```python
import torch

def _softmax(x, axis=-1):
    return torch.softmax(x, dim=axis)
def _gelu(x):
    return torch.nn.functional.gelu(x, approximate="tanh")
def _silu(x):
    return torch.nn.functional.silu(x)
def _sigmoid(x):
    return torch.sigmoid(x)
def _layernorm(x, axis=-1, eps=1e-5):
    m = x.mean(dim=axis, keepdim=True)
    v = x.var(dim=axis, keepdim=True, unbiased=False)
    return (x - m) / torch.sqrt(v + eps)
def _scatter_add(tgt, idx, src, axis=0):
    return tgt.clone().index_add_(axis, idx, src)
def _segment_sum(src, seg, n):
    out = torch.zeros((n,) + src.shape[1:], dtype=src.dtype)
    return out.index_add_(0, seg, src)
def _pad(x, pw):
    flat = tuple(v for pair in reversed(pw) for v in pair)
    return torch.nn.functional.pad(x, flat)

kernel_name = 'recurrent_scan__sliding_block_cached__dense_activation_silu__prompt'

def setup_inputs():

    _w = (lambda a: (lambda t: t.to(torch.bfloat16) if t.is_floating_point() else t)(torch.as_tensor(a)))
    def _A_setup():
        B, Sq, D = 1, 2048, 1024
        Hq, Hkv, Sk, Dh = 32, 4, 2048, 128
        assert Hq % Hkv == 0, (Hq, Hkv)
        return {
            "x": _w((lambda *s: torch.randn(*s, dtype=torch.bfloat16) * 0.05)(B, Sq, D)),
            "k_cache": _w((lambda *s: torch.randn(*s, dtype=torch.bfloat16) * 3.2)(B, Hkv, Sk, Dh)),
            "v_cache": _w((lambda *s: torch.randn(*s, dtype=torch.bfloat16) * 3.2)(B, Hkv, Sk, Dh)),
            "wq": _w((lambda *s: torch.randn(*s, dtype=torch.bfloat16) * 3.2)(D, Hq * Dh) * (D ** -0.5)),
            "wo": _w((lambda *s: torch.randn(*s, dtype=torch.bfloat16) * 3.2)(Hq * Dh, D) * ((Hq * Dh) ** -0.5)),
        }
    _A_in = _A_setup()

    def _B_setup():
        B, S, D, F = 1, 2048, 1024, 3584
        return {
            "x": _w((lambda *s: torch.randn(*s, dtype=torch.bfloat16) * 0.05)(B, S, D)),
            "w1": _w((lambda *s: torch.randn(*s, dtype=torch.bfloat16) * 3.2)(D, F) * (D ** -0.5)),
            "w2": _w((lambda *s: torch.randn(*s, dtype=torch.bfloat16) * 0.4)(F, D) * (F ** -0.5)),
        }
    _B_in = _B_setup()

    return {
        "A_x": _A_in["x"],
        "A_k_cache": _A_in["k_cache"],
        "A_v_cache": _A_in["v_cache"],
        "A_wq": _A_in["wq"],
        "A_wo": _A_in["wo"],
        "B_x": _B_in["x"],
        "B_w1": _B_in["w1"],
        "B_w2": _B_in["w2"],
    }

def reference(A_x, A_k_cache, A_v_cache, A_wq, A_wo, B_x, B_w1, B_w2):
    def _A_ref(x, k_cache, v_cache, wq, wo):
        B, Sq, D = x.shape
        Hq, Dh = 32, 128
        Hkv, Sk = k_cache.shape[1], k_cache.shape[2]
        W = min(4096, max(256, Sk // 4))
        rep = Hq // Hkv
        q = (lambda x, *d: x.permute(*d))((x @ wq).reshape(B, Sq, Hq, Dh), 0, 2, 1, 3)
        k = (lambda x, n, axis: torch.repeat_interleave(x, n, dim=axis))(k_cache[:, :, Sk - W:, :], rep, axis=1)
        v = (lambda x, n, axis: torch.repeat_interleave(x, n, dim=axis))(v_cache[:, :, Sk - W:, :], rep, axis=1)
        scores = torch.einsum("bhqd,bhkd->bhqk", q, k) * (Dh ** -0.5)
        p = _softmax(scores, axis=-1)
        o = torch.einsum("bhqk,bhkd->bhqd", p, v)
        return (lambda x, *d: x.permute(*d))(o, 0, 2, 1, 3).reshape(B, Sq, Hq * Dh) @ wo

    def _B_ref(x, w1, w2):
        h = torch.einsum("bsd,df->bsf", x, w1)
        h = _silu(h)
        out = torch.einsum("bsf,fd->bsd", h, w2)
        return out

    x = A_x
    xn = _layernorm(x, axis=-1)
    s = _A_ref(xn, A_k_cache, A_v_cache, A_wq, A_wo)
    g = _silu(_B_ref(xn, B_w1, B_w2))
    return x + s * g
```

```cpp
#include <hip/hip_runtime.h>
#include <hip/hip_bf16.h>
#include <stdint.h>

typedef unsigned short u16;
typedef short bf16x8 __attribute__((ext_vector_type(8)));
typedef short s16x4 __attribute__((ext_vector_type(4)));
typedef float f32x16 __attribute__((ext_vector_type(16)));
typedef float f32x2 __attribute__((ext_vector_type(2)));
typedef __bf16 bf16x2_t __attribute__((ext_vector_type(2)));
typedef unsigned u32x4 __attribute__((ext_vector_type(4)));
#define LAS __attribute__((address_space(3)))

constexpr int S_ = 2048, D_ = 1024, HQ = 32, HKV = 4, DH = 128, SK = 2048, WIN = 512, F_ = 3584;
constexpr size_t WS_CNT = 0;
constexpr size_t WS_XN  = 65536;
constexpr size_t WS_KV  = WS_XN + (size_t)S_ * D_ * 2;
constexpr size_t WS_O   = WS_KV + (size_t)HKV * WIN * DH * 2 * 2;
constexpr size_t WS_H   = WS_O + (size_t)S_ * HQ * DH * 2;
constexpr size_t WS_G   = WS_H + (size_t)S_ * F_ * 2;
constexpr size_t WS_END = WS_G + (size_t)S_ * D_ * 2 * 2;

__device__ __forceinline__ float bf2f(u16 v) { return __uint_as_float(((unsigned)v) << 16); }
__device__ __forceinline__ unsigned cvtpk(float lo, float hi) {
  f32x2 v = {lo, hi}; bf16x2_t b = __builtin_convertvector(v, bf16x2_t); return __builtin_bit_cast(unsigned, b);
}
__device__ __forceinline__ float rbf(float x) { return __uint_as_float(cvtpk(x, x) << 16); }
__device__ __forceinline__ int swap23(int i) { return (i & ~12) | ((i & 4) << 1) | ((i & 8) >> 1); }
__device__ __forceinline__ float silu_f(float x) { return x * __builtin_amdgcn_rcpf(1.0f + __builtin_amdgcn_exp2f(-1.4426950408889634f * x)); }

__global__ __launch_bounds__(256) void prep_kernel(const u16* __restrict__ x, const u16* __restrict__ kc, const u16* __restrict__ vc, char* __restrict__ ws) {
  __shared__ __attribute__((aligned(16))) char sm[64 * 272];
  const int tid = threadIdx.x, lane = tid & 63, w = tid >> 6, b = blockIdx.x;
  if (b < 256) {
    const int r = lane & 7, cg = lane >> 3, row = b * 8 + r;
    float* red = (float*)sm;
    u32x4 raw[4]; float v[32];
#pragma unroll
    for (int i = 0; i < 4; ++i) raw[i] = *(const u32x4*)(x + (size_t)row * D_ + (32 * w + cg + 8 * i) * 8);
    float s = 0.f;
#pragma unroll
    for (int i = 0; i < 4; ++i)
#pragma unroll
      for (int e = 0; e < 4; ++e) { v[i * 8 + 2 * e] = __uint_as_float(raw[i][e] << 16); v[i * 8 + 2 * e + 1] = __uint_as_float(raw[i][e] & 0xffff0000u); s += v[i * 8 + 2 * e] + v[i * 8 + 2 * e + 1]; }
    s += __shfl_xor(s, 8); s += __shfl_xor(s, 16); s += __shfl_xor(s, 32);
    if (lane < 8) red[w * 8 + r] = s;
    __syncthreads();
    const float mean = (red[r] + red[8 + r] + red[16 + r] + red[24 + r]) * (1.0f / D_);
    float q = 0.f;
#pragma unroll
    for (int i = 0; i < 32; ++i) { const float d = v[i] - mean; q += d * d; }
    q += __shfl_xor(q, 8); q += __shfl_xor(q, 16); q += __shfl_xor(q, 32);
    if (lane < 8) red[32 + w * 8 + r] = q;
    __syncthreads();
    const float var = (red[32 + r] + red[40 + r] + red[48 + r] + red[56 + r]) * (1.0f / D_);
    const float mb = rbf(mean), den = rbf(sqrtf(rbf(rbf(var) + 1e-5f)));
    char* xn = ws + WS_XN;
    const int tt = row >> 5, tok = row & 31;
#pragma unroll
    for (int i = 0; i < 4; ++i) {
      const int c = 32 * w + cg + 8 * i;
      u32x4 o;
#pragma unroll
      for (int e = 0; e < 4; ++e) o[e] = cvtpk(rbf(v[i * 8 + 2 * e] - mb) / den, rbf(v[i * 8 + 2 * e + 1] - mb) / den);
      *(u32x4*)(xn + ((size_t)tt * 64 + (c >> 1)) * 1024 + (c & 1) * 512 + tok * 16) = o;
    }
    if (b == 0 && tid < 128) *(unsigned*)(ws + WS_CNT + tid * 256) = 0u;
  } else {
    const int bb = b - 256, isv = bb & 1, tile = (bb >> 1) & 7, kvh = bb >> 4;
    const u16* src = (isv ? vc : kc) + ((size_t)kvh * SK + (SK - WIN) + tile * 64) * DH;
    char* dst = ws + WS_KV + ((size_t)(kvh * 8 + tile) * 2 + isv) * 16384;
#pragma unroll
    for (int i = 0; i < 4; ++i) { const int c = tid + 256 * i, row = c >> 4, ch = c & 15; *(u32x4*)(sm + row * 272 + ch * 16) = *(const u32x4*)(src + row * DH + ch * 8); }
    __syncthreads();
    const int i31 = lane & 31, h = lane >> 5;
#pragma unroll
    for (int i = 0; i < 4; ++i) {
      const int f = w + 4 * i;
      u32x4 o;
      if (!isv) {
        const int kt = f >> 3, ds = f & 7;
        o = *(const u32x4*)(sm + (32 * kt + swap23(i31)) * 272 + (16 * ds + 8 * h) * 2);
      } else {
        const int kst = f >> 2, dt = f & 3;
        const u16* p = (const u16*)(sm + (16 * kst + 8 * h) * 272) + 32 * dt + swap23(i31);
#pragma unroll
        for (int e = 0; e < 4; ++e) o[e] = (unsigned)p[(2 * e) * 136] | ((unsigned)p[(2 * e + 1) * 136] << 16);
      }
      *(u32x4*)(dst + f * 1024 + lane * 16) = o;
    }
  }
}

__device__ __forceinline__ bf16x8 trfrag(LAS char* p) {
  s16x4 a = __builtin_amdgcn_ds_read_tr16_b64_v4i16((LAS s16x4*)p);
  s16x4 b = __builtin_amdgcn_ds_read_tr16_b64_v4i16((LAS s16x4*)(p + 256));
  return (bf16x8){a[0], a[1], a[2], a[3], b[0], b[1], b[2], b[3]};
}
__device__ __forceinline__ bf16x8 pack8(const f32x16& a, int s, float sc) {
  u32x4 w = {cvtpk(a[8 * s] * sc, a[8 * s + 1] * sc), cvtpk(a[8 * s + 2] * sc, a[8 * s + 3] * sc), cvtpk(a[8 * s + 4] * sc, a[8 * s + 5] * sc), cvtpk(a[8 * s + 6] * sc, a[8 * s + 7] * sc)};
  return __builtin_bit_cast(bf16x8, w);
}
#define MFMA(a, b, c) __builtin_amdgcn_mfma_f32_32x32x16_bf16(a, b, c, 0, 0, 0)

__global__ __launch_bounds__(256, 1) void k1_ref(const u16* __restrict__ wq, const u16* __restrict__ w1, char* __restrict__ ws) {
  __shared__ __attribute__((aligned(16))) char sm[32768];
  const int tid = threadIdx.x, lane = tid & 63, w = tid >> 6, b = blockIdx.x;
  const int xcd = b & 7, idx = b >> 3, j = 4 * xcd + (idx & 3), m = idx >> 2;
  const int h = lane >> 5, p = lane & 3, q4 = (lane >> 2) & 3, blk = (lane >> 4) & 1;
  const int odd = j & 1, pr = j >> 1;
  const char* xn = ws + WS_XN;
  int tt[2]; tt[0] = 8 * m + 2 * w + odd; tt[1] = 8 * m + 2 * w + (odd ^ 1);
  f32x16 acc[8][2];
#pragma unroll
  for (int c = 0; c < 8; ++c) { acc[c][0] = f32x16{}; acc[c][1] = f32x16{}; }
  LAS char* rd = (LAS char*)sm + h * 4096 + q4 * 64 + blk * 32 + (p & 1) * 16 + (p >> 1) * 8;
  const int lt = 2 * (w & 1) + (lane >> 5);
  const u16* src; size_t rs;
  if (w < 2) { src = wq + j * 128 + lt * 32 + (lane & 3) * 8; rs = 4096; }
  else { const int gt = 7 * pr + (odd ? (lt == 3 ? 3 : 4 + lt) : lt); src = w1 + gt * 32 + (lane & 3) * 8; rs = F_; }
  src += (size_t)((lane & 31) >> 2) * rs;
  for (int st = 0; st < 16; ++st) {
    __syncthreads();
#pragma unroll
    for (int kg = 0; kg < 8; ++kg) *(u32x4*)(sm + kg * 4096 + w * 1024 + lane * 16) = *(const u32x4*)(src + (size_t)(st * 64 + kg * 8) * rs);
    __syncthreads();
#pragma unroll
    for (int ks = 0; ks < 4; ++ks) {
      bf16x8 xb[2];
#pragma unroll
      for (int u = 0; u < 2; ++u) xb[u] = *(const bf16x8*)(xn + ((size_t)tt[u] * 64 + st * 4 + ks) * 1024 + lane * 16);
#pragma unroll
      for (int c = 0; c < 8; ++c) {
        bf16x8 a = trfrag(rd + ks * 8192 + c * 512);
        acc[c][0] = MFMA(a, xb[0], acc[c][0]);
        if (c != 7) acc[c][1] = MFMA(a, xb[1], acc[c][1]);
      }
    }
  }
  char* hb = ws + WS_H;
#pragma unroll
  for (int c = 4; c < 8; ++c) {
    const int l2 = c - 4, gt = 7 * pr + (odd ? (l2 == 3 ? 3 : 4 + l2) : l2);
#pragma unroll
    for (int u = 0; u < 2; ++u) {
      if (c == 7 && u == 1) continue;
      f32x16 t;
#pragma unroll
      for (int r = 0; r < 16; ++r) t[r] = silu_f(rbf(acc[c][u][r]));
#pragma unroll
      for (int s = 0; s < 2; ++s) *(bf16x8*)(hb + ((size_t)tt[u] * 224 + 2 * gt + s) * 1024 + lane * 16) = pack8(t, s, 1.0f);
    }
  }
  const float CS = 0.088388347648318440f * 1.4426950408889634f;
  bf16x8 qf[2][8];
#pragma unroll
  for (int u = 0; u < 2; ++u)
#pragma unroll
    for (int c = 0; c < 4; ++c) { qf[u][2 * c] = pack8(acc[c][u], 0, CS); qf[u][2 * c + 1] = pack8(acc[c][u], 1, CS); }
  const char* kv = ws + WS_KV + (size_t)(j >> 3) * 8 * 32768;
  f32x16 o[4][2];
#pragma unroll
  for (int c = 0; c < 4; ++c) { o[c][0] = f32x16{}; o[c][1] = f32x16{}; }
  float mrun[2] = {-1e30f, -1e30f}, lsum[2] = {0.f, 0.f};
  for (int t = 0; t < 8; ++t) {
    __syncthreads();
#pragma unroll
    for (int i = 0; i < 8; ++i) *(u32x4*)(sm + i * 4096 + tid * 16) = *(const u32x4*)(kv + (size_t)t * 32768 + i * 4096 + tid * 16);
    __syncthreads();
    f32x16 s[2][2];
#pragma unroll
    for (int kt = 0; kt < 2; ++kt) { s[kt][0] = f32x16{}; s[kt][1] = f32x16{};
#pragma unroll
      for (int ds = 0; ds < 8; ++ds) { bf16x8 a = *(const bf16x8*)(sm + (kt * 8 + ds) * 1024 + lane * 16);
        s[kt][0] = MFMA(a, qf[0][ds], s[kt][0]); s[kt][1] = MFMA(a, qf[1][ds], s[kt][1]); } }
    bf16x8 pf[2][4];
#pragma unroll
    for (int u = 0; u < 2; ++u) {
      float mx = s[0][u][0];
#pragma unroll
      for (int r = 0; r < 16; ++r) { mx = fmaxf(mx, s[0][u][r]); mx = fmaxf(mx, s[1][u][r]); }
      mx = fmaxf(mx, __shfl_xor(mx, 32));
      const float mn = fmaxf(mrun[u], mx), al = __builtin_amdgcn_exp2f(mrun[u] - mn); mrun[u] = mn;
      float ps = 0.f;
#pragma unroll
      for (int kt = 0; kt < 2; ++kt)
#pragma unroll
        for (int r = 0; r < 16; ++r) { s[kt][u][r] = __builtin_amdgcn_exp2f(s[kt][u][r] - mn); ps += s[kt][u][r]; }
      lsum[u] = lsum[u] * al + ps;
#pragma unroll
      for (int c = 0; c < 4; ++c) o[c][u] *= al;
#pragma unroll
      for (int kt = 0; kt < 2; ++kt) { pf[u][2 * kt] = pack8(s[kt][u], 0, 1.0f); pf[u][2 * kt + 1] = pack8(s[kt][u], 1, 1.0f); }
    }
#pragma unroll
    for (int kst = 0; kst < 4; ++kst)
#pragma unroll
      for (int dt = 0; dt < 4; ++dt) { bf16x8 a = *(const bf16x8*)(sm + 16384 + (kst * 4 + dt) * 1024 + lane * 16);
        o[dt][0] = MFMA(a, pf[0][kst], o[dt][0]); o[dt][1] = MFMA(a, pf[1][kst], o[dt][1]); }
  }
  char* ob = ws + WS_O;
#pragma unroll
  for (int u = 0; u < 2; ++u) {
    const float l = lsum[u] + __shfl_xor(lsum[u], 32), rl = 1.0f / l;
#pragma unroll
    for (int dt = 0; dt < 4; ++dt)
#pragma unroll
      for (int s = 0; s < 2; ++s) *(bf16x8*)(ob + ((size_t)tt[u] * 256 + j * 8 + 2 * dt + s) * 1024 + lane * 16) = pack8(o[dt][u], s, rl);
  }
}

__global__ __launch_bounds__(256, 1) void k2_ref(const u16* __restrict__ x, const u16* __restrict__ wo, const u16* __restrict__ w2, const char* __restrict__ ws, u16* __restrict__ out) {
  __shared__ __attribute__((aligned(16))) char sm[16384];
  const int tid = threadIdx.x, lane = tid & 63, w = tid >> 6, b = blockIdx.x;
  const int nb = b & 7, tb = b >> 3;
  const int h = lane >> 5, p = lane & 3, q4 = (lane >> 2) & 3, blk = (lane >> 4) & 1;
  LAS char* rd = (LAS char*)sm + h * 2048 + q4 * 64 + blk * 32 + (p & 1) * 16 + (p >> 1) * 8;
  f32x16 acc[2][4];
  const int tt = 4 * tb + w;
#pragma unroll
  for (int g = 0; g < 2; ++g) {
    const u16* W = g ? w2 : wo; const int KD = g ? F_ : 4096; const char* act = ws + (g ? WS_H : WS_O) + (size_t)tt * (KD / 16) * 1024;
#pragma unroll
    for (int c = 0; c < 4; ++c) acc[g][c] = f32x16{};
    const u16* src = W + nb * 128 + (2 * (w & 1) + (lane >> 5)) * 32 + (lane & 3) * 8 + (size_t)((lane & 31) >> 2) * D_;
    for (int st = 0; st < KD / 64; ++st) {
      __syncthreads();
#pragma unroll
      for (int i = 0; i < 4; ++i) { const int kg = (w >> 1) + 2 * i; *(u32x4*)(sm + kg * 2048 + (w & 1) * 1024 + lane * 16) = *(const u32x4*)(src + (size_t)(st * 64 + kg * 8) * D_); }
      __syncthreads();
#pragma unroll
      for (int ks = 0; ks < 4; ++ks) {
        bf16x8 xb = *(const bf16x8*)(act + (size_t)(st * 4 + ks) * 1024 + lane * 16);
#pragma unroll
        for (int c = 0; c < 4; ++c) acc[g][c] = MFMA(trfrag(rd + ks * 4096 + c * 512), xb, acc[g][c]);
      }
    }
  }
  const int tok = tt * 32 + (lane & 31);
#pragma unroll
  for (int c = 0; c < 4; ++c)
#pragma unroll
    for (int s = 0; s < 2; ++s) {
      const size_t off = (size_t)tok * D_ + nb * 128 + 32 * c + 16 * s + 8 * h;
      u32x4 xv = *(const u32x4*)(x + off); u32x4 ov;
#pragma unroll
      for (int e = 0; e < 4; ++e) {
        float r2[2];
#pragma unroll
        for (int z = 0; z < 2; ++z) { const int r = 8 * s + 2 * e + z; const float sv = rbf(acc[0][c][r]), gv = rbf(silu_f(rbf(acc[1][c][r])));
          const float xf = z ? __uint_as_float(xv[e] & 0xffff0000u) : __uint_as_float(xv[e] << 16); r2[z] = xf + rbf(sv * gv); }
        ov[e] = cvtpk(r2[0], r2[1]);
      }
      *(u32x4*)(out + off) = ov;
    }
}

extern "C" void kernel_launch(void* const* d_in, const int* in_sizes, int n_in, void* d_out, int out_size, void* d_ws, size_t ws_size, hipStream_t stream) {
  const u16* x = (const u16*)d_in[0]; const u16* kc = (const u16*)d_in[1]; const u16* vc = (const u16*)d_in[2];
  const u16* wq = (const u16*)d_in[3]; const u16* wo = (const u16*)d_in[4]; const u16* w1 = (const u16*)d_in[6]; const u16* w2 = (const u16*)d_in[7];
  char* ws = (char*)d_ws;
  prep_kernel<<<256 + 64, 256, 0, stream>>>(x, kc, vc, ws);
  k1_ref<<<256, 256, 0, stream>>>(wq, w1, ws);
  k2_ref<<<128, 256, 0, stream>>>(x, wo, w2, ws, (u16*)d_out);
}
```

```cpp
#include <hip/hip_runtime.h>
#include <stdint.h>

typedef unsigned short u16;
typedef float f32x2 __attribute__((ext_vector_type(2)));
typedef __bf16 bf16x2_t __attribute__((ext_vector_type(2)));
typedef unsigned u32x4 __attribute__((ext_vector_type(4)));
typedef unsigned u32x8 __attribute__((ext_vector_type(8)));
#define LAS __attribute__((address_space(3)))

constexpr int S_ = 2048, D_ = 1024, HKV = 4, DH = 128, SK = 2048, WIN = 512, F_ = 3584;
constexpr unsigned XN_BS = (D_ / 32) * 4096 + 4096, O_BS = (4096 / 32) * 4096 + 4096 * 9, H_BS = (F_ / 32) * 4096 + 4096;
constexpr size_t WS_CNT = 0;
constexpr size_t WS_RDY = 65536;
constexpr size_t WS_XN  = 81920;
constexpr size_t WS_KV  = WS_XN + (size_t)32 * XN_BS;
constexpr size_t WS_O   = WS_KV + (size_t)HKV * WIN * DH * 2 * 2;
constexpr size_t WS_H   = WS_O + (size_t)32 * O_BS;
constexpr size_t WS_G   = WS_H + (size_t)32 * H_BS;
constexpr size_t WS_END = WS_G + (size_t)512 * 24576;

constexpr float KSCALE = 0.088388347648318440f * 1.4426950408889634f;
__device__ __forceinline__ unsigned cvtpk(float lo, float hi) {
  f32x2 v = {lo, hi}; bf16x2_t b = __builtin_convertvector(v, bf16x2_t); return __builtin_bit_cast(unsigned, b);
}
__device__ __forceinline__ float rbf(float x) { return __uint_as_float(cvtpk(x, x) << 16); }
__device__ __forceinline__ int ilv(int i, int t) { return 8 * (i >> 2) + 4 * t + (i & 3); }

__global__ __launch_bounds__(256) void prep_kernel(const u16* __restrict__ x, const u16* __restrict__ kc, const u16* __restrict__ vc, char* ws) {
  __shared__ __attribute__((aligned(16))) char sm[8 * 2064];
  typedef __attribute__((address_space(1))) const char* gptr;
  const int tid = threadIdx.x, lane = tid & 63, w = tid >> 6, b = blockIdx.x;
  const char *c0, *c1;
  asm volatile("s_getpc_b64 s[20:21]\n\ts_add_u32 s20, s20, k_code_begin@rel32@lo+4\n\ts_addc_u32 s21, s21, k_code_begin@rel32@hi+12\n\ts_mov_b64 %0, s[20:21]\n\t"
               "s_getpc_b64 s[20:21]\n\ts_add_u32 s20, s20, k_code_end@rel32@lo+4\n\ts_addc_u32 s21, s21, k_code_end@rel32@hi+12\n\ts_mov_b64 %1, s[20:21]" : "=&s"(c0), "=&s"(c1) : : "scc", "s20", "s21");
  { const char* pc; asm volatile("s_getpc_b64 %0" : "=s"(pc));
    c0 = (const char*)(((size_t)(c0 - pc) < 8192 ? (size_t)pc : (size_t)c0) & ~(size_t)15); }
  const int bb = b - 256, isv = bb & 1, st = (bb >> 1) & 15, kvh = (bb >> 5) & 3;
  const bool isln = b < 256, iskv = !isln && b < 384;
  const unsigned mln = 0u - (unsigned)isln, mkv = 0u - (unsigned)iskv, mcd = ~(mln | mkv);
  const unsigned lim = mln | (mkv & 8176u) | (mcd & ((unsigned)(c1 - c0) - 16u) & ~15u);
  const unsigned off0 = (mln & ((tid >> 5) * 2048 + (lane & 31) * 16)) | (~mln & (tid * 16 + (mcd & (((b - 384) >> 3) * 12288)))), stp = 4096 - (mln & 3584);
  const unsigned boff = (mln & (b * 16384)) | (mkv & ((kvh * SK + (SK - WIN) + st * 32) * (DH * 2)));
  unsigned o4[4];
#pragma unroll
  for (int i = 0; i < 4; ++i) { const unsigned o = off0 + i * stp; o4[i] = (o < lim ? o : lim) + boff; }
  size_t px = (size_t)x, pk = (size_t)kc, pv = (size_t)vc;
  asm volatile("" : "+v"(o4[0]), "+v"(o4[1]), "+v"(o4[2]), "+v"(o4[3]));
  asm volatile("" : "+s"(px), "+s"(pk), "+s"(pv), "+s"(ws));
  const size_t m1 = (size_t)(long)(int)mln, m2 = (size_t)(long)(int)(mkv & (0u - (unsigned)isv)), m3 = (size_t)(long)(int)(mkv & ((unsigned)isv - 1u)), m4 = (size_t)(long)(int)mcd;
  const gptr src = (gptr)((px & m1) | (pv & m2) | (pk & m3) | ((size_t)c0 & m4));
  u32x4 raw[4];
#pragma unroll
  for (int i = 0; i < 4; ++i) raw[i] = *(const __attribute__((address_space(1))) u32x4*)(src + o4[i]);
  __builtin_amdgcn_sched_barrier(0);
  auto st16 = [](char* p, u32x4 o) { asm volatile("global_store_dwordx4 %0, %1, off sc1\n\ts_nop 1" :: "v"(p), "v"(o) : "memory"); };
  if (__builtin_expect(isln, 1)) {
    f32x2 v[16], s2 = {0.f, 0.f};
#pragma unroll
    for (int i = 0; i < 4; ++i)
#pragma unroll
      for (int e = 0; e < 4; ++e) { v[i * 4 + e] = (f32x2){__uint_as_float(raw[i][e] << 16), __uint_as_float(raw[i][e] & 0xffff0000u)}; s2 += v[i * 4 + e]; }
    auto rsum = [](float a) {
      a += __uint_as_float(__builtin_amdgcn_mov_dpp(__float_as_uint(a), 0xB1, 0xf, 0xf, true));
      a += __uint_as_float(__builtin_amdgcn_mov_dpp(__float_as_uint(a), 0x4E, 0xf, 0xf, true));
      a += __uint_as_float(__builtin_amdgcn_mov_dpp(__float_as_uint(a), 0x124, 0xf, 0xf, true));
      a += __uint_as_float(__builtin_amdgcn_mov_dpp(__float_as_uint(a), 0x128, 0xf, 0xf, true));
      auto p = __builtin_amdgcn_permlane16_swap(__float_as_uint(a), __float_as_uint(a), false, false);
      return __uint_as_float(p[0]) + __uint_as_float(p[1]); };
    const float mean = rsum(s2[0] + s2[1]) * (1.0f / D_);
    f32x2 q2 = {0.f, 0.f};
#pragma unroll
    for (int i = 0; i < 16; ++i) { v[i] -= mean; q2 += v[i] * v[i]; }
    const float rstd = __builtin_amdgcn_rcpf(rbf(__builtin_amdgcn_sqrtf(rbf(rbf(rsum(q2[0] + q2[1]) * (1.0f / D_)) + 1e-5f)))), sh = mean - rbf(mean);
#pragma unroll
    for (int i = 0; i < 4; ++i) {
      u32x4 o;
#pragma unroll
      for (int e = 0; e < 4; ++e) { const f32x2 d = v[i * 4 + e] + sh; const unsigned d2 = cvtpk(d[0], d[1]);
        const f32x2 t = (f32x2){__uint_as_float(d2 << 16), __uint_as_float(d2 & 0xffff0000u)} * rstd; o[e] = cvtpk(t[0], t[1]); }
      *(u32x4*)(sm + (tid >> 5) * 2064 + (lane & 31) * 16 + i * 512) = o;
    }
    __syncthreads();
    const int row = b * 8 + (lane & 7), c = tid >> 3;
    char* xn = ws + WS_XN + (size_t)(row >> 6) * XN_BS + ((row >> 4) & 3) * 1024 + (row & 15) * 16 + (c >> 2) * 4096 + (c & 3) * 256;
    u32x4 o[4];
#pragma unroll
    for (int i = 0; i < 4; ++i) o[i] = *(const u32x4*)(sm + (lane & 7) * 2064 + c * 16 + i * 512);
#pragma unroll
    for (int i = 0; i < 4; ++i) st16(xn + i * 32768, o[i]);
    if (b < 2) *(__attribute__((address_space(1))) unsigned*)(ws + WS_CNT + (b * 256 + tid) * 128) = 0u;
    if (b >= 2 && b < 4) *(__attribute__((address_space(1))) unsigned*)(ws + WS_RDY + (((b - 2) * 256 + tid) >> 3) * 128 + (tid & 7) * 4) = 0u;
  } else if (iskv) {
    const int kb = st & 1;
    char* dst = ws + WS_KV + (size_t)(kvh * 8 + (st >> 1)) * 32768 + isv * 16384;
#pragma unroll
    for (int i = 0; i < 2; ++i) *(u32x4*)(sm + ((tid >> 4) + 16 * i) * 272 + (tid & 15) * 16) = raw[i];
    __syncthreads();
    const int i15 = lane & 15, g = lane >> 4;
#pragma unroll
    for (int i = 0; i < 2; ++i) {
      const int f = w + 4 * i;
      u32x4 o; int fi;
      if (!isv) {
        const int ds = f & 3, t = f >> 2; fi = (2 * kb + t) * 4 + ds;
        o = *(const u32x4*)(sm + ilv(i15, t) * 272 + (32 * ds + 8 * g) * 2);
#pragma unroll
        for (int e = 0; e < 4; ++e) o[e] = cvtpk(__uint_as_float(o[e] << 16) * KSCALE, __uint_as_float(o[e] & 0xffff0000u) * KSCALE);
      } else {
        fi = f * 2 + kb;
        const u16* p = (const u16*)(sm + (8 * g) * 272) + 32 * (f >> 1) + ilv(i15, f & 1);
#pragma unroll
        for (int e = 0; e < 4; ++e) o[e] = (unsigned)p[(2 * e) * 136] | ((unsigned)p[(2 * e + 1) * 136] << 16);
      }
      st16(dst + fi * 1024 + lane * 16, o);
    }
  } else {
    asm volatile("" :: "v"(raw[0]), "v"(raw[1]), "v"(raw[2]), "v"(raw[3]));
  }
}

__global__ __launch_bounds__(256, 1) __attribute__((aligned(4096))) void main_kernel(const u16* __restrict__ x, const u16* __restrict__ wq, const u16* __restrict__ w1, const u16* __restrict__ wo,
                                                      const u16* __restrict__ w2, char* __restrict__ ws, u16* __restrict__ out) {
  __shared__ __attribute__((aligned(64))) char sm[163840];
  int lane = threadIdx.x & 63, w = __builtin_amdgcn_readfirstlane(threadIdx.x >> 6), b = blockIdx.x;
  const unsigned sm0 = (unsigned)(size_t)(LAS char*)sm;
  {
    const unsigned rs = 8192u - (w >> 1) * (8192u - F_ * 2);
    const unsigned sWstep = 8 * rs, sLds = sm0 + w * 1024, vL16 = lane * 16;
    unsigned vW, vX, sWk = 0u - 8 * sWstep, sX = 0u - 4096u, tb2; u32x8 tb0, tb1;
    {
      const int idx = b >> 3, j = 4 * (b & 7) + (idx & 3), m = idx >> 2, odd = j & 1;
      const int lb = 2 * (w & 1) + (lane >> 5), ch = lane & 3;
      const int nq = w >> 1, sp = 4 - (nq & odd);
      const int c0 = j * 128 - nq * 32 * (j >> 1), mk = -(int)(lb == sp);
      const int col = c0 + ((((ch & 1) * 8 - 16) & mk) | ((lb * 32 + ch * 8) & ~mk));
      vW = ((lane & 31) >> 2) * rs + col * 2; vX = (unsigned)WS_XN + (4 * m + w) * XN_BS + vL16;
    }
    asm volatile(
    "k_code_begin:\n\t"
    "s_lshl_b32 s83, %[sWstep], 3\n\t"
    "s_xor_b32 s82, %[sRw], 0\n\t"
    "s_mul_i32 s84, s82, 4104\n\t"
    "s_mul_i32 s92, s82, %[sWstep]\n\t"
    "s_add_u32 s84, s84, %[sLds]\n\t"
    "s_xor_b32 s82, %[sRw], 1\n\t"
    "s_mul_i32 s85, s82, 4104\n\t"
    "s_mul_i32 s93, s82, %[sWstep]\n\t"
    "s_add_u32 s85, s85, %[sLds]\n\t"
    "s_xor_b32 s82, %[sRw], 2\n\t"
    "s_mul_i32 s86, s82, 4104\n\t"
    "s_mul_i32 s94, s82, %[sWstep]\n\t"
    "s_add_u32 s86, s86, %[sLds]\n\t"
    "s_xor_b32 s82, %[sRw], 3\n\t"
    "s_mul_i32 s87, s82, 4104\n\t"
    "s_mul_i32 s95, s82, %[sWstep]\n\t"
    "s_add_u32 s87, s87, %[sLds]\n\t"
    "s_xor_b32 s82, %[sRw], 4\n\t"
    "s_mul_i32 s88, s82, 4104\n\t"
    "s_mul_i32 s96, s82, %[sWstep]\n\t"
    "s_add_u32 s88, s88, %[sLds]\n\t"
    "s_xor_b32 s82, %[sRw], 5\n\t"
    "s_mul_i32 s89, s82, 4104\n\t"
    "s_mul_i32 s97, s82, %[sWstep]\n\t"
    "s_add_u32 s89, s89, %[sLds]\n\t"
    "s_xor_b32 s82, %[sRw], 6\n\t"
    "s_mul_i32 s90, s82, 4104\n\t"
    "s_mul_i32 s98, s82, %[sWstep]\n\t"
    "s_add_u32 s90, s90, %[sLds]\n\t"
    "s_xor_b32 s82, %[sRw], 7\n\t"
    "s_mul_i32 s91, s82, 4104\n\t"
    "s_mul_i32 s99, s82, %[sWstep]\n\t"
    "s_add_u32 s91, s91, %[sLds]\n\t"
    "s_nop 0\n\t"
      : "=&{s[84:91]}"(tb0), "=&{s[92:99]}"(tb1), "=&{s83}"(tb2)
      : [sWstep] "s"(sWstep), [sLds] "s"(sLds), [sRw] "s"(b >> 5), "s"(sWk), "s"(sX), "v"(vW), "v"(vX) : "scc", "s82");
    __builtin_amdgcn_sched_barrier(0);
    const u16* W = w < 2 ? wq : w1;
    const u32x4 srdW = {(unsigned)(size_t)W, (unsigned)((size_t)W >> 32) & 0xffffu, rs * D_, 0x20000u};
    const u32x4 srdWS = {(unsigned)(size_t)ws, (unsigned)((size_t)ws >> 32) & 0xffffu, (unsigned)WS_END, 0x20000u};
    asm volatile(
    "s_waitcnt lgkmcnt(0)\n\t"
    "s_nop 4\n\t"
    "s_add_u32 %[sWk], %[sWk], s83\n\t"
    "s_add_u32 m0, s84, 0\n\t"
    "s_add_u32 s82, %[sWk], s92\n\t"
    "buffer_load_dwordx4 %[vW], %[srdW], s82 offen lds\n\t"
    "s_add_u32 m0, s85, 0\n\t"
    "s_add_u32 s82, %[sWk], s93\n\t"
    "buffer_load_dwordx4 %[vW], %[srdW], s82 offen lds\n\t"
    "s_add_u32 m0, s86, 0\n\t"
    "s_add_u32 s82, %[sWk], s94\n\t"
    "buffer_load_dwordx4 %[vW], %[srdW], s82 offen lds\n\t"
    "s_add_u32 m0, s87, 0\n\t"
    "s_add_u32 s82, %[sWk], s95\n\t"
    "buffer_load_dwordx4 %[vW], %[srdW], s82 offen lds\n\t"
    "s_add_u32 m0, s88, 0\n\t"
    "s_add_u32 s82, %[sWk], s96\n\t"
    "buffer_load_dwordx4 %[vW], %[srdW], s82 offen lds\n\t"
    "s_add_u32 m0, s89, 0\n\t"
    "s_add_u32 s82, %[sWk], s97\n\t"
    "buffer_load_dwordx4 %[vW], %[srdW], s82 offen lds\n\t"
    "s_add_u32 m0, s90, 0\n\t"
    "s_add_u32 s82, %[sWk], s98\n\t"
    "buffer_load_dwordx4 %[vW], %[srdW], s82 offen lds\n\t"
    "s_add_u32 m0, s91, 0\n\t"
    "s_add_u32 s82, %[sWk], s99\n\t"
    "buffer_load_dwordx4 %[vW], %[srdW], s82 offen lds\n\t"
    "s_add_u32 %[sX], %[sX], 4096\n\t"
    "s_and_b32 %[sX], %[sX], 0x1ffff\n\t"
    "buffer_load_dwordx4 a[32:35], %[vX], %[srdWS], %[sX] offen offset:0\n\t"
    "buffer_load_dwordx4 a[36:39], %[vX], %[srdWS], %[sX] offen offset:1024\n\t"
    "buffer_load_dwordx4 a[40:43], %[vX], %[srdWS], %[sX] offen offset:2048\n\t"
    "buffer_load_dwordx4 a[44:47], %[vX], %[srdWS], %[sX] offen offset:3072\n\t"
    "s_add_u32 %[sX], %[sX], 4096\n\t"
    "buffer_load_dwordx4 a[48:51], %[vX], %[srdWS], %[sX] offen offset:0\n\t"
    "buffer_load_dwordx4 a[52:55], %[vX], %[srdWS], %[sX] offen offset:1024\n\t"
    "buffer_load_dwordx4 a[56:59], %[vX], %[srdWS], %[sX] offen offset:2048\n\t"
    "buffer_load_dwordx4 a[60:63], %[vX], %[srdWS], %[sX] offen offset:3072\n\t"
    "s_nop 0\n\t"
    "s_nop 0\n\t"
    "s_mov_b64 exec, 0\n\t"
    "s_cmp_eq_u32 %[wv], 0\n\t"
    "s_cbranch_scc1 k_pga0\n\t"
    "s_cmp_eq_u32 %[wv], 1\n\t"
    "s_cbranch_scc1 k_pga1\n\t"
    "s_cmp_eq_u32 %[wv], 2\n\t"
    "s_cbranch_scc1 k_pga2\n\t"
    "s_branch k_pga3\n\t"
    "k_pga_back:\n\t"
    "s_mov_b64 exec, -1\n\t"
    "s_add_u32 %[sWk], %[sWk], s83\n\t"
    "s_add_u32 m0, s84, 32832\n\t"
    "s_add_u32 s82, %[sWk], s92\n\t"
    "buffer_load_dwordx4 %[vW], %[srdW], s82 offen lds\n\t"
    "s_add_u32 m0, s85, 32832\n\t"
    "s_add_u32 s82, %[sWk], s93\n\t"
    "buffer_load_dwordx4 %[vW], %[srdW], s82 offen lds\n\t"
    "s_add_u32 m0, s86, 32832\n\t"
    "s_add_u32 s82, %[sWk], s94\n\t"
    "buffer_load_dwordx4 %[vW], %[srdW], s82 offen lds\n\t"
    "s_add_u32 m0, s87, 32832\n\t"
    "s_add_u32 s82, %[sWk], s95\n\t"
    "buffer_load_dwordx4 %[vW], %[srdW], s82 offen lds\n\t"
    "s_add_u32 m0, s88, 32832\n\t"
    "s_add_u32 s82, %[sWk], s96\n\t"
    "buffer_load_dwordx4 %[vW], %[srdW], s82 offen lds\n\t"
    "s_add_u32 m0, s89, 32832\n\t"
    "s_add_u32 s82, %[sWk], s97\n\t"
    "buffer_load_dwordx4 %[vW], %[srdW], s82 offen lds\n\t"
    "s_add_u32 m0, s90, 32832\n\t"
    "s_add_u32 s82, %[sWk], s98\n\t"
    "buffer_load_dwordx4 %[vW], %[srdW], s82 offen lds\n\t"
    "s_add_u32 m0, s91, 32832\n\t"
    "s_add_u32 s82, %[sWk], s99\n\t"
    "buffer_load_dwordx4 %[vW], %[srdW], s82 offen lds\n\t"
      : [sWk] "+&s"(sWk), [sX] "+&s"(sX), "+s"(b), [wv] "+s"(w), "+{s[84:91]}"(tb0), "+{s[92:99]}"(tb1), "+{s83}"(tb2)
      : [srdW] "s"(srdW), [srdWS] "s"(srdWS), [vW] "v"(vW), [vX] "v"(vX)
      : "memory", "scc", "s82",
      "a0","a1","a2","a3","a4","a5","a6","a7","a8","a9","a10","a11","a12","a13","a14","a15","a16","a17","a18","a19","a20","a21","a22","a23",
      "a24","a25","a26","a27","a28","a29","a30","a31","a32","a33","a34","a35","a36","a37","a38","a39","a40","a41","a42","a43","a44","a45","a46","a47",
      "a48","a49","a50","a51","a52","a53","a54","a55","a56","a57","a58","a59","a60","a61","a62","a63","a64","a65","a66","a67","a68","a69","a70","a71",
      "a72","a73","a74","a75","a76","a77","a78","a79","a80","a81","a82","a83","a84","a85","a86","a87","a88","a89","a90","a91","a92","a93","a94","a95",
      "a96","a97","a98","a99","a100","a101","a102","a103","a104","a105","a106","a107","a108","a109","a110","a111","a112","a113","a114","a115","a116","a117","a118","a119",
      "a120","a121","a122","a123","a124","a125","a126","a127","a128","a129","a130","a131","a132","a133","a134","a135","a136","a137","a138","a139","a140","a141","a142","a143",
      "a144","a145","a146","a147","a148","a149","a150","a151","a152","a153","a154","a155","a156","a157","a158","a159","a160","a161","a162","a163","a164","a165","a166","a167",
      "a168","a169","a170","a171","a172","a173","a174","a175","a176","a177","a178","a179","a180","a181","a182","a183","a184","a185","a186","a187","a188","a189","a190","a191",
      "a192","a193","a194","a195","a196","a197","a198","a199","a200","a201","a202","a203","a204","a205","a206","a207","a208","a209","a210","a211","a212","a213","a214","a215",
      "a216","a217","a218","a219","a220","a221","a222","a223","a224","a225","a226","a227","a228","a229","a230","a231","a232","a233","a234","a235","a236","a237","a238","a239",
      "a240","a241","a242","a243","a244","a245","a246","a247","a248","a249","a250","a251","a252","a253","a254","a255");
    asm volatile("" : "+v"(lane));
    const int idx = b >> 3, j = 4 * (b & 7) + (idx & 3), odd = j & 1, pr = j >> 1, g = lane >> 4, blk = 4 * (idx >> 2) + w;
    const unsigned vRdW = sm0 + g * 4104 + ((lane >> 2) & 3) * 64 + (lane & 3) * 16, vRdW2 = sm0 + g * 4104 + ((lane >> 2) & 3) * 64 + (lane & 3) * 8;
    const unsigned vH8 = ((lane & 15) + 16 * (2 * odd + (g >> 1))) * 16 + 8 * (g & 1);
    const unsigned sKV = (unsigned)WS_KV + (j >> 3) * 262144 + w * 4096;
    const unsigned sHa = (unsigned)WS_H + blk * H_BS + (7 * pr + 4 * odd) * 4096, sH2 = (unsigned)WS_H + blk * H_BS + (7 * pr + 3) * 4096;
    const unsigned sO = (unsigned)WS_O + blk * O_BS + j * 4 * 4096, sSig = (unsigned)WS_RDY + blk * 256 + (b & 7) * 4;
    const int gw = b * 4 + w; const u16* P2 = gw < 512 ? wo : w2;
    const u32x4 srdP2 = {(unsigned)(size_t)P2, (unsigned)((size_t)P2 >> 32) & 0xffffu, gw < 512 ? 4096u * D_ * 2 : (unsigned)(F_ * D_ * 2), 0x20000u};
    unsigned sP2 = (gw & 511) * 16384;
    asm volatile(
    "s_nop 4\n\t"
    "v_add_u32 v8, 65664, %[vRdW]\n\t"
    "v_add_u32 v9, 65664, %[vRdW2]\n\t"
    "v_add_u32 v10, %[sLdsB], %[vL16]\n\t"
    "v_add_u32 v11, 65664, v10\n\t"
    "s_mov_b32 s62, 0xbfb8aa3b\n\t"
    "s_mov_b32 s63, 0xbfb8aa3b\n\t"
    "s_mov_b32 s64, 0x5f800000\n\t"
    "s_waitcnt vmcnt(16)\n\t"
    "s_barrier\n\t"
    "ds_read_b64_tr_b16 a[0:1], %[vRdW] offset:0\n\t"
    "ds_read_b64_tr_b16 a[2:3], %[vRdW] offset:256\n\t"
    "ds_read_b64_tr_b16 a[4:5], %[vRdW] offset:16416\n\t"
    "ds_read_b64_tr_b16 a[6:7], %[vRdW] offset:16672\n\t"
    "ds_read_b64_tr_b16 a[8:9], %[vRdW] offset:8\n\t"
    "ds_read_b64_tr_b16 a[10:11], %[vRdW] offset:264\n\t"
    "ds_read_b64_tr_b16 a[12:13], %[vRdW] offset:16424\n\t"
    "ds_read_b64_tr_b16 a[14:15], %[vRdW] offset:16680\n\t"
    "ds_read_b64_tr_b16 a[16:17], %[vRdW] offset:512\n\t"
    "ds_read_b64_tr_b16 a[18:19], %[vRdW] offset:768\n\t"
    "ds_read_b64_tr_b16 a[20:21], %[vRdW] offset:16928\n\t"
    "ds_read_b64_tr_b16 a[22:23], %[vRdW] offset:17184\n\t"
    "s_waitcnt vmcnt(8) lgkmcnt(8)\n\t"
    "v_mfma_f32_16x16x32_bf16 v[16:19], a[0:3], a[32:35], 0\n\t"
    "ds_read_b64_tr_b16 a[24:25], %[vRdW] offset:520\n\t"
    "v_mfma_f32_16x16x32_bf16 v[16:19], a[4:7], a[48:51], v[16:19]\n\t"
    "ds_read_b64_tr_b16 a[26:27], %[vRdW] offset:776\n\t"
    "v_mfma_f32_16x16x32_bf16 v[20:23], a[4:7], a[52:55], 0\n\t"
    "s_add_u32 %[sX], %[sX], 4096\n\t"
    "s_and_b32 %[sX], %[sX], 0x1ffff\n\t"
    "v_mfma_f32_16x16x32_bf16 v[20:23], a[0:3], a[36:39], v[20:23]\n\t"
    "buffer_load_dwordx4 a[64:67], %[vX], %[srdWS], %[sX] offen offset:0\n\t"
    "v_mfma_f32_16x16x32_bf16 v[24:27], a[0:3], a[40:43], 0\n\t"
    "ds_read_b64_tr_b16 a[28:29], %[vRdW] offset:16936\n\t"
    "v_mfma_f32_16x16x32_bf16 v[24:27], a[4:7], a[56:59], v[24:27]\n\t"
    "ds_read_b64_tr_b16 a[30:31], %[vRdW] offset:17192\n\t"
    "v_mfma_f32_16x16x32_bf16 v[28:31], a[4:7], a[60:63], 0\n\t"
    "v_mfma_f32_16x16x32_bf16 v[28:31], a[0:3], a[44:47], v[28:31]\n\t"
    "buffer_load_dwordx4 a[68:71], %[vX], %[srdWS], %[sX] offen offset:1024\n\t"
    "s_waitcnt lgkmcnt(8)\n\t"
    "v_mfma_f32_16x16x32_bf16 v[32:35], a[8:11], a[32:35], 0\n\t"
    "ds_read_b64_tr_b16 a[0:1], %[vRdW] offset:1024\n\t"
    "v_mfma_f32_16x16x32_bf16 v[32:35], a[12:15], a[48:51], v[32:35]\n\t"
    "ds_read_b64_tr_b16 a[2:3], %[vRdW] offset:1280\n\t"
    "v_mfma_f32_16x16x32_bf16 v[36:39], a[12:15], a[52:55], 0\n\t"
    "v_mfma_f32_16x16x32_bf16 v[36:39], a[8:11], a[36:39], v[36:39]\n\t"
    "buffer_load_dwordx4 a[72:75], %[vX], %[srdWS], %[sX] offen offset:2048\n\t"
    "v_mfma_f32_16x16x32_bf16 v[40:43], a[8:11], a[40:43], 0\n\t"
    "ds_read_b64_tr_b16 a[4:5], %[vRdW] offset:17440\n\t"
    "v_mfma_f32_16x16x32_bf16 v[40:43], a[12:15], a[56:59], v[40:43]\n\t"
    "ds_read_b64_tr_b16 a[6:7], %[vRdW] offset:17696\n\t"
    "v_mfma_f32_16x16x32_bf16 v[44:47], a[12:15], a[60:63], 0\n\t"
    "v_mfma_f32_16x16x32_bf16 v[44:47], a[8:11], a[44:47], v[44:47]\n\t"
    "s_waitcnt lgkmcnt(8)\n\t"
    "v_mfma_f32_16x16x32_bf16 v[48:51], a[16:19], a[32:35], 0\n\t"
    "ds_read_b64_tr_b16 a[8:9], %[vRdW] offset:1032\n\t"
    "v_mfma_f32_16x16x32_bf16 v[48:51], a[20:23], a[48:51], v[48:51]\n\t"
    "ds_read_b64_tr_b16 a[10:11], %[vRdW] offset:1288\n\t"
    "v_mfma_f32_16x16x32_bf16 v[52:55], a[20:23], a[52:55], 0\n\t"
    "v_mfma_f32_16x16x32_bf16 v[52:55], a[16:19], a[36:39], v[52:55]\n\t"
    "buffer_load_dwordx4 a[76:79], %[vX], %[srdWS], %[sX] offen offset:3072\n\t"
    "v_mfma_f32_16x16x32_bf16 v[56:59], a[16:19], a[40:43], 0\n\t"
    "ds_read_b64_tr_b16 a[12:13], %[vRdW] offset:17448\n\t"
    "v_mfma_f32_16x16x32_bf16 v[56:59], a[20:23], a[56:59], v[56:59]\n\t"
    "ds_read_b64_tr_b16 a[14:15], %[vRdW] offset:17704\n\t"
    "v_mfma_f32_16x16x32_bf16 v[60:63], a[20:23], a[60:63], 0\n\t"
    "s_add_u32 %[sX], %[sX], 4096\n\t"
    "v_mfma_f32_16x16x32_bf16 v[60:63], a[16:19], a[44:47], v[60:63]\n\t"
    "buffer_load_dwordx4 a[80:83], %[vX], %[srdWS], %[sX] offen offset:0\n\t"
    "s_waitcnt lgkmcnt(8)\n\t"
    "v_mfma_f32_16x16x32_bf16 v[64:67], a[24:27], a[32:35], 0\n\t"
    "ds_read_b64_tr_b16 a[16:17], %[vRdW] offset:1536\n\t"
    "v_mfma_f32_16x16x32_bf16 v[64:67], a[28:31], a[48:51], v[64:67]\n\t"
    "ds_read_b64_tr_b16 a[18:19], %[vRdW] offset:1792\n\t"
    "v_mfma_f32_16x16x32_bf16 v[68:71], a[28:31], a[52:55], 0\n\t"
    "v_mfma_f32_16x16x32_bf16 v[68:71], a[24:27], a[36:39], v[68:71]\n\t"
    "buffer_load_dwordx4 a[84:87], %[vX], %[srdWS], %[sX] offen offset:1024\n\t"
    "v_mfma_f32_16x16x32_bf16 v[72:75], a[24:27], a[40:43], 0\n\t"
    "ds_read_b64_tr_b16 a[20:21], %[vRdW] offset:17952\n\t"
    "v_mfma_f32_16x16x32_bf16 v[72:75], a[28:31], a[56:59], v[72:75]\n\t"
    "ds_read_b64_tr_b16 a[22:23], %[vRdW] offset:18208\n\t"
    "v_mfma_f32_16x16x32_bf16 v[76:79], a[28:31], a[60:63], 0\n\t"
    "v_mfma_f32_16x16x32_bf16 v[76:79], a[24:27], a[44:47], v[76:79]\n\t"
    "s_waitcnt lgkmcnt(8)\n\t"
    "v_mfma_f32_16x16x32_bf16 v[80:83], a[0:3], a[32:35], 0\n\t"
    "ds_read_b64_tr_b16 a[24:25], %[vRdW] offset:1544\n\t"
    "v_mfma_f32_16x16x32_bf16 v[80:83], a[4:7], a[48:51], v[80:83]\n\t"
    "ds_read_b64_tr_b16 a[26:27], %[vRdW] offset:1800\n\t"
    "v_mfma_f32_16x16x32_bf16 v[84:87], a[4:7], a[52:55], 0\n\t"
    "v_mfma_f32_16x16x32_bf16 v[84:87], a[0:3], a[36:39], v[84:87]\n\t"
    "buffer_load_dwordx4 a[88:91], %[vX], %[srdWS], %[sX] offen offset:2048\n\t"
    "v_mfma_f32_16x16x32_bf16 v[88:91], a[0:3], a[40:43], 0\n\t"
    "ds_read_b64_tr_b16 a[28:29], %[vRdW] offset:17960\n\t"
    "v_mfma_f32_16x16x32_bf16 v[88:91], a[4:7], a[56:59], v[88:91]\n\t"
    "ds_read_b64_tr_b16 a[30:31], %[vRdW] offset:18216\n\t"
    "v_mfma_f32_16x16x32_bf16 v[92:95], a[4:7], a[60:63], 0\n\t"
    "v_mfma_f32_16x16x32_bf16 v[92:95], a[0:3], a[44:47], v[92:95]\n\t"
    "buffer_load_dwordx4 a[92:95], %[vX], %[srdWS], %[sX] offen offset:3072\n\t"
    "s_waitcnt lgkmcnt(8)\n\t"
    "v_mfma_f32_16x16x32_bf16 v[96:99], a[8:11], a[32:35], 0\n\t"
    "ds_read_b64_tr_b16 a[0:1], %[vRdW] offset:2048\n\t"
    "v_mfma_f32_16x16x32_bf16 v[96:99], a[12:15], a[48:51], v[96:99]\n\t"
    "ds_read_b64_tr_b16 a[2:3], %[vRdW] offset:2304\n\t"
    "v_mfma_f32_16x16x32_bf16 v[100:103], a[12:15], a[52:55], 0\n\t"
    "s_add_u32 %[sWk], %[sWk], s83\n\t"
    "s_add_u32 m0, s84, 65664\n\t"
    "s_add_u32 s82, %[sWk], s92\n\t"
    "v_mfma_f32_16x16x32_bf16 v[100:103], a[8:11], a[36:39], v[100:103]\n\t"
    "buffer_load_dwordx4 %[vW], %[srdW], s82 offen lds\n\t"
    "v_mfma_f32_16x16x32_bf16 v[104:107], a[8:11], a[40:43], 0\n\t"
    "ds_read_b64_tr_b16 a[4:5], %[vRdW] offset:18464\n\t"
    "v_mfma_f32_16x16x32_bf16 v[104:107], a[12:15], a[56:59], v[104:107]\n\t"
    "ds_read_b64_tr_b16 a[6:7], %[vRdW] offset:18720\n\t"
    "v_mfma_f32_16x16x32_bf16 v[108:111], a[12:15], a[60:63], 0\n\t"
    "v_mfma_f32_16x16x32_bf16 v[108:111], a[8:11], a[44:47], v[108:111]\n\t"
    "s_waitcnt lgkmcnt(8)\n\t"
    "v_mfma_f32_16x16x32_bf16 v[112:115], a[16:19], a[32:35], 0\n\t"
    "ds_read_b64_tr_b16 a[8:9], %[vRdW] offset:2056\n\t"
    "v_mfma_f32_16x16x32_bf16 v[112:115], a[20:23], a[48:51], v[112:115]\n\t"
    "ds_read_b64_tr_b16 a[10:11], %[vRdW] offset:2312\n\t"
    "v_mfma_f32_16x16x32_bf16 v[116:119], a[20:23], a[52:55], 0\n\t"
    "s_add_u32 m0, s85, 65664\n\t"
    "s_add_u32 s82, %[sWk], s93\n\t"
    "v_mfma_f32_16x16x32_bf16 v[116:119], a[16:19], a[36:39], v[116:119]\n\t"
    "buffer_load_dwordx4 %[vW], %[srdW], s82 offen lds\n\t"
    "v_mfma_f32_16x16x32_bf16 v[120:123], a[16:19], a[40:43], 0\n\t"
    "ds_read_b64_tr_b16 a[12:13], %[vRdW] offset:18472\n\t"
    "v_mfma_f32_16x16x32_bf16 v[120:123], a[20:23], a[56:59], v[120:123]\n\t"
    "ds_read_b64_tr_b16 a[14:15], %[vRdW] offset:18728\n\t"
    "v_mfma_f32_16x16x32_bf16 v[124:127], a[20:23], a[60:63], 0\n\t"
    "s_add_u32 m0, s86, 65664\n\t"
    "s_add_u32 s82, %[sWk], s94\n\t"
    "v_mfma_f32_16x16x32_bf16 v[124:127], a[16:19], a[44:47], v[124:127]\n\t"
    "buffer_load_dwordx4 %[vW], %[srdW], s82 offen lds\n\t"
    "s_waitcnt lgkmcnt(8)\n\t"
    "v_mfma_f32_16x16x32_bf16 v[128:131], a[24:27], a[32:35], 0\n\t"
    "ds_read_b64_tr_b16 a[16:17], %[vRdW] offset:2560\n\t"
    "v_mfma_f32_16x16x32_bf16 v[128:131], a[28:31], a[48:51], v[128:131]\n\t"
    "ds_read_b64_tr_b16 a[18:19], %[vRdW] offset:2816\n\t"
    "v_mfma_f32_16x16x32_bf16 v[132:135], a[28:31], a[52:55], 0\n\t"
    "s_add_u32 m0, s87, 65664\n\t"
    "s_add_u32 s82, %[sWk], s95\n\t"
    "v_mfma_f32_16x16x32_bf16 v[132:135], a[24:27], a[36:39], v[132:135]\n\t"
    "buffer_load_dwordx4 %[vW], %[srdW], s82 offen lds\n\t"
    "v_mfma_f32_16x16x32_bf16 v[136:139], a[24:27], a[40:43], 0\n\t"
    "ds_read_b64_tr_b16 a[20:21], %[vRdW] offset:18976\n\t"
    "v_mfma_f32_16x16x32_bf16 v[136:139], a[28:31], a[56:59], v[136:139]\n\t"
    "ds_read_b64_tr_b16 a[22:23], %[vRdW] offset:19232\n\t"
    "v_mfma_f32_16x16x32_bf16 v[140:143], a[28:31], a[60:63], 0\n\t"
    "v_mfma_f32_16x16x32_bf16 v[140:143], a[24:27], a[44:47], v[140:143]\n\t"
    "s_waitcnt lgkmcnt(8)\n\t"
    "v_mfma_f32_16x16x32_bf16 v[144:147], a[0:3], a[32:35], 0\n\t"
    "ds_read_b64_tr_b16 a[24:25], %[vRdW] offset:2568\n\t"
    "v_mfma_f32_16x16x32_bf16 v[144:147], a[4:7], a[48:51], v[144:147]\n\t"
    "ds_read_b64_tr_b16 a[26:27], %[vRdW] offset:2824\n\t"
    "v_mfma_f32_16x16x32_bf16 v[148:151], a[4:7], a[52:55], 0\n\t"
    "s_add_u32 m0, s88, 65664\n\t"
    "s_add_u32 s82, %[sWk], s96\n\t"
    "v_mfma_f32_16x16x32_bf16 v[148:151], a[0:3], a[36:39], v[148:151]\n\t"
    "buffer_load_dwordx4 %[vW], %[srdW], s82 offen lds\n\t"
    "v_mfma_f32_16x16x32_bf16 v[152:155], a[0:3], a[40:43], 0\n\t"
    "ds_read_b64_tr_b16 a[28:29], %[vRdW] offset:18984\n\t"
    "v_mfma_f32_16x16x32_bf16 v[152:155], a[4:7], a[56:59], v[152:155]\n\t"
    "ds_read_b64_tr_b16 a[30:31], %[vRdW] offset:19240\n\t"
    "v_mfma_f32_16x16x32_bf16 v[156:159], a[4:7], a[60:63], 0\n\t"
    "s_add_u32 m0, s89, 65664\n\t"
    "s_add_u32 s82, %[sWk], s97\n\t"
    "v_mfma_f32_16x16x32_bf16 v[156:159], a[0:3], a[44:47], v[156:159]\n\t"
    "buffer_load_dwordx4 %[vW], %[srdW], s82 offen lds\n\t"
    "s_waitcnt lgkmcnt(8)\n\t"
    "v_mfma_f32_16x16x32_bf16 v[160:163], a[8:11], a[32:35], 0\n\t"
    "ds_read_b64_tr_b16 a[0:1], %[vRdW] offset:3072\n\t"
    "v_mfma_f32_16x16x32_bf16 v[160:163], a[12:15], a[48:51], v[160:163]\n\t"
    "ds_read_b64_tr_b16 a[2:3], %[vRdW] offset:3328\n\t"
    "v_mfma_f32_16x16x32_bf16 v[164:167], a[12:15], a[52:55], 0\n\t"
    "s_add_u32 m0, s90, 65664\n\t"
    "s_add_u32 s82, %[sWk], s98\n\t"
    "v_mfma_f32_16x16x32_bf16 v[164:167], a[8:11], a[36:39], v[164:167]\n\t"
    "buffer_load_dwordx4 %[vW], %[srdW], s82 offen lds\n\t"
    "v_mfma_f32_16x16x32_bf16 v[168:171], a[8:11], a[40:43], 0\n\t"
    "ds_read_b64_tr_b16 a[4:5], %[vRdW] offset:19488\n\t"
    "v_mfma_f32_16x16x32_bf16 v[168:171], a[12:15], a[56:59], v[168:171]\n\t"
    "ds_read_b64_tr_b16 a[6:7], %[vRdW] offset:19744\n\t"
    "v_mfma_f32_16x16x32_bf16 v[172:175], a[12:15], a[60:63], 0\n\t"
    "v_mfma_f32_16x16x32_bf16 v[172:175], a[8:11], a[44:47], v[172:175]\n\t"
    "s_waitcnt lgkmcnt(8)\n\t"
    "v_mfma_f32_16x16x32_bf16 v[176:179], a[16:19], a[32:35], 0\n\t"
    "ds_read_b64_tr_b16 a[8:9], %[vRdW] offset:3080\n\t"
    "v_mfma_f32_16x16x32_bf16 v[176:179], a[20:23], a[48:51], v[176:179]\n\t"
    "ds_read_b64_tr_b16 a[10:11], %[vRdW] offset:3336\n\t"
    "v_mfma_f32_16x16x32_bf16 v[180:183], a[20:23], a[52:55], 0\n\t"
    "s_add_u32 m0, s91, 65664\n\t"
    "s_add_u32 s82, %[sWk], s99\n\t"
    "v_mfma_f32_16x16x32_bf16 v[180:183], a[16:19], a[36:39], v[180:183]\n\t"
    "buffer_load_dwordx4 %[vW], %[srdW], s82 offen lds\n\t"
    "v_mfma_f32_16x16x32_bf16 v[184:187], a[16:19], a[40:43], 0\n\t"
    "ds_read_b64_tr_b16 a[12:13], %[vRdW] offset:19496\n\t"
    "v_mfma_f32_16x16x32_bf16 v[184:187], a[20:23], a[56:59], v[184:187]\n\t"
    "ds_read_b64_tr_b16 a[14:15], %[vRdW] offset:19752\n\t"
    "v_mfma_f32_16x16x32_bf16 v[188:191], a[20:23], a[60:63], 0\n\t"
    "s_add_u32 %[sX], %[sX], 4096\n\t"
    "s_and_b32 %[sX], %[sX], 0x1ffff\n\t"
    "v_mfma_f32_16x16x32_bf16 v[188:191], a[16:19], a[44:47], v[188:191]\n\t"
    "buffer_load_dwordx4 a[96:99], %[vX], %[srdWS], %[sX] offen offset:0\n\t"
    "s_waitcnt lgkmcnt(8)\n\t"
    "v_mfma_f32_16x16x32_bf16 v[192:195], a[24:27], a[32:35], 0\n\t"
    "ds_read_b64_tr_b16 a[16:17], %[vRdW2] offset:3584\n\t"
    "v_mfma_f32_16x16x32_bf16 v[192:195], a[28:31], a[48:51], v[192:195]\n\t"
    "ds_read_b64_tr_b16 a[18:19], %[vRdW2] offset:3840\n\t"
    "v_mfma_f32_16x16x32_bf16 v[196:199], a[28:31], a[52:55], 0\n\t"
    "v_mfma_f32_16x16x32_bf16 v[196:199], a[24:27], a[36:39], v[196:199]\n\t"
    "buffer_load_dwordx4 a[100:103], %[vX], %[srdWS], %[sX] offen offset:1024\n\t"
    "v_mfma_f32_16x16x32_bf16 v[200:203], a[24:27], a[40:43], 0\n\t"
    "ds_read_b64_tr_b16 a[20:21], %[vRdW2] offset:20000\n\t"
    "v_mfma_f32_16x16x32_bf16 v[200:203], a[28:31], a[56:59], v[200:203]\n\t"
    "ds_read_b64_tr_b16 a[22:23], %[vRdW2] offset:20256\n\t"
    "v_mfma_f32_16x16x32_bf16 v[204:207], a[28:31], a[60:63], 0\n\t"
    "v_mfma_f32_16x16x32_bf16 v[204:207], a[24:27], a[44:47], v[204:207]\n\t"
    "s_waitcnt vmcnt(18) lgkmcnt(8)\n\t"
    "s_barrier\n\t"
    "v_mfma_f32_16x16x32_bf16 v[208:211], a[0:3], a[32:35], 0\n\t"
    "ds_read_b64_tr_b16 a[24:25], %[vRdW] offset:32832\n\t"
    "v_mfma_f32_16x16x32_bf16 v[208:211], a[4:7], a[48:51], v[208:211]\n\t"
    "ds_read_b64_tr_b16 a[26:27], %[vRdW] offset:33088\n\t"
    "v_mfma_f32_16x16x32_bf16 v[212:215], a[4:7], a[52:55], 0\n\t"
    "v_mfma_f32_16x16x32_bf16 v[212:215], a[0:3], a[36:39], v[212:215]\n\t"
    "buffer_load_dwordx4 a[104:107], %[vX], %[srdWS], %[sX] offen offset:2048\n\t"
    "v_mfma_f32_16x16x32_bf16 v[216:219], a[0:3], a[40:43], 0\n\t"
    "ds_read_b64_tr_b16 a[28:29], %[vRdW] offset:49248\n\t"
    "v_mfma_f32_16x16x32_bf16 v[216:219], a[4:7], a[56:59], v[216:219]\n\t"
    "ds_read_b64_tr_b16 a[30:31], %[vRdW] offset:49504\n\t"
    "v_mfma_f32_16x16x32_bf16 v[220:223], a[4:7], a[60:63], 0\n\t"
    "v_mfma_f32_16x16x32_bf16 v[220:223], a[0:3], a[44:47], v[220:223]\n\t"
    "buffer_load_dwordx4 a[108:111], %[vX], %[srdWS], %[sX] offen offset:3072\n\t"
    "s_waitcnt lgkmcnt(8)\n\t"
    "v_mfma_f32_16x16x32_bf16 v[224:227], a[8:11], a[32:35], 0\n\t"
    "ds_read_b64_tr_b16 a[0:1], %[vRdW] offset:32840\n\t"
    "v_mfma_f32_16x16x32_bf16 v[224:227], a[12:15], a[48:51], v[224:227]\n\t"
    "ds_read_b64_tr_b16 a[2:3], %[vRdW] offset:33096\n\t"
    "v_mfma_f32_16x16x32_bf16 v[228:231], a[12:15], a[52:55], 0\n\t"
    "s_add_u32 %[sX], %[sX], 4096\n\t"
    "v_mfma_f32_16x16x32_bf16 v[228:231], a[8:11], a[36:39], v[228:231]\n\t"
    "buffer_load_dwordx4 a[112:115], %[vX], %[srdWS], %[sX] offen offset:0\n\t"
    "v_mfma_f32_16x16x32_bf16 v[232:235], a[8:11], a[40:43], 0\n\t"
    "ds_read_b64_tr_b16 a[4:5], %[vRdW] offset:49256\n\t"
    "v_mfma_f32_16x16x32_bf16 v[232:235], a[12:15], a[56:59], v[232:235]\n\t"
    "ds_read_b64_tr_b16 a[6:7], %[vRdW] offset:49512\n\t"
    "v_mfma_f32_16x16x32_bf16 v[236:239], a[12:15], a[60:63], 0\n\t"
    "v_mfma_f32_16x16x32_bf16 v[236:239], a[8:11], a[44:47], v[236:239]\n\t"
    "s_waitcnt lgkmcnt(8)\n\t"
    "v_mfma_f32_16x16x32_bf16 v[240:243], a[16:19], a[32:35], 0\n\t"
    "ds_read_b64_tr_b16 a[8:9], %[vRdW] offset:33344\n\t"
    "v_mfma_f32_16x16x32_bf16 v[240:243], a[20:23], a[48:51], v[240:243]\n\t"
    "ds_read_b64_tr_b16 a[10:11], %[vRdW] offset:33600\n\t"
    "v_mfma_f32_16x16x32_bf16 v[244:247], a[20:23], a[52:55], 0\n\t"
    "v_mfma_f32_16x16x32_bf16 v[244:247], a[16:19], a[36:39], v[244:247]\n\t"
    "buffer_load_dwordx4 a[116:119], %[vX], %[srdWS], %[sX] offen offset:1024\n\t"
    "v_mfma_f32_16x16x32_bf16 v[248:251], a[16:19], a[40:43], 0\n\t"
    "ds_read_b64_tr_b16 a[12:13], %[vRdW] offset:49760\n\t"
    "v_mfma_f32_16x16x32_bf16 v[248:251], a[20:23], a[56:59], v[248:251]\n\t"
    "ds_read_b64_tr_b16 a[14:15], %[vRdW] offset:50016\n\t"
    "v_mfma_f32_16x16x32_bf16 v[252:255], a[20:23], a[60:63], 0\n\t"
    "v_mfma_f32_16x16x32_bf16 v[252:255], a[16:19], a[44:47], v[252:255]\n\t"
    "buffer_load_dwordx4 a[120:123], %[vX], %[srdWS], %[sX] offen offset:2048\n\t"
    "s_waitcnt vmcnt(15) lgkmcnt(8)\n\t"
    "v_mfma_f32_16x16x32_bf16 v[16:19], a[24:27], a[64:67], v[16:19]\n\t"
    "ds_read_b64_tr_b16 a[16:17], %[vRdW] offset:33352\n\t"
    "v_mfma_f32_16x16x32_bf16 v[16:19], a[28:31], a[80:83], v[16:19]\n\t"
    "ds_read_b64_tr_b16 a[18:19], %[vRdW] offset:33608\n\t"
    "v_mfma_f32_16x16x32_bf16 v[20:23], a[28:31], a[84:87], v[20:23]\n\t"
    "v_mfma_f32_16x16x32_bf16 v[20:23], a[24:27], a[68:71], v[20:23]\n\t"
    "buffer_load_dwordx4 a[124:127], %[vX], %[srdWS], %[sX] offen offset:3072\n\t"
    "v_mfma_f32_16x16x32_bf16 v[24:27], a[24:27], a[72:75], v[24:27]\n\t"
    "ds_read_b64_tr_b16 a[20:21], %[vRdW] offset:49768\n\t"
    "v_mfma_f32_16x16x32_bf16 v[24:27], a[28:31], a[88:91], v[24:27]\n\t"
    "ds_read_b64_tr_b16 a[22:23], %[vRdW] offset:50024\n\t"
    "v_mfma_f32_16x16x32_bf16 v[28:31], a[28:31], a[92:95], v[28:31]\n\t"
    "s_add_u32 %[sWk], %[sWk], s83\n\t"
    "s_add_u32 m0, s84, 98496\n\t"
    "s_add_u32 s82, %[sWk], s92\n\t"
    "v_mfma_f32_16x16x32_bf16 v[28:31], a[24:27], a[76:79], v[28:31]\n\t"
    "buffer_load_dwordx4 %[vW], %[srdW], s82 offen lds\n\t"
    "s_waitcnt lgkmcnt(8)\n\t"
    "s_barrier\n\t"
    "v_mfma_f32_16x16x32_bf16 v[32:35], a[0:3], a[64:67], v[32:35]\n\t"
    "ds_read_b64_tr_b16 a[24:25], %[vRdW] offset:33856\n\t"
    "v_mfma_f32_16x16x32_bf16 v[32:35], a[4:7], a[80:83], v[32:35]\n\t"
    "ds_read_b64_tr_b16 a[26:27], %[vRdW] offset:34112\n\t"
    "v_mfma_f32_16x16x32_bf16 v[36:39], a[4:7], a[84:87], v[36:39]\n\t"
    "s_add_u32 m0, s85, 98496\n\t"
    "s_add_u32 s82, %[sWk], s93\n\t"
    "v_mfma_f32_16x16x32_bf16 v[36:39], a[0:3], a[68:71], v[36:39]\n\t"
    "buffer_load_dwordx4 %[vW], %[srdW], s82 offen lds\n\t"
    "v_mfma_f32_16x16x32_bf16 v[40:43], a[0:3], a[72:75], v[40:43]\n\t"
    "ds_read_b64_tr_b16 a[28:29], %[vRdW] offset:50272\n\t"
    "v_mfma_f32_16x16x32_bf16 v[40:43], a[4:7], a[88:91], v[40:43]\n\t"
    "ds_read_b64_tr_b16 a[30:31], %[vRdW] offset:50528\n\t"
    "v_mfma_f32_16x16x32_bf16 v[44:47], a[4:7], a[92:95], v[44:47]\n\t"
    "v_mfma_f32_16x16x32_bf16 v[44:47], a[0:3], a[76:79], v[44:47]\n\t"
    "s_waitcnt lgkmcnt(8)\n\t"
    "v_mfma_f32_16x16x32_bf16 v[48:51], a[8:11], a[64:67], v[48:51]\n\t"
    "ds_read_b64_tr_b16 a[0:1], %[vRdW] offset:33864\n\t"
    "v_mfma_f32_16x16x32_bf16 v[48:51], a[12:15], a[80:83], v[48:51]\n\t"
    "ds_read_b64_tr_b16 a[2:3], %[vRdW] offset:34120\n\t"
    "v_mfma_f32_16x16x32_bf16 v[52:55], a[12:15], a[84:87], v[52:55]\n\t"
    "s_add_u32 m0, s86, 98496\n\t"
    "s_add_u32 s82, %[sWk], s94\n\t"
    "v_mfma_f32_16x16x32_bf16 v[52:55], a[8:11], a[68:71], v[52:55]\n\t"
    "buffer_load_dwordx4 %[vW], %[srdW], s82 offen lds\n\t"
    "v_mfma_f32_16x16x32_bf16 v[56:59], a[8:11], a[72:75], v[56:59]\n\t"
    "ds_read_b64_tr_b16 a[4:5], %[vRdW] offset:50280\n\t"
    "v_mfma_f32_16x16x32_bf16 v[56:59], a[12:15], a[88:91], v[56:59]\n\t"
    "ds_read_b64_tr_b16 a[6:7], %[vRdW] offset:50536\n\t"
    "v_mfma_f32_16x16x32_bf16 v[60:63], a[12:15], a[92:95], v[60:63]\n\t"
    "s_add_u32 m0, s87, 98496\n\t"
    "s_add_u32 s82, %[sWk], s95\n\t"
    "v_mfma_f32_16x16x32_bf16 v[60:63], a[8:11], a[76:79], v[60:63]\n\t"
    "buffer_load_dwordx4 %[vW], %[srdW], s82 offen lds\n\t"
    "s_waitcnt lgkmcnt(8)\n\t"
    "v_mfma_f32_16x16x32_bf16 v[64:67], a[16:19], a[64:67], v[64:67]\n\t"
    "ds_read_b64_tr_b16 a[8:9], %[vRdW] offset:34368\n\t"
    "v_mfma_f32_16x16x32_bf16 v[64:67], a[20:23], a[80:83], v[64:67]\n\t"
    "ds_read_b64_tr_b16 a[10:11], %[vRdW] offset:34624\n\t"
    "v_mfma_f32_16x16x32_bf16 v[68:71], a[20:23], a[84:87], v[68:71]\n\t"
    "s_add_u32 m0, s88, 98496\n\t"
    "s_add_u32 s82, %[sWk], s96\n\t"
    "v_mfma_f32_16x16x32_bf16 v[68:71], a[16:19], a[68:71], v[68:71]\n\t"
    "buffer_load_dwordx4 %[vW], %[srdW], s82 offen lds\n\t"
    "v_mfma_f32_16x16x32_bf16 v[72:75], a[16:19], a[72:75], v[72:75]\n\t"
    "ds_read_b64_tr_b16 a[12:13], %[vRdW] offset:50784\n\t"
    "v_mfma_f32_16x16x32_bf16 v[72:75], a[20:23], a[88:91], v[72:75]\n\t"
    "ds_read_b64_tr_b16 a[14:15], %[vRdW] offset:51040\n\t"
    "v_mfma_f32_16x16x32_bf16 v[76:79], a[20:23], a[92:95], v[76:79]\n\t"
    "v_mfma_f32_16x16x32_bf16 v[76:79], a[16:19], a[76:79], v[76:79]\n\t"
    "s_waitcnt lgkmcnt(8)\n\t"
    "v_mfma_f32_16x16x32_bf16 v[80:83], a[24:27], a[64:67], v[80:83]\n\t"
    "ds_read_b64_tr_b16 a[16:17], %[vRdW] offset:34376\n\t"
    "v_mfma_f32_16x16x32_bf16 v[80:83], a[28:31], a[80:83], v[80:83]\n\t"
    "ds_read_b64_tr_b16 a[18:19], %[vRdW] offset:34632\n\t"
    "v_mfma_f32_16x16x32_bf16 v[84:87], a[28:31], a[84:87], v[84:87]\n\t"
    "s_add_u32 m0, s89, 98496\n\t"
    "s_add_u32 s82, %[sWk], s97\n\t"
    "v_mfma_f32_16x16x32_bf16 v[84:87], a[24:27], a[68:71], v[84:87]\n\t"
    "buffer_load_dwordx4 %[vW], %[srdW], s82 offen lds\n\t"
    "v_mfma_f32_16x16x32_bf16 v[88:91], a[24:27], a[72:75], v[88:91]\n\t"
    "ds_read_b64_tr_b16 a[20:21], %[vRdW] offset:50792\n\t"
    "v_mfma_f32_16x16x32_bf16 v[88:91], a[28:31], a[88:91], v[88:91]\n\t"
    "ds_read_b64_tr_b16 a[22:23], %[vRdW] offset:51048\n\t"
    "v_mfma_f32_16x16x32_bf16 v[92:95], a[28:31], a[92:95], v[92:95]\n\t"
    "s_add_u32 m0, s90, 98496\n\t"
    "s_add_u32 s82, %[sWk], s98\n\t"
    "v_mfma_f32_16x16x32_bf16 v[92:95], a[24:27], a[76:79], v[92:95]\n\t"
    "buffer_load_dwordx4 %[vW], %[srdW], s82 offen lds\n\t"
    "s_waitcnt lgkmcnt(8)\n\t"
    "v_mfma_f32_16x16x32_bf16 v[96:99], a[0:3], a[64:67], v[96:99]\n\t"
    "ds_read_b64_tr_b16 a[24:25], %[vRdW] offset:34880\n\t"
    "v_mfma_f32_16x16x32_bf16 v[96:99], a[4:7], a[80:83], v[96:99]\n\t"
    "ds_read_b64_tr_b16 a[26:27], %[vRdW] offset:35136\n\t"
    "v_mfma_f32_16x16x32_bf16 v[100:103], a[4:7], a[84:87], v[100:103]\n\t"
    "s_add_u32 m0, s91, 98496\n\t"
    "s_add_u32 s82, %[sWk], s99\n\t"
    "v_mfma_f32_16x16x32_bf16 v[100:103], a[0:3], a[68:71], v[100:103]\n\t"
    "buffer_load_dwordx4 %[vW], %[srdW], s82 offen lds\n\t"
    "v_mfma_f32_16x16x32_bf16 v[104:107], a[0:3], a[72:75], v[104:107]\n\t"
    "ds_read_b64_tr_b16 a[28:29], %[vRdW] offset:51296\n\t"
    "v_mfma_f32_16x16x32_bf16 v[104:107], a[4:7], a[88:91], v[104:107]\n\t"
    "ds_read_b64_tr_b16 a[30:31], %[vRdW] offset:51552\n\t"
    "v_mfma_f32_16x16x32_bf16 v[108:111], a[4:7], a[92:95], v[108:111]\n\t"
    "v_mfma_f32_16x16x32_bf16 v[108:111], a[0:3], a[76:79], v[108:111]\n\t"
    "s_waitcnt lgkmcnt(8)\n\t"
    "v_mfma_f32_16x16x32_bf16 v[112:115], a[8:11], a[64:67], v[112:115]\n\t"
    "ds_read_b64_tr_b16 a[0:1], %[vRdW] offset:34888\n\t"
    "v_mfma_f32_16x16x32_bf16 v[112:115], a[12:15], a[80:83], v[112:115]\n\t"
    "ds_read_b64_tr_b16 a[2:3], %[vRdW] offset:35144\n\t"
    "v_mfma_f32_16x16x32_bf16 v[116:119], a[12:15], a[84:87], v[116:119]\n\t"
    "s_add_u32 %[sX], %[sX], 4096\n\t"
    "s_and_b32 %[sX], %[sX], 0x1ffff\n\t"
    "v_mfma_f32_16x16x32_bf16 v[116:119], a[8:11], a[68:71], v[116:119]\n\t"
    "buffer_load_dwordx4 a[128:131], %[vX], %[srdWS], %[sX] offen offset:0\n\t"
    "k_pga0: s_cbranch_execz k_pga4\n\t"
    "v_mfma_f32_16x16x32_bf16 v[120:123], a[8:11], a[72:75], v[120:123]\n\t"
    "ds_read_b64_tr_b16 a[4:5], %[vRdW] offset:51304\n\t"
    "v_mfma_f32_16x16x32_bf16 v[120:123], a[12:15], a[88:91], v[120:123]\n\t"
    "ds_read_b64_tr_b16 a[6:7], %[vRdW] offset:51560\n\t"
    "v_mfma_f32_16x16x32_bf16 v[124:127], a[12:15], a[92:95], v[124:127]\n\t"
    "v_mfma_f32_16x16x32_bf16 v[124:127], a[8:11], a[76:79], v[124:127]\n\t"
    "buffer_load_dwordx4 a[132:135], %[vX], %[srdWS], %[sX] offen offset:1024\n\t"
    "s_waitcnt lgkmcnt(8)\n\t"
    "v_mfma_f32_16x16x32_bf16 v[128:131], a[16:19], a[64:67], v[128:131]\n\t"
    "ds_read_b64_tr_b16 a[8:9], %[vRdW] offset:35392\n\t"
    "v_mfma_f32_16x16x32_bf16 v[128:131], a[20:23], a[80:83], v[128:131]\n\t"
    "ds_read_b64_tr_b16 a[10:11], %[vRdW] offset:35648\n\t"
    "v_mfma_f32_16x16x32_bf16 v[132:135], a[20:23], a[84:87], v[132:135]\n\t"
    "v_mfma_f32_16x16x32_bf16 v[132:135], a[16:19], a[68:71], v[132:135]\n\t"
    "buffer_load_dwordx4 a[136:139], %[vX], %[srdWS], %[sX] offen offset:2048\n\t"
    "v_mfma_f32_16x16x32_bf16 v[136:139], a[16:19], a[72:75], v[136:139]\n\t"
    "ds_read_b64_tr_b16 a[12:13], %[vRdW] offset:51808\n\t"
    "v_mfma_f32_16x16x32_bf16 v[136:139], a[20:23], a[88:91], v[136:139]\n\t"
    "ds_read_b64_tr_b16 a[14:15], %[vRdW] offset:52064\n\t"
    "v_mfma_f32_16x16x32_bf16 v[140:143], a[20:23], a[92:95], v[140:143]\n\t"
    "v_mfma_f32_16x16x32_bf16 v[140:143], a[16:19], a[76:79], v[140:143]\n\t"
    "s_waitcnt lgkmcnt(8)\n\t"
    "v_mfma_f32_16x16x32_bf16 v[144:147], a[24:27], a[64:67], v[144:147]\n\t"
    "ds_read_b64_tr_b16 a[16:17], %[vRdW] offset:35400\n\t"
    "v_mfma_f32_16x16x32_bf16 v[144:147], a[28:31], a[80:83], v[144:147]\n\t"
    "ds_read_b64_tr_b16 a[18:19], %[vRdW] offset:35656\n\t"
    "v_mfma_f32_16x16x32_bf16 v[148:151], a[28:31], a[84:87], v[148:151]\n\t"
    "v_mfma_f32_16x16x32_bf16 v[148:151], a[24:27], a[68:71], v[148:151]\n\t"
    "buffer_load_dwordx4 a[140:143], %[vX], %[srdWS], %[sX] offen offset:3072\n\t"
    "v_mfma_f32_16x16x32_bf16 v[152:155], a[24:27], a[72:75], v[152:155]\n\t"
    "ds_read_b64_tr_b16 a[20:21], %[vRdW] offset:51816\n\t"
    "v_mfma_f32_16x16x32_bf16 v[152:155], a[28:31], a[88:91], v[152:155]\n\t"
    "ds_read_b64_tr_b16 a[22:23], %[vRdW] offset:52072\n\t"
    "v_mfma_f32_16x16x32_bf16 v[156:159], a[28:31], a[92:95], v[156:159]\n\t"
    "s_add_u32 %[sX], %[sX], 4096\n\t"
    "v_mfma_f32_16x16x32_bf16 v[156:159], a[24:27], a[76:79], v[156:159]\n\t"
    "buffer_load_dwordx4 a[144:147], %[vX], %[srdWS], %[sX] offen offset:0\n\t"
    "s_waitcnt lgkmcnt(8)\n\t"
    "v_mfma_f32_16x16x32_bf16 v[160:163], a[0:3], a[64:67], v[160:163]\n\t"
    "ds_read_b64_tr_b16 a[24:25], %[vRdW] offset:35904\n\t"
    "v_mfma_f32_16x16x32_bf16 v[160:163], a[4:7], a[80:83], v[160:163]\n\t"
    "ds_read_b64_tr_b16 a[26:27], %[vRdW] offset:36160\n\t"
    "v_mfma_f32_16x16x32_bf16 v[164:167], a[4:7], a[84:87], v[164:167]\n\t"
    "v_mfma_f32_16x16x32_bf16 v[164:167], a[0:3], a[68:71], v[164:167]\n\t"
    "buffer_load_dwordx4 a[148:151], %[vX], %[srdWS], %[sX] offen offset:1024\n\t"
    "v_mfma_f32_16x16x32_bf16 v[168:171], a[0:3], a[72:75], v[168:171]\n\t"
    "ds_read_b64_tr_b16 a[28:29], %[vRdW] offset:52320\n\t"
    "v_mfma_f32_16x16x32_bf16 v[168:171], a[4:7], a[88:91], v[168:171]\n\t"
    "ds_read_b64_tr_b16 a[30:31], %[vRdW] offset:52576\n\t"
    "v_mfma_f32_16x16x32_bf16 v[172:175], a[4:7], a[92:95], v[172:175]\n\t"
    "v_mfma_f32_16x16x32_bf16 v[172:175], a[0:3], a[76:79], v[172:175]\n\t"
    "s_waitcnt lgkmcnt(8)\n\t"
    "v_mfma_f32_16x16x32_bf16 v[176:179], a[8:11], a[64:67], v[176:179]\n\t"
    "ds_read_b64_tr_b16 a[0:1], %[vRdW] offset:35912\n\t"
    "v_mfma_f32_16x16x32_bf16 v[176:179], a[12:15], a[80:83], v[176:179]\n\t"
    "ds_read_b64_tr_b16 a[2:3], %[vRdW] offset:36168\n\t"
    "v_mfma_f32_16x16x32_bf16 v[180:183], a[12:15], a[84:87], v[180:183]\n\t"
    "v_mfma_f32_16x16x32_bf16 v[180:183], a[8:11], a[68:71], v[180:183]\n\t"
    "buffer_load_dwordx4 a[152:155], %[vX], %[srdWS], %[sX] offen offset:2048\n\t"
    "v_mfma_f32_16x16x32_bf16 v[184:187], a[8:11], a[72:75], v[184:187]\n\t"
    "ds_read_b64_tr_b16 a[4:5], %[vRdW] offset:52328\n\t"
    "v_mfma_f32_16x16x32_bf16 v[184:187], a[12:15], a[88:91], v[184:187]\n\t"
    "ds_read_b64_tr_b16 a[6:7], %[vRdW] offset:52584\n\t"
    "v_mfma_f32_16x16x32_bf16 v[188:191], a[12:15], a[92:95], v[188:191]\n\t"
    "v_mfma_f32_16x16x32_bf16 v[188:191], a[8:11], a[76:79], v[188:191]\n\t"
    "buffer_load_dwordx4 a[156:159], %[vX], %[srdWS], %[sX] offen offset:3072\n\t"
    "s_waitcnt lgkmcnt(8)\n\t"
    "v_mfma_f32_16x16x32_bf16 v[192:195], a[16:19], a[64:67], v[192:195]\n\t"
    "ds_read_b64_tr_b16 a[8:9], %[vRdW2] offset:36416\n\t"
    "v_mfma_f32_16x16x32_bf16 v[192:195], a[20:23], a[80:83], v[192:195]\n\t"
    "ds_read_b64_tr_b16 a[10:11], %[vRdW2] offset:36672\n\t"
    "v_mfma_f32_16x16x32_bf16 v[196:199], a[20:23], a[84:87], v[196:199]\n\t"
    "s_add_u32 %[sWk], %[sWk], s83\n\t"
    "s_add_u32 m0, s84, 0\n\t"
    "s_add_u32 s82, %[sWk], s92\n\t"
    "v_mfma_f32_16x16x32_bf16 v[196:199], a[16:19], a[68:71], v[196:199]\n\t"
    "buffer_load_dwordx4 %[vW], %[srdW], s82 offen lds\n\t"
    "v_mfma_f32_16x16x32_bf16 v[200:203], a[16:19], a[72:75], v[200:203]\n\t"
    "ds_read_b64_tr_b16 a[12:13], %[vRdW2] offset:52832\n\t"
    "v_mfma_f32_16x16x32_bf16 v[200:203], a[20:23], a[88:91], v[200:203]\n\t"
    "ds_read_b64_tr_b16 a[14:15], %[vRdW2] offset:53088\n\t"
    "v_mfma_f32_16x16x32_bf16 v[204:207], a[20:23], a[92:95], v[204:207]\n\t"
    "v_mfma_f32_16x16x32_bf16 v[204:207], a[16:19], a[76:79], v[204:207]\n\t"
    "s_waitcnt vmcnt(25) lgkmcnt(8)\n\t"
    "s_barrier\n\t"
    "v_mfma_f32_16x16x32_bf16 v[208:211], a[24:27], a[64:67], v[208:211]\n\t"
    "ds_read_b64_tr_b16 a[16:17], v8 offset:0\n\t"
    "v_mfma_f32_16x16x32_bf16 v[208:211], a[28:31], a[80:83], v[208:211]\n\t"
    "ds_read_b64_tr_b16 a[18:19], v8 offset:256\n\t"
    "v_mfma_f32_16x16x32_bf16 v[212:215], a[28:31], a[84:87], v[212:215]\n\t"
    "s_add_u32 m0, s85, 0\n\t"
    "s_add_u32 s82, %[sWk], s93\n\t"
    "v_mfma_f32_16x16x32_bf16 v[212:215], a[24:27], a[68:71], v[212:215]\n\t"
    "buffer_load_dwordx4 %[vW], %[srdW], s82 offen lds\n\t"
    "v_mfma_f32_16x16x32_bf16 v[216:219], a[24:27], a[72:75], v[216:219]\n\t"
    "ds_read_b64_tr_b16 a[20:21], v8 offset:16416\n\t"
    "v_mfma_f32_16x16x32_bf16 v[216:219], a[28:31], a[88:91], v[216:219]\n\t"
    "ds_read_b64_tr_b16 a[22:23], v8 offset:16672\n\t"
    "v_mfma_f32_16x16x32_bf16 v[220:223], a[28:31], a[92:95], v[220:223]\n\t"
    "s_add_u32 m0, s86, 0\n\t"
    "s_add_u32 s82, %[sWk], s94\n\t"
    "v_mfma_f32_16x16x32_bf16 v[220:223], a[24:27], a[76:79], v[220:223]\n\t"
    "buffer_load_dwordx4 %[vW], %[srdW], s82 offen lds\n\t"
    "s_waitcnt lgkmcnt(8)\n\t"
    "v_mfma_f32_16x16x32_bf16 v[224:227], a[0:3], a[64:67], v[224:227]\n\t"
    "ds_read_b64_tr_b16 a[24:25], v8 offset:8\n\t"
    "v_mfma_f32_16x16x32_bf16 v[224:227], a[4:7], a[80:83], v[224:227]\n\t"
    "ds_read_b64_tr_b16 a[26:27], v8 offset:264\n\t"
    "v_mfma_f32_16x16x32_bf16 v[228:231], a[4:7], a[84:87], v[228:231]\n\t"
    "s_add_u32 m0, s87, 0\n\t"
    "s_add_u32 s82, %[sWk], s95\n\t"
    "v_mfma_f32_16x16x32_bf16 v[228:231], a[0:3], a[68:71], v[228:231]\n\t"
    "buffer_load_dwordx4 %[vW], %[srdW], s82 offen lds\n\t"
    "v_mfma_f32_16x16x32_bf16 v[232:235], a[0:3], a[72:75], v[232:235]\n\t"
    "ds_read_b64_tr_b16 a[28:29], v8 offset:16424\n\t"
    "v_mfma_f32_16x16x32_bf16 v[232:235], a[4:7], a[88:91], v[232:235]\n\t"
    "ds_read_b64_tr_b16 a[30:31], v8 offset:16680\n\t"
    "v_mfma_f32_16x16x32_bf16 v[236:239], a[4:7], a[92:95], v[236:239]\n\t"
    "v_mfma_f32_16x16x32_bf16 v[236:239], a[0:3], a[76:79], v[236:239]\n\t"
    "s_waitcnt lgkmcnt(8)\n\t"
    "v_mfma_f32_16x16x32_bf16 v[240:243], a[8:11], a[64:67], v[240:243]\n\t"
    "ds_read_b64_tr_b16 a[0:1], v8 offset:512\n\t"
    "v_mfma_f32_16x16x32_bf16 v[240:243], a[12:15], a[80:83], v[240:243]\n\t"
    "ds_read_b64_tr_b16 a[2:3], v8 offset:768\n\t"
    "v_mfma_f32_16x16x32_bf16 v[244:247], a[12:15], a[84:87], v[244:247]\n\t"
    "s_add_u32 m0, s88, 0\n\t"
    "s_add_u32 s82, %[sWk], s96\n\t"
    "v_mfma_f32_16x16x32_bf16 v[244:247], a[8:11], a[68:71], v[244:247]\n\t"
    "buffer_load_dwordx4 %[vW], %[srdW], s82 offen lds\n\t"
    "v_mfma_f32_16x16x32_bf16 v[248:251], a[8:11], a[72:75], v[248:251]\n\t"
    "ds_read_b64_tr_b16 a[4:5], v8 offset:16928\n\t"
    "v_mfma_f32_16x16x32_bf16 v[248:251], a[12:15], a[88:91], v[248:251]\n\t"
    "ds_read_b64_tr_b16 a[6:7], v8 offset:17184\n\t"
    "v_mfma_f32_16x16x32_bf16 v[252:255], a[12:15], a[92:95], v[252:255]\n\t"
    "s_add_u32 m0, s89, 0\n\t"
    "s_add_u32 s82, %[sWk], s97\n\t"
    "v_mfma_f32_16x16x32_bf16 v[252:255], a[8:11], a[76:79], v[252:255]\n\t"
    "buffer_load_dwordx4 %[vW], %[srdW], s82 offen lds\n\t"
    "s_waitcnt vmcnt(22) lgkmcnt(8)\n\t"
    "v_mfma_f32_16x16x32_bf16 v[16:19], a[16:19], a[96:99], v[16:19]\n\t"
    "ds_read_b64_tr_b16 a[8:9], v8 offset:520\n\t"
    "v_mfma_f32_16x16x32_bf16 v[16:19], a[20:23], a[112:115], v[16:19]\n\t"
    "ds_read_b64_tr_b16 a[10:11], v8 offset:776\n\t"
    "v_mfma_f32_16x16x32_bf16 v[20:23], a[20:23], a[116:119], v[20:23]\n\t"
    "s_add_u32 m0, s90, 0\n\t"
    "s_add_u32 s82, %[sWk], s98\n\t"
    "v_mfma_f32_16x16x32_bf16 v[20:23], a[16:19], a[100:103], v[20:23]\n\t"
    "buffer_load_dwordx4 %[vW], %[srdW], s82 offen lds\n\t"
    "v_mfma_f32_16x16x32_bf16 v[24:27], a[16:19], a[104:107], v[24:27]\n\t"
    "ds_read_b64_tr_b16 a[12:13], v8 offset:16936\n\t"
    "v_mfma_f32_16x16x32_bf16 v[24:27], a[20:23], a[120:123], v[24:27]\n\t"
    "ds_read_b64_tr_b16 a[14:15], v8 offset:17192\n\t"
    "v_mfma_f32_16x16x32_bf16 v[28:31], a[20:23], a[124:127], v[28:31]\n\t"
    "s_add_u32 m0, s91, 0\n\t"
    "s_add_u32 s82, %[sWk], s99\n\t"
    "v_mfma_f32_16x16x32_bf16 v[28:31], a[16:19], a[108:111], v[28:31]\n\t"
    "buffer_load_dwordx4 %[vW], %[srdW], s82 offen lds\n\t"
    "s_waitcnt lgkmcnt(8)\n\t"
    "s_barrier\n\t"
    "v_mfma_f32_16x16x32_bf16 v[32:35], a[24:27], a[96:99], v[32:35]\n\t"
    "ds_read_b64_tr_b16 a[16:17], v8 offset:1024\n\t"
    "v_mfma_f32_16x16x32_bf16 v[32:35], a[28:31], a[112:115], v[32:35]\n\t"
    "ds_read_b64_tr_b16 a[18:19], v8 offset:1280\n\t"
    "v_mfma_f32_16x16x32_bf16 v[36:39], a[28:31], a[116:119], v[36:39]\n\t"
    "s_add_u32 %[sX], %[sX], 4096\n\t"
    "s_and_b32 %[sX], %[sX], 0x1ffff\n\t"
    "v_mfma_f32_16x16x32_bf16 v[36:39], a[24:27], a[100:103], v[36:39]\n\t"
    "buffer_load_dwordx4 a[32:35], %[vX], %[srdWS], %[sX] offen offset:0\n\t"
    "v_mfma_f32_16x16x32_bf16 v[40:43], a[24:27], a[104:107], v[40:43]\n\t"
    "ds_read_b64_tr_b16 a[20:21], v8 offset:17440\n\t"
    "v_mfma_f32_16x16x32_bf16 v[40:43], a[28:31], a[120:123], v[40:43]\n\t"
    "ds_read_b64_tr_b16 a[22:23], v8 offset:17696\n\t"
    "v_mfma_f32_16x16x32_bf16 v[44:47], a[28:31], a[124:127], v[44:47]\n\t"
    "v_mfma_f32_16x16x32_bf16 v[44:47], a[24:27], a[108:111], v[44:47]\n\t"
    "s_waitcnt lgkmcnt(8)\n\t"
    "v_mfma_f32_16x16x32_bf16 v[48:51], a[0:3], a[96:99], v[48:51]\n\t"
    "ds_read_b64_tr_b16 a[24:25], v8 offset:1032\n\t"
    "v_mfma_f32_16x16x32_bf16 v[48:51], a[4:7], a[112:115], v[48:51]\n\t"
    "ds_read_b64_tr_b16 a[26:27], v8 offset:1288\n\t"
    "v_mfma_f32_16x16x32_bf16 v[52:55], a[4:7], a[116:119], v[52:55]\n\t"
    "v_mfma_f32_16x16x32_bf16 v[52:55], a[0:3], a[100:103], v[52:55]\n\t"
    "buffer_load_dwordx4 a[36:39], %[vX], %[srdWS], %[sX] offen offset:1024\n\t"
    "v_mfma_f32_16x16x32_bf16 v[56:59], a[0:3], a[104:107], v[56:59]\n\t"
    "ds_read_b64_tr_b16 a[28:29], v8 offset:17448\n\t"
    "v_mfma_f32_16x16x32_bf16 v[56:59], a[4:7], a[120:123], v[56:59]\n\t"
    "ds_read_b64_tr_b16 a[30:31], v8 offset:17704\n\t"
    "v_mfma_f32_16x16x32_bf16 v[60:63], a[4:7], a[124:127], v[60:63]\n\t"
    "v_mfma_f32_16x16x32_bf16 v[60:63], a[0:3], a[108:111], v[60:63]\n\t"
    "buffer_load_dwordx4 a[40:43], %[vX], %[srdWS], %[sX] offen offset:2048\n\t"
    "s_waitcnt lgkmcnt(8)\n\t"
    "v_mfma_f32_16x16x32_bf16 v[64:67], a[8:11], a[96:99], v[64:67]\n\t"
    "ds_read_b64_tr_b16 a[0:1], v8 offset:1536\n\t"
    "v_mfma_f32_16x16x32_bf16 v[64:67], a[12:15], a[112:115], v[64:67]\n\t"
    "ds_read_b64_tr_b16 a[2:3], v8 offset:1792\n\t"
    "v_mfma_f32_16x16x32_bf16 v[68:71], a[12:15], a[116:119], v[68:71]\n\t"
    "v_mfma_f32_16x16x32_bf16 v[68:71], a[8:11], a[100:103], v[68:71]\n\t"
    "buffer_load_dwordx4 a[44:47], %[vX], %[srdWS], %[sX] offen offset:3072\n\t"
    "v_mfma_f32_16x16x32_bf16 v[72:75], a[8:11], a[104:107], v[72:75]\n\t"
    "ds_read_b64_tr_b16 a[4:5], v8 offset:17952\n\t"
    "v_mfma_f32_16x16x32_bf16 v[72:75], a[12:15], a[120:123], v[72:75]\n\t"
    "ds_read_b64_tr_b16 a[6:7], v8 offset:18208\n\t"
    "v_mfma_f32_16x16x32_bf16 v[76:79], a[12:15], a[124:127], v[76:79]\n\t"
    "v_mfma_f32_16x16x32_bf16 v[76:79], a[8:11], a[108:111], v[76:79]\n\t"
    "s_waitcnt lgkmcnt(8)\n\t"
    "v_mfma_f32_16x16x32_bf16 v[80:83], a[16:19], a[96:99], v[80:83]\n\t"
    "ds_read_b64_tr_b16 a[8:9], v8 offset:1544\n\t"
    "v_mfma_f32_16x16x32_bf16 v[80:83], a[20:23], a[112:115], v[80:83]\n\t"
    "ds_read_b64_tr_b16 a[10:11], v8 offset:1800\n\t"
    "v_mfma_f32_16x16x32_bf16 v[84:87], a[20:23], a[116:119], v[84:87]\n\t"
    "s_add_u32 %[sX], %[sX], 4096\n\t"
    "v_mfma_f32_16x16x32_bf16 v[84:87], a[16:19], a[100:103], v[84:87]\n\t"
    "buffer_load_dwordx4 a[48:51], %[vX], %[srdWS], %[sX] offen offset:0\n\t"
    "v_mfma_f32_16x16x32_bf16 v[88:91], a[16:19], a[104:107], v[88:91]\n\t"
    "ds_read_b64_tr_b16 a[12:13], v8 offset:17960\n\t"
    "v_mfma_f32_16x16x32_bf16 v[88:91], a[20:23], a[120:123], v[88:91]\n\t"
    "ds_read_b64_tr_b16 a[14:15], v8 offset:18216\n\t"
    "v_mfma_f32_16x16x32_bf16 v[92:95], a[20:23], a[124:127], v[92:95]\n\t"
    "v_mfma_f32_16x16x32_bf16 v[92:95], a[16:19], a[108:111], v[92:95]\n\t"
    "buffer_load_dwordx4 a[52:55], %[vX], %[srdWS], %[sX] offen offset:1024\n\t"
    "s_waitcnt lgkmcnt(8)\n\t"
    "v_mfma_f32_16x16x32_bf16 v[96:99], a[24:27], a[96:99], v[96:99]\n\t"
    "ds_read_b64_tr_b16 a[16:17], v8 offset:2048\n\t"
    "v_mfma_f32_16x16x32_bf16 v[96:99], a[28:31], a[112:115], v[96:99]\n\t"
    "ds_read_b64_tr_b16 a[18:19], v8 offset:2304\n\t"
    "v_mfma_f32_16x16x32_bf16 v[100:103], a[28:31], a[116:119], v[100:103]\n\t"
    "v_mfma_f32_16x16x32_bf16 v[100:103], a[24:27], a[100:103], v[100:103]\n\t"
    "buffer_load_dwordx4 a[56:59], %[vX], %[srdWS], %[sX] offen offset:2048\n\t"
    "v_mfma_f32_16x16x32_bf16 v[104:107], a[24:27], a[104:107], v[104:107]\n\t"
    "ds_read_b64_tr_b16 a[20:21], v8 offset:18464\n\t"
    "v_mfma_f32_16x16x32_bf16 v[104:107], a[28:31], a[120:123], v[104:107]\n\t"
    "ds_read_b64_tr_b16 a[22:23], v8 offset:18720\n\t"
    "v_mfma_f32_16x16x32_bf16 v[108:111], a[28:31], a[124:127], v[108:111]\n\t"
    "v_mfma_f32_16x16x32_bf16 v[108:111], a[24:27], a[108:111], v[108:111]\n\t"
    "s_waitcnt lgkmcnt(8)\n\t"
    "v_mfma_f32_16x16x32_bf16 v[112:115], a[0:3], a[96:99], v[112:115]\n\t"
    "ds_read_b64_tr_b16 a[24:25], v8 offset:2056\n\t"
    "v_mfma_f32_16x16x32_bf16 v[112:115], a[4:7], a[112:115], v[112:115]\n\t"
    "ds_read_b64_tr_b16 a[26:27], v8 offset:2312\n\t"
    "v_mfma_f32_16x16x32_bf16 v[116:119], a[4:7], a[116:119], v[116:119]\n\t"
    "v_mfma_f32_16x16x32_bf16 v[116:119], a[0:3], a[100:103], v[116:119]\n\t"
    "buffer_load_dwordx4 a[60:63], %[vX], %[srdWS], %[sX] offen offset:3072\n\t"
    "v_mfma_f32_16x16x32_bf16 v[120:123], a[0:3], a[104:107], v[120:123]\n\t"
    "ds_read_b64_tr_b16 a[28:29], v8 offset:18472\n\t"
    "v_mfma_f32_16x16x32_bf16 v[120:123], a[4:7], a[120:123], v[120:123]\n\t"
    "ds_read_b64_tr_b16 a[30:31], v8 offset:18728\n\t"
    "v_mfma_f32_16x16x32_bf16 v[124:127], a[4:7], a[124:127], v[124:127]\n\t"
    "s_add_u32 %[sWk], %[sWk], s83\n\t"
    "s_add_u32 m0, s84, 32832\n\t"
    "s_add_u32 s82, %[sWk], s92\n\t"
    "v_mfma_f32_16x16x32_bf16 v[124:127], a[0:3], a[108:111], v[124:127]\n\t"
    "buffer_load_dwordx4 %[vW], %[srdW], s82 offen lds\n\t"
    "s_waitcnt lgkmcnt(8)\n\t"
    "v_mfma_f32_16x16x32_bf16 v[128:131], a[8:11], a[96:99], v[128:131]\n\t"
    "ds_read_b64_tr_b16 a[0:1], v8 offset:2560\n\t"
    "v_mfma_f32_16x16x32_bf16 v[128:131], a[12:15], a[112:115], v[128:131]\n\t"
    "ds_read_b64_tr_b16 a[2:3], v8 offset:2816\n\t"
    "v_mfma_f32_16x16x32_bf16 v[132:135], a[12:15], a[116:119], v[132:135]\n\t"
    "s_add_u32 m0, s85, 32832\n\t"
    "s_add_u32 s82, %[sWk], s93\n\t"
    "v_mfma_f32_16x16x32_bf16 v[132:135], a[8:11], a[100:103], v[132:135]\n\t"
    "buffer_load_dwordx4 %[vW], %[srdW], s82 offen lds\n\t"
    "v_mfma_f32_16x16x32_bf16 v[136:139], a[8:11], a[104:107], v[136:139]\n\t"
    "ds_read_b64_tr_b16 a[4:5], v8 offset:18976\n\t"
    "v_mfma_f32_16x16x32_bf16 v[136:139], a[12:15], a[120:123], v[136:139]\n\t"
    "ds_read_b64_tr_b16 a[6:7], v8 offset:19232\n\t"
    "v_mfma_f32_16x16x32_bf16 v[140:143], a[12:15], a[124:127], v[140:143]\n\t"
    "v_mfma_f32_16x16x32_bf16 v[140:143], a[8:11], a[108:111], v[140:143]\n\t"
    "s_waitcnt lgkmcnt(8)\n\t"
    "v_mfma_f32_16x16x32_bf16 v[144:147], a[16:19], a[96:99], v[144:147]\n\t"
    "ds_read_b64_tr_b16 a[8:9], v8 offset:2568\n\t"
    "v_mfma_f32_16x16x32_bf16 v[144:147], a[20:23], a[112:115], v[144:147]\n\t"
    "ds_read_b64_tr_b16 a[10:11], v8 offset:2824\n\t"
    "v_mfma_f32_16x16x32_bf16 v[148:151], a[20:23], a[116:119], v[148:151]\n\t"
    "s_add_u32 m0, s86, 32832\n\t"
    "s_add_u32 s82, %[sWk], s94\n\t"
    "v_mfma_f32_16x16x32_bf16 v[148:151], a[16:19], a[100:103], v[148:151]\n\t"
    "buffer_load_dwordx4 %[vW], %[srdW], s82 offen lds\n\t"
    "v_mfma_f32_16x16x32_bf16 v[152:155], a[16:19], a[104:107], v[152:155]\n\t"
    "ds_read_b64_tr_b16 a[12:13], v8 offset:18984\n\t"
    "v_mfma_f32_16x16x32_bf16 v[152:155], a[20:23], a[120:123], v[152:155]\n\t"
    "ds_read_b64_tr_b16 a[14:15], v8 offset:19240\n\t"
    "v_mfma_f32_16x16x32_bf16 v[156:159], a[20:23], a[124:127], v[156:159]\n\t"
    "s_add_u32 m0, s87, 32832\n\t"
    "s_add_u32 s82, %[sWk], s95\n\t"
    "v_mfma_f32_16x16x32_bf16 v[156:159], a[16:19], a[108:111], v[156:159]\n\t"
    "buffer_load_dwordx4 %[vW], %[srdW], s82 offen lds\n\t"
    "s_waitcnt lgkmcnt(8)\n\t"
    "v_mfma_f32_16x16x32_bf16 v[160:163], a[24:27], a[96:99], v[160:163]\n\t"
    "ds_read_b64_tr_b16 a[16:17], v8 offset:3072\n\t"
    "v_mfma_f32_16x16x32_bf16 v[160:163], a[28:31], a[112:115], v[160:163]\n\t"
    "ds_read_b64_tr_b16 a[18:19], v8 offset:3328\n\t"
    "v_mfma_f32_16x16x32_bf16 v[164:167], a[28:31], a[116:119], v[164:167]\n\t"
    "s_add_u32 m0, s88, 32832\n\t"
    "s_add_u32 s82, %[sWk], s96\n\t"
    "v_mfma_f32_16x16x32_bf16 v[164:167], a[24:27], a[100:103], v[164:167]\n\t"
    "buffer_load_dwordx4 %[vW], %[srdW], s82 offen lds\n\t"
    "v_mfma_f32_16x16x32_bf16 v[168:171], a[24:27], a[104:107], v[168:171]\n\t"
    "ds_read_b64_tr_b16 a[20:21], v8 offset:19488\n\t"
    "v_mfma_f32_16x16x32_bf16 v[168:171], a[28:31], a[120:123], v[168:171]\n\t"
    "ds_read_b64_tr_b16 a[22:23], v8 offset:19744\n\t"
    "v_mfma_f32_16x16x32_bf16 v[172:175], a[28:31], a[124:127], v[172:175]\n\t"
    "v_mfma_f32_16x16x32_bf16 v[172:175], a[24:27], a[108:111], v[172:175]\n\t"
    "s_waitcnt lgkmcnt(8)\n\t"
    "v_mfma_f32_16x16x32_bf16 v[176:179], a[0:3], a[96:99], v[176:179]\n\t"
    "ds_read_b64_tr_b16 a[24:25], v8 offset:3080\n\t"
    "v_mfma_f32_16x16x32_bf16 v[176:179], a[4:7], a[112:115], v[176:179]\n\t"
    "ds_read_b64_tr_b16 a[26:27], v8 offset:3336\n\t"
    "v_mfma_f32_16x16x32_bf16 v[180:183], a[4:7], a[116:119], v[180:183]\n\t"
    "s_add_u32 m0, s89, 32832\n\t"
    "s_add_u32 s82, %[sWk], s97\n\t"
    "v_mfma_f32_16x16x32_bf16 v[180:183], a[0:3], a[100:103], v[180:183]\n\t"
    "buffer_load_dwordx4 %[vW], %[srdW], s82 offen lds\n\t"
    "v_mfma_f32_16x16x32_bf16 v[184:187], a[0:3], a[104:107], v[184:187]\n\t"
    "ds_read_b64_tr_b16 a[28:29], v8 offset:19496\n\t"
    "v_mfma_f32_16x16x32_bf16 v[184:187], a[4:7], a[120:123], v[184:187]\n\t"
    "ds_read_b64_tr_b16 a[30:31], v8 offset:19752\n\t"
    "v_mfma_f32_16x16x32_bf16 v[188:191], a[4:7], a[124:127], v[188:191]\n\t"
    "s_add_u32 m0, s90, 32832\n\t"
    "s_add_u32 s82, %[sWk], s98\n\t"
    "v_mfma_f32_16x16x32_bf16 v[188:191], a[0:3], a[108:111], v[188:191]\n\t"
    "buffer_load_dwordx4 %[vW], %[srdW], s82 offen lds\n\t"
    "s_waitcnt lgkmcnt(8)\n\t"
    "v_mfma_f32_16x16x32_bf16 v[192:195], a[8:11], a[96:99], v[192:195]\n\t"
    "ds_read_b64_tr_b16 a[0:1], v9 offset:3584\n\t"
    "v_mfma_f32_16x16x32_bf16 v[192:195], a[12:15], a[112:115], v[192:195]\n\t"
    "ds_read_b64_tr_b16 a[2:3], v9 offset:3840\n\t"
    "v_mfma_f32_16x16x32_bf16 v[196:199], a[12:15], a[116:119], v[196:199]\n\t"
    "s_add_u32 %[sX], %[sX], 4096\n\t"
    "s_and_b32 %[sX], %[sX], 0x1ffff\n\t"
    "v_mfma_f32_16x16x32_bf16 v[196:199], a[8:11], a[100:103], v[196:199]\n\t"
    "buffer_load_dwordx4 a[64:67], %[vX], %[srdWS], %[sX] offen offset:0\n\t"
    "v_mfma_f32_16x16x32_bf16 v[200:203], a[8:11], a[104:107], v[200:203]\n\t"
    "ds_read_b64_tr_b16 a[4:5], v9 offset:20000\n\t"
    "v_mfma_f32_16x16x32_bf16 v[200:203], a[12:15], a[120:123], v[200:203]\n\t"
    "ds_read_b64_tr_b16 a[6:7], v9 offset:20256\n\t"
    "v_mfma_f32_16x16x32_bf16 v[204:207], a[12:15], a[124:127], v[204:207]\n\t"
    "v_mfma_f32_16x16x32_bf16 v[204:207], a[8:11], a[108:111], v[204:207]\n\t"
    "s_waitcnt vmcnt(32) lgkmcnt(8)\n\t"
    "s_barrier\n\t"
    "v_mfma_f32_16x16x32_bf16 v[208:211], a[16:19], a[96:99], v[208:211]\n\t"
    "ds_read_b64_tr_b16 a[8:9], v8 offset:32832\n\t"
    "v_mfma_f32_16x16x32_bf16 v[208:211], a[20:23], a[112:115], v[208:211]\n\t"
    "ds_read_b64_tr_b16 a[10:11], v8 offset:33088\n\t"
    "v_mfma_f32_16x16x32_bf16 v[212:215], a[20:23], a[116:119], v[212:215]\n\t"
    "s_add_u32 m0, s91, 32832\n\t"
    "s_add_u32 s82, %[sWk], s99\n\t"
    "v_mfma_f32_16x16x32_bf16 v[212:215], a[16:19], a[100:103], v[212:215]\n\t"
    "buffer_load_dwordx4 %[vW], %[srdW], s82 offen lds\n\t"
    "v_mfma_f32_16x16x32_bf16 v[216:219], a[16:19], a[104:107], v[216:219]\n\t"
    "ds_read_b64_tr_b16 a[12:13], v8 offset:49248\n\t"
    "v_mfma_f32_16x16x32_bf16 v[216:219], a[20:23], a[120:123], v[216:219]\n\t"
    "ds_read_b64_tr_b16 a[14:15], v8 offset:49504\n\t"
    "v_mfma_f32_16x16x32_bf16 v[220:223], a[20:23], a[124:127], v[220:223]\n\t"
    "v_mfma_f32_16x16x32_bf16 v[220:223], a[16:19], a[108:111], v[220:223]\n\t"
    "buffer_load_dwordx4 a[68:71], %[vX], %[srdWS], %[sX] offen offset:1024\n\t"
    "s_waitcnt lgkmcnt(8)\n\t"
    "v_mfma_f32_16x16x32_bf16 v[224:227], a[24:27], a[96:99], v[224:227]\n\t"
    "ds_read_b64_tr_b16 a[16:17], v8 offset:32840\n\t"
    "v_mfma_f32_16x16x32_bf16 v[224:227], a[28:31], a[112:115], v[224:227]\n\t"
    "ds_read_b64_tr_b16 a[18:19], v8 offset:33096\n\t"
    "v_mfma_f32_16x16x32_bf16 v[228:231], a[28:31], a[116:119], v[228:231]\n\t"
    "v_mfma_f32_16x16x32_bf16 v[228:231], a[24:27], a[100:103], v[228:231]\n\t"
    "buffer_load_dwordx4 a[72:75], %[vX], %[srdWS], %[sX] offen offset:2048\n\t"
    "v_mfma_f32_16x16x32_bf16 v[232:235], a[24:27], a[104:107], v[232:235]\n\t"
    "ds_read_b64_tr_b16 a[20:21], v8 offset:49256\n\t"
    "v_mfma_f32_16x16x32_bf16 v[232:235], a[28:31], a[120:123], v[232:235]\n\t"
    "ds_read_b64_tr_b16 a[22:23], v8 offset:49512\n\t"
    "v_mfma_f32_16x16x32_bf16 v[236:239], a[28:31], a[124:127], v[236:239]\n\t"
    "v_mfma_f32_16x16x32_bf16 v[236:239], a[24:27], a[108:111], v[236:239]\n\t"
    "s_waitcnt lgkmcnt(8)\n\t"
    "v_mfma_f32_16x16x32_bf16 v[240:243], a[0:3], a[96:99], v[240:243]\n\t"
    "ds_read_b64_tr_b16 a[24:25], v8 offset:33344\n\t"
    "v_mfma_f32_16x16x32_bf16 v[240:243], a[4:7], a[112:115], v[240:243]\n\t"
    "ds_read_b64_tr_b16 a[26:27], v8 offset:33600\n\t"
    "v_mfma_f32_16x16x32_bf16 v[244:247], a[4:7], a[116:119], v[244:247]\n\t"
    "v_mfma_f32_16x16x32_bf16 v[244:247], a[0:3], a[100:103], v[244:247]\n\t"
    "buffer_load_dwordx4 a[76:79], %[vX], %[srdWS], %[sX] offen offset:3072\n\t"
    "v_mfma_f32_16x16x32_bf16 v[248:251], a[0:3], a[104:107], v[248:251]\n\t"
    "ds_read_b64_tr_b16 a[28:29], v8 offset:49760\n\t"
    "v_mfma_f32_16x16x32_bf16 v[248:251], a[4:7], a[120:123], v[248:251]\n\t"
    "ds_read_b64_tr_b16 a[30:31], v8 offset:50016\n\t"
    "v_mfma_f32_16x16x32_bf16 v[252:255], a[4:7], a[124:127], v[252:255]\n\t"
    "s_add_u32 %[sX], %[sX], 4096\n\t"
    "v_mfma_f32_16x16x32_bf16 v[252:255], a[0:3], a[108:111], v[252:255]\n\t"
    "buffer_load_dwordx4 a[80:83], %[vX], %[srdWS], %[sX] offen offset:0\n\t"
    "s_waitcnt vmcnt(29) lgkmcnt(8)\n\t"
    "v_mfma_f32_16x16x32_bf16 v[16:19], a[8:11], a[128:131], v[16:19]\n\t"
    "ds_read_b64_tr_b16 a[0:1], v8 offset:33352\n\t"
    "v_mfma_f32_16x16x32_bf16 v[16:19], a[12:15], a[144:147], v[16:19]\n\t"
    "ds_read_b64_tr_b16 a[2:3], v8 offset:33608\n\t"
    "v_mfma_f32_16x16x32_bf16 v[20:23], a[12:15], a[148:151], v[20:23]\n\t"
    "v_mfma_f32_16x16x32_bf16 v[20:23], a[8:11], a[132:135], v[20:23]\n\t"
    "buffer_load_dwordx4 a[84:87], %[vX], %[srdWS], %[sX] offen offset:1024\n\t"
    "v_mfma_f32_16x16x32_bf16 v[24:27], a[8:11], a[136:139], v[24:27]\n\t"
    "ds_read_b64_tr_b16 a[4:5], v8 offset:49768\n\t"
    "v_mfma_f32_16x16x32_bf16 v[24:27], a[12:15], a[152:155], v[24:27]\n\t"
    "ds_read_b64_tr_b16 a[6:7], v8 offset:50024\n\t"
    "v_mfma_f32_16x16x32_bf16 v[28:31], a[12:15], a[156:159], v[28:31]\n\t"
    "v_mfma_f32_16x16x32_bf16 v[28:31], a[8:11], a[140:143], v[28:31]\n\t"
    "buffer_load_dwordx4 a[88:91], %[vX], %[srdWS], %[sX] offen offset:2048\n\t"
    "s_waitcnt lgkmcnt(8)\n\t"
    "s_barrier\n\t"
    "v_mfma_f32_16x16x32_bf16 v[32:35], a[16:19], a[128:131], v[32:35]\n\t"
    "ds_read_b64_tr_b16 a[8:9], v8 offset:33856\n\t"
    "v_mfma_f32_16x16x32_bf16 v[32:35], a[20:23], a[144:147], v[32:35]\n\t"
    "ds_read_b64_tr_b16 a[10:11], v8 offset:34112\n\t"
    "v_mfma_f32_16x16x32_bf16 v[36:39], a[20:23], a[148:151], v[36:39]\n\t"
    "v_mfma_f32_16x16x32_bf16 v[36:39], a[16:19], a[132:135], v[36:39]\n\t"
    "buffer_load_dwordx4 a[92:95], %[vX], %[srdWS], %[sX] offen offset:3072\n\t"
    "v_mfma_f32_16x16x32_bf16 v[40:43], a[16:19], a[136:139], v[40:43]\n\t"
    "ds_read_b64_tr_b16 a[12:13], v8 offset:50272\n\t"
    "v_mfma_f32_16x16x32_bf16 v[40:43], a[20:23], a[152:155], v[40:43]\n\t"
    "ds_read_b64_tr_b16 a[14:15], v8 offset:50528\n\t"
    "v_mfma_f32_16x16x32_bf16 v[44:47], a[20:23], a[156:159], v[44:47]\n\t"
    "s_add_u32 %[sWk], %[sWk], s83\n\t"
    "s_add_u32 m0, s84, 65664\n\t"
    "s_add_u32 s82, %[sWk], s92\n\t"
    "v_mfma_f32_16x16x32_bf16 v[44:47], a[16:19], a[140:143], v[44:47]\n\t"
    "buffer_load_dwordx4 %[vW], %[srdW], s82 offen lds\n\t"
    "s_waitcnt lgkmcnt(8)\n\t"
    "v_mfma_f32_16x16x32_bf16 v[48:51], a[24:27], a[128:131], v[48:51]\n\t"
    "ds_read_b64_tr_b16 a[16:17], v8 offset:33864\n\t"
    "v_mfma_f32_16x16x32_bf16 v[48:51], a[28:31], a[144:147], v[48:51]\n\t"
    "ds_read_b64_tr_b16 a[18:19], v8 offset:34120\n\t"
    "v_mfma_f32_16x16x32_bf16 v[52:55], a[28:31], a[148:151], v[52:55]\n\t"
    "s_add_u32 %[sX], %[sX], 4096\n\t"
    "s_and_b32 %[sX], %[sX], 0x1ffff\n\t"
    "v_mfma_f32_16x16x32_bf16 v[52:55], a[24:27], a[132:135], v[52:55]\n\t"
    "buffer_load_dwordx4 a[96:99], %[vX], %[srdWS], %[sX] offen offset:0\n\t"
    "v_mfma_f32_16x16x32_bf16 v[56:59], a[24:27], a[136:139], v[56:59]\n\t"
    "ds_read_b64_tr_b16 a[20:21], v8 offset:50280\n\t"
    "v_mfma_f32_16x16x32_bf16 v[56:59], a[28:31], a[152:155], v[56:59]\n\t"
    "ds_read_b64_tr_b16 a[22:23], v8 offset:50536\n\t"
    "v_mfma_f32_16x16x32_bf16 v[60:63], a[28:31], a[156:159], v[60:63]\n\t"
    "s_add_u32 m0, s85, 65664\n\t"
    "s_add_u32 s82, %[sWk], s93\n\t"
    "v_mfma_f32_16x16x32_bf16 v[60:63], a[24:27], a[140:143], v[60:63]\n\t"
    "buffer_load_dwordx4 %[vW], %[srdW], s82 offen lds\n\t"
    "s_waitcnt lgkmcnt(8)\n\t"
    "v_mfma_f32_16x16x32_bf16 v[64:67], a[0:3], a[128:131], v[64:67]\n\t"
    "ds_read_b64_tr_b16 a[24:25], v8 offset:34368\n\t"
    "v_mfma_f32_16x16x32_bf16 v[64:67], a[4:7], a[144:147], v[64:67]\n\t"
    "ds_read_b64_tr_b16 a[26:27], v8 offset:34624\n\t"
    "v_mfma_f32_16x16x32_bf16 v[68:71], a[4:7], a[148:151], v[68:71]\n\t"
    "v_mfma_f32_16x16x32_bf16 v[68:71], a[0:3], a[132:135], v[68:71]\n\t"
    "buffer_load_dwordx4 a[100:103], %[vX], %[srdWS], %[sX] offen offset:1024\n\t"
    "v_mfma_f32_16x16x32_bf16 v[72:75], a[0:3], a[136:139], v[72:75]\n\t"
    "ds_read_b64_tr_b16 a[28:29], v8 offset:50784\n\t"
    "v_mfma_f32_16x16x32_bf16 v[72:75], a[4:7], a[152:155], v[72:75]\n\t"
    "ds_read_b64_tr_b16 a[30:31], v8 offset:51040\n\t"
    "v_mfma_f32_16x16x32_bf16 v[76:79], a[4:7], a[156:159], v[76:79]\n\t"
    "v_mfma_f32_16x16x32_bf16 v[76:79], a[0:3], a[140:143], v[76:79]\n\t"
    "buffer_load_dwordx4 a[104:107], %[vX], %[srdWS], %[sX] offen offset:2048\n\t"
    "s_waitcnt lgkmcnt(8)\n\t"
    "v_mfma_f32_16x16x32_bf16 v[80:83], a[8:11], a[128:131], v[80:83]\n\t"
    "ds_read_b64_tr_b16 a[0:1], v8 offset:34376\n\t"
    "v_mfma_f32_16x16x32_bf16 v[80:83], a[12:15], a[144:147], v[80:83]\n\t"
    "ds_read_b64_tr_b16 a[2:3], v8 offset:34632\n\t"
    "v_mfma_f32_16x16x32_bf16 v[84:87], a[12:15], a[148:151], v[84:87]\n\t"
    "s_add_u32 m0, s86, 65664\n\t"
    "s_add_u32 s82, %[sWk], s94\n\t"
    "v_mfma_f32_16x16x32_bf16 v[84:87], a[8:11], a[132:135], v[84:87]\n\t"
    "buffer_load_dwordx4 %[vW], %[srdW], s82 offen lds\n\t"
    "v_mfma_f32_16x16x32_bf16 v[88:91], a[8:11], a[136:139], v[88:91]\n\t"
    "ds_read_b64_tr_b16 a[4:5], v8 offset:50792\n\t"
    "v_mfma_f32_16x16x32_bf16 v[88:91], a[12:15], a[152:155], v[88:91]\n\t"
    "ds_read_b64_tr_b16 a[6:7], v8 offset:51048\n\t"
    "v_mfma_f32_16x16x32_bf16 v[92:95], a[12:15], a[156:159], v[92:95]\n\t"
    "v_mfma_f32_16x16x32_bf16 v[92:95], a[8:11], a[140:143], v[92:95]\n\t"
    "s_waitcnt lgkmcnt(8)\n\t"
    "v_mfma_f32_16x16x32_bf16 v[96:99], a[16:19], a[128:131], v[96:99]\n\t"
    "ds_read_b64_tr_b16 a[8:9], v8 offset:34880\n\t"
    "v_mfma_f32_16x16x32_bf16 v[96:99], a[20:23], a[144:147], v[96:99]\n\t"
    "ds_read_b64_tr_b16 a[10:11], v8 offset:35136\n\t"
    "v_mfma_f32_16x16x32_bf16 v[100:103], a[20:23], a[148:151], v[100:103]\n\t"
    "v_mfma_f32_16x16x32_bf16 v[100:103], a[16:19], a[132:135], v[100:103]\n\t"
    "v_mfma_f32_16x16x32_bf16 v[104:107], a[16:19], a[136:139], v[104:107]\n\t"
    "ds_read_b64_tr_b16 a[12:13], v8 offset:51296\n\t"
    "v_mfma_f32_16x16x32_bf16 v[104:107], a[20:23], a[152:155], v[104:107]\n\t"
    "ds_read_b64_tr_b16 a[14:15], v8 offset:51552\n\t"
    "v_mfma_f32_16x16x32_bf16 v[108:111], a[20:23], a[156:159], v[108:111]\n\t"
    "s_add_u32 m0, s87, 65664\n\t"
    "s_add_u32 s82, %[sWk], s95\n\t"
    "v_mfma_f32_16x16x32_bf16 v[108:111], a[16:19], a[140:143], v[108:111]\n\t"
    "buffer_load_dwordx4 %[vW], %[srdW], s82 offen lds\n\t"
    "s_waitcnt lgkmcnt(8)\n\t"
    "v_mfma_f32_16x16x32_bf16 v[112:115], a[24:27], a[128:131], v[112:115]\n\t"
    "ds_read_b64_tr_b16 a[16:17], v8 offset:34888\n\t"
    "v_mfma_f32_16x16x32_bf16 v[112:115], a[28:31], a[144:147], v[112:115]\n\t"
    "ds_read_b64_tr_b16 a[18:19], v8 offset:35144\n\t"
    "v_mfma_f32_16x16x32_bf16 v[116:119], a[28:31], a[148:151], v[116:119]\n\t"
    "v_mfma_f32_16x16x32_bf16 v[116:119], a[24:27], a[132:135], v[116:119]\n\t"
    "buffer_load_dwordx4 a[108:111], %[vX], %[srdWS], %[sX] offen offset:3072\n\t"
    "v_mfma_f32_16x16x32_bf16 v[120:123], a[24:27], a[136:139], v[120:123]\n\t"
    "ds_read_b64_tr_b16 a[20:21], v8 offset:51304\n\t"
    "v_mfma_f32_16x16x32_bf16 v[120:123], a[28:31], a[152:155], v[120:123]\n\t"
    "ds_read_b64_tr_b16 a[22:23], v8 offset:51560\n\t"
    "v_mfma_f32_16x16x32_bf16 v[124:127], a[28:31], a[156:159], v[124:127]\n\t"
    "v_mfma_f32_16x16x32_bf16 v[124:127], a[24:27], a[140:143], v[124:127]\n\t"
    "s_waitcnt lgkmcnt(8)\n\t"
    "v_mfma_f32_16x16x32_bf16 v[128:131], a[0:3], a[128:131], v[128:131]\n\t"
    "ds_read_b64_tr_b16 a[24:25], v8 offset:35392\n\t"
    "v_mfma_f32_16x16x32_bf16 v[128:131], a[4:7], a[144:147], v[128:131]\n\t"
    "ds_read_b64_tr_b16 a[26:27], v8 offset:35648\n\t"
    "v_mfma_f32_16x16x32_bf16 v[132:135], a[4:7], a[148:151], v[132:135]\n\t"
    "s_add_u32 m0, s88, 65664\n\t"
    "s_add_u32 s82, %[sWk], s96\n\t"
    "v_mfma_f32_16x16x32_bf16 v[132:135], a[0:3], a[132:135], v[132:135]\n\t"
    "buffer_load_dwordx4 %[vW], %[srdW], s82 offen lds\n\t"
    "v_mfma_f32_16x16x32_bf16 v[136:139], a[0:3], a[136:139], v[136:139]\n\t"
    "ds_read_b64_tr_b16 a[28:29], v8 offset:51808\n\t"
    "v_mfma_f32_16x16x32_bf16 v[136:139], a[4:7], a[152:155], v[136:139]\n\t"
    "ds_read_b64_tr_b16 a[30:31], v8 offset:52064\n\t"
    "v_mfma_f32_16x16x32_bf16 v[140:143], a[4:7], a[156:159], v[140:143]\n\t"
    "v_mfma_f32_16x16x32_bf16 v[140:143], a[0:3], a[140:143], v[140:143]\n\t"
    "s_waitcnt lgkmcnt(8)\n\t"
    "v_mfma_f32_16x16x32_bf16 v[144:147], a[8:11], a[128:131], v[144:147]\n\t"
    "ds_read_b64_tr_b16 a[0:1], v8 offset:35400\n\t"
    "v_mfma_f32_16x16x32_bf16 v[144:147], a[12:15], a[144:147], v[144:147]\n\t"
    "ds_read_b64_tr_b16 a[2:3], v8 offset:35656\n\t"
    "v_mfma_f32_16x16x32_bf16 v[148:151], a[12:15], a[148:151], v[148:151]\n\t"
    "s_add_u32 %[sX], %[sX], 4096\n\t"
    "v_mfma_f32_16x16x32_bf16 v[148:151], a[8:11], a[132:135], v[148:151]\n\t"
    "buffer_load_dwordx4 a[112:115], %[vX], %[srdWS], %[sX] offen offset:0\n\t"
    "v_mfma_f32_16x16x32_bf16 v[152:155], a[8:11], a[136:139], v[152:155]\n\t"
    "ds_read_b64_tr_b16 a[4:5], v8 offset:51816\n\t"
    "v_mfma_f32_16x16x32_bf16 v[152:155], a[12:15], a[152:155], v[152:155]\n\t"
    "ds_read_b64_tr_b16 a[6:7], v8 offset:52072\n\t"
    "v_mfma_f32_16x16x32_bf16 v[156:159], a[12:15], a[156:159], v[156:159]\n\t"
    "s_add_u32 m0, s89, 65664\n\t"
    "s_add_u32 s82, %[sWk], s97\n\t"
    "v_mfma_f32_16x16x32_bf16 v[156:159], a[8:11], a[140:143], v[156:159]\n\t"
    "buffer_load_dwordx4 %[vW], %[srdW], s82 offen lds\n\t"
    "s_waitcnt lgkmcnt(8)\n\t"
    "v_mfma_f32_16x16x32_bf16 v[160:163], a[16:19], a[128:131], v[160:163]\n\t"
    "ds_read_b64_tr_b16 a[8:9], v8 offset:35904\n\t"
    "v_mfma_f32_16x16x32_bf16 v[160:163], a[20:23], a[144:147], v[160:163]\n\t"
    "ds_read_b64_tr_b16 a[10:11], v8 offset:36160\n\t"
    "v_mfma_f32_16x16x32_bf16 v[164:167], a[20:23], a[148:151], v[164:167]\n\t"
    "v_mfma_f32_16x16x32_bf16 v[164:167], a[16:19], a[132:135], v[164:167]\n\t"
    "v_mfma_f32_16x16x32_bf16 v[168:171], a[16:19], a[136:139], v[168:171]\n\t"
    "ds_read_b64_tr_b16 a[12:13], v8 offset:52320\n\t"
    "v_mfma_f32_16x16x32_bf16 v[168:171], a[20:23], a[152:155], v[168:171]\n\t"
    "ds_read_b64_tr_b16 a[14:15], v8 offset:52576\n\t"
    "v_mfma_f32_16x16x32_bf16 v[172:175], a[20:23], a[156:159], v[172:175]\n\t"
    "k_pga1: s_cbranch_execz k_pga5\n\t"
    "v_mfma_f32_16x16x32_bf16 v[172:175], a[16:19], a[140:143], v[172:175]\n\t"
    "buffer_load_dwordx4 a[116:119], %[vX], %[srdWS], %[sX] offen offset:1024\n\t"
    "s_waitcnt lgkmcnt(8)\n\t"
    "v_mfma_f32_16x16x32_bf16 v[176:179], a[24:27], a[128:131], v[176:179]\n\t"
    "ds_read_b64_tr_b16 a[16:17], v8 offset:35912\n\t"
    "v_mfma_f32_16x16x32_bf16 v[176:179], a[28:31], a[144:147], v[176:179]\n\t"
    "ds_read_b64_tr_b16 a[18:19], v8 offset:36168\n\t"
    "v_mfma_f32_16x16x32_bf16 v[180:183], a[28:31], a[148:151], v[180:183]\n\t"
    "s_add_u32 m0, s90, 65664\n\t"
    "s_add_u32 s82, %[sWk], s98\n\t"
    "v_mfma_f32_16x16x32_bf16 v[180:183], a[24:27], a[132:135], v[180:183]\n\t"
    "buffer_load_dwordx4 %[vW], %[srdW], s82 offen lds\n\t"
    "v_mfma_f32_16x16x32_bf16 v[184:187], a[24:27], a[136:139], v[184:187]\n\t"
    "ds_read_b64_tr_b16 a[20:21], v8 offset:52328\n\t"
    "v_mfma_f32_16x16x32_bf16 v[184:187], a[28:31], a[152:155], v[184:187]\n\t"
    "ds_read_b64_tr_b16 a[22:23], v8 offset:52584\n\t"
    "v_mfma_f32_16x16x32_bf16 v[188:191], a[28:31], a[156:159], v[188:191]\n\t"
    "v_mfma_f32_16x16x32_bf16 v[188:191], a[24:27], a[140:143], v[188:191]\n\t"
    "s_waitcnt lgkmcnt(8)\n\t"
    "v_mfma_f32_16x16x32_bf16 v[192:195], a[0:3], a[128:131], v[192:195]\n\t"
    "ds_read_b64_tr_b16 a[24:25], v9 offset:36416\n\t"
    "v_mfma_f32_16x16x32_bf16 v[192:195], a[4:7], a[144:147], v[192:195]\n\t"
    "ds_read_b64_tr_b16 a[26:27], v9 offset:36672\n\t"
    "v_mfma_f32_16x16x32_bf16 v[196:199], a[4:7], a[148:151], v[196:199]\n\t"
    "v_mfma_f32_16x16x32_bf16 v[196:199], a[0:3], a[132:135], v[196:199]\n\t"
    "v_mfma_f32_16x16x32_bf16 v[200:203], a[0:3], a[136:139], v[200:203]\n\t"
    "ds_read_b64_tr_b16 a[28:29], v9 offset:52832\n\t"
    "v_mfma_f32_16x16x32_bf16 v[200:203], a[4:7], a[152:155], v[200:203]\n\t"
    "ds_read_b64_tr_b16 a[30:31], v9 offset:53088\n\t"
    "v_mfma_f32_16x16x32_bf16 v[204:207], a[4:7], a[156:159], v[204:207]\n\t"
    "s_add_u32 m0, s91, 65664\n\t"
    "s_add_u32 s82, %[sWk], s99\n\t"
    "v_mfma_f32_16x16x32_bf16 v[204:207], a[0:3], a[140:143], v[204:207]\n\t"
    "buffer_load_dwordx4 %[vW], %[srdW], s82 offen lds\n\t"
    "s_waitcnt vmcnt(38) lgkmcnt(8)\n\t"
    "s_barrier\n\t"
    "v_mfma_f32_16x16x32_bf16 v[208:211], a[8:11], a[128:131], v[208:211]\n\t"
    "ds_read_b64_tr_b16 a[0:1], %[vRdW] offset:0\n\t"
    "v_mfma_f32_16x16x32_bf16 v[208:211], a[12:15], a[144:147], v[208:211]\n\t"
    "ds_read_b64_tr_b16 a[2:3], %[vRdW] offset:256\n\t"
    "v_mfma_f32_16x16x32_bf16 v[212:215], a[12:15], a[148:151], v[212:215]\n\t"
    "v_mfma_f32_16x16x32_bf16 v[212:215], a[8:11], a[132:135], v[212:215]\n\t"
    "buffer_load_dwordx4 a[120:123], %[vX], %[srdWS], %[sX] offen offset:2048\n\t"
    "v_mfma_f32_16x16x32_bf16 v[216:219], a[8:11], a[136:139], v[216:219]\n\t"
    "ds_read_b64_tr_b16 a[4:5], %[vRdW] offset:16416\n\t"
    "v_mfma_f32_16x16x32_bf16 v[216:219], a[12:15], a[152:155], v[216:219]\n\t"
    "ds_read_b64_tr_b16 a[6:7], %[vRdW] offset:16672\n\t"
    "v_mfma_f32_16x16x32_bf16 v[220:223], a[12:15], a[156:159], v[220:223]\n\t"
    "v_mfma_f32_16x16x32_bf16 v[220:223], a[8:11], a[140:143], v[220:223]\n\t"
    "s_waitcnt lgkmcnt(8)\n\t"
    "v_mfma_f32_16x16x32_bf16 v[224:227], a[16:19], a[128:131], v[224:227]\n\t"
    "ds_read_b64_tr_b16 a[8:9], %[vRdW] offset:8\n\t"
    "v_mfma_f32_16x16x32_bf16 v[224:227], a[20:23], a[144:147], v[224:227]\n\t"
    "ds_read_b64_tr_b16 a[10:11], %[vRdW] offset:264\n\t"
    "v_mfma_f32_16x16x32_bf16 v[228:231], a[20:23], a[148:151], v[228:231]\n\t"
    "v_mfma_f32_16x16x32_bf16 v[228:231], a[16:19], a[132:135], v[228:231]\n\t"
    "v_mfma_f32_16x16x32_bf16 v[232:235], a[16:19], a[136:139], v[232:235]\n\t"
    "ds_read_b64_tr_b16 a[12:13], %[vRdW] offset:16424\n\t"
    "v_mfma_f32_16x16x32_bf16 v[232:235], a[20:23], a[152:155], v[232:235]\n\t"
    "ds_read_b64_tr_b16 a[14:15], %[vRdW] offset:16680\n\t"
    "v_mfma_f32_16x16x32_bf16 v[236:239], a[20:23], a[156:159], v[236:239]\n\t"
    "v_mfma_f32_16x16x32_bf16 v[236:239], a[16:19], a[140:143], v[236:239]\n\t"
    "buffer_load_dwordx4 a[124:127], %[vX], %[srdWS], %[sX] offen offset:3072\n\t"
    "s_waitcnt lgkmcnt(8)\n\t"
    "v_mfma_f32_16x16x32_bf16 v[240:243], a[24:27], a[128:131], v[240:243]\n\t"
    "ds_read_b64_tr_b16 a[16:17], %[vRdW] offset:512\n\t"
    "v_mfma_f32_16x16x32_bf16 v[240:243], a[28:31], a[144:147], v[240:243]\n\t"
    "ds_read_b64_tr_b16 a[18:19], %[vRdW] offset:768\n\t"
    "v_mfma_f32_16x16x32_bf16 v[244:247], a[28:31], a[148:151], v[244:247]\n\t"
    "v_mfma_f32_16x16x32_bf16 v[244:247], a[24:27], a[132:135], v[244:247]\n\t"
    "v_mfma_f32_16x16x32_bf16 v[248:251], a[24:27], a[136:139], v[248:251]\n\t"
    "ds_read_b64_tr_b16 a[20:21], %[vRdW] offset:16928\n\t"
    "v_mfma_f32_16x16x32_bf16 v[248:251], a[28:31], a[152:155], v[248:251]\n\t"
    "ds_read_b64_tr_b16 a[22:23], %[vRdW] offset:17184\n\t"
    "v_mfma_f32_16x16x32_bf16 v[252:255], a[28:31], a[156:159], v[252:255]\n\t"
    "v_mfma_f32_16x16x32_bf16 v[252:255], a[24:27], a[140:143], v[252:255]\n\t"
    "s_mov_b32 s67, 2\n\t"
    "L_k1_loop_%=:\n\t"
    "s_waitcnt vmcnt(32) lgkmcnt(8)\n\t"
    "v_mfma_f32_16x16x32_bf16 v[16:19], a[0:3], a[32:35], v[16:19]\n\t"
    "ds_read_b64_tr_b16 a[24:25], %[vRdW] offset:520\n\t"
    "v_mfma_f32_16x16x32_bf16 v[16:19], a[4:7], a[48:51], v[16:19]\n\t"
    "ds_read_b64_tr_b16 a[26:27], %[vRdW] offset:776\n\t"
    "v_mfma_f32_16x16x32_bf16 v[20:23], a[4:7], a[52:55], v[20:23]\n\t"
    "s_add_u32 %[sX], %[sX], 4096\n\t"
    "s_and_b32 %[sX], %[sX], 0x1ffff\n\t"
    "v_mfma_f32_16x16x32_bf16 v[20:23], a[0:3], a[36:39], v[20:23]\n\t"
    "buffer_load_dwordx4 a[128:131], %[vX], %[srdWS], %[sX] offen offset:0\n\t"
    "v_mfma_f32_16x16x32_bf16 v[24:27], a[0:3], a[40:43], v[24:27]\n\t"
    "ds_read_b64_tr_b16 a[28:29], %[vRdW] offset:16936\n\t"
    "v_mfma_f32_16x16x32_bf16 v[24:27], a[4:7], a[56:59], v[24:27]\n\t"
    "ds_read_b64_tr_b16 a[30:31], %[vRdW] offset:17192\n\t"
    "v_mfma_f32_16x16x32_bf16 v[28:31], a[4:7], a[60:63], v[28:31]\n\t"
    "v_mfma_f32_16x16x32_bf16 v[28:31], a[0:3], a[44:47], v[28:31]\n\t"
    "s_waitcnt lgkmcnt(8)\n\t"
    "s_barrier\n\t"
    "v_mfma_f32_16x16x32_bf16 v[32:35], a[8:11], a[32:35], v[32:35]\n\t"
    "ds_read_b64_tr_b16 a[0:1], %[vRdW] offset:1024\n\t"
    "v_mfma_f32_16x16x32_bf16 v[32:35], a[12:15], a[48:51], v[32:35]\n\t"
    "ds_read_b64_tr_b16 a[2:3], %[vRdW] offset:1280\n\t"
    "v_mfma_f32_16x16x32_bf16 v[36:39], a[12:15], a[52:55], v[36:39]\n\t"
    "s_add_u32 %[sWk], %[sWk], s83\n\t"
    "s_add_u32 m0, s84, 98496\n\t"
    "s_add_u32 s82, %[sWk], s92\n\t"
    "v_mfma_f32_16x16x32_bf16 v[36:39], a[8:11], a[36:39], v[36:39]\n\t"
    "buffer_load_dwordx4 %[vW], %[srdW], s82 offen lds\n\t"
    "v_mfma_f32_16x16x32_bf16 v[40:43], a[8:11], a[40:43], v[40:43]\n\t"
    "ds_read_b64_tr_b16 a[4:5], %[vRdW] offset:17440\n\t"
    "v_mfma_f32_16x16x32_bf16 v[40:43], a[12:15], a[56:59], v[40:43]\n\t"
    "ds_read_b64_tr_b16 a[6:7], %[vRdW] offset:17696\n\t"
    "v_mfma_f32_16x16x32_bf16 v[44:47], a[12:15], a[60:63], v[44:47]\n\t"
    "v_mfma_f32_16x16x32_bf16 v[44:47], a[8:11], a[44:47], v[44:47]\n\t"
    "buffer_load_dwordx4 a[132:135], %[vX], %[srdWS], %[sX] offen offset:1024\n\t"
    "s_waitcnt lgkmcnt(8)\n\t"
    "v_mfma_f32_16x16x32_bf16 v[48:51], a[16:19], a[32:35], v[48:51]\n\t"
    "ds_read_b64_tr_b16 a[8:9], %[vRdW] offset:1032\n\t"
    "v_mfma_f32_16x16x32_bf16 v[48:51], a[20:23], a[48:51], v[48:51]\n\t"
    "ds_read_b64_tr_b16 a[10:11], %[vRdW] offset:1288\n\t"
    "v_mfma_f32_16x16x32_bf16 v[52:55], a[20:23], a[52:55], v[52:55]\n\t"
    "v_mfma_f32_16x16x32_bf16 v[52:55], a[16:19], a[36:39], v[52:55]\n\t"
    "v_mfma_f32_16x16x32_bf16 v[56:59], a[16:19], a[40:43], v[56:59]\n\t"
    "ds_read_b64_tr_b16 a[12:13], %[vRdW] offset:17448\n\t"
    "v_mfma_f32_16x16x32_bf16 v[56:59], a[20:23], a[56:59], v[56:59]\n\t"
    "ds_read_b64_tr_b16 a[14:15], %[vRdW] offset:17704\n\t"
    "v_mfma_f32_16x16x32_bf16 v[60:63], a[20:23], a[60:63], v[60:63]\n\t"
    "s_add_u32 m0, s85, 98496\n\t"
    "s_add_u32 s82, %[sWk], s93\n\t"
    "v_mfma_f32_16x16x32_bf16 v[60:63], a[16:19], a[44:47], v[60:63]\n\t"
    "buffer_load_dwordx4 %[vW], %[srdW], s82 offen lds\n\t"
    "s_waitcnt lgkmcnt(8)\n\t"
    "v_mfma_f32_16x16x32_bf16 v[64:67], a[24:27], a[32:35], v[64:67]\n\t"
    "ds_read_b64_tr_b16 a[16:17], %[vRdW] offset:1536\n\t"
    "v_mfma_f32_16x16x32_bf16 v[64:67], a[28:31], a[48:51], v[64:67]\n\t"
    "ds_read_b64_tr_b16 a[18:19], %[vRdW] offset:1792\n\t"
    "v_mfma_f32_16x16x32_bf16 v[68:71], a[28:31], a[52:55], v[68:71]\n\t"
    "v_mfma_f32_16x16x32_bf16 v[68:71], a[24:27], a[36:39], v[68:71]\n\t"
    "v_mfma_f32_16x16x32_bf16 v[72:75], a[24:27], a[40:43], v[72:75]\n\t"
    "ds_read_b64_tr_b16 a[20:21], %[vRdW] offset:17952\n\t"
    "v_mfma_f32_16x16x32_bf16 v[72:75], a[28:31], a[56:59], v[72:75]\n\t"
    "ds_read_b64_tr_b16 a[22:23], %[vRdW] offset:18208\n\t"
    "v_mfma_f32_16x16x32_bf16 v[76:79], a[28:31], a[60:63], v[76:79]\n\t"
    "v_mfma_f32_16x16x32_bf16 v[76:79], a[24:27], a[44:47], v[76:79]\n\t"
    "buffer_load_dwordx4 a[136:139], %[vX], %[srdWS], %[sX] offen offset:2048\n\t"
    "s_waitcnt lgkmcnt(8)\n\t"
    "v_mfma_f32_16x16x32_bf16 v[80:83], a[0:3], a[32:35], v[80:83]\n\t"
    "ds_read_b64_tr_b16 a[24:25], %[vRdW] offset:1544\n\t"
    "v_mfma_f32_16x16x32_bf16 v[80:83], a[4:7], a[48:51], v[80:83]\n\t"
    "ds_read_b64_tr_b16 a[26:27], %[vRdW] offset:1800\n\t"
    "v_mfma_f32_16x16x32_bf16 v[84:87], a[4:7], a[52:55], v[84:87]\n\t"
    "s_add_u32 m0, s86, 98496\n\t"
    "s_add_u32 s82, %[sWk], s94\n\t"
    "v_mfma_f32_16x16x32_bf16 v[84:87], a[0:3], a[36:39], v[84:87]\n\t"
    "buffer_load_dwordx4 %[vW], %[srdW], s82 offen lds\n\t"
    "v_mfma_f32_16x16x32_bf16 v[88:91], a[0:3], a[40:43], v[88:91]\n\t"
    "ds_read_b64_tr_b16 a[28:29], %[vRdW] offset:17960\n\t"
    "v_mfma_f32_16x16x32_bf16 v[88:91], a[4:7], a[56:59], v[88:91]\n\t"
    "ds_read_b64_tr_b16 a[30:31], %[vRdW] offset:18216\n\t"
    "v_mfma_f32_16x16x32_bf16 v[92:95], a[4:7], a[60:63], v[92:95]\n\t"
    "v_mfma_f32_16x16x32_bf16 v[92:95], a[0:3], a[44:47], v[92:95]\n\t"
    "s_waitcnt lgkmcnt(8)\n\t"
    "v_mfma_f32_16x16x32_bf16 v[96:99], a[8:11], a[32:35], v[96:99]\n\t"
    "ds_read_b64_tr_b16 a[0:1], %[vRdW] offset:2048\n\t"
    "v_mfma_f32_16x16x32_bf16 v[96:99], a[12:15], a[48:51], v[96:99]\n\t"
    "ds_read_b64_tr_b16 a[2:3], %[vRdW] offset:2304\n\t"
    "v_mfma_f32_16x16x32_bf16 v[100:103], a[12:15], a[52:55], v[100:103]\n\t"
    "v_mfma_f32_16x16x32_bf16 v[100:103], a[8:11], a[36:39], v[100:103]\n\t"
    "v_mfma_f32_16x16x32_bf16 v[104:107], a[8:11], a[40:43], v[104:107]\n\t"
    "ds_read_b64_tr_b16 a[4:5], %[vRdW] offset:18464\n\t"
    "v_mfma_f32_16x16x32_bf16 v[104:107], a[12:15], a[56:59], v[104:107]\n\t"
    "ds_read_b64_tr_b16 a[6:7], %[vRdW] offset:18720\n\t"
    "v_mfma_f32_16x16x32_bf16 v[108:111], a[12:15], a[60:63], v[108:111]\n\t"
    "s_add_u32 m0, s87, 98496\n\t"
    "s_add_u32 s82, %[sWk], s95\n\t"
    "v_mfma_f32_16x16x32_bf16 v[108:111], a[8:11], a[44:47], v[108:111]\n\t"
    "buffer_load_dwordx4 %[vW], %[srdW], s82 offen lds\n\t"
    "s_waitcnt lgkmcnt(8)\n\t"
    "v_mfma_f32_16x16x32_bf16 v[112:115], a[16:19], a[32:35], v[112:115]\n\t"
    "ds_read_b64_tr_b16 a[8:9], %[vRdW] offset:2056\n\t"
    "v_mfma_f32_16x16x32_bf16 v[112:115], a[20:23], a[48:51], v[112:115]\n\t"
    "ds_read_b64_tr_b16 a[10:11], %[vRdW] offset:2312\n\t"
    "v_mfma_f32_16x16x32_bf16 v[116:119], a[20:23], a[52:55], v[116:119]\n\t"
    "v_mfma_f32_16x16x32_bf16 v[116:119], a[16:19], a[36:39], v[116:119]\n\t"
    "buffer_load_dwordx4 a[140:143], %[vX], %[srdWS], %[sX] offen offset:3072\n\t"
    "v_mfma_f32_16x16x32_bf16 v[120:123], a[16:19], a[40:43], v[120:123]\n\t"
    "ds_read_b64_tr_b16 a[12:13], %[vRdW] offset:18472\n\t"
    "v_mfma_f32_16x16x32_bf16 v[120:123], a[20:23], a[56:59], v[120:123]\n\t"
    "ds_read_b64_tr_b16 a[14:15], %[vRdW] offset:18728\n\t"
    "v_mfma_f32_16x16x32_bf16 v[124:127], a[20:23], a[60:63], v[124:127]\n\t"
    "v_mfma_f32_16x16x32_bf16 v[124:127], a[16:19], a[44:47], v[124:127]\n\t"
    "s_waitcnt lgkmcnt(8)\n\t"
    "v_mfma_f32_16x16x32_bf16 v[128:131], a[24:27], a[32:35], v[128:131]\n\t"
    "ds_read_b64_tr_b16 a[16:17], %[vRdW] offset:2560\n\t"
    "v_mfma_f32_16x16x32_bf16 v[128:131], a[28:31], a[48:51], v[128:131]\n\t"
    "ds_read_b64_tr_b16 a[18:19], %[vRdW] offset:2816\n\t"
    "v_mfma_f32_16x16x32_bf16 v[132:135], a[28:31], a[52:55], v[132:135]\n\t"
    "s_add_u32 m0, s88, 98496\n\t"
    "s_add_u32 s82, %[sWk], s96\n\t"
    "v_mfma_f32_16x16x32_bf16 v[132:135], a[24:27], a[36:39], v[132:135]\n\t"
    "buffer_load_dwordx4 %[vW], %[srdW], s82 offen lds\n\t"
    "v_mfma_f32_16x16x32_bf16 v[136:139], a[24:27], a[40:43], v[136:139]\n\t"
    "ds_read_b64_tr_b16 a[20:21], %[vRdW] offset:18976\n\t"
    "v_mfma_f32_16x16x32_bf16 v[136:139], a[28:31], a[56:59], v[136:139]\n\t"
    "ds_read_b64_tr_b16 a[22:23], %[vRdW] offset:19232\n\t"
    "v_mfma_f32_16x16x32_bf16 v[140:143], a[28:31], a[60:63], v[140:143]\n\t"
    "v_mfma_f32_16x16x32_bf16 v[140:143], a[24:27], a[44:47], v[140:143]\n\t"
    "s_waitcnt lgkmcnt(8)\n\t"
    "v_mfma_f32_16x16x32_bf16 v[144:147], a[0:3], a[32:35], v[144:147]\n\t"
    "ds_read_b64_tr_b16 a[24:25], %[vRdW] offset:2568\n\t"
    "v_mfma_f32_16x16x32_bf16 v[144:147], a[4:7], a[48:51], v[144:147]\n\t"
    "ds_read_b64_tr_b16 a[26:27], %[vRdW] offset:2824\n\t"
    "v_mfma_f32_16x16x32_bf16 v[148:151], a[4:7], a[52:55], v[148:151]\n\t"
    "s_add_u32 %[sX], %[sX], 4096\n\t"
    "v_mfma_f32_16x16x32_bf16 v[148:151], a[0:3], a[36:39], v[148:151]\n\t"
    "buffer_load_dwordx4 a[144:147], %[vX], %[srdWS], %[sX] offen offset:0\n\t"
    "v_mfma_f32_16x16x32_bf16 v[152:155], a[0:3], a[40:43], v[152:155]\n\t"
    "ds_read_b64_tr_b16 a[28:29], %[vRdW] offset:18984\n\t"
    "v_mfma_f32_16x16x32_bf16 v[152:155], a[4:7], a[56:59], v[152:155]\n\t"
    "ds_read_b64_tr_b16 a[30:31], %[vRdW] offset:19240\n\t"
    "v_mfma_f32_16x16x32_bf16 v[156:159], a[4:7], a[60:63], v[156:159]\n\t"
    "s_add_u32 m0, s89, 98496\n\t"
    "s_add_u32 s82, %[sWk], s97\n\t"
    "v_mfma_f32_16x16x32_bf16 v[156:159], a[0:3], a[44:47], v[156:159]\n\t"
    "buffer_load_dwordx4 %[vW], %[srdW], s82 offen lds\n\t"
    "s_waitcnt lgkmcnt(8)\n\t"
    "v_mfma_f32_16x16x32_bf16 v[160:163], a[8:11], a[32:35], v[160:163]\n\t"
    "ds_read_b64_tr_b16 a[0:1], %[vRdW] offset:3072\n\t"
    "v_mfma_f32_16x16x32_bf16 v[160:163], a[12:15], a[48:51], v[160:163]\n\t"
    "ds_read_b64_tr_b16 a[2:3], %[vRdW] offset:3328\n\t"
    "v_mfma_f32_16x16x32_bf16 v[164:167], a[12:15], a[52:55], v[164:167]\n\t"
    "v_mfma_f32_16x16x32_bf16 v[164:167], a[8:11], a[36:39], v[164:167]\n\t"
    "v_mfma_f32_16x16x32_bf16 v[168:171], a[8:11], a[40:43], v[168:171]\n\t"
    "ds_read_b64_tr_b16 a[4:5], %[vRdW] offset:19488\n\t"
    "v_mfma_f32_16x16x32_bf16 v[168:171], a[12:15], a[56:59], v[168:171]\n\t"
    "ds_read_b64_tr_b16 a[6:7], %[vRdW] offset:19744\n\t"
    "v_mfma_f32_16x16x32_bf16 v[172:175], a[12:15], a[60:63], v[172:175]\n\t"
    "v_mfma_f32_16x16x32_bf16 v[172:175], a[8:11], a[44:47], v[172:175]\n\t"
    "buffer_load_dwordx4 a[148:151], %[vX], %[srdWS], %[sX] offen offset:1024\n\t"
    "s_waitcnt lgkmcnt(8)\n\t"
    "v_mfma_f32_16x16x32_bf16 v[176:179], a[16:19], a[32:35], v[176:179]\n\t"
    "ds_read_b64_tr_b16 a[8:9], %[vRdW] offset:3080\n\t"
    "v_mfma_f32_16x16x32_bf16 v[176:179], a[20:23], a[48:51], v[176:179]\n\t"
    "ds_read_b64_tr_b16 a[10:11], %[vRdW] offset:3336\n\t"
    "v_mfma_f32_16x16x32_bf16 v[180:183], a[20:23], a[52:55], v[180:183]\n\t"
    "s_add_u32 m0, s90, 98496\n\t"
    "s_add_u32 s82, %[sWk], s98\n\t"
    "v_mfma_f32_16x16x32_bf16 v[180:183], a[16:19], a[36:39], v[180:183]\n\t"
    "buffer_load_dwordx4 %[vW], %[srdW], s82 offen lds\n\t"
    "v_mfma_f32_16x16x32_bf16 v[184:187], a[16:19], a[40:43], v[184:187]\n\t"
    "ds_read_b64_tr_b16 a[12:13], %[vRdW] offset:19496\n\t"
    "v_mfma_f32_16x16x32_bf16 v[184:187], a[20:23], a[56:59], v[184:187]\n\t"
    "ds_read_b64_tr_b16 a[14:15], %[vRdW] offset:19752\n\t"
    "v_mfma_f32_16x16x32_bf16 v[188:191], a[20:23], a[60:63], v[188:191]\n\t"
    "v_mfma_f32_16x16x32_bf16 v[188:191], a[16:19], a[44:47], v[188:191]\n\t"
    "s_waitcnt lgkmcnt(8)\n\t"
    "v_mfma_f32_16x16x32_bf16 v[192:195], a[24:27], a[32:35], v[192:195]\n\t"
    "ds_read_b64_tr_b16 a[16:17], %[vRdW2] offset:3584\n\t"
    "v_mfma_f32_16x16x32_bf16 v[192:195], a[28:31], a[48:51], v[192:195]\n\t"
    "ds_read_b64_tr_b16 a[18:19], %[vRdW2] offset:3840\n\t"
    "v_mfma_f32_16x16x32_bf16 v[196:199], a[28:31], a[52:55], v[196:199]\n\t"
    "v_mfma_f32_16x16x32_bf16 v[196:199], a[24:27], a[36:39], v[196:199]\n\t"
    "v_mfma_f32_16x16x32_bf16 v[200:203], a[24:27], a[40:43], v[200:203]\n\t"
    "ds_read_b64_tr_b16 a[20:21], %[vRdW2] offset:20000\n\t"
    "v_mfma_f32_16x16x32_bf16 v[200:203], a[28:31], a[56:59], v[200:203]\n\t"
    "ds_read_b64_tr_b16 a[22:23], %[vRdW2] offset:20256\n\t"
    "v_mfma_f32_16x16x32_bf16 v[204:207], a[28:31], a[60:63], v[204:207]\n\t"
    "s_add_u32 m0, s91, 98496\n\t"
    "s_add_u32 s82, %[sWk], s99\n\t"
    "v_mfma_f32_16x16x32_bf16 v[204:207], a[24:27], a[44:47], v[204:207]\n\t"
    "buffer_load_dwordx4 %[vW], %[srdW], s82 offen lds\n\t"
    "s_waitcnt vmcnt(32) lgkmcnt(8)\n\t"
    "s_barrier\n\t"
    "v_mfma_f32_16x16x32_bf16 v[208:211], a[0:3], a[32:35], v[208:211]\n\t"
    "ds_read_b64_tr_b16 a[24:25], %[vRdW] offset:32832\n\t"
    "v_mfma_f32_16x16x32_bf16 v[208:211], a[4:7], a[48:51], v[208:211]\n\t"
    "ds_read_b64_tr_b16 a[26:27], %[vRdW] offset:33088\n\t"
    "v_mfma_f32_16x16x32_bf16 v[212:215], a[4:7], a[52:55], v[212:215]\n\t"
    "v_mfma_f32_16x16x32_bf16 v[212:215], a[0:3], a[36:39], v[212:215]\n\t"
    "buffer_load_dwordx4 a[152:155], %[vX], %[srdWS], %[sX] offen offset:2048\n\t"
    "v_mfma_f32_16x16x32_bf16 v[216:219], a[0:3], a[40:43], v[216:219]\n\t"
    "ds_read_b64_tr_b16 a[28:29], %[vRdW] offset:49248\n\t"
    "v_mfma_f32_16x16x32_bf16 v[216:219], a[4:7], a[56:59], v[216:219]\n\t"
    "ds_read_b64_tr_b16 a[30:31], %[vRdW] offset:49504\n\t"
    "v_mfma_f32_16x16x32_bf16 v[220:223], a[4:7], a[60:63], v[220:223]\n\t"
    "v_mfma_f32_16x16x32_bf16 v[220:223], a[0:3], a[44:47], v[220:223]\n\t"
    "s_waitcnt lgkmcnt(8)\n\t"
    "v_mfma_f32_16x16x32_bf16 v[224:227], a[8:11], a[32:35], v[224:227]\n\t"
    "ds_read_b64_tr_b16 a[0:1], %[vRdW] offset:32840\n\t"
    "v_mfma_f32_16x16x32_bf16 v[224:227], a[12:15], a[48:51], v[224:227]\n\t"
    "ds_read_b64_tr_b16 a[2:3], %[vRdW] offset:33096\n\t"
    "v_mfma_f32_16x16x32_bf16 v[228:231], a[12:15], a[52:55], v[228:231]\n\t"
    "v_mfma_f32_16x16x32_bf16 v[228:231], a[8:11], a[36:39], v[228:231]\n\t"
    "v_mfma_f32_16x16x32_bf16 v[232:235], a[8:11], a[40:43], v[232:235]\n\t"
    "ds_read_b64_tr_b16 a[4:5], %[vRdW] offset:49256\n\t"
    "v_mfma_f32_16x16x32_bf16 v[232:235], a[12:15], a[56:59], v[232:235]\n\t"
    "ds_read_b64_tr_b16 a[6:7], %[vRdW] offset:49512\n\t"
    "v_mfma_f32_16x16x32_bf16 v[236:239], a[12:15], a[60:63], v[236:239]\n\t"
    "v_mfma_f32_16x16x32_bf16 v[236:239], a[8:11], a[44:47], v[236:239]\n\t"
    "buffer_load_dwordx4 a[156:159], %[vX], %[srdWS], %[sX] offen offset:3072\n\t"
    "s_waitcnt lgkmcnt(8)\n\t"
    "v_mfma_f32_16x16x32_bf16 v[240:243], a[16:19], a[32:35], v[240:243]\n\t"
    "ds_read_b64_tr_b16 a[8:9], %[vRdW] offset:33344\n\t"
    "v_mfma_f32_16x16x32_bf16 v[240:243], a[20:23], a[48:51], v[240:243]\n\t"
    "ds_read_b64_tr_b16 a[10:11], %[vRdW] offset:33600\n\t"
    "v_mfma_f32_16x16x32_bf16 v[244:247], a[20:23], a[52:55], v[244:247]\n\t"
    "v_mfma_f32_16x16x32_bf16 v[244:247], a[16:19], a[36:39], v[244:247]\n\t"
    "v_mfma_f32_16x16x32_bf16 v[248:251], a[16:19], a[40:43], v[248:251]\n\t"
    "ds_read_b64_tr_b16 a[12:13], %[vRdW] offset:49760\n\t"
    "v_mfma_f32_16x16x32_bf16 v[248:251], a[20:23], a[56:59], v[248:251]\n\t"
    "ds_read_b64_tr_b16 a[14:15], %[vRdW] offset:50016\n\t"
    "v_mfma_f32_16x16x32_bf16 v[252:255], a[20:23], a[60:63], v[252:255]\n\t"
    "v_mfma_f32_16x16x32_bf16 v[252:255], a[16:19], a[44:47], v[252:255]\n\t"
    "s_waitcnt vmcnt(32) lgkmcnt(8)\n\t"
    "v_mfma_f32_16x16x32_bf16 v[16:19], a[24:27], a[64:67], v[16:19]\n\t"
    "ds_read_b64_tr_b16 a[16:17], %[vRdW] offset:33352\n\t"
    "v_mfma_f32_16x16x32_bf16 v[16:19], a[28:31], a[80:83], v[16:19]\n\t"
    "ds_read_b64_tr_b16 a[18:19], %[vRdW] offset:33608\n\t"
    "v_mfma_f32_16x16x32_bf16 v[20:23], a[28:31], a[84:87], v[20:23]\n\t"
    "s_add_u32 %[sX], %[sX], 4096\n\t"
    "s_and_b32 %[sX], %[sX], 0x1ffff\n\t"
    "v_mfma_f32_16x16x32_bf16 v[20:23], a[24:27], a[68:71], v[20:23]\n\t"
    "buffer_load_dwordx4 a[32:35], %[vX], %[srdWS], %[sX] offen offset:0\n\t"
    "v_mfma_f32_16x16x32_bf16 v[24:27], a[24:27], a[72:75], v[24:27]\n\t"
    "ds_read_b64_tr_b16 a[20:21], %[vRdW] offset:49768\n\t"
    "v_mfma_f32_16x16x32_bf16 v[24:27], a[28:31], a[88:91], v[24:27]\n\t"
    "ds_read_b64_tr_b16 a[22:23], %[vRdW] offset:50024\n\t"
    "v_mfma_f32_16x16x32_bf16 v[28:31], a[28:31], a[92:95], v[28:31]\n\t"
    "v_mfma_f32_16x16x32_bf16 v[28:31], a[24:27], a[76:79], v[28:31]\n\t"
    "s_waitcnt lgkmcnt(8)\n\t"
    "s_barrier\n\t"
    "v_mfma_f32_16x16x32_bf16 v[32:35], a[0:3], a[64:67], v[32:35]\n\t"
    "ds_read_b64_tr_b16 a[24:25], %[vRdW] offset:33856\n\t"
    "v_mfma_f32_16x16x32_bf16 v[32:35], a[4:7], a[80:83], v[32:35]\n\t"
    "ds_read_b64_tr_b16 a[26:27], %[vRdW] offset:34112\n\t"
    "v_mfma_f32_16x16x32_bf16 v[36:39], a[4:7], a[84:87], v[36:39]\n\t"
    "s_add_u32 %[sWk], %[sWk], s83\n\t"
    "s_add_u32 m0, s84, 0\n\t"
    "s_add_u32 s82, %[sWk], s92\n\t"
    "v_mfma_f32_16x16x32_bf16 v[36:39], a[0:3], a[68:71], v[36:39]\n\t"
    "buffer_load_dwordx4 %[vW], %[srdW], s82 offen lds\n\t"
    "v_mfma_f32_16x16x32_bf16 v[40:43], a[0:3], a[72:75], v[40:43]\n\t"
    "ds_read_b64_tr_b16 a[28:29], %[vRdW] offset:50272\n\t"
    "v_mfma_f32_16x16x32_bf16 v[40:43], a[4:7], a[88:91], v[40:43]\n\t"
    "ds_read_b64_tr_b16 a[30:31], %[vRdW] offset:50528\n\t"
    "v_mfma_f32_16x16x32_bf16 v[44:47], a[4:7], a[92:95], v[44:47]\n\t"
    "v_mfma_f32_16x16x32_bf16 v[44:47], a[0:3], a[76:79], v[44:47]\n\t"
    "buffer_load_dwordx4 a[36:39], %[vX], %[srdWS], %[sX] offen offset:1024\n\t"
    "s_waitcnt lgkmcnt(8)\n\t"
    "v_mfma_f32_16x16x32_bf16 v[48:51], a[8:11], a[64:67], v[48:51]\n\t"
    "ds_read_b64_tr_b16 a[0:1], %[vRdW] offset:33864\n\t"
    "v_mfma_f32_16x16x32_bf16 v[48:51], a[12:15], a[80:83], v[48:51]\n\t"
    "ds_read_b64_tr_b16 a[2:3], %[vRdW] offset:34120\n\t"
    "v_mfma_f32_16x16x32_bf16 v[52:55], a[12:15], a[84:87], v[52:55]\n\t"
    "v_mfma_f32_16x16x32_bf16 v[52:55], a[8:11], a[68:71], v[52:55]\n\t"
    "v_mfma_f32_16x16x32_bf16 v[56:59], a[8:11], a[72:75], v[56:59]\n\t"
    "ds_read_b64_tr_b16 a[4:5], %[vRdW] offset:50280\n\t"
    "v_mfma_f32_16x16x32_bf16 v[56:59], a[12:15], a[88:91], v[56:59]\n\t"
    "ds_read_b64_tr_b16 a[6:7], %[vRdW] offset:50536\n\t"
    "v_mfma_f32_16x16x32_bf16 v[60:63], a[12:15], a[92:95], v[60:63]\n\t"
    "s_add_u32 m0, s85, 0\n\t"
    "s_add_u32 s82, %[sWk], s93\n\t"
    "v_mfma_f32_16x16x32_bf16 v[60:63], a[8:11], a[76:79], v[60:63]\n\t"
    "buffer_load_dwordx4 %[vW], %[srdW], s82 offen lds\n\t"
    "s_waitcnt lgkmcnt(8)\n\t"
    "v_mfma_f32_16x16x32_bf16 v[64:67], a[16:19], a[64:67], v[64:67]\n\t"
    "ds_read_b64_tr_b16 a[8:9], %[vRdW] offset:34368\n\t"
    "v_mfma_f32_16x16x32_bf16 v[64:67], a[20:23], a[80:83], v[64:67]\n\t"
    "ds_read_b64_tr_b16 a[10:11], %[vRdW] offset:34624\n\t"
    "v_mfma_f32_16x16x32_bf16 v[68:71], a[20:23], a[84:87], v[68:71]\n\t"
    "v_mfma_f32_16x16x32_bf16 v[68:71], a[16:19], a[68:71], v[68:71]\n\t"
    "v_mfma_f32_16x16x32_bf16 v[72:75], a[16:19], a[72:75], v[72:75]\n\t"
    "ds_read_b64_tr_b16 a[12:13], %[vRdW] offset:50784\n\t"
    "v_mfma_f32_16x16x32_bf16 v[72:75], a[20:23], a[88:91], v[72:75]\n\t"
    "ds_read_b64_tr_b16 a[14:15], %[vRdW] offset:51040\n\t"
    "v_mfma_f32_16x16x32_bf16 v[76:79], a[20:23], a[92:95], v[76:79]\n\t"
    "v_mfma_f32_16x16x32_bf16 v[76:79], a[16:19], a[76:79], v[76:79]\n\t"
    "buffer_load_dwordx4 a[40:43], %[vX], %[srdWS], %[sX] offen offset:2048\n\t"
    "s_waitcnt lgkmcnt(8)\n\t"
    "v_mfma_f32_16x16x32_bf16 v[80:83], a[24:27], a[64:67], v[80:83]\n\t"
    "ds_read_b64_tr_b16 a[16:17], %[vRdW] offset:34376\n\t"
    "v_mfma_f32_16x16x32_bf16 v[80:83], a[28:31], a[80:83], v[80:83]\n\t"
    "ds_read_b64_tr_b16 a[18:19], %[vRdW] offset:34632\n\t"
    "v_mfma_f32_16x16x32_bf16 v[84:87], a[28:31], a[84:87], v[84:87]\n\t"
    "s_add_u32 m0, s86, 0\n\t"
    "s_add_u32 s82, %[sWk], s94\n\t"
    "v_mfma_f32_16x16x32_bf16 v[84:87], a[24:27], a[68:71], v[84:87]\n\t"
    "buffer_load_dwordx4 %[vW], %[srdW], s82 offen lds\n\t"
    "v_mfma_f32_16x16x32_bf16 v[88:91], a[24:27], a[72:75], v[88:91]\n\t"
    "ds_read_b64_tr_b16 a[20:21], %[vRdW] offset:50792\n\t"
    "v_mfma_f32_16x16x32_bf16 v[88:91], a[28:31], a[88:91], v[88:91]\n\t"
    "ds_read_b64_tr_b16 a[22:23], %[vRdW] offset:51048\n\t"
    "v_mfma_f32_16x16x32_bf16 v[92:95], a[28:31], a[92:95], v[92:95]\n\t"
    "v_mfma_f32_16x16x32_bf16 v[92:95], a[24:27], a[76:79], v[92:95]\n\t"
    "s_waitcnt lgkmcnt(8)\n\t"
    "v_mfma_f32_16x16x32_bf16 v[96:99], a[0:3], a[64:67], v[96:99]\n\t"
    "ds_read_b64_tr_b16 a[24:25], %[vRdW] offset:34880\n\t"
    "v_mfma_f32_16x16x32_bf16 v[96:99], a[4:7], a[80:83], v[96:99]\n\t"
    "ds_read_b64_tr_b16 a[26:27], %[vRdW] offset:35136\n\t"
    "v_mfma_f32_16x16x32_bf16 v[100:103], a[4:7], a[84:87], v[100:103]\n\t"
    "v_mfma_f32_16x16x32_bf16 v[100:103], a[0:3], a[68:71], v[100:103]\n\t"
    "v_mfma_f32_16x16x32_bf16 v[104:107], a[0:3], a[72:75], v[104:107]\n\t"
    "ds_read_b64_tr_b16 a[28:29], %[vRdW] offset:51296\n\t"
    "v_mfma_f32_16x16x32_bf16 v[104:107], a[4:7], a[88:91], v[104:107]\n\t"
    "ds_read_b64_tr_b16 a[30:31], %[vRdW] offset:51552\n\t"
    "v_mfma_f32_16x16x32_bf16 v[108:111], a[4:7], a[92:95], v[108:111]\n\t"
    "s_add_u32 m0, s87, 0\n\t"
    "s_add_u32 s82, %[sWk], s95\n\t"
    "v_mfma_f32_16x16x32_bf16 v[108:111], a[0:3], a[76:79], v[108:111]\n\t"
    "buffer_load_dwordx4 %[vW], %[srdW], s82 offen lds\n\t"
    "s_waitcnt lgkmcnt(8)\n\t"
    "v_mfma_f32_16x16x32_bf16 v[112:115], a[8:11], a[64:67], v[112:115]\n\t"
    "ds_read_b64_tr_b16 a[0:1], %[vRdW] offset:34888\n\t"
    "v_mfma_f32_16x16x32_bf16 v[112:115], a[12:15], a[80:83], v[112:115]\n\t"
    "ds_read_b64_tr_b16 a[2:3], %[vRdW] offset:35144\n\t"
    "v_mfma_f32_16x16x32_bf16 v[116:119], a[12:15], a[84:87], v[116:119]\n\t"
    "v_mfma_f32_16x16x32_bf16 v[116:119], a[8:11], a[68:71], v[116:119]\n\t"
    "buffer_load_dwordx4 a[44:47], %[vX], %[srdWS], %[sX] offen offset:3072\n\t"
    "v_mfma_f32_16x16x32_bf16 v[120:123], a[8:11], a[72:75], v[120:123]\n\t"
    "ds_read_b64_tr_b16 a[4:5], %[vRdW] offset:51304\n\t"
    "v_mfma_f32_16x16x32_bf16 v[120:123], a[12:15], a[88:91], v[120:123]\n\t"
    "ds_read_b64_tr_b16 a[6:7], %[vRdW] offset:51560\n\t"
    "v_mfma_f32_16x16x32_bf16 v[124:127], a[12:15], a[92:95], v[124:127]\n\t"
    "v_mfma_f32_16x16x32_bf16 v[124:127], a[8:11], a[76:79], v[124:127]\n\t"
    "s_waitcnt lgkmcnt(8)\n\t"
    "v_mfma_f32_16x16x32_bf16 v[128:131], a[16:19], a[64:67], v[128:131]\n\t"
    "ds_read_b64_tr_b16 a[8:9], %[vRdW] offset:35392\n\t"
    "v_mfma_f32_16x16x32_bf16 v[128:131], a[20:23], a[80:83], v[128:131]\n\t"
    "ds_read_b64_tr_b16 a[10:11], %[vRdW] offset:35648\n\t"
    "v_mfma_f32_16x16x32_bf16 v[132:135], a[20:23], a[84:87], v[132:135]\n\t"
    "s_add_u32 m0, s88, 0\n\t"
    "s_add_u32 s82, %[sWk], s96\n\t"
    "v_mfma_f32_16x16x32_bf16 v[132:135], a[16:19], a[68:71], v[132:135]\n\t"
    "buffer_load_dwordx4 %[vW], %[srdW], s82 offen lds\n\t"
    "v_mfma_f32_16x16x32_bf16 v[136:139], a[16:19], a[72:75], v[136:139]\n\t"
    "ds_read_b64_tr_b16 a[12:13], %[vRdW] offset:51808\n\t"
    "v_mfma_f32_16x16x32_bf16 v[136:139], a[20:23], a[88:91], v[136:139]\n\t"
    "ds_read_b64_tr_b16 a[14:15], %[vRdW] offset:52064\n\t"
    "v_mfma_f32_16x16x32_bf16 v[140:143], a[20:23], a[92:95], v[140:143]\n\t"
    "v_mfma_f32_16x16x32_bf16 v[140:143], a[16:19], a[76:79], v[140:143]\n\t"
    "s_waitcnt lgkmcnt(8)\n\t"
    "v_mfma_f32_16x16x32_bf16 v[144:147], a[24:27], a[64:67], v[144:147]\n\t"
    "ds_read_b64_tr_b16 a[16:17], %[vRdW] offset:35400\n\t"
    "v_mfma_f32_16x16x32_bf16 v[144:147], a[28:31], a[80:83], v[144:147]\n\t"
    "ds_read_b64_tr_b16 a[18:19], %[vRdW] offset:35656\n\t"
    "v_mfma_f32_16x16x32_bf16 v[148:151], a[28:31], a[84:87], v[148:151]\n\t"
    "s_add_u32 %[sX], %[sX], 4096\n\t"
    "v_mfma_f32_16x16x32_bf16 v[148:151], a[24:27], a[68:71], v[148:151]\n\t"
    "buffer_load_dwordx4 a[48:51], %[vX], %[srdWS], %[sX] offen offset:0\n\t"
    "v_mfma_f32_16x16x32_bf16 v[152:155], a[24:27], a[72:75], v[152:155]\n\t"
    "ds_read_b64_tr_b16 a[20:21], %[vRdW] offset:51816\n\t"
    "v_mfma_f32_16x16x32_bf16 v[152:155], a[28:31], a[88:91], v[152:155]\n\t"
    "ds_read_b64_tr_b16 a[22:23], %[vRdW] offset:52072\n\t"
    "v_mfma_f32_16x16x32_bf16 v[156:159], a[28:31], a[92:95], v[156:159]\n\t"
    "s_add_u32 m0, s89, 0\n\t"
    "s_add_u32 s82, %[sWk], s97\n\t"
    "v_mfma_f32_16x16x32_bf16 v[156:159], a[24:27], a[76:79], v[156:159]\n\t"
    "buffer_load_dwordx4 %[vW], %[srdW], s82 offen lds\n\t"
    "s_waitcnt lgkmcnt(8)\n\t"
    "v_mfma_f32_16x16x32_bf16 v[160:163], a[0:3], a[64:67], v[160:163]\n\t"
    "ds_read_b64_tr_b16 a[24:25], %[vRdW] offset:35904\n\t"
    "v_mfma_f32_16x16x32_bf16 v[160:163], a[4:7], a[80:83], v[160:163]\n\t"
    "ds_read_b64_tr_b16 a[26:27], %[vRdW] offset:36160\n\t"
    "v_mfma_f32_16x16x32_bf16 v[164:167], a[4:7], a[84:87], v[164:167]\n\t"
    "v_mfma_f32_16x16x32_bf16 v[164:167], a[0:3], a[68:71], v[164:167]\n\t"
    "v_mfma_f32_16x16x32_bf16 v[168:171], a[0:3], a[72:75], v[168:171]\n\t"
    "ds_read_b64_tr_b16 a[28:29], %[vRdW] offset:52320\n\t"
    "v_mfma_f32_16x16x32_bf16 v[168:171], a[4:7], a[88:91], v[168:171]\n\t"
    "ds_read_b64_tr_b16 a[30:31], %[vRdW] offset:52576\n\t"
    "v_mfma_f32_16x16x32_bf16 v[172:175], a[4:7], a[92:95], v[172:175]\n\t"
    "v_mfma_f32_16x16x32_bf16 v[172:175], a[0:3], a[76:79], v[172:175]\n\t"
    "buffer_load_dwordx4 a[52:55], %[vX], %[srdWS], %[sX] offen offset:1024\n\t"
    "s_waitcnt lgkmcnt(8)\n\t"
    "v_mfma_f32_16x16x32_bf16 v[176:179], a[8:11], a[64:67], v[176:179]\n\t"
    "ds_read_b64_tr_b16 a[0:1], %[vRdW] offset:35912\n\t"
    "v_mfma_f32_16x16x32_bf16 v[176:179], a[12:15], a[80:83], v[176:179]\n\t"
    "ds_read_b64_tr_b16 a[2:3], %[vRdW] offset:36168\n\t"
    "v_mfma_f32_16x16x32_bf16 v[180:183], a[12:15], a[84:87], v[180:183]\n\t"
    "s_add_u32 m0, s90, 0\n\t"
    "s_add_u32 s82, %[sWk], s98\n\t"
    "v_mfma_f32_16x16x32_bf16 v[180:183], a[8:11], a[68:71], v[180:183]\n\t"
    "buffer_load_dwordx4 %[vW], %[srdW], s82 offen lds\n\t"
    "v_mfma_f32_16x16x32_bf16 v[184:187], a[8:11], a[72:75], v[184:187]\n\t"
    "ds_read_b64_tr_b16 a[4:5], %[vRdW] offset:52328\n\t"
    "v_mfma_f32_16x16x32_bf16 v[184:187], a[12:15], a[88:91], v[184:187]\n\t"
    "ds_read_b64_tr_b16 a[6:7], %[vRdW] offset:52584\n\t"
    "v_mfma_f32_16x16x32_bf16 v[188:191], a[12:15], a[92:95], v[188:191]\n\t"
    "v_mfma_f32_16x16x32_bf16 v[188:191], a[8:11], a[76:79], v[188:191]\n\t"
    "s_waitcnt lgkmcnt(8)\n\t"
    "v_mfma_f32_16x16x32_bf16 v[192:195], a[16:19], a[64:67], v[192:195]\n\t"
    "ds_read_b64_tr_b16 a[8:9], %[vRdW2] offset:36416\n\t"
    "v_mfma_f32_16x16x32_bf16 v[192:195], a[20:23], a[80:83], v[192:195]\n\t"
    "ds_read_b64_tr_b16 a[10:11], %[vRdW2] offset:36672\n\t"
    "v_mfma_f32_16x16x32_bf16 v[196:199], a[20:23], a[84:87], v[196:199]\n\t"
    "v_mfma_f32_16x16x32_bf16 v[196:199], a[16:19], a[68:71], v[196:199]\n\t"
    "v_mfma_f32_16x16x32_bf16 v[200:203], a[16:19], a[72:75], v[200:203]\n\t"
    "ds_read_b64_tr_b16 a[12:13], %[vRdW2] offset:52832\n\t"
    "v_mfma_f32_16x16x32_bf16 v[200:203], a[20:23], a[88:91], v[200:203]\n\t"
    "ds_read_b64_tr_b16 a[14:15], %[vRdW2] offset:53088\n\t"
    "v_mfma_f32_16x16x32_bf16 v[204:207], a[20:23], a[92:95], v[204:207]\n\t"
    "s_add_u32 m0, s91, 0\n\t"
    "s_add_u32 s82, %[sWk], s99\n\t"
    "v_mfma_f32_16x16x32_bf16 v[204:207], a[16:19], a[76:79], v[204:207]\n\t"
    "buffer_load_dwordx4 %[vW], %[srdW], s82 offen lds\n\t"
    "s_waitcnt vmcnt(32) lgkmcnt(8)\n\t"
    "s_barrier\n\t"
    "v_mfma_f32_16x16x32_bf16 v[208:211], a[24:27], a[64:67], v[208:211]\n\t"
    "ds_read_b64_tr_b16 a[16:17], v8 offset:0\n\t"
    "v_mfma_f32_16x16x32_bf16 v[208:211], a[28:31], a[80:83], v[208:211]\n\t"
    "ds_read_b64_tr_b16 a[18:19], v8 offset:256\n\t"
    "v_mfma_f32_16x16x32_bf16 v[212:215], a[28:31], a[84:87], v[212:215]\n\t"
    "v_mfma_f32_16x16x32_bf16 v[212:215], a[24:27], a[68:71], v[212:215]\n\t"
    "buffer_load_dwordx4 a[56:59], %[vX], %[srdWS], %[sX] offen offset:2048\n\t"
    "v_mfma_f32_16x16x32_bf16 v[216:219], a[24:27], a[72:75], v[216:219]\n\t"
    "ds_read_b64_tr_b16 a[20:21], v8 offset:16416\n\t"
    "v_mfma_f32_16x16x32_bf16 v[216:219], a[28:31], a[88:91], v[216:219]\n\t"
    "ds_read_b64_tr_b16 a[22:23], v8 offset:16672\n\t"
    "v_mfma_f32_16x16x32_bf16 v[220:223], a[28:31], a[92:95], v[220:223]\n\t"
    "v_mfma_f32_16x16x32_bf16 v[220:223], a[24:27], a[76:79], v[220:223]\n\t"
    "s_waitcnt lgkmcnt(8)\n\t"
    "v_mfma_f32_16x16x32_bf16 v[224:227], a[0:3], a[64:67], v[224:227]\n\t"
    "ds_read_b64_tr_b16 a[24:25], v8 offset:8\n\t"
    "v_mfma_f32_16x16x32_bf16 v[224:227], a[4:7], a[80:83], v[224:227]\n\t"
    "ds_read_b64_tr_b16 a[26:27], v8 offset:264\n\t"
    "v_mfma_f32_16x16x32_bf16 v[228:231], a[4:7], a[84:87], v[228:231]\n\t"
    "v_mfma_f32_16x16x32_bf16 v[228:231], a[0:3], a[68:71], v[228:231]\n\t"
    "v_mfma_f32_16x16x32_bf16 v[232:235], a[0:3], a[72:75], v[232:235]\n\t"
    "ds_read_b64_tr_b16 a[28:29], v8 offset:16424\n\t"
    "v_mfma_f32_16x16x32_bf16 v[232:235], a[4:7], a[88:91], v[232:235]\n\t"
    "ds_read_b64_tr_b16 a[30:31], v8 offset:16680\n\t"
    "v_mfma_f32_16x16x32_bf16 v[236:239], a[4:7], a[92:95], v[236:239]\n\t"
    "v_mfma_f32_16x16x32_bf16 v[236:239], a[0:3], a[76:79], v[236:239]\n\t"
    "buffer_load_dwordx4 a[60:63], %[vX], %[srdWS], %[sX] offen offset:3072\n\t"
    "s_waitcnt lgkmcnt(8)\n\t"
    "v_mfma_f32_16x16x32_bf16 v[240:243], a[8:11], a[64:67], v[240:243]\n\t"
    "ds_read_b64_tr_b16 a[0:1], v8 offset:512\n\t"
    "v_mfma_f32_16x16x32_bf16 v[240:243], a[12:15], a[80:83], v[240:243]\n\t"
    "ds_read_b64_tr_b16 a[2:3], v8 offset:768\n\t"
    "v_mfma_f32_16x16x32_bf16 v[244:247], a[12:15], a[84:87], v[244:247]\n\t"
    "v_mfma_f32_16x16x32_bf16 v[244:247], a[8:11], a[68:71], v[244:247]\n\t"
    "v_mfma_f32_16x16x32_bf16 v[248:251], a[8:11], a[72:75], v[248:251]\n\t"
    "ds_read_b64_tr_b16 a[4:5], v8 offset:16928\n\t"
    "v_mfma_f32_16x16x32_bf16 v[248:251], a[12:15], a[88:91], v[248:251]\n\t"
    "ds_read_b64_tr_b16 a[6:7], v8 offset:17184\n\t"
    "v_mfma_f32_16x16x32_bf16 v[252:255], a[12:15], a[92:95], v[252:255]\n\t"
    "v_mfma_f32_16x16x32_bf16 v[252:255], a[8:11], a[76:79], v[252:255]\n\t"
    "s_waitcnt vmcnt(32) lgkmcnt(8)\n\t"
    "v_mfma_f32_16x16x32_bf16 v[16:19], a[16:19], a[96:99], v[16:19]\n\t"
    "ds_read_b64_tr_b16 a[8:9], v8 offset:520\n\t"
    "v_mfma_f32_16x16x32_bf16 v[16:19], a[20:23], a[112:115], v[16:19]\n\t"
    "k_pga2: s_cbranch_execz k_pga6\n\t"
    "ds_read_b64_tr_b16 a[10:11], v8 offset:776\n\t"
    "v_mfma_f32_16x16x32_bf16 v[20:23], a[20:23], a[116:119], v[20:23]\n\t"
    "s_add_u32 %[sX], %[sX], 4096\n\t"
    "s_and_b32 %[sX], %[sX], 0x1ffff\n\t"
    "v_mfma_f32_16x16x32_bf16 v[20:23], a[16:19], a[100:103], v[20:23]\n\t"
    "buffer_load_dwordx4 a[64:67], %[vX], %[srdWS], %[sX] offen offset:0\n\t"
    "v_mfma_f32_16x16x32_bf16 v[24:27], a[16:19], a[104:107], v[24:27]\n\t"
    "ds_read_b64_tr_b16 a[12:13], v8 offset:16936\n\t"
    "v_mfma_f32_16x16x32_bf16 v[24:27], a[20:23], a[120:123], v[24:27]\n\t"
    "ds_read_b64_tr_b16 a[14:15], v8 offset:17192\n\t"
    "v_mfma_f32_16x16x32_bf16 v[28:31], a[20:23], a[124:127], v[28:31]\n\t"
    "v_mfma_f32_16x16x32_bf16 v[28:31], a[16:19], a[108:111], v[28:31]\n\t"
    "s_waitcnt lgkmcnt(8)\n\t"
    "s_barrier\n\t"
    "v_mfma_f32_16x16x32_bf16 v[32:35], a[24:27], a[96:99], v[32:35]\n\t"
    "ds_read_b64_tr_b16 a[16:17], v8 offset:1024\n\t"
    "v_mfma_f32_16x16x32_bf16 v[32:35], a[28:31], a[112:115], v[32:35]\n\t"
    "ds_read_b64_tr_b16 a[18:19], v8 offset:1280\n\t"
    "v_mfma_f32_16x16x32_bf16 v[36:39], a[28:31], a[116:119], v[36:39]\n\t"
    "s_add_u32 %[sWk], %[sWk], s83\n\t"
    "s_add_u32 m0, s84, 32832\n\t"
    "s_add_u32 s82, %[sWk], s92\n\t"
    "v_mfma_f32_16x16x32_bf16 v[36:39], a[24:27], a[100:103], v[36:39]\n\t"
    "buffer_load_dwordx4 %[vW], %[srdW], s82 offen lds\n\t"
    "v_mfma_f32_16x16x32_bf16 v[40:43], a[24:27], a[104:107], v[40:43]\n\t"
    "ds_read_b64_tr_b16 a[20:21], v8 offset:17440\n\t"
    "v_mfma_f32_16x16x32_bf16 v[40:43], a[28:31], a[120:123], v[40:43]\n\t"
    "ds_read_b64_tr_b16 a[22:23], v8 offset:17696\n\t"
    "v_mfma_f32_16x16x32_bf16 v[44:47], a[28:31], a[124:127], v[44:47]\n\t"
    "v_mfma_f32_16x16x32_bf16 v[44:47], a[24:27], a[108:111], v[44:47]\n\t"
    "buffer_load_dwordx4 a[68:71], %[vX], %[srdWS], %[sX] offen offset:1024\n\t"
    "s_waitcnt lgkmcnt(8)\n\t"
    "v_mfma_f32_16x16x32_bf16 v[48:51], a[0:3], a[96:99], v[48:51]\n\t"
    "ds_read_b64_tr_b16 a[24:25], v8 offset:1032\n\t"
    "v_mfma_f32_16x16x32_bf16 v[48:51], a[4:7], a[112:115], v[48:51]\n\t"
    "ds_read_b64_tr_b16 a[26:27], v8 offset:1288\n\t"
    "v_mfma_f32_16x16x32_bf16 v[52:55], a[4:7], a[116:119], v[52:55]\n\t"
    "v_mfma_f32_16x16x32_bf16 v[52:55], a[0:3], a[100:103], v[52:55]\n\t"
    "v_mfma_f32_16x16x32_bf16 v[56:59], a[0:3], a[104:107], v[56:59]\n\t"
    "ds_read_b64_tr_b16 a[28:29], v8 offset:17448\n\t"
    "v_mfma_f32_16x16x32_bf16 v[56:59], a[4:7], a[120:123], v[56:59]\n\t"
    "ds_read_b64_tr_b16 a[30:31], v8 offset:17704\n\t"
    "v_mfma_f32_16x16x32_bf16 v[60:63], a[4:7], a[124:127], v[60:63]\n\t"
    "s_add_u32 m0, s85, 32832\n\t"
    "s_add_u32 s82, %[sWk], s93\n\t"
    "v_mfma_f32_16x16x32_bf16 v[60:63], a[0:3], a[108:111], v[60:63]\n\t"
    "buffer_load_dwordx4 %[vW], %[srdW], s82 offen lds\n\t"
    "s_waitcnt lgkmcnt(8)\n\t"
    "v_mfma_f32_16x16x32_bf16 v[64:67], a[8:11], a[96:99], v[64:67]\n\t"
    "ds_read_b64_tr_b16 a[0:1], v8 offset:1536\n\t"
    "v_mfma_f32_16x16x32_bf16 v[64:67], a[12:15], a[112:115], v[64:67]\n\t"
    "ds_read_b64_tr_b16 a[2:3], v8 offset:1792\n\t"
    "v_mfma_f32_16x16x32_bf16 v[68:71], a[12:15], a[116:119], v[68:71]\n\t"
    "v_mfma_f32_16x16x32_bf16 v[68:71], a[8:11], a[100:103], v[68:71]\n\t"
    "v_mfma_f32_16x16x32_bf16 v[72:75], a[8:11], a[104:107], v[72:75]\n\t"
    "ds_read_b64_tr_b16 a[4:5], v8 offset:17952\n\t"
    "v_mfma_f32_16x16x32_bf16 v[72:75], a[12:15], a[120:123], v[72:75]\n\t"
    "ds_read_b64_tr_b16 a[6:7], v8 offset:18208\n\t"
    "v_mfma_f32_16x16x32_bf16 v[76:79], a[12:15], a[124:127], v[76:79]\n\t"
    "v_mfma_f32_16x16x32_bf16 v[76:79], a[8:11], a[108:111], v[76:79]\n\t"
    "buffer_load_dwordx4 a[72:75], %[vX], %[srdWS], %[sX] offen offset:2048\n\t"
    "s_waitcnt lgkmcnt(8)\n\t"
    "v_mfma_f32_16x16x32_bf16 v[80:83], a[16:19], a[96:99], v[80:83]\n\t"
    "ds_read_b64_tr_b16 a[8:9], v8 offset:1544\n\t"
    "v_mfma_f32_16x16x32_bf16 v[80:83], a[20:23], a[112:115], v[80:83]\n\t"
    "ds_read_b64_tr_b16 a[10:11], v8 offset:1800\n\t"
    "v_mfma_f32_16x16x32_bf16 v[84:87], a[20:23], a[116:119], v[84:87]\n\t"
    "s_add_u32 m0, s86, 32832\n\t"
    "s_add_u32 s82, %[sWk], s94\n\t"
    "v_mfma_f32_16x16x32_bf16 v[84:87], a[16:19], a[100:103], v[84:87]\n\t"
    "buffer_load_dwordx4 %[vW], %[srdW], s82 offen lds\n\t"
    "v_mfma_f32_16x16x32_bf16 v[88:91], a[16:19], a[104:107], v[88:91]\n\t"
    "ds_read_b64_tr_b16 a[12:13], v8 offset:17960\n\t"
    "v_mfma_f32_16x16x32_bf16 v[88:91], a[20:23], a[120:123], v[88:91]\n\t"
    "ds_read_b64_tr_b16 a[14:15], v8 offset:18216\n\t"
    "v_mfma_f32_16x16x32_bf16 v[92:95], a[20:23], a[124:127], v[92:95]\n\t"
    "v_mfma_f32_16x16x32_bf16 v[92:95], a[16:19], a[108:111], v[92:95]\n\t"
    "s_waitcnt lgkmcnt(8)\n\t"
    "v_mfma_f32_16x16x32_bf16 v[96:99], a[24:27], a[96:99], v[96:99]\n\t"
    "ds_read_b64_tr_b16 a[16:17], v8 offset:2048\n\t"
    "v_mfma_f32_16x16x32_bf16 v[96:99], a[28:31], a[112:115], v[96:99]\n\t"
    "ds_read_b64_tr_b16 a[18:19], v8 offset:2304\n\t"
    "v_mfma_f32_16x16x32_bf16 v[100:103], a[28:31], a[116:119], v[100:103]\n\t"
    "v_mfma_f32_16x16x32_bf16 v[100:103], a[24:27], a[100:103], v[100:103]\n\t"
    "v_mfma_f32_16x16x32_bf16 v[104:107], a[24:27], a[104:107], v[104:107]\n\t"
    "ds_read_b64_tr_b16 a[20:21], v8 offset:18464\n\t"
    "v_mfma_f32_16x16x32_bf16 v[104:107], a[28:31], a[120:123], v[104:107]\n\t"
    "ds_read_b64_tr_b16 a[22:23], v8 offset:18720\n\t"
    "v_mfma_f32_16x16x32_bf16 v[108:111], a[28:31], a[124:127], v[108:111]\n\t"
    "s_add_u32 m0, s87, 32832\n\t"
    "s_add_u32 s82, %[sWk], s95\n\t"
    "v_mfma_f32_16x16x32_bf16 v[108:111], a[24:27], a[108:111], v[108:111]\n\t"
    "buffer_load_dwordx4 %[vW], %[srdW], s82 offen lds\n\t"
    "s_waitcnt lgkmcnt(8)\n\t"
    "v_mfma_f32_16x16x32_bf16 v[112:115], a[0:3], a[96:99], v[112:115]\n\t"
    "ds_read_b64_tr_b16 a[24:25], v8 offset:2056\n\t"
    "v_mfma_f32_16x16x32_bf16 v[112:115], a[4:7], a[112:115], v[112:115]\n\t"
    "ds_read_b64_tr_b16 a[26:27], v8 offset:2312\n\t"
    "v_mfma_f32_16x16x32_bf16 v[116:119], a[4:7], a[116:119], v[116:119]\n\t"
    "v_mfma_f32_16x16x32_bf16 v[116:119], a[0:3], a[100:103], v[116:119]\n\t"
    "buffer_load_dwordx4 a[76:79], %[vX], %[srdWS], %[sX] offen offset:3072\n\t"
    "v_mfma_f32_16x16x32_bf16 v[120:123], a[0:3], a[104:107], v[120:123]\n\t"
    "ds_read_b64_tr_b16 a[28:29], v8 offset:18472\n\t"
    "v_mfma_f32_16x16x32_bf16 v[120:123], a[4:7], a[120:123], v[120:123]\n\t"
    "ds_read_b64_tr_b16 a[30:31], v8 offset:18728\n\t"
    "v_mfma_f32_16x16x32_bf16 v[124:127], a[4:7], a[124:127], v[124:127]\n\t"
    "v_mfma_f32_16x16x32_bf16 v[124:127], a[0:3], a[108:111], v[124:127]\n\t"
    "s_waitcnt lgkmcnt(8)\n\t"
    "v_mfma_f32_16x16x32_bf16 v[128:131], a[8:11], a[96:99], v[128:131]\n\t"
    "ds_read_b64_tr_b16 a[0:1], v8 offset:2560\n\t"
    "v_mfma_f32_16x16x32_bf16 v[128:131], a[12:15], a[112:115], v[128:131]\n\t"
    "ds_read_b64_tr_b16 a[2:3], v8 offset:2816\n\t"
    "v_mfma_f32_16x16x32_bf16 v[132:135], a[12:15], a[116:119], v[132:135]\n\t"
    "s_add_u32 m0, s88, 32832\n\t"
    "s_add_u32 s82, %[sWk], s96\n\t"
    "v_mfma_f32_16x16x32_bf16 v[132:135], a[8:11], a[100:103], v[132:135]\n\t"
    "buffer_load_dwordx4 %[vW], %[srdW], s82 offen lds\n\t"
    "v_mfma_f32_16x16x32_bf16 v[136:139], a[8:11], a[104:107], v[136:139]\n\t"
    "ds_read_b64_tr_b16 a[4:5], v8 offset:18976\n\t"
    "v_mfma_f32_16x16x32_bf16 v[136:139], a[12:15], a[120:123], v[136:139]\n\t"
    "ds_read_b64_tr_b16 a[6:7], v8 offset:19232\n\t"
    "v_mfma_f32_16x16x32_bf16 v[140:143], a[12:15], a[124:127], v[140:143]\n\t"
    "v_mfma_f32_16x16x32_bf16 v[140:143], a[8:11], a[108:111], v[140:143]\n\t"
    "s_waitcnt lgkmcnt(8)\n\t"
    "v_mfma_f32_16x16x32_bf16 v[144:147], a[16:19], a[96:99], v[144:147]\n\t"
    "ds_read_b64_tr_b16 a[8:9], v8 offset:2568\n\t"
    "v_mfma_f32_16x16x32_bf16 v[144:147], a[20:23], a[112:115], v[144:147]\n\t"
    "ds_read_b64_tr_b16 a[10:11], v8 offset:2824\n\t"
    "v_mfma_f32_16x16x32_bf16 v[148:151], a[20:23], a[116:119], v[148:151]\n\t"
    "s_add_u32 %[sX], %[sX], 4096\n\t"
    "v_mfma_f32_16x16x32_bf16 v[148:151], a[16:19], a[100:103], v[148:151]\n\t"
    "buffer_load_dwordx4 a[80:83], %[vX], %[srdWS], %[sX] offen offset:0\n\t"
    "v_mfma_f32_16x16x32_bf16 v[152:155], a[16:19], a[104:107], v[152:155]\n\t"
    "ds_read_b64_tr_b16 a[12:13], v8 offset:18984\n\t"
    "v_mfma_f32_16x16x32_bf16 v[152:155], a[20:23], a[120:123], v[152:155]\n\t"
    "ds_read_b64_tr_b16 a[14:15], v8 offset:19240\n\t"
    "v_mfma_f32_16x16x32_bf16 v[156:159], a[20:23], a[124:127], v[156:159]\n\t"
    "s_add_u32 m0, s89, 32832\n\t"
    "s_add_u32 s82, %[sWk], s97\n\t"
    "v_mfma_f32_16x16x32_bf16 v[156:159], a[16:19], a[108:111], v[156:159]\n\t"
    "buffer_load_dwordx4 %[vW], %[srdW], s82 offen lds\n\t"
    "s_waitcnt lgkmcnt(8)\n\t"
    "v_mfma_f32_16x16x32_bf16 v[160:163], a[24:27], a[96:99], v[160:163]\n\t"
    "ds_read_b64_tr_b16 a[16:17], v8 offset:3072\n\t"
    "v_mfma_f32_16x16x32_bf16 v[160:163], a[28:31], a[112:115], v[160:163]\n\t"
    "ds_read_b64_tr_b16 a[18:19], v8 offset:3328\n\t"
    "v_mfma_f32_16x16x32_bf16 v[164:167], a[28:31], a[116:119], v[164:167]\n\t"
    "v_mfma_f32_16x16x32_bf16 v[164:167], a[24:27], a[100:103], v[164:167]\n\t"
    "v_mfma_f32_16x16x32_bf16 v[168:171], a[24:27], a[104:107], v[168:171]\n\t"
    "ds_read_b64_tr_b16 a[20:21], v8 offset:19488\n\t"
    "v_mfma_f32_16x16x32_bf16 v[168:171], a[28:31], a[120:123], v[168:171]\n\t"
    "ds_read_b64_tr_b16 a[22:23], v8 offset:19744\n\t"
    "v_mfma_f32_16x16x32_bf16 v[172:175], a[28:31], a[124:127], v[172:175]\n\t"
    "v_mfma_f32_16x16x32_bf16 v[172:175], a[24:27], a[108:111], v[172:175]\n\t"
    "buffer_load_dwordx4 a[84:87], %[vX], %[srdWS], %[sX] offen offset:1024\n\t"
    "s_waitcnt lgkmcnt(8)\n\t"
    "v_mfma_f32_16x16x32_bf16 v[176:179], a[0:3], a[96:99], v[176:179]\n\t"
    "ds_read_b64_tr_b16 a[24:25], v8 offset:3080\n\t"
    "v_mfma_f32_16x16x32_bf16 v[176:179], a[4:7], a[112:115], v[176:179]\n\t"
    "ds_read_b64_tr_b16 a[26:27], v8 offset:3336\n\t"
    "v_mfma_f32_16x16x32_bf16 v[180:183], a[4:7], a[116:119], v[180:183]\n\t"
    "s_add_u32 m0, s90, 32832\n\t"
    "s_add_u32 s82, %[sWk], s98\n\t"
    "v_mfma_f32_16x16x32_bf16 v[180:183], a[0:3], a[100:103], v[180:183]\n\t"
    "buffer_load_dwordx4 %[vW], %[srdW], s82 offen lds\n\t"
    "v_mfma_f32_16x16x32_bf16 v[184:187], a[0:3], a[104:107], v[184:187]\n\t"
    "ds_read_b64_tr_b16 a[28:29], v8 offset:19496\n\t"
    "v_mfma_f32_16x16x32_bf16 v[184:187], a[4:7], a[120:123], v[184:187]\n\t"
    "ds_read_b64_tr_b16 a[30:31], v8 offset:19752\n\t"
    "v_mfma_f32_16x16x32_bf16 v[188:191], a[4:7], a[124:127], v[188:191]\n\t"
    "v_mfma_f32_16x16x32_bf16 v[188:191], a[0:3], a[108:111], v[188:191]\n\t"
    "s_waitcnt lgkmcnt(8)\n\t"
    "v_mfma_f32_16x16x32_bf16 v[192:195], a[8:11], a[96:99], v[192:195]\n\t"
    "ds_read_b64_tr_b16 a[0:1], v9 offset:3584\n\t"
    "v_mfma_f32_16x16x32_bf16 v[192:195], a[12:15], a[112:115], v[192:195]\n\t"
    "ds_read_b64_tr_b16 a[2:3], v9 offset:3840\n\t"
    "v_mfma_f32_16x16x32_bf16 v[196:199], a[12:15], a[116:119], v[196:199]\n\t"
    "v_mfma_f32_16x16x32_bf16 v[196:199], a[8:11], a[100:103], v[196:199]\n\t"
    "v_mfma_f32_16x16x32_bf16 v[200:203], a[8:11], a[104:107], v[200:203]\n\t"
    "ds_read_b64_tr_b16 a[4:5], v9 offset:20000\n\t"
    "v_mfma_f32_16x16x32_bf16 v[200:203], a[12:15], a[120:123], v[200:203]\n\t"
    "ds_read_b64_tr_b16 a[6:7], v9 offset:20256\n\t"
    "v_mfma_f32_16x16x32_bf16 v[204:207], a[12:15], a[124:127], v[204:207]\n\t"
    "s_add_u32 m0, s91, 32832\n\t"
    "s_add_u32 s82, %[sWk], s99\n\t"
    "v_mfma_f32_16x16x32_bf16 v[204:207], a[8:11], a[108:111], v[204:207]\n\t"
    "buffer_load_dwordx4 %[vW], %[srdW], s82 offen lds\n\t"
    "s_waitcnt vmcnt(32) lgkmcnt(8)\n\t"
    "s_barrier\n\t"
    "v_mfma_f32_16x16x32_bf16 v[208:211], a[16:19], a[96:99], v[208:211]\n\t"
    "ds_read_b64_tr_b16 a[8:9], v8 offset:32832\n\t"
    "v_mfma_f32_16x16x32_bf16 v[208:211], a[20:23], a[112:115], v[208:211]\n\t"
    "ds_read_b64_tr_b16 a[10:11], v8 offset:33088\n\t"
    "v_mfma_f32_16x16x32_bf16 v[212:215], a[20:23], a[116:119], v[212:215]\n\t"
    "v_mfma_f32_16x16x32_bf16 v[212:215], a[16:19], a[100:103], v[212:215]\n\t"
    "buffer_load_dwordx4 a[88:91], %[vX], %[srdWS], %[sX] offen offset:2048\n\t"
    "v_mfma_f32_16x16x32_bf16 v[216:219], a[16:19], a[104:107], v[216:219]\n\t"
    "ds_read_b64_tr_b16 a[12:13], v8 offset:49248\n\t"
    "v_mfma_f32_16x16x32_bf16 v[216:219], a[20:23], a[120:123], v[216:219]\n\t"
    "ds_read_b64_tr_b16 a[14:15], v8 offset:49504\n\t"
    "v_mfma_f32_16x16x32_bf16 v[220:223], a[20:23], a[124:127], v[220:223]\n\t"
    "v_mfma_f32_16x16x32_bf16 v[220:223], a[16:19], a[108:111], v[220:223]\n\t"
    "s_waitcnt lgkmcnt(8)\n\t"
    "v_mfma_f32_16x16x32_bf16 v[224:227], a[24:27], a[96:99], v[224:227]\n\t"
    "ds_read_b64_tr_b16 a[16:17], v8 offset:32840\n\t"
    "v_mfma_f32_16x16x32_bf16 v[224:227], a[28:31], a[112:115], v[224:227]\n\t"
    "ds_read_b64_tr_b16 a[18:19], v8 offset:33096\n\t"
    "v_mfma_f32_16x16x32_bf16 v[228:231], a[28:31], a[116:119], v[228:231]\n\t"
    "v_mfma_f32_16x16x32_bf16 v[228:231], a[24:27], a[100:103], v[228:231]\n\t"
    "v_mfma_f32_16x16x32_bf16 v[232:235], a[24:27], a[104:107], v[232:235]\n\t"
    "ds_read_b64_tr_b16 a[20:21], v8 offset:49256\n\t"
    "v_mfma_f32_16x16x32_bf16 v[232:235], a[28:31], a[120:123], v[232:235]\n\t"
    "ds_read_b64_tr_b16 a[22:23], v8 offset:49512\n\t"
    "v_mfma_f32_16x16x32_bf16 v[236:239], a[28:31], a[124:127], v[236:239]\n\t"
    "v_mfma_f32_16x16x32_bf16 v[236:239], a[24:27], a[108:111], v[236:239]\n\t"
    "buffer_load_dwordx4 a[92:95], %[vX], %[srdWS], %[sX] offen offset:3072\n\t"
    "s_waitcnt lgkmcnt(8)\n\t"
    "v_mfma_f32_16x16x32_bf16 v[240:243], a[0:3], a[96:99], v[240:243]\n\t"
    "ds_read_b64_tr_b16 a[24:25], v8 offset:33344\n\t"
    "v_mfma_f32_16x16x32_bf16 v[240:243], a[4:7], a[112:115], v[240:243]\n\t"
    "ds_read_b64_tr_b16 a[26:27], v8 offset:33600\n\t"
    "v_mfma_f32_16x16x32_bf16 v[244:247], a[4:7], a[116:119], v[244:247]\n\t"
    "v_mfma_f32_16x16x32_bf16 v[244:247], a[0:3], a[100:103], v[244:247]\n\t"
    "v_mfma_f32_16x16x32_bf16 v[248:251], a[0:3], a[104:107], v[248:251]\n\t"
    "ds_read_b64_tr_b16 a[28:29], v8 offset:49760\n\t"
    "v_mfma_f32_16x16x32_bf16 v[248:251], a[4:7], a[120:123], v[248:251]\n\t"
    "ds_read_b64_tr_b16 a[30:31], v8 offset:50016\n\t"
    "v_mfma_f32_16x16x32_bf16 v[252:255], a[4:7], a[124:127], v[252:255]\n\t"
    "v_mfma_f32_16x16x32_bf16 v[252:255], a[0:3], a[108:111], v[252:255]\n\t"
    "s_waitcnt vmcnt(32) lgkmcnt(8)\n\t"
    "v_mfma_f32_16x16x32_bf16 v[16:19], a[8:11], a[128:131], v[16:19]\n\t"
    "ds_read_b64_tr_b16 a[0:1], v8 offset:33352\n\t"
    "v_mfma_f32_16x16x32_bf16 v[16:19], a[12:15], a[144:147], v[16:19]\n\t"
    "ds_read_b64_tr_b16 a[2:3], v8 offset:33608\n\t"
    "v_mfma_f32_16x16x32_bf16 v[20:23], a[12:15], a[148:151], v[20:23]\n\t"
    "s_add_u32 %[sX], %[sX], 4096\n\t"
    "s_and_b32 %[sX], %[sX], 0x1ffff\n\t"
    "v_mfma_f32_16x16x32_bf16 v[20:23], a[8:11], a[132:135], v[20:23]\n\t"
    "buffer_load_dwordx4 a[96:99], %[vX], %[srdWS], %[sX] offen offset:0\n\t"
    "v_mfma_f32_16x16x32_bf16 v[24:27], a[8:11], a[136:139], v[24:27]\n\t"
    "ds_read_b64_tr_b16 a[4:5], v8 offset:49768\n\t"
    "v_mfma_f32_16x16x32_bf16 v[24:27], a[12:15], a[152:155], v[24:27]\n\t"
    "ds_read_b64_tr_b16 a[6:7], v8 offset:50024\n\t"
    "v_mfma_f32_16x16x32_bf16 v[28:31], a[12:15], a[156:159], v[28:31]\n\t"
    "v_mfma_f32_16x16x32_bf16 v[28:31], a[8:11], a[140:143], v[28:31]\n\t"
    "s_waitcnt lgkmcnt(8)\n\t"
    "s_barrier\n\t"
    "v_mfma_f32_16x16x32_bf16 v[32:35], a[16:19], a[128:131], v[32:35]\n\t"
    "ds_read_b64_tr_b16 a[8:9], v8 offset:33856\n\t"
    "v_mfma_f32_16x16x32_bf16 v[32:35], a[20:23], a[144:147], v[32:35]\n\t"
    "ds_read_b64_tr_b16 a[10:11], v8 offset:34112\n\t"
    "v_mfma_f32_16x16x32_bf16 v[36:39], a[20:23], a[148:151], v[36:39]\n\t"
    "s_add_u32 %[sWk], %[sWk], s83\n\t"
    "s_add_u32 m0, s84, 65664\n\t"
    "s_add_u32 s82, %[sWk], s92\n\t"
    "v_mfma_f32_16x16x32_bf16 v[36:39], a[16:19], a[132:135], v[36:39]\n\t"
    "buffer_load_dwordx4 %[vW], %[srdW], s82 offen lds\n\t"
    "v_mfma_f32_16x16x32_bf16 v[40:43], a[16:19], a[136:139], v[40:43]\n\t"
    "ds_read_b64_tr_b16 a[12:13], v8 offset:50272\n\t"
    "v_mfma_f32_16x16x32_bf16 v[40:43], a[20:23], a[152:155], v[40:43]\n\t"
    "ds_read_b64_tr_b16 a[14:15], v8 offset:50528\n\t"
    "v_mfma_f32_16x16x32_bf16 v[44:47], a[20:23], a[156:159], v[44:47]\n\t"
    "v_mfma_f32_16x16x32_bf16 v[44:47], a[16:19], a[140:143], v[44:47]\n\t"
    "buffer_load_dwordx4 a[100:103], %[vX], %[srdWS], %[sX] offen offset:1024\n\t"
    "s_waitcnt lgkmcnt(8)\n\t"
    "v_mfma_f32_16x16x32_bf16 v[48:51], a[24:27], a[128:131], v[48:51]\n\t"
    "ds_read_b64_tr_b16 a[16:17], v8 offset:33864\n\t"
    "v_mfma_f32_16x16x32_bf16 v[48:51], a[28:31], a[144:147], v[48:51]\n\t"
    "ds_read_b64_tr_b16 a[18:19], v8 offset:34120\n\t"
    "v_mfma_f32_16x16x32_bf16 v[52:55], a[28:31], a[148:151], v[52:55]\n\t"
    "v_mfma_f32_16x16x32_bf16 v[52:55], a[24:27], a[132:135], v[52:55]\n\t"
    "v_mfma_f32_16x16x32_bf16 v[56:59], a[24:27], a[136:139], v[56:59]\n\t"
    "ds_read_b64_tr_b16 a[20:21], v8 offset:50280\n\t"
    "v_mfma_f32_16x16x32_bf16 v[56:59], a[28:31], a[152:155], v[56:59]\n\t"
    "ds_read_b64_tr_b16 a[22:23], v8 offset:50536\n\t"
    "v_mfma_f32_16x16x32_bf16 v[60:63], a[28:31], a[156:159], v[60:63]\n\t"
    "s_add_u32 m0, s85, 65664\n\t"
    "s_add_u32 s82, %[sWk], s93\n\t"
    "v_mfma_f32_16x16x32_bf16 v[60:63], a[24:27], a[140:143], v[60:63]\n\t"
    "buffer_load_dwordx4 %[vW], %[srdW], s82 offen lds\n\t"
    "s_waitcnt lgkmcnt(8)\n\t"
    "v_mfma_f32_16x16x32_bf16 v[64:67], a[0:3], a[128:131], v[64:67]\n\t"
    "ds_read_b64_tr_b16 a[24:25], v8 offset:34368\n\t"
    "v_mfma_f32_16x16x32_bf16 v[64:67], a[4:7], a[144:147], v[64:67]\n\t"
    "ds_read_b64_tr_b16 a[26:27], v8 offset:34624\n\t"
    "v_mfma_f32_16x16x32_bf16 v[68:71], a[4:7], a[148:151], v[68:71]\n\t"
    "v_mfma_f32_16x16x32_bf16 v[68:71], a[0:3], a[132:135], v[68:71]\n\t"
    "v_mfma_f32_16x16x32_bf16 v[72:75], a[0:3], a[136:139], v[72:75]\n\t"
    "ds_read_b64_tr_b16 a[28:29], v8 offset:50784\n\t"
    "v_mfma_f32_16x16x32_bf16 v[72:75], a[4:7], a[152:155], v[72:75]\n\t"
    "ds_read_b64_tr_b16 a[30:31], v8 offset:51040\n\t"
    "v_mfma_f32_16x16x32_bf16 v[76:79], a[4:7], a[156:159], v[76:79]\n\t"
    "v_mfma_f32_16x16x32_bf16 v[76:79], a[0:3], a[140:143], v[76:79]\n\t"
    "buffer_load_dwordx4 a[104:107], %[vX], %[srdWS], %[sX] offen offset:2048\n\t"
    "s_waitcnt lgkmcnt(8)\n\t"
    "v_mfma_f32_16x16x32_bf16 v[80:83], a[8:11], a[128:131], v[80:83]\n\t"
    "ds_read_b64_tr_b16 a[0:1], v8 offset:34376\n\t"
    "v_mfma_f32_16x16x32_bf16 v[80:83], a[12:15], a[144:147], v[80:83]\n\t"
    "ds_read_b64_tr_b16 a[2:3], v8 offset:34632\n\t"
    "v_mfma_f32_16x16x32_bf16 v[84:87], a[12:15], a[148:151], v[84:87]\n\t"
    "s_add_u32 m0, s86, 65664\n\t"
    "s_add_u32 s82, %[sWk], s94\n\t"
    "v_mfma_f32_16x16x32_bf16 v[84:87], a[8:11], a[132:135], v[84:87]\n\t"
    "buffer_load_dwordx4 %[vW], %[srdW], s82 offen lds\n\t"
    "v_mfma_f32_16x16x32_bf16 v[88:91], a[8:11], a[136:139], v[88:91]\n\t"
    "ds_read_b64_tr_b16 a[4:5], v8 offset:50792\n\t"
    "v_mfma_f32_16x16x32_bf16 v[88:91], a[12:15], a[152:155], v[88:91]\n\t"
    "ds_read_b64_tr_b16 a[6:7], v8 offset:51048\n\t"
    "v_mfma_f32_16x16x32_bf16 v[92:95], a[12:15], a[156:159], v[92:95]\n\t"
    "v_mfma_f32_16x16x32_bf16 v[92:95], a[8:11], a[140:143], v[92:95]\n\t"
    "s_waitcnt lgkmcnt(8)\n\t"
    "v_mfma_f32_16x16x32_bf16 v[96:99], a[16:19], a[128:131], v[96:99]\n\t"
    "ds_read_b64_tr_b16 a[8:9], v8 offset:34880\n\t"
    "v_mfma_f32_16x16x32_bf16 v[96:99], a[20:23], a[144:147], v[96:99]\n\t"
    "ds_read_b64_tr_b16 a[10:11], v8 offset:35136\n\t"
    "v_mfma_f32_16x16x32_bf16 v[100:103], a[20:23], a[148:151], v[100:103]\n\t"
    "v_mfma_f32_16x16x32_bf16 v[100:103], a[16:19], a[132:135], v[100:103]\n\t"
    "v_mfma_f32_16x16x32_bf16 v[104:107], a[16:19], a[136:139], v[104:107]\n\t"
    "ds_read_b64_tr_b16 a[12:13], v8 offset:51296\n\t"
    "v_mfma_f32_16x16x32_bf16 v[104:107], a[20:23], a[152:155], v[104:107]\n\t"
    "ds_read_b64_tr_b16 a[14:15], v8 offset:51552\n\t"
    "v_mfma_f32_16x16x32_bf16 v[108:111], a[20:23], a[156:159], v[108:111]\n\t"
    "s_add_u32 m0, s87, 65664\n\t"
    "s_add_u32 s82, %[sWk], s95\n\t"
    "v_mfma_f32_16x16x32_bf16 v[108:111], a[16:19], a[140:143], v[108:111]\n\t"
    "buffer_load_dwordx4 %[vW], %[srdW], s82 offen lds\n\t"
    "s_waitcnt lgkmcnt(8)\n\t"
    "v_mfma_f32_16x16x32_bf16 v[112:115], a[24:27], a[128:131], v[112:115]\n\t"
    "ds_read_b64_tr_b16 a[16:17], v8 offset:34888\n\t"
    "v_mfma_f32_16x16x32_bf16 v[112:115], a[28:31], a[144:147], v[112:115]\n\t"
    "ds_read_b64_tr_b16 a[18:19], v8 offset:35144\n\t"
    "v_mfma_f32_16x16x32_bf16 v[116:119], a[28:31], a[148:151], v[116:119]\n\t"
    "v_mfma_f32_16x16x32_bf16 v[116:119], a[24:27], a[132:135], v[116:119]\n\t"
    "buffer_load_dwordx4 a[108:111], %[vX], %[srdWS], %[sX] offen offset:3072\n\t"
    "v_mfma_f32_16x16x32_bf16 v[120:123], a[24:27], a[136:139], v[120:123]\n\t"
    "ds_read_b64_tr_b16 a[20:21], v8 offset:51304\n\t"
    "v_mfma_f32_16x16x32_bf16 v[120:123], a[28:31], a[152:155], v[120:123]\n\t"
    "ds_read_b64_tr_b16 a[22:23], v8 offset:51560\n\t"
    "v_mfma_f32_16x16x32_bf16 v[124:127], a[28:31], a[156:159], v[124:127]\n\t"
    "v_mfma_f32_16x16x32_bf16 v[124:127], a[24:27], a[140:143], v[124:127]\n\t"
    "s_waitcnt lgkmcnt(8)\n\t"
    "v_mfma_f32_16x16x32_bf16 v[128:131], a[0:3], a[128:131], v[128:131]\n\t"
    "ds_read_b64_tr_b16 a[24:25], v8 offset:35392\n\t"
    "v_mfma_f32_16x16x32_bf16 v[128:131], a[4:7], a[144:147], v[128:131]\n\t"
    "ds_read_b64_tr_b16 a[26:27], v8 offset:35648\n\t"
    "v_mfma_f32_16x16x32_bf16 v[132:135], a[4:7], a[148:151], v[132:135]\n\t"
    "s_add_u32 m0, s88, 65664\n\t"
    "s_add_u32 s82, %[sWk], s96\n\t"
    "v_mfma_f32_16x16x32_bf16 v[132:135], a[0:3], a[132:135], v[132:135]\n\t"
    "buffer_load_dwordx4 %[vW], %[srdW], s82 offen lds\n\t"
    "v_mfma_f32_16x16x32_bf16 v[136:139], a[0:3], a[136:139], v[136:139]\n\t"
    "ds_read_b64_tr_b16 a[28:29], v8 offset:51808\n\t"
    "v_mfma_f32_16x16x32_bf16 v[136:139], a[4:7], a[152:155], v[136:139]\n\t"
    "ds_read_b64_tr_b16 a[30:31], v8 offset:52064\n\t"
    "v_mfma_f32_16x16x32_bf16 v[140:143], a[4:7], a[156:159], v[140:143]\n\t"
    "v_mfma_f32_16x16x32_bf16 v[140:143], a[0:3], a[140:143], v[140:143]\n\t"
    "s_waitcnt lgkmcnt(8)\n\t"
    "v_mfma_f32_16x16x32_bf16 v[144:147], a[8:11], a[128:131], v[144:147]\n\t"
    "ds_read_b64_tr_b16 a[0:1], v8 offset:35400\n\t"
    "v_mfma_f32_16x16x32_bf16 v[144:147], a[12:15], a[144:147], v[144:147]\n\t"
    "ds_read_b64_tr_b16 a[2:3], v8 offset:35656\n\t"
    "v_mfma_f32_16x16x32_bf16 v[148:151], a[12:15], a[148:151], v[148:151]\n\t"
    "s_add_u32 %[sX], %[sX], 4096\n\t"
    "v_mfma_f32_16x16x32_bf16 v[148:151], a[8:11], a[132:135], v[148:151]\n\t"
    "buffer_load_dwordx4 a[112:115], %[vX], %[srdWS], %[sX] offen offset:0\n\t"
    "v_mfma_f32_16x16x32_bf16 v[152:155], a[8:11], a[136:139], v[152:155]\n\t"
    "ds_read_b64_tr_b16 a[4:5], v8 offset:51816\n\t"
    "v_mfma_f32_16x16x32_bf16 v[152:155], a[12:15], a[152:155], v[152:155]\n\t"
    "ds_read_b64_tr_b16 a[6:7], v8 offset:52072\n\t"
    "v_mfma_f32_16x16x32_bf16 v[156:159], a[12:15], a[156:159], v[156:159]\n\t"
    "s_add_u32 m0, s89, 65664\n\t"
    "s_add_u32 s82, %[sWk], s97\n\t"
    "v_mfma_f32_16x16x32_bf16 v[156:159], a[8:11], a[140:143], v[156:159]\n\t"
    "buffer_load_dwordx4 %[vW], %[srdW], s82 offen lds\n\t"
    "s_waitcnt lgkmcnt(8)\n\t"
    "v_mfma_f32_16x16x32_bf16 v[160:163], a[16:19], a[128:131], v[160:163]\n\t"
    "ds_read_b64_tr_b16 a[8:9], v8 offset:35904\n\t"
    "v_mfma_f32_16x16x32_bf16 v[160:163], a[20:23], a[144:147], v[160:163]\n\t"
    "ds_read_b64_tr_b16 a[10:11], v8 offset:36160\n\t"
    "v_mfma_f32_16x16x32_bf16 v[164:167], a[20:23], a[148:151], v[164:167]\n\t"
    "v_mfma_f32_16x16x32_bf16 v[164:167], a[16:19], a[132:135], v[164:167]\n\t"
    "v_mfma_f32_16x16x32_bf16 v[168:171], a[16:19], a[136:139], v[168:171]\n\t"
    "ds_read_b64_tr_b16 a[12:13], v8 offset:52320\n\t"
    "v_mfma_f32_16x16x32_bf16 v[168:171], a[20:23], a[152:155], v[168:171]\n\t"
    "ds_read_b64_tr_b16 a[14:15], v8 offset:52576\n\t"
    "v_mfma_f32_16x16x32_bf16 v[172:175], a[20:23], a[156:159], v[172:175]\n\t"
    "v_mfma_f32_16x16x32_bf16 v[172:175], a[16:19], a[140:143], v[172:175]\n\t"
    "buffer_load_dwordx4 a[116:119], %[vX], %[srdWS], %[sX] offen offset:1024\n\t"
    "s_waitcnt lgkmcnt(8)\n\t"
    "v_mfma_f32_16x16x32_bf16 v[176:179], a[24:27], a[128:131], v[176:179]\n\t"
    "ds_read_b64_tr_b16 a[16:17], v8 offset:35912\n\t"
    "v_mfma_f32_16x16x32_bf16 v[176:179], a[28:31], a[144:147], v[176:179]\n\t"
    "ds_read_b64_tr_b16 a[18:19], v8 offset:36168\n\t"
    "v_mfma_f32_16x16x32_bf16 v[180:183], a[28:31], a[148:151], v[180:183]\n\t"
    "s_add_u32 m0, s90, 65664\n\t"
    "s_add_u32 s82, %[sWk], s98\n\t"
    "v_mfma_f32_16x16x32_bf16 v[180:183], a[24:27], a[132:135], v[180:183]\n\t"
    "buffer_load_dwordx4 %[vW], %[srdW], s82 offen lds\n\t"
    "v_mfma_f32_16x16x32_bf16 v[184:187], a[24:27], a[136:139], v[184:187]\n\t"
    "ds_read_b64_tr_b16 a[20:21], v8 offset:52328\n\t"
    "v_mfma_f32_16x16x32_bf16 v[184:187], a[28:31], a[152:155], v[184:187]\n\t"
    "ds_read_b64_tr_b16 a[22:23], v8 offset:52584\n\t"
    "v_mfma_f32_16x16x32_bf16 v[188:191], a[28:31], a[156:159], v[188:191]\n\t"
    "v_mfma_f32_16x16x32_bf16 v[188:191], a[24:27], a[140:143], v[188:191]\n\t"
    "s_waitcnt lgkmcnt(8)\n\t"
    "v_mfma_f32_16x16x32_bf16 v[192:195], a[0:3], a[128:131], v[192:195]\n\t"
    "ds_read_b64_tr_b16 a[24:25], v9 offset:36416\n\t"
    "v_mfma_f32_16x16x32_bf16 v[192:195], a[4:7], a[144:147], v[192:195]\n\t"
    "ds_read_b64_tr_b16 a[26:27], v9 offset:36672\n\t"
    "v_mfma_f32_16x16x32_bf16 v[196:199], a[4:7], a[148:151], v[196:199]\n\t"
    "v_mfma_f32_16x16x32_bf16 v[196:199], a[0:3], a[132:135], v[196:199]\n\t"
    "v_mfma_f32_16x16x32_bf16 v[200:203], a[0:3], a[136:139], v[200:203]\n\t"
    "ds_read_b64_tr_b16 a[28:29], v9 offset:52832\n\t"
    "v_mfma_f32_16x16x32_bf16 v[200:203], a[4:7], a[152:155], v[200:203]\n\t"
    "ds_read_b64_tr_b16 a[30:31], v9 offset:53088\n\t"
    "v_mfma_f32_16x16x32_bf16 v[204:207], a[4:7], a[156:159], v[204:207]\n\t"
    "s_add_u32 m0, s91, 65664\n\t"
    "s_add_u32 s82, %[sWk], s99\n\t"
    "v_mfma_f32_16x16x32_bf16 v[204:207], a[0:3], a[140:143], v[204:207]\n\t"
    "buffer_load_dwordx4 %[vW], %[srdW], s82 offen lds\n\t"
    "s_waitcnt vmcnt(32) lgkmcnt(8)\n\t"
    "s_barrier\n\t"
    "v_mfma_f32_16x16x32_bf16 v[208:211], a[8:11], a[128:131], v[208:211]\n\t"
    "ds_read_b64_tr_b16 a[0:1], %[vRdW] offset:0\n\t"
    "v_mfma_f32_16x16x32_bf16 v[208:211], a[12:15], a[144:147], v[208:211]\n\t"
    "ds_read_b64_tr_b16 a[2:3], %[vRdW] offset:256\n\t"
    "v_mfma_f32_16x16x32_bf16 v[212:215], a[12:15], a[148:151], v[212:215]\n\t"
    "v_mfma_f32_16x16x32_bf16 v[212:215], a[8:11], a[132:135], v[212:215]\n\t"
    "buffer_load_dwordx4 a[120:123], %[vX], %[srdWS], %[sX] offen offset:2048\n\t"
    "v_mfma_f32_16x16x32_bf16 v[216:219], a[8:11], a[136:139], v[216:219]\n\t"
    "ds_read_b64_tr_b16 a[4:5], %[vRdW] offset:16416\n\t"
    "v_mfma_f32_16x16x32_bf16 v[216:219], a[12:15], a[152:155], v[216:219]\n\t"
    "ds_read_b64_tr_b16 a[6:7], %[vRdW] offset:16672\n\t"
    "v_mfma_f32_16x16x32_bf16 v[220:223], a[12:15], a[156:159], v[220:223]\n\t"
    "v_mfma_f32_16x16x32_bf16 v[220:223], a[8:11], a[140:143], v[220:223]\n\t"
    "s_waitcnt lgkmcnt(8)\n\t"
    "v_mfma_f32_16x16x32_bf16 v[224:227], a[16:19], a[128:131], v[224:227]\n\t"
    "ds_read_b64_tr_b16 a[8:9], %[vRdW] offset:8\n\t"
    "v_mfma_f32_16x16x32_bf16 v[224:227], a[20:23], a[144:147], v[224:227]\n\t"
    "ds_read_b64_tr_b16 a[10:11], %[vRdW] offset:264\n\t"
    "v_mfma_f32_16x16x32_bf16 v[228:231], a[20:23], a[148:151], v[228:231]\n\t"
    "v_mfma_f32_16x16x32_bf16 v[228:231], a[16:19], a[132:135], v[228:231]\n\t"
    "v_mfma_f32_16x16x32_bf16 v[232:235], a[16:19], a[136:139], v[232:235]\n\t"
    "ds_read_b64_tr_b16 a[12:13], %[vRdW] offset:16424\n\t"
    "v_mfma_f32_16x16x32_bf16 v[232:235], a[20:23], a[152:155], v[232:235]\n\t"
    "ds_read_b64_tr_b16 a[14:15], %[vRdW] offset:16680\n\t"
    "v_mfma_f32_16x16x32_bf16 v[236:239], a[20:23], a[156:159], v[236:239]\n\t"
    "v_mfma_f32_16x16x32_bf16 v[236:239], a[16:19], a[140:143], v[236:239]\n\t"
    "buffer_load_dwordx4 a[124:127], %[vX], %[srdWS], %[sX] offen offset:3072\n\t"
    "s_waitcnt lgkmcnt(8)\n\t"
    "v_mfma_f32_16x16x32_bf16 v[240:243], a[24:27], a[128:131], v[240:243]\n\t"
    "ds_read_b64_tr_b16 a[16:17], %[vRdW] offset:512\n\t"
    "v_mfma_f32_16x16x32_bf16 v[240:243], a[28:31], a[144:147], v[240:243]\n\t"
    "ds_read_b64_tr_b16 a[18:19], %[vRdW] offset:768\n\t"
    "v_mfma_f32_16x16x32_bf16 v[244:247], a[28:31], a[148:151], v[244:247]\n\t"
    "v_mfma_f32_16x16x32_bf16 v[244:247], a[24:27], a[132:135], v[244:247]\n\t"
    "v_mfma_f32_16x16x32_bf16 v[248:251], a[24:27], a[136:139], v[248:251]\n\t"
    "ds_read_b64_tr_b16 a[20:21], %[vRdW] offset:16928\n\t"
    "v_mfma_f32_16x16x32_bf16 v[248:251], a[28:31], a[152:155], v[248:251]\n\t"
    "ds_read_b64_tr_b16 a[22:23], %[vRdW] offset:17184\n\t"
    "v_mfma_f32_16x16x32_bf16 v[252:255], a[28:31], a[156:159], v[252:255]\n\t"
    "v_mfma_f32_16x16x32_bf16 v[252:255], a[24:27], a[140:143], v[252:255]\n\t"
    "s_sub_u32 s67, s67, 1\n\t"
    "s_cmp_lg_u32 s67, 0\n\t"
    "s_cbranch_scc1 L_k1_loop_%=\n\t"
    "s_waitcnt vmcnt(32) lgkmcnt(8)\n\t"
    "v_mfma_f32_16x16x32_bf16 v[16:19], a[0:3], a[32:35], v[16:19]\n\t"
    "ds_read_b64_tr_b16 a[24:25], %[vRdW] offset:520\n\t"
    "v_mfma_f32_16x16x32_bf16 v[16:19], a[4:7], a[48:51], v[16:19]\n\t"
    "ds_read_b64_tr_b16 a[26:27], %[vRdW] offset:776\n\t"
    "v_mfma_f32_16x16x32_bf16 v[20:23], a[4:7], a[52:55], v[20:23]\n\t"
    "v_mfma_f32_16x16x32_bf16 v[20:23], a[0:3], a[36:39], v[20:23]\n\t"
    "v_mfma_f32_16x16x32_bf16 v[24:27], a[0:3], a[40:43], v[24:27]\n\t"
    "ds_read_b64_tr_b16 a[28:29], %[vRdW] offset:16936\n\t"
    "v_mfma_f32_16x16x32_bf16 v[24:27], a[4:7], a[56:59], v[24:27]\n\t"
    "ds_read_b64_tr_b16 a[30:31], %[vRdW] offset:17192\n\t"
    "v_mfma_f32_16x16x32_bf16 v[28:31], a[4:7], a[60:63], v[28:31]\n\t"
    "v_mfma_f32_16x16x32_bf16 v[28:31], a[0:3], a[44:47], v[28:31]\n\t"
    "s_waitcnt lgkmcnt(8)\n\t"
    "s_barrier\n\t"
    "v_mfma_f32_16x16x32_bf16 v[32:35], a[8:11], a[32:35], v[32:35]\n\t"
    "ds_read_b64_tr_b16 a[0:1], %[vRdW] offset:1024\n\t"
    "s_add_u32 %[sWk], %[sWk], s83\n\t"
    "s_add_u32 m0, s84, 98496\n\t"
    "s_add_u32 s82, %[sWk], s92\n\t"
    "v_mfma_f32_16x16x32_bf16 v[32:35], a[12:15], a[48:51], v[32:35]\n\t"
    "ds_read_b64_tr_b16 a[2:3], %[vRdW] offset:1280\n\t"
    "buffer_load_dwordx4 %[vW], %[srdW], s82 offen lds\n\t"
    "v_mfma_f32_16x16x32_bf16 v[36:39], a[12:15], a[52:55], v[36:39]\n\t"
    "s_add_u32 m0, s85, 98496\n\t"
    "s_add_u32 s82, %[sWk], s93\n\t"
    "v_mfma_f32_16x16x32_bf16 v[36:39], a[8:11], a[36:39], v[36:39]\n\t"
    "v_mfma_f32_16x16x32_bf16 v[40:43], a[8:11], a[40:43], v[40:43]\n\t"
    "ds_read_b64_tr_b16 a[4:5], %[vRdW] offset:17440\n\t"
    "v_mfma_f32_16x16x32_bf16 v[40:43], a[12:15], a[56:59], v[40:43]\n\t"
    "ds_read_b64_tr_b16 a[6:7], %[vRdW] offset:17696\n\t"
    "v_mfma_f32_16x16x32_bf16 v[44:47], a[12:15], a[60:63], v[44:47]\n\t"
    "v_mfma_f32_16x16x32_bf16 v[44:47], a[8:11], a[44:47], v[44:47]\n\t"
    "buffer_load_dwordx4 %[vW], %[srdW], s82 offen lds\n\t"
    "s_waitcnt lgkmcnt(8)\n\t"
    "v_mfma_f32_16x16x32_bf16 v[48:51], a[16:19], a[32:35], v[48:51]\n\t"
    "ds_read_b64_tr_b16 a[8:9], %[vRdW] offset:1032\n\t"
    "s_add_u32 m0, s86, 98496\n\t"
    "s_add_u32 s82, %[sWk], s94\n\t"
    "v_mfma_f32_16x16x32_bf16 v[48:51], a[20:23], a[48:51], v[48:51]\n\t"
    "ds_read_b64_tr_b16 a[10:11], %[vRdW] offset:1288\n\t"
    "v_mfma_f32_16x16x32_bf16 v[52:55], a[20:23], a[52:55], v[52:55]\n\t"
    "v_mfma_f32_16x16x32_bf16 v[52:55], a[16:19], a[36:39], v[52:55]\n\t"
    "v_mfma_f32_16x16x32_bf16 v[56:59], a[16:19], a[40:43], v[56:59]\n\t"
    "ds_read_b64_tr_b16 a[12:13], %[vRdW] offset:17448\n\t"
    "v_mfma_f32_16x16x32_bf16 v[56:59], a[20:23], a[56:59], v[56:59]\n\t"
    "ds_read_b64_tr_b16 a[14:15], %[vRdW] offset:17704\n\t"
    "buffer_load_dwordx4 %[vW], %[srdW], s82 offen lds\n\t"
    "v_mfma_f32_16x16x32_bf16 v[60:63], a[20:23], a[60:63], v[60:63]\n\t"
    "s_add_u32 m0, s87, 98496\n\t"
    "s_add_u32 s82, %[sWk], s95\n\t"
    "v_mfma_f32_16x16x32_bf16 v[60:63], a[16:19], a[44:47], v[60:63]\n\t"
    "s_waitcnt lgkmcnt(8)\n\t"
    "v_mfma_f32_16x16x32_bf16 v[64:67], a[24:27], a[32:35], v[64:67]\n\t"
    "ds_read_b64_tr_b16 a[16:17], %[vRdW] offset:1536\n\t"
    "v_mfma_f32_16x16x32_bf16 v[64:67], a[28:31], a[48:51], v[64:67]\n\t"
    "ds_read_b64_tr_b16 a[18:19], %[vRdW] offset:1792\n\t"
    "v_mfma_f32_16x16x32_bf16 v[68:71], a[28:31], a[52:55], v[68:71]\n\t"
    "v_mfma_f32_16x16x32_bf16 v[68:71], a[24:27], a[36:39], v[68:71]\n\t"
    "buffer_load_dwordx4 %[vW], %[srdW], s82 offen lds\n\t"
    "s_add_u32 m0, s88, 98496\n\t"
    "s_add_u32 s82, %[sWk], s96\n\t"
    "v_mfma_f32_16x16x32_bf16 v[72:75], a[24:27], a[40:43], v[72:75]\n\t"
    "ds_read_b64_tr_b16 a[20:21], %[vRdW] offset:17952\n\t"
    "v_mfma_f32_16x16x32_bf16 v[72:75], a[28:31], a[56:59], v[72:75]\n\t"
    "ds_read_b64_tr_b16 a[22:23], %[vRdW] offset:18208\n\t"
    "v_mfma_f32_16x16x32_bf16 v[76:79], a[28:31], a[60:63], v[76:79]\n\t"
    "v_mfma_f32_16x16x32_bf16 v[76:79], a[24:27], a[44:47], v[76:79]\n\t"
    "s_waitcnt lgkmcnt(8)\n\t"
    "v_mfma_f32_16x16x32_bf16 v[80:83], a[0:3], a[32:35], v[80:83]\n\t"
    "ds_read_b64_tr_b16 a[24:25], %[vRdW] offset:1544\n\t"
    "v_mfma_f32_16x16x32_bf16 v[80:83], a[4:7], a[48:51], v[80:83]\n\t"
    "ds_read_b64_tr_b16 a[26:27], %[vRdW] offset:1800\n\t"
    "buffer_load_dwordx4 %[vW], %[srdW], s82 offen lds\n\t"
    "v_mfma_f32_16x16x32_bf16 v[84:87], a[4:7], a[52:55], v[84:87]\n\t"
    "s_add_u32 m0, s89, 98496\n\t"
    "s_add_u32 s82, %[sWk], s97\n\t"
    "v_mfma_f32_16x16x32_bf16 v[84:87], a[0:3], a[36:39], v[84:87]\n\t"
    "v_mfma_f32_16x16x32_bf16 v[88:91], a[0:3], a[40:43], v[88:91]\n\t"
    "ds_read_b64_tr_b16 a[28:29], %[vRdW] offset:17960\n\t"
    "k_pga3: s_cbranch_execz k_pga7\n\t"
    "v_mfma_f32_16x16x32_bf16 v[88:91], a[4:7], a[56:59], v[88:91]\n\t"
    "ds_read_b64_tr_b16 a[30:31], %[vRdW] offset:18216\n\t"
    "v_mfma_f32_16x16x32_bf16 v[92:95], a[4:7], a[60:63], v[92:95]\n\t"
    "v_mfma_f32_16x16x32_bf16 v[92:95], a[0:3], a[44:47], v[92:95]\n\t"
    "buffer_load_dwordx4 %[vW], %[srdW], s82 offen lds\n\t"
    "s_waitcnt lgkmcnt(8)\n\t"
    "v_mfma_f32_16x16x32_bf16 v[96:99], a[8:11], a[32:35], v[96:99]\n\t"
    "ds_read_b64_tr_b16 a[0:1], %[vRdW] offset:2048\n\t"
    "s_add_u32 m0, s90, 98496\n\t"
    "s_add_u32 s82, %[sWk], s98\n\t"
    "v_mfma_f32_16x16x32_bf16 v[96:99], a[12:15], a[48:51], v[96:99]\n\t"
    "ds_read_b64_tr_b16 a[2:3], %[vRdW] offset:2304\n\t"
    "v_mfma_f32_16x16x32_bf16 v[100:103], a[12:15], a[52:55], v[100:103]\n\t"
    "v_mfma_f32_16x16x32_bf16 v[100:103], a[8:11], a[36:39], v[100:103]\n\t"
    "v_mfma_f32_16x16x32_bf16 v[104:107], a[8:11], a[40:43], v[104:107]\n\t"
    "ds_read_b64_tr_b16 a[4:5], %[vRdW] offset:18464\n\t"
    "v_mfma_f32_16x16x32_bf16 v[104:107], a[12:15], a[56:59], v[104:107]\n\t"
    "ds_read_b64_tr_b16 a[6:7], %[vRdW] offset:18720\n\t"
    "buffer_load_dwordx4 %[vW], %[srdW], s82 offen lds\n\t"
    "v_mfma_f32_16x16x32_bf16 v[108:111], a[12:15], a[60:63], v[108:111]\n\t"
    "s_add_u32 m0, s91, 98496\n\t"
    "s_add_u32 s82, %[sWk], s99\n\t"
    "v_mfma_f32_16x16x32_bf16 v[108:111], a[8:11], a[44:47], v[108:111]\n\t"
    "s_waitcnt lgkmcnt(8)\n\t"
    "v_mfma_f32_16x16x32_bf16 v[112:115], a[16:19], a[32:35], v[112:115]\n\t"
    "ds_read_b64_tr_b16 a[8:9], %[vRdW] offset:2056\n\t"
    "v_mfma_f32_16x16x32_bf16 v[112:115], a[20:23], a[48:51], v[112:115]\n\t"
    "ds_read_b64_tr_b16 a[10:11], %[vRdW] offset:2312\n\t"
    "v_mfma_f32_16x16x32_bf16 v[116:119], a[20:23], a[52:55], v[116:119]\n\t"
    "v_mfma_f32_16x16x32_bf16 v[116:119], a[16:19], a[36:39], v[116:119]\n\t"
    "buffer_load_dwordx4 %[vW], %[srdW], s82 offen lds\n\t"
    "s_add_u32 %[sX], %[sX], 4096\n\t"
    "s_and_b32 %[sX], %[sX], 0x1ffff\n\t"
    "v_mfma_f32_16x16x32_bf16 v[120:123], a[16:19], a[40:43], v[120:123]\n\t"
    "ds_read_b64_tr_b16 a[12:13], %[vRdW] offset:18472\n\t"
    "v_mfma_f32_16x16x32_bf16 v[120:123], a[20:23], a[56:59], v[120:123]\n\t"
    "ds_read_b64_tr_b16 a[14:15], %[vRdW] offset:18728\n\t"
    "v_mfma_f32_16x16x32_bf16 v[124:127], a[20:23], a[60:63], v[124:127]\n\t"
    "v_mfma_f32_16x16x32_bf16 v[124:127], a[16:19], a[44:47], v[124:127]\n\t"
    "s_waitcnt lgkmcnt(8)\n\t"
    "v_mfma_f32_16x16x32_bf16 v[128:131], a[24:27], a[32:35], v[128:131]\n\t"
    "ds_read_b64_tr_b16 a[16:17], %[vRdW] offset:2560\n\t"
    "v_mfma_f32_16x16x32_bf16 v[128:131], a[28:31], a[48:51], v[128:131]\n\t"
    "ds_read_b64_tr_b16 a[18:19], %[vRdW] offset:2816\n\t"
    "buffer_load_dwordx4 a[128:131], %[vX], %[srdWS], %[sX] offen offset:0\n\t"
    "v_mfma_f32_16x16x32_bf16 v[132:135], a[28:31], a[52:55], v[132:135]\n\t"
    "v_mfma_f32_16x16x32_bf16 v[132:135], a[24:27], a[36:39], v[132:135]\n\t"
    "v_mfma_f32_16x16x32_bf16 v[136:139], a[24:27], a[40:43], v[136:139]\n\t"
    "ds_read_b64_tr_b16 a[20:21], %[vRdW] offset:18976\n\t"
    "v_mfma_f32_16x16x32_bf16 v[136:139], a[28:31], a[56:59], v[136:139]\n\t"
    "ds_read_b64_tr_b16 a[22:23], %[vRdW] offset:19232\n\t"
    "v_mfma_f32_16x16x32_bf16 v[140:143], a[28:31], a[60:63], v[140:143]\n\t"
    "v_mfma_f32_16x16x32_bf16 v[140:143], a[24:27], a[44:47], v[140:143]\n\t"
    "buffer_load_dwordx4 a[132:135], %[vX], %[srdWS], %[sX] offen offset:1024\n\t"
    "s_waitcnt lgkmcnt(8)\n\t"
    "v_mfma_f32_16x16x32_bf16 v[144:147], a[0:3], a[32:35], v[144:147]\n\t"
    "ds_read_b64_tr_b16 a[24:25], %[vRdW] offset:2568\n\t"
    "v_mfma_f32_16x16x32_bf16 v[144:147], a[4:7], a[48:51], v[144:147]\n\t"
    "ds_read_b64_tr_b16 a[26:27], %[vRdW] offset:2824\n\t"
    "v_mfma_f32_16x16x32_bf16 v[148:151], a[4:7], a[52:55], v[148:151]\n\t"
    "v_mfma_f32_16x16x32_bf16 v[148:151], a[0:3], a[36:39], v[148:151]\n\t"
    "v_mfma_f32_16x16x32_bf16 v[152:155], a[0:3], a[40:43], v[152:155]\n\t"
    "ds_read_b64_tr_b16 a[28:29], %[vRdW] offset:18984\n\t"
    "v_mfma_f32_16x16x32_bf16 v[152:155], a[4:7], a[56:59], v[152:155]\n\t"
    "ds_read_b64_tr_b16 a[30:31], %[vRdW] offset:19240\n\t"
    "buffer_load_dwordx4 a[136:139], %[vX], %[srdWS], %[sX] offen offset:2048\n\t"
    "v_mfma_f32_16x16x32_bf16 v[156:159], a[4:7], a[60:63], v[156:159]\n\t"
    "v_mfma_f32_16x16x32_bf16 v[156:159], a[0:3], a[44:47], v[156:159]\n\t"
    "s_waitcnt lgkmcnt(8)\n\t"
    "v_mfma_f32_16x16x32_bf16 v[160:163], a[8:11], a[32:35], v[160:163]\n\t"
    "ds_read_b64_tr_b16 a[0:1], %[vRdW] offset:3072\n\t"
    "v_mfma_f32_16x16x32_bf16 v[160:163], a[12:15], a[48:51], v[160:163]\n\t"
    "ds_read_b64_tr_b16 a[2:3], %[vRdW] offset:3328\n\t"
    "v_mfma_f32_16x16x32_bf16 v[164:167], a[12:15], a[52:55], v[164:167]\n\t"
    "v_mfma_f32_16x16x32_bf16 v[164:167], a[8:11], a[36:39], v[164:167]\n\t"
    "buffer_load_dwordx4 a[140:143], %[vX], %[srdWS], %[sX] offen offset:3072\n\t"
    "s_add_u32 %[sX], %[sX], 4096\n\t"
    "v_mfma_f32_16x16x32_bf16 v[168:171], a[8:11], a[40:43], v[168:171]\n\t"
    "ds_read_b64_tr_b16 a[4:5], %[vRdW] offset:19488\n\t"
    "v_mfma_f32_16x16x32_bf16 v[168:171], a[12:15], a[56:59], v[168:171]\n\t"
    "ds_read_b64_tr_b16 a[6:7], %[vRdW] offset:19744\n\t"
    "v_mfma_f32_16x16x32_bf16 v[172:175], a[12:15], a[60:63], v[172:175]\n\t"
    "v_mfma_f32_16x16x32_bf16 v[172:175], a[8:11], a[44:47], v[172:175]\n\t"
    "s_waitcnt lgkmcnt(8)\n\t"
    "v_mfma_f32_16x16x32_bf16 v[176:179], a[16:19], a[32:35], v[176:179]\n\t"
    "ds_read_b64_tr_b16 a[8:9], %[vRdW] offset:3080\n\t"
    "v_mfma_f32_16x16x32_bf16 v[176:179], a[20:23], a[48:51], v[176:179]\n\t"
    "ds_read_b64_tr_b16 a[10:11], %[vRdW] offset:3336\n\t"
    "buffer_load_dwordx4 a[144:147], %[vX], %[srdWS], %[sX] offen offset:0\n\t"
    "v_mfma_f32_16x16x32_bf16 v[180:183], a[20:23], a[52:55], v[180:183]\n\t"
    "v_mfma_f32_16x16x32_bf16 v[180:183], a[16:19], a[36:39], v[180:183]\n\t"
    "v_mfma_f32_16x16x32_bf16 v[184:187], a[16:19], a[40:43], v[184:187]\n\t"
    "ds_read_b64_tr_b16 a[12:13], %[vRdW] offset:19496\n\t"
    "v_mfma_f32_16x16x32_bf16 v[184:187], a[20:23], a[56:59], v[184:187]\n\t"
    "ds_read_b64_tr_b16 a[14:15], %[vRdW] offset:19752\n\t"
    "v_mfma_f32_16x16x32_bf16 v[188:191], a[20:23], a[60:63], v[188:191]\n\t"
    "v_mfma_f32_16x16x32_bf16 v[188:191], a[16:19], a[44:47], v[188:191]\n\t"
    "buffer_load_dwordx4 a[148:151], %[vX], %[srdWS], %[sX] offen offset:1024\n\t"
    "s_waitcnt lgkmcnt(8)\n\t"
    "v_mfma_f32_16x16x32_bf16 v[192:195], a[24:27], a[32:35], v[192:195]\n\t"
    "ds_read_b64_tr_b16 a[16:17], %[vRdW2] offset:3584\n\t"
    "v_mfma_f32_16x16x32_bf16 v[192:195], a[28:31], a[48:51], v[192:195]\n\t"
    "ds_read_b64_tr_b16 a[18:19], %[vRdW2] offset:3840\n\t"
    "v_mfma_f32_16x16x32_bf16 v[196:199], a[28:31], a[52:55], v[196:199]\n\t"
    "v_mfma_f32_16x16x32_bf16 v[196:199], a[24:27], a[36:39], v[196:199]\n\t"
    "v_mfma_f32_16x16x32_bf16 v[200:203], a[24:27], a[40:43], v[200:203]\n\t"
    "ds_read_b64_tr_b16 a[20:21], %[vRdW2] offset:20000\n\t"
    "v_mfma_f32_16x16x32_bf16 v[200:203], a[28:31], a[56:59], v[200:203]\n\t"
    "ds_read_b64_tr_b16 a[22:23], %[vRdW2] offset:20256\n\t"
    "buffer_load_dwordx4 a[152:155], %[vX], %[srdWS], %[sX] offen offset:2048\n\t"
    "v_mfma_f32_16x16x32_bf16 v[204:207], a[28:31], a[60:63], v[204:207]\n\t"
    "v_mfma_f32_16x16x32_bf16 v[204:207], a[24:27], a[44:47], v[204:207]\n\t"
    "s_waitcnt vmcnt(33) lgkmcnt(8)\n\t"
    "s_barrier\n\t"
    "v_mfma_f32_16x16x32_bf16 v[208:211], a[0:3], a[32:35], v[208:211]\n\t"
    "ds_read_b64_tr_b16 a[24:25], %[vRdW2] offset:36416\n\t"
    "v_mfma_f32_16x16x32_bf16 v[208:211], a[4:7], a[48:51], v[208:211]\n\t"
    "ds_read_b64_tr_b16 a[26:27], %[vRdW2] offset:36672\n\t"
    "v_mfma_f32_16x16x32_bf16 v[212:215], a[4:7], a[52:55], v[212:215]\n\t"
    "v_mfma_f32_16x16x32_bf16 v[212:215], a[0:3], a[36:39], v[212:215]\n\t"
    "buffer_load_dwordx4 a[156:159], %[vX], %[srdWS], %[sX] offen offset:3072\n\t"
    "v_mfma_f32_16x16x32_bf16 v[216:219], a[0:3], a[40:43], v[216:219]\n\t"
    "ds_read_b64_tr_b16 a[28:29], %[vRdW2] offset:52832\n\t"
    "v_mfma_f32_16x16x32_bf16 v[216:219], a[4:7], a[56:59], v[216:219]\n\t"
    "ds_read_b64_tr_b16 a[30:31], %[vRdW2] offset:53088\n\t"
    "v_mfma_f32_16x16x32_bf16 v[220:223], a[4:7], a[60:63], v[220:223]\n\t"
    "v_mfma_f32_16x16x32_bf16 v[220:223], a[0:3], a[44:47], v[220:223]\n\t"
    "s_waitcnt lgkmcnt(8)\n\t"
    "v_mfma_f32_16x16x32_bf16 v[224:227], a[8:11], a[32:35], v[224:227]\n\t"
    "ds_read_b64_tr_b16 a[0:1], %[vRdW] offset:34880\n\t"
    "v_mfma_f32_16x16x32_bf16 v[224:227], a[12:15], a[48:51], v[224:227]\n\t"
    "ds_read_b64_tr_b16 a[2:3], %[vRdW] offset:35136\n\t"
    "v_mfma_f32_16x16x32_bf16 v[228:231], a[12:15], a[52:55], v[228:231]\n\t"
    "v_mfma_f32_16x16x32_bf16 v[228:231], a[8:11], a[36:39], v[228:231]\n\t"
    "v_mfma_f32_16x16x32_bf16 v[232:235], a[8:11], a[40:43], v[232:235]\n\t"
    "ds_read_b64_tr_b16 a[4:5], %[vRdW] offset:51296\n\t"
    "v_mfma_f32_16x16x32_bf16 v[232:235], a[12:15], a[56:59], v[232:235]\n\t"
    "ds_read_b64_tr_b16 a[6:7], %[vRdW] offset:51552\n\t"
    "v_mfma_f32_16x16x32_bf16 v[236:239], a[12:15], a[60:63], v[236:239]\n\t"
    "v_mfma_f32_16x16x32_bf16 v[236:239], a[8:11], a[44:47], v[236:239]\n\t"
    "s_waitcnt lgkmcnt(8)\n\t"
    "v_mfma_f32_16x16x32_bf16 v[240:243], a[16:19], a[32:35], v[240:243]\n\t"
    "ds_read_b64_tr_b16 a[8:9], %[vRdW] offset:34888\n\t"
    "v_mfma_f32_16x16x32_bf16 v[240:243], a[20:23], a[48:51], v[240:243]\n\t"
    "ds_read_b64_tr_b16 a[10:11], %[vRdW] offset:35144\n\t"
    "v_mfma_f32_16x16x32_bf16 v[244:247], a[20:23], a[52:55], v[244:247]\n\t"
    "v_mfma_f32_16x16x32_bf16 v[244:247], a[16:19], a[36:39], v[244:247]\n\t"
    "v_mfma_f32_16x16x32_bf16 v[248:251], a[16:19], a[40:43], v[248:251]\n\t"
    "ds_read_b64_tr_b16 a[12:13], %[vRdW] offset:51304\n\t"
    "v_mfma_f32_16x16x32_bf16 v[248:251], a[20:23], a[56:59], v[248:251]\n\t"
    "ds_read_b64_tr_b16 a[14:15], %[vRdW] offset:51560\n\t"
    "v_mfma_f32_16x16x32_bf16 v[252:255], a[20:23], a[60:63], v[252:255]\n\t"
    "v_mfma_f32_16x16x32_bf16 v[252:255], a[16:19], a[44:47], v[252:255]\n\t"
    "s_waitcnt vmcnt(32) lgkmcnt(8)\n\t"
    "v_mfma_f32_16x16x32_bf16 v[240:243], a[24:27], a[64:67], v[240:243]\n\t"
    "ds_read_b64_tr_b16 a[16:17], %[vRdW] offset:35392\n\t"
    "v_mfma_f32_16x16x32_bf16 v[240:243], a[28:31], a[80:83], v[240:243]\n\t"
    "ds_read_b64_tr_b16 a[18:19], %[vRdW] offset:35648\n\t"
    "v_mfma_f32_16x16x32_bf16 v[244:247], a[28:31], a[84:87], v[244:247]\n\t"
    "v_mfma_f32_16x16x32_bf16 v[244:247], a[24:27], a[68:71], v[244:247]\n\t"
    "v_mfma_f32_16x16x32_bf16 v[248:251], a[24:27], a[72:75], v[248:251]\n\t"
    "ds_read_b64_tr_b16 a[20:21], %[vRdW] offset:51808\n\t"
    "v_mfma_f32_16x16x32_bf16 v[248:251], a[28:31], a[88:91], v[248:251]\n\t"
    "ds_read_b64_tr_b16 a[22:23], %[vRdW] offset:52064\n\t"
    "v_mfma_f32_16x16x32_bf16 v[252:255], a[28:31], a[92:95], v[252:255]\n\t"
    "v_mfma_f32_16x16x32_bf16 v[252:255], a[24:27], a[76:79], v[252:255]\n\t"
    "s_waitcnt lgkmcnt(8)\n\t"
    "v_mfma_f32_16x16x32_bf16 v[144:147], a[0:3], a[64:67], v[144:147]\n\t"
    "ds_read_b64_tr_b16 a[24:25], %[vRdW] offset:35400\n\t"
    "v_mfma_f32_16x16x32_bf16 v[144:147], a[4:7], a[80:83], v[144:147]\n\t"
    "ds_read_b64_tr_b16 a[26:27], %[vRdW] offset:35656\n\t"
    "v_mfma_f32_16x16x32_bf16 v[148:151], a[4:7], a[84:87], v[148:151]\n\t"
    "v_mfma_f32_16x16x32_bf16 v[148:151], a[0:3], a[68:71], v[148:151]\n\t"
    "v_mfma_f32_16x16x32_bf16 v[152:155], a[0:3], a[72:75], v[152:155]\n\t"
    "ds_read_b64_tr_b16 a[28:29], %[vRdW] offset:51816\n\t"
    "v_mfma_f32_16x16x32_bf16 v[152:155], a[4:7], a[88:91], v[152:155]\n\t"
    "ds_read_b64_tr_b16 a[30:31], %[vRdW] offset:52072\n\t"
    "v_mfma_f32_16x16x32_bf16 v[156:159], a[4:7], a[92:95], v[156:159]\n\t"
    "v_mfma_f32_16x16x32_bf16 v[156:159], a[0:3], a[76:79], v[156:159]\n\t"
    "s_waitcnt lgkmcnt(8)\n\t"
    "v_mfma_f32_16x16x32_bf16 v[160:163], a[8:11], a[64:67], v[160:163]\n\t"
    "ds_read_b64_tr_b16 a[0:1], %[vRdW] offset:35904\n\t"
    "v_mfma_f32_16x16x32_bf16 v[160:163], a[12:15], a[80:83], v[160:163]\n\t"
    "ds_read_b64_tr_b16 a[2:3], %[vRdW] offset:36160\n\t"
    "v_mfma_f32_16x16x32_bf16 v[164:167], a[12:15], a[84:87], v[164:167]\n\t"
    "v_mfma_f32_16x16x32_bf16 v[164:167], a[8:11], a[68:71], v[164:167]\n\t"
    "v_mfma_f32_16x16x32_bf16 v[168:171], a[8:11], a[72:75], v[168:171]\n\t"
    "ds_read_b64_tr_b16 a[4:5], %[vRdW] offset:52320\n\t"
    "v_mfma_f32_16x16x32_bf16 v[168:171], a[12:15], a[88:91], v[168:171]\n\t"
    "ds_read_b64_tr_b16 a[6:7], %[vRdW] offset:52576\n\t"
    "v_mfma_f32_16x16x32_bf16 v[172:175], a[12:15], a[92:95], v[172:175]\n\t"
    "v_mfma_f32_16x16x32_bf16 v[172:175], a[8:11], a[76:79], v[172:175]\n\t"
    "s_waitcnt lgkmcnt(8)\n\t"
    "v_mfma_f32_16x16x32_bf16 v[176:179], a[16:19], a[64:67], v[176:179]\n\t"
    "ds_read_b64_tr_b16 a[8:9], %[vRdW] offset:35912\n\t"
    "v_mfma_f32_16x16x32_bf16 v[176:179], a[20:23], a[80:83], v[176:179]\n\t"
    "ds_read_b64_tr_b16 a[10:11], %[vRdW] offset:36168\n\t"
    "v_mfma_f32_16x16x32_bf16 v[180:183], a[20:23], a[84:87], v[180:183]\n\t"
    "v_mfma_f32_16x16x32_bf16 v[180:183], a[16:19], a[68:71], v[180:183]\n\t"
    "v_mfma_f32_16x16x32_bf16 v[184:187], a[16:19], a[72:75], v[184:187]\n\t"
    "ds_read_b64_tr_b16 a[12:13], %[vRdW] offset:52328\n\t"
    "v_mfma_f32_16x16x32_bf16 v[184:187], a[20:23], a[88:91], v[184:187]\n\t"
    "ds_read_b64_tr_b16 a[14:15], %[vRdW] offset:52584\n\t"
    "v_mfma_f32_16x16x32_bf16 v[188:191], a[20:23], a[92:95], v[188:191]\n\t"
    "v_mfma_f32_16x16x32_bf16 v[188:191], a[16:19], a[76:79], v[188:191]\n\t"
    "s_waitcnt vmcnt(18) lgkmcnt(8)\n\t"
    "s_barrier\n\t"
    "v_mfma_f32_16x16x32_bf16 v[192:195], a[24:27], a[64:67], v[192:195]\n\t"
    "ds_read_b64_tr_b16 a[16:17], v9 offset:3584\n\t"
    "s_add_u32 m0, %[sLds4], 0\n\t"
    "s_add_u32 s65, %[sKV], 0\n\t"
    "v_mfma_f32_16x16x32_bf16 v[192:195], a[28:31], a[80:83], v[192:195]\n\t"
    "ds_read_b64_tr_b16 a[18:19], v9 offset:3840\n\t"
    "buffer_load_dwordx4 %[vL16], %[srdWS], s65 offen offset:0 lds\n\t"
    "v_mfma_f32_16x16x32_bf16 v[196:199], a[28:31], a[84:87], v[196:199]\n\t"
    "v_mfma_f32_16x16x32_bf16 v[196:199], a[24:27], a[68:71], v[196:199]\n\t"
    "v_mfma_f32_16x16x32_bf16 v[200:203], a[24:27], a[72:75], v[200:203]\n\t"
    "ds_read_b64_tr_b16 a[20:21], v9 offset:20000\n\t"
    "v_mfma_f32_16x16x32_bf16 v[200:203], a[28:31], a[88:91], v[200:203]\n\t"
    "ds_read_b64_tr_b16 a[22:23], v9 offset:20256\n\t"
    "v_mfma_f32_16x16x32_bf16 v[204:207], a[28:31], a[92:95], v[204:207]\n\t"
    "v_mfma_f32_16x16x32_bf16 v[204:207], a[24:27], a[76:79], v[204:207]\n\t"
    "buffer_load_dwordx4 %[vL16], %[srdWS], s65 offen offset:1024 lds\n\t"
    "s_waitcnt lgkmcnt(8)\n\t"
    "v_mfma_f32_16x16x32_bf16 v[208:211], a[0:3], a[64:67], v[208:211]\n\t"
    "ds_read_b64_tr_b16 a[24:25], v8 offset:2048\n\t"
    "v_mfma_f32_16x16x32_bf16 v[208:211], a[4:7], a[80:83], v[208:211]\n\t"
    "ds_read_b64_tr_b16 a[26:27], v8 offset:2304\n\t"
    "v_mfma_f32_16x16x32_bf16 v[212:215], a[4:7], a[84:87], v[212:215]\n\t"
    "v_mfma_f32_16x16x32_bf16 v[212:215], a[0:3], a[68:71], v[212:215]\n\t"
    "v_mfma_f32_16x16x32_bf16 v[216:219], a[0:3], a[72:75], v[216:219]\n\t"
    "ds_read_b64_tr_b16 a[28:29], v8 offset:18464\n\t"
    "v_mfma_f32_16x16x32_bf16 v[216:219], a[4:7], a[88:91], v[216:219]\n\t"
    "ds_read_b64_tr_b16 a[30:31], v8 offset:18720\n\t"
    "buffer_load_dwordx4 %[vL16], %[srdWS], s65 offen offset:2048 lds\n\t"
    "v_mfma_f32_16x16x32_bf16 v[220:223], a[4:7], a[92:95], v[220:223]\n\t"
    "v_mfma_f32_16x16x32_bf16 v[220:223], a[0:3], a[76:79], v[220:223]\n\t"
    "s_waitcnt lgkmcnt(8)\n\t"
    "v_mfma_f32_16x16x32_bf16 v[224:227], a[8:11], a[64:67], v[224:227]\n\t"
    "ds_read_b64_tr_b16 a[0:1], v8 offset:2056\n\t"
    "v_mfma_f32_16x16x32_bf16 v[224:227], a[12:15], a[80:83], v[224:227]\n\t"
    "ds_read_b64_tr_b16 a[2:3], v8 offset:2312\n\t"
    "v_mfma_f32_16x16x32_bf16 v[228:231], a[12:15], a[84:87], v[228:231]\n\t"
    "v_mfma_f32_16x16x32_bf16 v[228:231], a[8:11], a[68:71], v[228:231]\n\t"
    "buffer_load_dwordx4 %[vL16], %[srdWS], s65 offen offset:3072 lds\n\t"
    "v_mfma_f32_16x16x32_bf16 v[232:235], a[8:11], a[72:75], v[232:235]\n\t"
    "ds_read_b64_tr_b16 a[4:5], v8 offset:18472\n\t"
    "v_mfma_f32_16x16x32_bf16 v[232:235], a[12:15], a[88:91], v[232:235]\n\t"
    "ds_read_b64_tr_b16 a[6:7], v8 offset:18728\n\t"
    "v_mfma_f32_16x16x32_bf16 v[236:239], a[12:15], a[92:95], v[236:239]\n\t"
    "v_mfma_f32_16x16x32_bf16 v[236:239], a[8:11], a[76:79], v[236:239]\n\t"
    "s_waitcnt vmcnt(20) lgkmcnt(8)\n\t"
    "v_mfma_f32_16x16x32_bf16 v[240:243], a[16:19], a[96:99], v[240:243]\n\t"
    "ds_read_b64_tr_b16 a[8:9], v8 offset:2560\n\t"
    "v_mfma_f32_16x16x32_bf16 v[240:243], a[20:23], a[112:115], v[240:243]\n\t"
    "ds_read_b64_tr_b16 a[10:11], v8 offset:2816\n\t"
    "v_mfma_f32_16x16x32_bf16 v[244:247], a[20:23], a[116:119], v[244:247]\n\t"
    "v_mfma_f32_16x16x32_bf16 v[244:247], a[16:19], a[100:103], v[244:247]\n\t"
    "v_mfma_f32_16x16x32_bf16 v[248:251], a[16:19], a[104:107], v[248:251]\n\t"
    "ds_read_b64_tr_b16 a[12:13], v8 offset:18976\n\t"
    "v_mfma_f32_16x16x32_bf16 v[248:251], a[20:23], a[120:123], v[248:251]\n\t"
    "ds_read_b64_tr_b16 a[14:15], v8 offset:19232\n\t"
    "v_mfma_f32_16x16x32_bf16 v[252:255], a[20:23], a[124:127], v[252:255]\n\t"
    "v_mfma_f32_16x16x32_bf16 v[252:255], a[16:19], a[108:111], v[252:255]\n\t"
    "s_waitcnt lgkmcnt(8)\n\t"
    "v_mfma_f32_16x16x32_bf16 v[144:147], a[24:27], a[96:99], v[144:147]\n\t"
    "ds_read_b64_tr_b16 a[16:17], v8 offset:2568\n\t"
    "v_mfma_f32_16x16x32_bf16 v[144:147], a[28:31], a[112:115], v[144:147]\n\t"
    "ds_read_b64_tr_b16 a[18:19], v8 offset:2824\n\t"
    "v_mfma_f32_16x16x32_bf16 v[148:151], a[28:31], a[116:119], v[148:151]\n\t"
    "v_mfma_f32_16x16x32_bf16 v[148:151], a[24:27], a[100:103], v[148:151]\n\t"
    "v_mfma_f32_16x16x32_bf16 v[152:155], a[24:27], a[104:107], v[152:155]\n\t"
    "ds_read_b64_tr_b16 a[20:21], v8 offset:18984\n\t"
    "v_mfma_f32_16x16x32_bf16 v[152:155], a[28:31], a[120:123], v[152:155]\n\t"
    "ds_read_b64_tr_b16 a[22:23], v8 offset:19240\n\t"
    "v_mfma_f32_16x16x32_bf16 v[156:159], a[28:31], a[124:127], v[156:159]\n\t"
    "v_mfma_f32_16x16x32_bf16 v[156:159], a[24:27], a[108:111], v[156:159]\n\t"
    "s_waitcnt lgkmcnt(8)\n\t"
    "v_mfma_f32_16x16x32_bf16 v[160:163], a[0:3], a[96:99], v[160:163]\n\t"
    "ds_read_b64_tr_b16 a[24:25], v8 offset:3072\n\t"
    "v_mfma_f32_16x16x32_bf16 v[160:163], a[4:7], a[112:115], v[160:163]\n\t"
    "ds_read_b64_tr_b16 a[26:27], v8 offset:3328\n\t"
    "v_mfma_f32_16x16x32_bf16 v[164:167], a[4:7], a[116:119], v[164:167]\n\t"
    "v_mfma_f32_16x16x32_bf16 v[164:167], a[0:3], a[100:103], v[164:167]\n\t"
    "v_mfma_f32_16x16x32_bf16 v[168:171], a[0:3], a[104:107], v[168:171]\n\t"
    "ds_read_b64_tr_b16 a[28:29], v8 offset:19488\n\t"
    "v_mfma_f32_16x16x32_bf16 v[168:171], a[4:7], a[120:123], v[168:171]\n\t"
    "ds_read_b64_tr_b16 a[30:31], v8 offset:19744\n\t"
    "v_mfma_f32_16x16x32_bf16 v[172:175], a[4:7], a[124:127], v[172:175]\n\t"
    "v_mfma_f32_16x16x32_bf16 v[172:175], a[0:3], a[108:111], v[172:175]\n\t"
    "s_waitcnt lgkmcnt(8)\n\t"
    "v_mfma_f32_16x16x32_bf16 v[176:179], a[8:11], a[96:99], v[176:179]\n\t"
    "ds_read_b64_tr_b16 a[0:1], v8 offset:3080\n\t"
    "v_mfma_f32_16x16x32_bf16 v[176:179], a[12:15], a[112:115], v[176:179]\n\t"
    "ds_read_b64_tr_b16 a[2:3], v8 offset:3336\n\t"
    "v_mfma_f32_16x16x32_bf16 v[180:183], a[12:15], a[116:119], v[180:183]\n\t"
    "v_mfma_f32_16x16x32_bf16 v[180:183], a[8:11], a[100:103], v[180:183]\n\t"
    "v_mfma_f32_16x16x32_bf16 v[184:187], a[8:11], a[104:107], v[184:187]\n\t"
    "ds_read_b64_tr_b16 a[4:5], v8 offset:19496\n\t"
    "v_mfma_f32_16x16x32_bf16 v[184:187], a[12:15], a[120:123], v[184:187]\n\t"
    "ds_read_b64_tr_b16 a[6:7], v8 offset:19752\n\t"
    "v_mfma_f32_16x16x32_bf16 v[188:191], a[12:15], a[124:127], v[188:191]\n\t"
    "v_mfma_f32_16x16x32_bf16 v[188:191], a[8:11], a[108:111], v[188:191]\n\t"
    "s_waitcnt vmcnt(12) lgkmcnt(8)\n\t"
    "s_barrier\n\t"
    "v_mfma_f32_16x16x32_bf16 v[192:195], a[16:19], a[96:99], v[192:195]\n\t"
    "ds_read_b64_tr_b16 a[8:9], v9 offset:36416\n\t"
    "v_mfma_f32_16x16x32_bf16 v[192:195], a[20:23], a[112:115], v[192:195]\n\t"
    "ds_read_b64_tr_b16 a[10:11], v9 offset:36672\n\t"
    "v_mfma_f32_16x16x32_bf16 v[196:199], a[20:23], a[116:119], v[196:199]\n\t"
    "v_mfma_f32_16x16x32_bf16 v[196:199], a[16:19], a[100:103], v[196:199]\n\t"
    "v_mfma_f32_16x16x32_bf16 v[200:203], a[16:19], a[104:107], v[200:203]\n\t"
    "ds_read_b64_tr_b16 a[12:13], v9 offset:52832\n\t"
    "v_mfma_f32_16x16x32_bf16 v[200:203], a[20:23], a[120:123], v[200:203]\n\t"
    "ds_read_b64_tr_b16 a[14:15], v9 offset:53088\n\t"
    "v_mfma_f32_16x16x32_bf16 v[204:207], a[20:23], a[124:127], v[204:207]\n\t"
    "v_mfma_f32_16x16x32_bf16 v[204:207], a[16:19], a[108:111], v[204:207]\n\t"
    "s_waitcnt lgkmcnt(8)\n\t"
    "v_mfma_f32_16x16x32_bf16 v[208:211], a[24:27], a[96:99], v[208:211]\n\t"
    "ds_read_b64_tr_b16 a[16:17], v8 offset:34880\n\t"
    "v_mfma_f32_16x16x32_bf16 v[208:211], a[28:31], a[112:115], v[208:211]\n\t"
    "ds_read_b64_tr_b16 a[18:19], v8 offset:35136\n\t"
    "v_mfma_f32_16x16x32_bf16 v[212:215], a[28:31], a[116:119], v[212:215]\n\t"
    "v_mfma_f32_16x16x32_bf16 v[212:215], a[24:27], a[100:103], v[212:215]\n\t"
    "v_mfma_f32_16x16x32_bf16 v[216:219], a[24:27], a[104:107], v[216:219]\n\t"
    "ds_read_b64_tr_b16 a[20:21], v8 offset:51296\n\t"
    "v_mfma_f32_16x16x32_bf16 v[216:219], a[28:31], a[120:123], v[216:219]\n\t"
    "ds_read_b64_tr_b16 a[22:23], v8 offset:51552\n\t"
    "v_mfma_f32_16x16x32_bf16 v[220:223], a[28:31], a[124:127], v[220:223]\n\t"
    "v_mfma_f32_16x16x32_bf16 v[220:223], a[24:27], a[108:111], v[220:223]\n\t"
    "s_waitcnt lgkmcnt(8)\n\t"
    "v_mfma_f32_16x16x32_bf16 v[224:227], a[0:3], a[96:99], v[224:227]\n\t"
    "ds_read_b64_tr_b16 a[24:25], v8 offset:34888\n\t"
    "v_mfma_f32_16x16x32_bf16 v[224:227], a[4:7], a[112:115], v[224:227]\n\t"
    "ds_read_b64_tr_b16 a[26:27], v8 offset:35144\n\t"
    "v_mfma_f32_16x16x32_bf16 v[228:231], a[4:7], a[116:119], v[228:231]\n\t"
    "v_mfma_f32_16x16x32_bf16 v[228:231], a[0:3], a[100:103], v[228:231]\n\t"
    "v_mfma_f32_16x16x32_bf16 v[232:235], a[0:3], a[104:107], v[232:235]\n\t"
    "ds_read_b64_tr_b16 a[28:29], v8 offset:51304\n\t"
    "v_mfma_f32_16x16x32_bf16 v[232:235], a[4:7], a[120:123], v[232:235]\n\t"
    "ds_read_b64_tr_b16 a[30:31], v8 offset:51560\n\t"
    "v_mfma_f32_16x16x32_bf16 v[236:239], a[4:7], a[124:127], v[236:239]\n\t"
    "v_mfma_f32_16x16x32_bf16 v[236:239], a[0:3], a[108:111], v[236:239]\n\t"
    "s_waitcnt vmcnt(4) lgkmcnt(8)\n\t"
    "v_mfma_f32_16x16x32_bf16 v[240:243], a[8:11], a[128:131], v[240:243]\n\t"
    "ds_read_b64_tr_b16 a[0:1], v8 offset:35392\n\t"
    "v_mfma_f32_16x16x32_bf16 v[240:243], a[12:15], a[144:147], v[240:243]\n\t"
    "ds_read_b64_tr_b16 a[2:3], v8 offset:35648\n\t"
    "v_mfma_f32_16x16x32_bf16 v[244:247], a[12:15], a[148:151], v[244:247]\n\t"
    "v_mfma_f32_16x16x32_bf16 v[244:247], a[8:11], a[132:135], v[244:247]\n\t"
    "v_mfma_f32_16x16x32_bf16 v[248:251], a[8:11], a[136:139], v[248:251]\n\t"
    "ds_read_b64_tr_b16 a[4:5], v8 offset:51808\n\t"
    "v_mfma_f32_16x16x32_bf16 v[248:251], a[12:15], a[152:155], v[248:251]\n\t"
    "ds_read_b64_tr_b16 a[6:7], v8 offset:52064\n\t"
    "v_mfma_f32_16x16x32_bf16 v[252:255], a[12:15], a[156:159], v[252:255]\n\t"
    "v_mfma_f32_16x16x32_bf16 v[252:255], a[8:11], a[140:143], v[252:255]\n\t"
    "s_waitcnt lgkmcnt(8)\n\t"
    "v_mfma_f32_16x16x32_bf16 v[144:147], a[16:19], a[128:131], v[144:147]\n\t"
    "ds_read_b64_tr_b16 a[8:9], v8 offset:35400\n\t"
    "v_mfma_f32_16x16x32_bf16 v[144:147], a[20:23], a[144:147], v[144:147]\n\t"
    "ds_read_b64_tr_b16 a[10:11], v8 offset:35656\n\t"
    "v_mfma_f32_16x16x32_bf16 v[148:151], a[20:23], a[148:151], v[148:151]\n\t"
    "v_mfma_f32_16x16x32_bf16 v[148:151], a[16:19], a[132:135], v[148:151]\n\t"
    "v_mul_f32 v12, s62, v240\n\t"
    "v_mul_f32 v13, s63, v241\n\t"
    "v_mfma_f32_16x16x32_bf16 v[152:155], a[16:19], a[136:139], v[152:155]\n\t"
    "ds_read_b64_tr_b16 a[12:13], v8 offset:51816\n\t"
    "v_mul_f32 v14, s62, v242\n\t"
    "v_mfma_f32_16x16x32_bf16 v[152:155], a[20:23], a[152:155], v[152:155]\n\t"
    "ds_read_b64_tr_b16 a[14:15], v8 offset:52072\n\t"
    "v_mul_f32 v15, s63, v243\n\t"
    "v_mfma_f32_16x16x32_bf16 v[156:159], a[20:23], a[156:159], v[156:159]\n\t"
    "v_exp_f32 v12, v12\n\t"
    "v_mfma_f32_16x16x32_bf16 v[156:159], a[16:19], a[140:143], v[156:159]\n\t"
    "v_exp_f32 v13, v13\n\t"
    "s_waitcnt lgkmcnt(8)\n\t"
    "v_mfma_f32_16x16x32_bf16 v[160:163], a[24:27], a[128:131], v[160:163]\n\t"
    "ds_read_b64_tr_b16 a[16:17], v8 offset:35904\n\t"
    "v_exp_f32 v14, v14\n\t"
    "v_mfma_f32_16x16x32_bf16 v[160:163], a[28:31], a[144:147], v[160:163]\n\t"
    "ds_read_b64_tr_b16 a[18:19], v8 offset:36160\n\t"
    "v_exp_f32 v15, v15\n\t"
    "v_mfma_f32_16x16x32_bf16 v[164:167], a[28:31], a[148:151], v[164:167]\n\t"
    "v_add_f32 v12, 1.0, v12\n\t"
    "v_add_f32 v13, 1.0, v13\n\t"
    "v_mfma_f32_16x16x32_bf16 v[164:167], a[24:27], a[132:135], v[164:167]\n\t"
    "v_add_f32 v14, 1.0, v14\n\t"
    "v_add_f32 v15, 1.0, v15\n\t"
    "v_mfma_f32_16x16x32_bf16 v[168:171], a[24:27], a[136:139], v[168:171]\n\t"
    "ds_read_b64_tr_b16 a[20:21], v8 offset:52320\n\t"
    "v_rcp_f32 v12, v12\n\t"
    "v_mfma_f32_16x16x32_bf16 v[168:171], a[28:31], a[152:155], v[168:171]\n\t"
    "ds_read_b64_tr_b16 a[22:23], v8 offset:52576\n\t"
    "v_rcp_f32 v13, v13\n\t"
    "v_mfma_f32_16x16x32_bf16 v[172:175], a[28:31], a[156:159], v[172:175]\n\t"
    "v_rcp_f32 v14, v14\n\t"
    "v_mfma_f32_16x16x32_bf16 v[172:175], a[24:27], a[140:143], v[172:175]\n\t"
    "v_rcp_f32 v15, v15\n\t"
    "s_waitcnt lgkmcnt(8)\n\t"
    "v_mfma_f32_16x16x32_bf16 v[176:179], a[0:3], a[128:131], v[176:179]\n\t"
    "ds_read_b64_tr_b16 a[24:25], v8 offset:35912\n\t"
    "v_mul_f32 v12, v240, v12\n\t"
    "v_mfma_f32_16x16x32_bf16 v[176:179], a[4:7], a[144:147], v[176:179]\n\t"
    "ds_read_b64_tr_b16 a[26:27], v8 offset:36168\n\t"
    "v_mul_f32 v13, v241, v13\n\t"
    "v_mfma_f32_16x16x32_bf16 v[180:183], a[4:7], a[148:151], v[180:183]\n\t"
    "v_mul_f32 v14, v242, v14\n\t"
    "v_mul_f32 v15, v243, v15\n\t"
    "v_mfma_f32_16x16x32_bf16 v[180:183], a[0:3], a[132:135], v[180:183]\n\t"
    "v_cvt_pk_bf16_f32 v240, v12, v13\n\t"
    "v_cvt_pk_bf16_f32 v241, v14, v15\n\t"
    "v_mfma_f32_16x16x32_bf16 v[184:187], a[0:3], a[136:139], v[184:187]\n\t"
    "ds_read_b64_tr_b16 a[28:29], v8 offset:52328\n\t"
    "buffer_store_dwordx2 v[240:241], %[vH8], %[srdWS], %[sH2] offen offset:0 sc1\n\t"
    "v_mfma_f32_16x16x32_bf16 v[184:187], a[4:7], a[152:155], v[184:187]\n\t"
    "ds_read_b64_tr_b16 a[30:31], v8 offset:52584\n\t"
    "v_mul_f32 v12, s62, v248\n\t"
    "v_mfma_f32_16x16x32_bf16 v[188:191], a[4:7], a[156:159], v[188:191]\n\t"
    "v_mul_f32 v13, s63, v249\n\t"
    "v_mul_f32 v14, s62, v250\n\t"
    "v_mfma_f32_16x16x32_bf16 v[188:191], a[0:3], a[140:143], v[188:191]\n\t"
    "v_mul_f32 v15, s63, v251\n\t"
    "s_waitcnt lgkmcnt(8)\n\t"
    "v_mfma_f32_16x16x32_bf16 v[192:195], a[8:11], a[128:131], v[192:195]\n\t"
    "ds_read_b64_tr_b16 a[0:1], %[vRdW] offset:32832\n\t"
    "v_mul_f32 v240, s62, v244\n\t"
    "v_mfma_f32_16x16x32_bf16 v[192:195], a[12:15], a[144:147], v[192:195]\n\t"
    "ds_read_b64_tr_b16 a[2:3], %[vRdW] offset:33088\n\t"
    "v_mul_f32 v241, s63, v245\n\t"
    "v_mfma_f32_16x16x32_bf16 v[196:199], a[12:15], a[148:151], v[196:199]\n\t"
    "v_exp_f32 v12, v12\n\t"
    "v_mfma_f32_16x16x32_bf16 v[196:199], a[8:11], a[132:135], v[196:199]\n\t"
    "v_exp_f32 v13, v13\n\t"
    "v_mfma_f32_16x16x32_bf16 v[200:203], a[8:11], a[136:139], v[200:203]\n\t"
    "ds_read_b64_tr_b16 a[4:5], %[vRdW] offset:49248\n\t"
    "v_mul_f32 v242, s62, v246\n\t"
    "v_mfma_f32_16x16x32_bf16 v[200:203], a[12:15], a[152:155], v[200:203]\n\t"
    "ds_read_b64_tr_b16 a[6:7], %[vRdW] offset:49504\n\t"
    "v_mul_f32 v243, s63, v247\n\t"
    "v_mfma_f32_16x16x32_bf16 v[204:207], a[12:15], a[156:159], v[204:207]\n\t"
    "v_exp_f32 v14, v14\n\t"
    "v_mfma_f32_16x16x32_bf16 v[204:207], a[8:11], a[140:143], v[204:207]\n\t"
    "v_exp_f32 v15, v15\n\t"
    "s_waitcnt lgkmcnt(8)\n\t"
    "v_mfma_f32_16x16x32_bf16 v[208:211], a[16:19], a[128:131], v[208:211]\n\t"
    "ds_read_b64_tr_b16 a[8:9], %[vRdW] offset:32840\n\t"
    "v_add_f32 v12, 1.0, v12\n\t"
    "v_mfma_f32_16x16x32_bf16 v[208:211], a[20:23], a[144:147], v[208:211]\n\t"
    "ds_read_b64_tr_b16 a[10:11], %[vRdW] offset:33096\n\t"
    "v_add_f32 v13, 1.0, v13\n\t"
    "v_mfma_f32_16x16x32_bf16 v[212:215], a[20:23], a[148:151], v[212:215]\n\t"
    "v_exp_f32 v240, v240\n\t"
    "v_mfma_f32_16x16x32_bf16 v[212:215], a[16:19], a[132:135], v[212:215]\n\t"
    "v_exp_f32 v241, v241\n\t"
    "v_mfma_f32_16x16x32_bf16 v[216:219], a[16:19], a[136:139], v[216:219]\n\t"
    "ds_read_b64_tr_b16 a[12:13], %[vRdW] offset:49256\n\t"
    "v_add_f32 v14, 1.0, v14\n\t"
    "v_mfma_f32_16x16x32_bf16 v[216:219], a[20:23], a[152:155], v[216:219]\n\t"
    "ds_read_b64_tr_b16 a[14:15], %[vRdW] offset:49512\n\t"
    "v_add_f32 v15, 1.0, v15\n\t"
    "v_mfma_f32_16x16x32_bf16 v[220:223], a[20:23], a[156:159], v[220:223]\n\t"
    "v_rcp_f32 v12, v12\n\t"
    "v_mfma_f32_16x16x32_bf16 v[220:223], a[16:19], a[140:143], v[220:223]\n\t"
    "v_rcp_f32 v13, v13\n\t"
    "s_waitcnt lgkmcnt(8)\n\t"
    "v_mfma_f32_16x16x32_bf16 v[224:227], a[24:27], a[128:131], v[224:227]\n\t"
    "ds_read_b64_tr_b16 a[16:17], %[vRdW] offset:33344\n\t"
    "v_rcp_f32 v14, v14\n\t"
    "v_mfma_f32_16x16x32_bf16 v[224:227], a[28:31], a[144:147], v[224:227]\n\t"
    "ds_read_b64_tr_b16 a[18:19], %[vRdW] offset:33600\n\t"
    "v_rcp_f32 v15, v15\n\t"
    "v_mfma_f32_16x16x32_bf16 v[228:231], a[28:31], a[148:151], v[228:231]\n\t"
    "v_exp_f32 v242, v242\n\t"
    "v_mfma_f32_16x16x32_bf16 v[228:231], a[24:27], a[132:135], v[228:231]\n\t"
    "v_exp_f32 v243, v243\n\t"
    "v_mfma_f32_16x16x32_bf16 v[232:235], a[24:27], a[136:139], v[232:235]\n\t"
    "ds_read_b64_tr_b16 a[20:21], %[vRdW] offset:49760\n\t"
    "v_mul_f32 v12, v248, v12\n\t"
    "v_mfma_f32_16x16x32_bf16 v[232:235], a[28:31], a[152:155], v[232:235]\n\t"
    "ds_read_b64_tr_b16 a[22:23], %[vRdW] offset:50016\n\t"
    "v_mul_f32 v13, v249, v13\n\t"
    "v_mfma_f32_16x16x32_bf16 v[236:239], a[28:31], a[156:159], v[236:239]\n\t"
    "v_mul_f32 v14, v250, v14\n\t"
    "v_mul_f32 v15, v251, v15\n\t"
    "v_mfma_f32_16x16x32_bf16 v[236:239], a[24:27], a[140:143], v[236:239]\n\t"
    "v_cvt_pk_bf16_f32 v248, v12, v13\n\t"
    "s_waitcnt lgkmcnt(8)\n\t"
    "v_mfma_f32_16x16x32_bf16 v[16:19], a[0:3], a[64:67], v[16:19]\n\t"
    "ds_read_b64_tr_b16 a[24:25], %[vRdW] offset:33352\n\t"
    "v_cvt_pk_bf16_f32 v249, v14, v15\n\t"
    "v_mfma_f32_16x16x32_bf16 v[16:19], a[4:7], a[80:83], v[16:19]\n\t"
    "ds_read_b64_tr_b16 a[26:27], %[vRdW] offset:33608\n\t"
    "buffer_store_dwordx2 v[248:249], %[vH8], %[srdWS], %[sH2] offen offset:2048 sc1\n\t"
    "v_mfma_f32_16x16x32_bf16 v[20:23], a[4:7], a[84:87], v[20:23]\n\t"
    "v_add_f32 v240, 1.0, v240\n\t"
    "v_add_f32 v241, 1.0, v241\n\t"
    "v_mfma_f32_16x16x32_bf16 v[20:23], a[0:3], a[68:71], v[20:23]\n\t"
    "v_add_u32 %[vX], %[sPark], v10\n\t"
    "v_add_f32 v242, 1.0, v242\n\t"
    "v_mfma_f32_16x16x32_bf16 v[24:27], a[0:3], a[72:75], v[24:27]\n\t"
    "ds_read_b64_tr_b16 a[28:29], %[vRdW] offset:49768\n\t"
    "v_add_f32 v243, 1.0, v243\n\t"
    "v_mfma_f32_16x16x32_bf16 v[24:27], a[4:7], a[88:91], v[24:27]\n\t"
    "ds_read_b64_tr_b16 a[30:31], %[vRdW] offset:50024\n\t"
    "ds_write_b128 %[vX], v[208:211] offset:0\n\t"
    "v_mfma_f32_16x16x32_bf16 v[28:31], a[4:7], a[92:95], v[28:31]\n\t"
    "ds_write_b128 %[vX], v[224:227] offset:1024\n\t"
    "v_mfma_f32_16x16x32_bf16 v[28:31], a[0:3], a[76:79], v[28:31]\n\t"
    "ds_write_b128 %[vX], v[212:215] offset:2048\n\t"
    "s_waitcnt lgkmcnt(11)\n\t"
    "v_mfma_f32_16x16x32_bf16 v[32:35], a[8:11], a[64:67], v[32:35]\n\t"
    "ds_read_b64_tr_b16 a[0:1], %[vRdW] offset:33856\n\t"
    "ds_write_b128 %[vX], v[228:231] offset:3072\n\t"
    "v_mfma_f32_16x16x32_bf16 v[32:35], a[12:15], a[80:83], v[32:35]\n\t"
    "ds_read_b64_tr_b16 a[2:3], %[vRdW] offset:34112\n\t"
    "ds_write_b128 %[vX], v[216:219] offset:4096\n\t"
    "v_mfma_f32_16x16x32_bf16 v[36:39], a[12:15], a[84:87], v[36:39]\n\t"
    "ds_write_b128 %[vX], v[232:235] offset:5120\n\t"
    "v_mfma_f32_16x16x32_bf16 v[36:39], a[8:11], a[68:71], v[36:39]\n\t"
    "v_rcp_f32 v240, v240\n\t"
    "v_mfma_f32_16x16x32_bf16 v[40:43], a[8:11], a[72:75], v[40:43]\n\t"
    "ds_read_b64_tr_b16 a[4:5], %[vRdW] offset:50272\n\t"
    "v_rcp_f32 v241, v241\n\t"
    "v_mfma_f32_16x16x32_bf16 v[40:43], a[12:15], a[88:91], v[40:43]\n\t"
    "ds_read_b64_tr_b16 a[6:7], %[vRdW] offset:50528\n\t"
    "v_rcp_f32 v242, v242\n\t"
    "v_mfma_f32_16x16x32_bf16 v[44:47], a[12:15], a[92:95], v[44:47]\n\t"
    "v_rcp_f32 v243, v243\n\t"
    "v_mfma_f32_16x16x32_bf16 v[44:47], a[8:11], a[76:79], v[44:47]\n\t"
    "v_mul_f32 v240, v244, v240\n\t"
    "s_waitcnt lgkmcnt(14)\n\t"
    "v_mfma_f32_16x16x32_bf16 v[48:51], a[16:19], a[64:67], v[48:51]\n\t"
    "ds_read_b64_tr_b16 a[8:9], %[vRdW] offset:33864\n\t"
    "v_mul_f32 v241, v245, v241\n\t"
    "v_mfma_f32_16x16x32_bf16 v[48:51], a[20:23], a[80:83], v[48:51]\n\t"
    "ds_read_b64_tr_b16 a[10:11], %[vRdW] offset:34120\n\t"
    "v_mul_f32 v242, v246, v242\n\t"
    "v_mfma_f32_16x16x32_bf16 v[52:55], a[20:23], a[84:87], v[52:55]\n\t"
    "v_mul_f32 v243, v247, v243\n\t"
    "v_cvt_pk_bf16_f32 v244, v240, v241\n\t"
    "v_mfma_f32_16x16x32_bf16 v[52:55], a[16:19], a[68:71], v[52:55]\n\t"
    "v_cvt_pk_bf16_f32 v245, v242, v243\n\t"
    "buffer_store_dwordx2 v[244:245], %[vH8], %[srdWS], %[sH2] offen offset:1024 sc1\n\t"
    "v_mfma_f32_16x16x32_bf16 v[56:59], a[16:19], a[72:75], v[56:59]\n\t"
    "ds_read_b64_tr_b16 a[12:13], %[vRdW] offset:50280\n\t"
    "v_mul_f32 v248, s62, v252\n\t"
    "v_mfma_f32_16x16x32_bf16 v[56:59], a[20:23], a[88:91], v[56:59]\n\t"
    "ds_read_b64_tr_b16 a[14:15], %[vRdW] offset:50536\n\t"
    "v_mul_f32 v249, s63, v253\n\t"
    "v_mfma_f32_16x16x32_bf16 v[60:63], a[20:23], a[92:95], v[60:63]\n\t"
    "v_mul_f32 v250, s62, v254\n\t"
    "v_mul_f32 v251, s63, v255\n\t"
    "v_mfma_f32_16x16x32_bf16 v[60:63], a[16:19], a[76:79], v[60:63]\n\t"
    "v_exp_f32 v248, v248\n\t"
    "s_waitcnt lgkmcnt(14)\n\t"
    "v_mfma_f32_16x16x32_bf16 v[64:67], a[24:27], a[64:67], v[64:67]\n\t"
    "ds_read_b64_tr_b16 a[16:17], %[vRdW] offset:34368\n\t"
    "v_exp_f32 v249, v249\n\t"
    "v_mfma_f32_16x16x32_bf16 v[64:67], a[28:31], a[80:83], v[64:67]\n\t"
    "ds_read_b64_tr_b16 a[18:19], %[vRdW] offset:34624\n\t"
    "v_exp_f32 v250, v250\n\t"
    "v_mfma_f32_16x16x32_bf16 v[68:71], a[28:31], a[84:87], v[68:71]\n\t"
    "v_exp_f32 v251, v251\n\t"
    "v_mfma_f32_16x16x32_bf16 v[68:71], a[24:27], a[68:71], v[68:71]\n\t"
    "v_add_f32 v248, 1.0, v248\n\t"
    "v_add_f32 v249, 1.0, v249\n\t"
    "v_mfma_f32_16x16x32_bf16 v[72:75], a[24:27], a[72:75], v[72:75]\n\t"
    "ds_read_b64_tr_b16 a[20:21], %[vRdW] offset:50784\n\t"
    "k_pga4: s_cbranch_execz k_pga8\n\t"
    "v_add_f32 v250, 1.0, v250\n\t"
    "v_mfma_f32_16x16x32_bf16 v[72:75], a[28:31], a[88:91], v[72:75]\n\t"
    "ds_read_b64_tr_b16 a[22:23], %[vRdW] offset:51040\n\t"
    "v_add_f32 v251, 1.0, v251\n\t"
    "v_mfma_f32_16x16x32_bf16 v[76:79], a[28:31], a[92:95], v[76:79]\n\t"
    "v_rcp_f32 v248, v248\n\t"
    "v_mfma_f32_16x16x32_bf16 v[76:79], a[24:27], a[76:79], v[76:79]\n\t"
    "v_rcp_f32 v249, v249\n\t"
    "s_waitcnt lgkmcnt(8)\n\t"
    "v_mfma_f32_16x16x32_bf16 v[80:83], a[0:3], a[64:67], v[80:83]\n\t"
    "ds_read_b64_tr_b16 a[24:25], %[vRdW] offset:34376\n\t"
    "v_rcp_f32 v250, v250\n\t"
    "v_mfma_f32_16x16x32_bf16 v[80:83], a[4:7], a[80:83], v[80:83]\n\t"
    "ds_read_b64_tr_b16 a[26:27], %[vRdW] offset:34632\n\t"
    "v_rcp_f32 v251, v251\n\t"
    "v_mfma_f32_16x16x32_bf16 v[84:87], a[4:7], a[84:87], v[84:87]\n\t"
    "v_mul_f32 v248, v252, v248\n\t"
    "v_mul_f32 v249, v253, v249\n\t"
    "v_mfma_f32_16x16x32_bf16 v[84:87], a[0:3], a[68:71], v[84:87]\n\t"
    "v_mul_f32 v250, v254, v250\n\t"
    "v_mul_f32 v251, v255, v251\n\t"
    "v_mfma_f32_16x16x32_bf16 v[88:91], a[0:3], a[72:75], v[88:91]\n\t"
    "ds_read_b64_tr_b16 a[28:29], %[vRdW] offset:50792\n\t"
    "v_cvt_pk_bf16_f32 v252, v248, v249\n\t"
    "v_mfma_f32_16x16x32_bf16 v[88:91], a[4:7], a[88:91], v[88:91]\n\t"
    "ds_read_b64_tr_b16 a[30:31], %[vRdW] offset:51048\n\t"
    "v_cvt_pk_bf16_f32 v253, v250, v251\n\t"
    "v_mfma_f32_16x16x32_bf16 v[92:95], a[4:7], a[92:95], v[92:95]\n\t"
    "buffer_store_dwordx2 v[252:253], %[vH8], %[srdWS], %[sH2] offen offset:3072 sc1\n\t"
    "v_mul_f32 v240, s62, v144\n\t"
    "v_mfma_f32_16x16x32_bf16 v[92:95], a[0:3], a[76:79], v[92:95]\n\t"
    "v_mul_f32 v241, s63, v145\n\t"
    "s_waitcnt lgkmcnt(8)\n\t"
    "v_mfma_f32_16x16x32_bf16 v[96:99], a[8:11], a[64:67], v[96:99]\n\t"
    "ds_read_b64_tr_b16 a[0:1], v8 offset:0\n\t"
    "v_mul_f32 v242, s62, v146\n\t"
    "v_mfma_f32_16x16x32_bf16 v[96:99], a[12:15], a[80:83], v[96:99]\n\t"
    "ds_read_b64_tr_b16 a[2:3], v8 offset:256\n\t"
    "v_mul_f32 v243, s63, v147\n\t"
    "v_mfma_f32_16x16x32_bf16 v[100:103], a[12:15], a[84:87], v[100:103]\n\t"
    "v_mul_f32 v248, s62, v160\n\t"
    "v_mul_f32 v249, s63, v161\n\t"
    "v_mfma_f32_16x16x32_bf16 v[100:103], a[8:11], a[68:71], v[100:103]\n\t"
    "v_mul_f32 v250, s62, v162\n\t"
    "v_mul_f32 v251, s63, v163\n\t"
    "v_mfma_f32_16x16x32_bf16 v[104:107], a[8:11], a[72:75], v[104:107]\n\t"
    "ds_read_b64_tr_b16 a[4:5], v8 offset:16416\n\t"
    "v_mul_f32 v244, s62, v148\n\t"
    "v_mfma_f32_16x16x32_bf16 v[104:107], a[12:15], a[88:91], v[104:107]\n\t"
    "ds_read_b64_tr_b16 a[6:7], v8 offset:16672\n\t"
    "v_mul_f32 v245, s63, v149\n\t"
    "v_mfma_f32_16x16x32_bf16 v[108:111], a[12:15], a[92:95], v[108:111]\n\t"
    "v_exp_f32 v240, v240\n\t"
    "v_mfma_f32_16x16x32_bf16 v[108:111], a[8:11], a[76:79], v[108:111]\n\t"
    "v_mul_f32 v246, s62, v150\n\t"
    "s_waitcnt lgkmcnt(8)\n\t"
    "v_mfma_f32_16x16x32_bf16 v[112:115], a[16:19], a[64:67], v[112:115]\n\t"
    "ds_read_b64_tr_b16 a[8:9], v8 offset:8\n\t"
    "v_mul_f32 v247, s63, v151\n\t"
    "v_mfma_f32_16x16x32_bf16 v[112:115], a[20:23], a[80:83], v[112:115]\n\t"
    "ds_read_b64_tr_b16 a[10:11], v8 offset:264\n\t"
    "v_mul_f32 v252, s62, v164\n\t"
    "v_mfma_f32_16x16x32_bf16 v[116:119], a[20:23], a[84:87], v[116:119]\n\t"
    "v_exp_f32 v241, v241\n\t"
    "v_mfma_f32_16x16x32_bf16 v[116:119], a[16:19], a[68:71], v[116:119]\n\t"
    "v_exp_f32 v242, v242\n\t"
    "v_mfma_f32_16x16x32_bf16 v[120:123], a[16:19], a[72:75], v[120:123]\n\t"
    "ds_read_b64_tr_b16 a[12:13], v8 offset:16424\n\t"
    "v_mul_f32 v253, s63, v165\n\t"
    "v_mfma_f32_16x16x32_bf16 v[120:123], a[20:23], a[88:91], v[120:123]\n\t"
    "ds_read_b64_tr_b16 a[14:15], v8 offset:16680\n\t"
    "v_mul_f32 v254, s62, v166\n\t"
    "v_mfma_f32_16x16x32_bf16 v[124:127], a[20:23], a[92:95], v[124:127]\n\t"
    "v_exp_f32 v243, v243\n\t"
    "v_mfma_f32_16x16x32_bf16 v[124:127], a[16:19], a[76:79], v[124:127]\n\t"
    "v_mul_f32 v255, s63, v167\n\t"
    "s_waitcnt lgkmcnt(8)\n\t"
    "v_mfma_f32_16x16x32_bf16 v[128:131], a[24:27], a[64:67], v[128:131]\n\t"
    "ds_read_b64_tr_b16 a[16:17], v8 offset:512\n\t"
    "v_exp_f32 v248, v248\n\t"
    "v_mfma_f32_16x16x32_bf16 v[128:131], a[28:31], a[80:83], v[128:131]\n\t"
    "ds_read_b64_tr_b16 a[18:19], v8 offset:768\n\t"
    "v_exp_f32 v249, v249\n\t"
    "v_mfma_f32_16x16x32_bf16 v[132:135], a[28:31], a[84:87], v[132:135]\n\t"
    "v_exp_f32 v250, v250\n\t"
    "v_mfma_f32_16x16x32_bf16 v[132:135], a[24:27], a[68:71], v[132:135]\n\t"
    "v_exp_f32 v251, v251\n\t"
    "v_mfma_f32_16x16x32_bf16 v[136:139], a[24:27], a[72:75], v[136:139]\n\t"
    "ds_read_b64_tr_b16 a[20:21], v8 offset:16928\n\t"
    "v_add_f32 v240, 1.0, v240\n\t"
    "v_mfma_f32_16x16x32_bf16 v[136:139], a[28:31], a[88:91], v[136:139]\n\t"
    "ds_read_b64_tr_b16 a[22:23], v8 offset:17184\n\t"
    "v_add_f32 v241, 1.0, v241\n\t"
    "v_mfma_f32_16x16x32_bf16 v[140:143], a[28:31], a[92:95], v[140:143]\n\t"
    "v_exp_f32 v244, v244\n\t"
    "v_mfma_f32_16x16x32_bf16 v[140:143], a[24:27], a[76:79], v[140:143]\n\t"
    "v_add_f32 v242, 1.0, v242\n\t"
    "s_waitcnt lgkmcnt(8)\n\t"
    "v_mfma_f32_16x16x32_bf16 v[16:19], a[0:3], a[96:99], v[16:19]\n\t"
    "ds_read_b64_tr_b16 a[24:25], v8 offset:520\n\t"
    "v_add_f32 v243, 1.0, v243\n\t"
    "v_mfma_f32_16x16x32_bf16 v[16:19], a[4:7], a[112:115], v[16:19]\n\t"
    "ds_read_b64_tr_b16 a[26:27], v8 offset:776\n\t"
    "v_add_f32 v248, 1.0, v248\n\t"
    "v_mfma_f32_16x16x32_bf16 v[20:23], a[4:7], a[116:119], v[20:23]\n\t"
    "v_exp_f32 v245, v245\n\t"
    "v_mfma_f32_16x16x32_bf16 v[20:23], a[0:3], a[100:103], v[20:23]\n\t"
    "v_exp_f32 v246, v246\n\t"
    "v_mfma_f32_16x16x32_bf16 v[24:27], a[0:3], a[104:107], v[24:27]\n\t"
    "ds_read_b64_tr_b16 a[28:29], v8 offset:16936\n\t"
    "v_add_f32 v249, 1.0, v249\n\t"
    "v_mfma_f32_16x16x32_bf16 v[24:27], a[4:7], a[120:123], v[24:27]\n\t"
    "ds_read_b64_tr_b16 a[30:31], v8 offset:17192\n\t"
    "v_add_f32 v250, 1.0, v250\n\t"
    "v_mfma_f32_16x16x32_bf16 v[28:31], a[4:7], a[124:127], v[28:31]\n\t"
    "v_exp_f32 v247, v247\n\t"
    "v_mfma_f32_16x16x32_bf16 v[28:31], a[0:3], a[108:111], v[28:31]\n\t"
    "v_add_f32 v251, 1.0, v251\n\t"
    "s_waitcnt lgkmcnt(8)\n\t"
    "s_barrier\n\t"
    "v_mfma_f32_16x16x32_bf16 v[32:35], a[8:11], a[96:99], v[32:35]\n\t"
    "ds_read_b64_tr_b16 a[0:1], v8 offset:1024\n\t"
    "s_add_u32 m0, %[sLds4], 32832\n\t"
    "s_add_u32 s65, %[sKV], 32768\n\t"
    "v_mfma_f32_16x16x32_bf16 v[32:35], a[12:15], a[112:115], v[32:35]\n\t"
    "ds_read_b64_tr_b16 a[2:3], v8 offset:1280\n\t"
    "buffer_load_dwordx4 %[vL16], %[srdWS], s65 offen offset:0 lds\n\t"
    "v_mfma_f32_16x16x32_bf16 v[36:39], a[12:15], a[116:119], v[36:39]\n\t"
    "v_rcp_f32 v240, v240\n\t"
    "v_mfma_f32_16x16x32_bf16 v[36:39], a[8:11], a[100:103], v[36:39]\n\t"
    "v_rcp_f32 v241, v241\n\t"
    "v_mfma_f32_16x16x32_bf16 v[40:43], a[8:11], a[104:107], v[40:43]\n\t"
    "ds_read_b64_tr_b16 a[4:5], v8 offset:17440\n\t"
    "v_rcp_f32 v242, v242\n\t"
    "v_mfma_f32_16x16x32_bf16 v[40:43], a[12:15], a[120:123], v[40:43]\n\t"
    "ds_read_b64_tr_b16 a[6:7], v8 offset:17696\n\t"
    "v_rcp_f32 v243, v243\n\t"
    "v_mfma_f32_16x16x32_bf16 v[44:47], a[12:15], a[124:127], v[44:47]\n\t"
    "v_rcp_f32 v248, v248\n\t"
    "v_mfma_f32_16x16x32_bf16 v[44:47], a[8:11], a[108:111], v[44:47]\n\t"
    "buffer_load_dwordx4 %[vL16], %[srdWS], s65 offen offset:1024 lds\n\t"
    "s_waitcnt lgkmcnt(8)\n\t"
    "v_mfma_f32_16x16x32_bf16 v[48:51], a[16:19], a[96:99], v[48:51]\n\t"
    "ds_read_b64_tr_b16 a[8:9], v8 offset:1032\n\t"
    "v_rcp_f32 v249, v249\n\t"
    "v_mfma_f32_16x16x32_bf16 v[48:51], a[20:23], a[112:115], v[48:51]\n\t"
    "ds_read_b64_tr_b16 a[10:11], v8 offset:1288\n\t"
    "v_rcp_f32 v250, v250\n\t"
    "v_mfma_f32_16x16x32_bf16 v[52:55], a[20:23], a[116:119], v[52:55]\n\t"
    "v_rcp_f32 v251, v251\n\t"
    "v_mfma_f32_16x16x32_bf16 v[52:55], a[16:19], a[100:103], v[52:55]\n\t"
    "v_exp_f32 v252, v252\n\t"
    "v_mfma_f32_16x16x32_bf16 v[56:59], a[16:19], a[104:107], v[56:59]\n\t"
    "ds_read_b64_tr_b16 a[12:13], v8 offset:17448\n\t"
    "v_mul_f32 v240, v144, v240\n\t"
    "v_mfma_f32_16x16x32_bf16 v[56:59], a[20:23], a[120:123], v[56:59]\n\t"
    "ds_read_b64_tr_b16 a[14:15], v8 offset:17704\n\t"
    "buffer_load_dwordx4 %[vL16], %[srdWS], s65 offen offset:2048 lds\n\t"
    "v_mfma_f32_16x16x32_bf16 v[60:63], a[20:23], a[124:127], v[60:63]\n\t"
    "v_exp_f32 v253, v253\n\t"
    "v_mfma_f32_16x16x32_bf16 v[60:63], a[16:19], a[108:111], v[60:63]\n\t"
    "v_mul_f32 v241, v145, v241\n\t"
    "s_waitcnt lgkmcnt(8)\n\t"
    "v_mfma_f32_16x16x32_bf16 v[64:67], a[24:27], a[96:99], v[64:67]\n\t"
    "ds_read_b64_tr_b16 a[16:17], v8 offset:1536\n\t"
    "v_mul_f32 v242, v146, v242\n\t"
    "v_mfma_f32_16x16x32_bf16 v[64:67], a[28:31], a[112:115], v[64:67]\n\t"
    "ds_read_b64_tr_b16 a[18:19], v8 offset:1792\n\t"
    "v_mul_f32 v243, v147, v243\n\t"
    "v_mfma_f32_16x16x32_bf16 v[68:71], a[28:31], a[116:119], v[68:71]\n\t"
    "v_exp_f32 v254, v254\n\t"
    "v_mfma_f32_16x16x32_bf16 v[68:71], a[24:27], a[100:103], v[68:71]\n\t"
    "buffer_load_dwordx4 %[vL16], %[srdWS], s65 offen offset:3072 lds\n\t"
    "v_mul_f32 v248, v160, v248\n\t"
    "v_mfma_f32_16x16x32_bf16 v[72:75], a[24:27], a[104:107], v[72:75]\n\t"
    "ds_read_b64_tr_b16 a[20:21], v8 offset:17952\n\t"
    "v_mul_f32 v249, v161, v249\n\t"
    "v_mfma_f32_16x16x32_bf16 v[72:75], a[28:31], a[120:123], v[72:75]\n\t"
    "ds_read_b64_tr_b16 a[22:23], v8 offset:18208\n\t"
    "v_mul_f32 v250, v162, v250\n\t"
    "v_mfma_f32_16x16x32_bf16 v[76:79], a[28:31], a[124:127], v[76:79]\n\t"
    "v_exp_f32 v255, v255\n\t"
    "v_mfma_f32_16x16x32_bf16 v[76:79], a[24:27], a[108:111], v[76:79]\n\t"
    "v_mul_f32 v251, v163, v251\n\t"
    "s_waitcnt lgkmcnt(8)\n\t"
    "v_mfma_f32_16x16x32_bf16 v[80:83], a[0:3], a[96:99], v[80:83]\n\t"
    "ds_read_b64_tr_b16 a[24:25], v8 offset:1544\n\t"
    "v_cvt_pk_bf16_f32 v144, v240, v241\n\t"
    "v_mfma_f32_16x16x32_bf16 v[80:83], a[4:7], a[112:115], v[80:83]\n\t"
    "ds_read_b64_tr_b16 a[26:27], v8 offset:1800\n\t"
    "v_cvt_pk_bf16_f32 v145, v242, v243\n\t"
    "v_mfma_f32_16x16x32_bf16 v[84:87], a[4:7], a[116:119], v[84:87]\n\t"
    "v_cvt_pk_bf16_f32 v146, v248, v249\n\t"
    "v_cvt_pk_bf16_f32 v147, v250, v251\n\t"
    "v_mfma_f32_16x16x32_bf16 v[84:87], a[0:3], a[100:103], v[84:87]\n\t"
    "buffer_store_dwordx4 v[144:147], %[vL16], %[srdWS], %[sHa] offen offset:0 sc1\n\t"
    "v_mul_f32 v240, s62, v152\n\t"
    "v_mfma_f32_16x16x32_bf16 v[88:91], a[0:3], a[104:107], v[88:91]\n\t"
    "ds_read_b64_tr_b16 a[28:29], v8 offset:17960\n\t"
    "v_mul_f32 v241, s63, v153\n\t"
    "v_mfma_f32_16x16x32_bf16 v[88:91], a[4:7], a[120:123], v[88:91]\n\t"
    "ds_read_b64_tr_b16 a[30:31], v8 offset:18216\n\t"
    "v_mul_f32 v242, s62, v154\n\t"
    "v_mfma_f32_16x16x32_bf16 v[92:95], a[4:7], a[124:127], v[92:95]\n\t"
    "v_mul_f32 v243, s63, v155\n\t"
    "v_mul_f32 v248, s62, v168\n\t"
    "v_mfma_f32_16x16x32_bf16 v[92:95], a[0:3], a[108:111], v[92:95]\n\t"
    "v_mul_f32 v249, s63, v169\n\t"
    "s_waitcnt lgkmcnt(8)\n\t"
    "v_mfma_f32_16x16x32_bf16 v[96:99], a[8:11], a[96:99], v[96:99]\n\t"
    "ds_read_b64_tr_b16 a[0:1], v8 offset:32832\n\t"
    "v_mul_f32 v250, s62, v170\n\t"
    "v_mfma_f32_16x16x32_bf16 v[96:99], a[12:15], a[112:115], v[96:99]\n\t"
    "ds_read_b64_tr_b16 a[2:3], v8 offset:33088\n\t"
    "v_mul_f32 v251, s63, v171\n\t"
    "v_mfma_f32_16x16x32_bf16 v[100:103], a[12:15], a[116:119], v[100:103]\n\t"
    "v_exp_f32 v240, v240\n\t"
    "v_mfma_f32_16x16x32_bf16 v[100:103], a[8:11], a[100:103], v[100:103]\n\t"
    "v_exp_f32 v241, v241\n\t"
    "v_mfma_f32_16x16x32_bf16 v[104:107], a[8:11], a[104:107], v[104:107]\n\t"
    "ds_read_b64_tr_b16 a[4:5], v8 offset:49248\n\t"
    "v_add_f32 v244, 1.0, v244\n\t"
    "v_mfma_f32_16x16x32_bf16 v[104:107], a[12:15], a[120:123], v[104:107]\n\t"
    "ds_read_b64_tr_b16 a[6:7], v8 offset:49504\n\t"
    "v_add_f32 v245, 1.0, v245\n\t"
    "v_mfma_f32_16x16x32_bf16 v[108:111], a[12:15], a[124:127], v[108:111]\n\t"
    "v_exp_f32 v242, v242\n\t"
    "v_mfma_f32_16x16x32_bf16 v[108:111], a[8:11], a[108:111], v[108:111]\n\t"
    "v_add_f32 v246, 1.0, v246\n\t"
    "s_waitcnt lgkmcnt(8)\n\t"
    "v_mfma_f32_16x16x32_bf16 v[112:115], a[16:19], a[96:99], v[112:115]\n\t"
    "ds_read_b64_tr_b16 a[8:9], v8 offset:32840\n\t"
    "v_add_f32 v247, 1.0, v247\n\t"
    "v_mfma_f32_16x16x32_bf16 v[112:115], a[20:23], a[112:115], v[112:115]\n\t"
    "ds_read_b64_tr_b16 a[10:11], v8 offset:33096\n\t"
    "v_add_f32 v252, 1.0, v252\n\t"
    "v_mfma_f32_16x16x32_bf16 v[116:119], a[20:23], a[116:119], v[116:119]\n\t"
    "v_exp_f32 v243, v243\n\t"
    "v_mfma_f32_16x16x32_bf16 v[116:119], a[16:19], a[100:103], v[116:119]\n\t"
    "v_exp_f32 v248, v248\n\t"
    "v_mfma_f32_16x16x32_bf16 v[120:123], a[16:19], a[104:107], v[120:123]\n\t"
    "ds_read_b64_tr_b16 a[12:13], v8 offset:49256\n\t"
    "v_add_f32 v253, 1.0, v253\n\t"
    "v_mfma_f32_16x16x32_bf16 v[120:123], a[20:23], a[120:123], v[120:123]\n\t"
    "ds_read_b64_tr_b16 a[14:15], v8 offset:49512\n\t"
    "v_add_f32 v254, 1.0, v254\n\t"
    "v_mfma_f32_16x16x32_bf16 v[124:127], a[20:23], a[124:127], v[124:127]\n\t"
    "v_exp_f32 v249, v249\n\t"
    "v_mfma_f32_16x16x32_bf16 v[124:127], a[16:19], a[108:111], v[124:127]\n\t"
    "v_add_f32 v255, 1.0, v255\n\t"
    "s_waitcnt lgkmcnt(8)\n\t"
    "v_mfma_f32_16x16x32_bf16 v[128:131], a[24:27], a[96:99], v[128:131]\n\t"
    "ds_read_b64_tr_b16 a[16:17], v8 offset:33344\n\t"
    "v_exp_f32 v250, v250\n\t"
    "v_mfma_f32_16x16x32_bf16 v[128:131], a[28:31], a[112:115], v[128:131]\n\t"
    "ds_read_b64_tr_b16 a[18:19], v8 offset:33600\n\t"
    "v_exp_f32 v251, v251\n\t"
    "v_mfma_f32_16x16x32_bf16 v[132:135], a[28:31], a[116:119], v[132:135]\n\t"
    "v_rcp_f32 v244, v244\n\t"
    "v_mfma_f32_16x16x32_bf16 v[132:135], a[24:27], a[100:103], v[132:135]\n\t"
    "v_rcp_f32 v245, v245\n\t"
    "v_mfma_f32_16x16x32_bf16 v[136:139], a[24:27], a[104:107], v[136:139]\n\t"
    "ds_read_b64_tr_b16 a[20:21], v8 offset:49760\n\t"
    "v_add_f32 v240, 1.0, v240\n\t"
    "v_mfma_f32_16x16x32_bf16 v[136:139], a[28:31], a[120:123], v[136:139]\n\t"
    "ds_read_b64_tr_b16 a[22:23], v8 offset:50016\n\t"
    "v_add_f32 v241, 1.0, v241\n\t"
    "v_mfma_f32_16x16x32_bf16 v[140:143], a[28:31], a[124:127], v[140:143]\n\t"
    "v_rcp_f32 v246, v246\n\t"
    "v_mfma_f32_16x16x32_bf16 v[140:143], a[24:27], a[108:111], v[140:143]\n\t"
    "v_add_f32 v242, 1.0, v242\n\t"
    "s_waitcnt lgkmcnt(8)\n\t"
    "v_mfma_f32_16x16x32_bf16 v[16:19], a[0:3], a[128:131], v[16:19]\n\t"
    "ds_read_b64_tr_b16 a[24:25], v8 offset:33352\n\t"
    "v_accvgpr_write_b32 a112, v220\n\t"
    "v_mfma_f32_16x16x32_bf16 v[16:19], a[4:7], a[144:147], v[16:19]\n\t"
    "ds_read_b64_tr_b16 a[26:27], v8 offset:33608\n\t"
    "v_accvgpr_write_b32 a113, v221\n\t"
    "v_mfma_f32_16x16x32_bf16 v[20:23], a[4:7], a[148:151], v[20:23]\n\t"
    "v_rcp_f32 v247, v247\n\t"
    "v_mfma_f32_16x16x32_bf16 v[20:23], a[0:3], a[132:135], v[20:23]\n\t"
    "v_rcp_f32 v252, v252\n\t"
    "v_mfma_f32_16x16x32_bf16 v[24:27], a[0:3], a[136:139], v[24:27]\n\t"
    "ds_read_b64_tr_b16 a[28:29], v8 offset:49768\n\t"
    "v_accvgpr_write_b32 a114, v222\n\t"
    "v_mfma_f32_16x16x32_bf16 v[24:27], a[4:7], a[152:155], v[24:27]\n\t"
    "ds_read_b64_tr_b16 a[30:31], v8 offset:50024\n\t"
    "v_accvgpr_write_b32 a115, v223\n\t"
    "v_mfma_f32_16x16x32_bf16 v[28:31], a[4:7], a[156:159], v[28:31]\n\t"
    "v_rcp_f32 v253, v253\n\t"
    "v_mfma_f32_16x16x32_bf16 v[28:31], a[0:3], a[140:143], v[28:31]\n\t"
    "v_accvgpr_write_b32 a116, v236\n\t"
    "s_waitcnt lgkmcnt(8)\n\t"
    "s_barrier\n\t"
    "v_mfma_f32_16x16x32_bf16 v[32:35], a[8:11], a[128:131], v[32:35]\n\t"
    "ds_read_b64_tr_b16 a[0:1], v8 offset:33856\n\t"
    "v_accvgpr_write_b32 a117, v237\n\t"
    "v_mfma_f32_16x16x32_bf16 v[32:35], a[12:15], a[144:147], v[32:35]\n\t"
    "ds_read_b64_tr_b16 a[2:3], v8 offset:34112\n\t"
    "v_accvgpr_write_b32 a118, v238\n\t"
    "v_mfma_f32_16x16x32_bf16 v[36:39], a[12:15], a[148:151], v[36:39]\n\t"
    "s_add_u32 m0, %[sLds4], 65664\n\t"
    "s_add_u32 s65, %[sKV], 65536\n\t"
    "v_mfma_f32_16x16x32_bf16 v[36:39], a[8:11], a[132:135], v[36:39]\n\t"
    "buffer_load_dwordx4 %[vL16], %[srdWS], s65 offen offset:0 lds\n\t"
    "v_accvgpr_write_b32 a119, v239\n\t"
    "v_mfma_f32_16x16x32_bf16 v[40:43], a[8:11], a[136:139], v[40:43]\n\t"
    "ds_read_b64_tr_b16 a[4:5], v8 offset:50272\n\t"
    "v_add_f32 v243, 1.0, v243\n\t"
    "v_mfma_f32_16x16x32_bf16 v[40:43], a[12:15], a[152:155], v[40:43]\n\t"
    "ds_read_b64_tr_b16 a[6:7], v8 offset:50528\n\t"
    "v_add_f32 v248, 1.0, v248\n\t"
    "v_mfma_f32_16x16x32_bf16 v[44:47], a[12:15], a[156:159], v[44:47]\n\t"
    "v_rcp_f32 v254, v254\n\t"
    "v_mfma_f32_16x16x32_bf16 v[44:47], a[8:11], a[140:143], v[44:47]\n\t"
    "v_add_f32 v249, 1.0, v249\n\t"
    "s_waitcnt lgkmcnt(8)\n\t"
    "v_mfma_f32_16x16x32_bf16 v[48:51], a[16:19], a[128:131], v[48:51]\n\t"
    "ds_read_b64_tr_b16 a[8:9], v8 offset:33864\n\t"
    "v_add_f32 v250, 1.0, v250\n\t"
    "v_mfma_f32_16x16x32_bf16 v[48:51], a[20:23], a[144:147], v[48:51]\n\t"
    "ds_read_b64_tr_b16 a[10:11], v8 offset:34120\n\t"
    "buffer_load_dwordx4 %[vL16], %[srdWS], s65 offen offset:1024 lds\n\t"
    "v_mfma_f32_16x16x32_bf16 v[52:55], a[20:23], a[148:151], v[52:55]\n\t"
    "v_rcp_f32 v255, v255\n\t"
    "v_mfma_f32_16x16x32_bf16 v[52:55], a[16:19], a[132:135], v[52:55]\n\t"
    "v_cvt_pk_bf16_f32 v16, v16, v17\n\t"
    "v_cvt_pk_bf16_f32 v18, v18, v19\n\t"
    "v_mfma_f32_16x16x32_bf16 v[56:59], a[16:19], a[136:139], v[56:59]\n\t"
    "ds_read_b64_tr_b16 a[12:13], v8 offset:50280\n\t"
    "v_cvt_pk_bf16_f32 v32, v32, v33\n\t"
    "v_mfma_f32_16x16x32_bf16 v[56:59], a[20:23], a[152:155], v[56:59]\n\t"
    "ds_read_b64_tr_b16 a[14:15], v8 offset:50536\n\t"
    "v_cvt_pk_bf16_f32 v34, v34, v35\n\t"
    "v_mfma_f32_16x16x32_bf16 v[60:63], a[20:23], a[156:159], v[60:63]\n\t"
    "v_accvgpr_write_b32 a32, v16\n\t"
    "v_accvgpr_write_b32 a33, v18\n\t"
    "v_mfma_f32_16x16x32_bf16 v[60:63], a[16:19], a[140:143], v[60:63]\n\t"
    "buffer_load_dwordx4 %[vL16], %[srdWS], s65 offen offset:2048 lds\n\t"
    "s_waitcnt lgkmcnt(8)\n\t"
    "v_mfma_f32_16x16x32_bf16 v[64:67], a[24:27], a[128:131], v[64:67]\n\t"
    "ds_read_b64_tr_b16 a[16:17], v8 offset:34368\n\t"
    "v_accvgpr_write_b32 a34, v32\n\t"
    "v_mfma_f32_16x16x32_bf16 v[64:67], a[28:31], a[144:147], v[64:67]\n\t"
    "ds_read_b64_tr_b16 a[18:19], v8 offset:34624\n\t"
    "v_accvgpr_write_b32 a35, v34\n\t"
    "v_mfma_f32_16x16x32_bf16 v[68:71], a[28:31], a[148:151], v[68:71]\n\t"
    "v_cvt_pk_bf16_f32 v20, v20, v21\n\t"
    "v_cvt_pk_bf16_f32 v22, v22, v23\n\t"
    "v_mfma_f32_16x16x32_bf16 v[68:71], a[24:27], a[132:135], v[68:71]\n\t"
    "v_cvt_pk_bf16_f32 v36, v36, v37\n\t"
    "v_cvt_pk_bf16_f32 v38, v38, v39\n\t"
    "v_mfma_f32_16x16x32_bf16 v[72:75], a[24:27], a[136:139], v[72:75]\n\t"
    "ds_read_b64_tr_b16 a[20:21], v8 offset:50784\n\t"
    "v_accvgpr_write_b32 a36, v20\n\t"
    "v_mfma_f32_16x16x32_bf16 v[72:75], a[28:31], a[152:155], v[72:75]\n\t"
    "ds_read_b64_tr_b16 a[22:23], v8 offset:51040\n\t"
    "buffer_load_dwordx4 %[vL16], %[srdWS], s65 offen offset:3072 lds\n\t"
    "v_mfma_f32_16x16x32_bf16 v[76:79], a[28:31], a[156:159], v[76:79]\n\t"
    "s_add_u32 m0, %[sLds4], 82048\n\t"
    "s_add_u32 s66, %[sKV], 16384\n\t"
    "v_mfma_f32_16x16x32_bf16 v[76:79], a[24:27], a[140:143], v[76:79]\n\t"
    "v_accvgpr_write_b32 a37, v22\n\t"
    "s_waitcnt lgkmcnt(8)\n\t"
    "v_mfma_f32_16x16x32_bf16 v[80:83], a[0:3], a[128:131], v[80:83]\n\t"
    "ds_read_b64_tr_b16 a[24:25], v8 offset:34376\n\t"
    "v_accvgpr_write_b32 a38, v36\n\t"
    "v_mfma_f32_16x16x32_bf16 v[80:83], a[4:7], a[144:147], v[80:83]\n\t"
    "ds_read_b64_tr_b16 a[26:27], v8 offset:34632\n\t"
    "v_accvgpr_write_b32 a39, v38\n\t"
    "v_mfma_f32_16x16x32_bf16 v[84:87], a[4:7], a[148:151], v[84:87]\n\t"
    "v_cvt_pk_bf16_f32 v24, v24, v25\n\t"
    "v_cvt_pk_bf16_f32 v26, v26, v27\n\t"
    "v_mfma_f32_16x16x32_bf16 v[84:87], a[0:3], a[132:135], v[84:87]\n\t"
    "buffer_load_dwordx4 %[vL16], %[srdWS], s66 offen offset:0 lds\n\t"
    "v_cvt_pk_bf16_f32 v40, v40, v41\n\t"
    "v_mfma_f32_16x16x32_bf16 v[88:91], a[0:3], a[136:139], v[88:91]\n\t"
    "ds_read_b64_tr_b16 a[28:29], v8 offset:50792\n\t"
    "v_cvt_pk_bf16_f32 v42, v42, v43\n\t"
    "v_mfma_f32_16x16x32_bf16 v[88:91], a[4:7], a[152:155], v[88:91]\n\t"
    "ds_read_b64_tr_b16 a[30:31], v8 offset:51048\n\t"
    "v_accvgpr_write_b32 a40, v24\n\t"
    "v_mfma_f32_16x16x32_bf16 v[92:95], a[4:7], a[156:159], v[92:95]\n\t"
    "v_accvgpr_write_b32 a41, v26\n\t"
    "v_accvgpr_write_b32 a42, v40\n\t"
    "v_mfma_f32_16x16x32_bf16 v[92:95], a[0:3], a[140:143], v[92:95]\n\t"
    "v_accvgpr_write_b32 a43, v42\n\t"
    "s_waitcnt vmcnt(9) lgkmcnt(8)\n\t"
    "s_barrier\n\t"
    "v_mfma_f32_16x16x32_bf16 v[96:99], a[8:11], a[128:131], v[96:99]\n\t"
    "ds_read_b128 a[0:3], v10 offset:0\n\t"
    "v_cvt_pk_bf16_f32 v28, v28, v29\n\t"
    "v_mfma_f32_16x16x32_bf16 v[96:99], a[12:15], a[144:147], v[96:99]\n\t"
    "buffer_load_dwordx4 %[vL16], %[srdWS], s66 offen offset:1024 lds\n\t"
    "v_cvt_pk_bf16_f32 v30, v30, v31\n\t"
    "v_mfma_f32_16x16x32_bf16 v[100:103], a[12:15], a[148:151], v[100:103]\n\t"
    "v_cvt_pk_bf16_f32 v44, v44, v45\n\t"
    "v_cvt_pk_bf16_f32 v46, v46, v47\n\t"
    "v_mfma_f32_16x16x32_bf16 v[100:103], a[8:11], a[132:135], v[100:103]\n\t"
    "v_accvgpr_write_b32 a44, v28\n\t"
    "v_accvgpr_write_b32 a45, v30\n\t"
    "v_mfma_f32_16x16x32_bf16 v[104:107], a[8:11], a[136:139], v[104:107]\n\t"
    "ds_read_b128 a[4:7], v10 offset:1024\n\t"
    "v_accvgpr_write_b32 a46, v44\n\t"
    "v_mfma_f32_16x16x32_bf16 v[104:107], a[12:15], a[152:155], v[104:107]\n\t"
    "v_accvgpr_write_b32 a47, v46\n\t"
    "v_cvt_pk_bf16_f32 v48, v48, v49\n\t"
    "v_mfma_f32_16x16x32_bf16 v[108:111], a[12:15], a[156:159], v[108:111]\n\t"
    "v_cvt_pk_bf16_f32 v50, v50, v51\n\t"
    "v_cvt_pk_bf16_f32 v64, v64, v65\n\t"
    "v_mfma_f32_16x16x32_bf16 v[108:111], a[8:11], a[140:143], v[108:111]\n\t"
    "buffer_load_dwordx4 %[vL16], %[srdWS], s66 offen offset:2048 lds\n\t"
    "s_waitcnt lgkmcnt(6)\n\t"
    "v_mfma_f32_16x16x32_bf16 v[112:115], a[16:19], a[128:131], v[112:115]\n\t"
    "ds_read_b128 a[8:11], v10 offset:2048\n\t"
    "v_cvt_pk_bf16_f32 v66, v66, v67\n\t"
    "v_mfma_f32_16x16x32_bf16 v[112:115], a[20:23], a[144:147], v[112:115]\n\t"
    "v_accvgpr_write_b32 a48, v48\n\t"
    "v_accvgpr_write_b32 a49, v50\n\t"
    "v_mfma_f32_16x16x32_bf16 v[116:119], a[20:23], a[148:151], v[116:119]\n\t"
    "v_accvgpr_write_b32 a50, v64\n\t"
    "v_accvgpr_write_b32 a51, v66\n\t"
    "v_mfma_f32_16x16x32_bf16 v[116:119], a[16:19], a[132:135], v[116:119]\n\t"
    "v_cvt_pk_bf16_f32 v52, v52, v53\n\t"
    "v_cvt_pk_bf16_f32 v54, v54, v55\n\t"
    "v_mfma_f32_16x16x32_bf16 v[120:123], a[16:19], a[136:139], v[120:123]\n\t"
    "ds_read_b128 a[12:15], v10 offset:3072\n\t"
    "v_cvt_pk_bf16_f32 v68, v68, v69\n\t"
    "v_mfma_f32_16x16x32_bf16 v[120:123], a[20:23], a[152:155], v[120:123]\n\t"
    "buffer_load_dwordx4 %[vL16], %[srdWS], s66 offen offset:3072 lds\n\t"
    "v_cvt_pk_bf16_f32 v70, v70, v71\n\t"
    "v_mfma_f32_16x16x32_bf16 v[124:127], a[20:23], a[156:159], v[124:127]\n\t"
    "v_accvgpr_write_b32 a52, v52\n\t"
    "v_accvgpr_write_b32 a53, v54\n\t"
    "v_mfma_f32_16x16x32_bf16 v[124:127], a[16:19], a[140:143], v[124:127]\n\t"
    "v_accvgpr_write_b32 a54, v68\n\t"
    "s_waitcnt lgkmcnt(4)\n\t"
    "v_mfma_f32_16x16x32_bf16 v[128:131], a[24:27], a[128:131], v[128:131]\n\t"
    "ds_read_b128 a[16:19], v10 offset:4096\n\t"
    "v_accvgpr_write_b32 a55, v70\n\t"
    "v_mfma_f32_16x16x32_bf16 v[128:131], a[28:31], a[144:147], v[128:131]\n\t"
    "v_cvt_pk_bf16_f32 v56, v56, v57\n\t"
    "v_cvt_pk_bf16_f32 v58, v58, v59\n\t"
    "v_mfma_f32_16x16x32_bf16 v[132:135], a[28:31], a[148:151], v[132:135]\n\t"
    "v_cvt_pk_bf16_f32 v72, v72, v73\n\t"
    "v_cvt_pk_bf16_f32 v74, v74, v75\n\t"
    "v_mfma_f32_16x16x32_bf16 v[132:135], a[24:27], a[132:135], v[132:135]\n\t"
    "v_accvgpr_write_b32 a56, v56\n\t"
    "v_accvgpr_write_b32 a57, v58\n\t"
    "v_mfma_f32_16x16x32_bf16 v[136:139], a[24:27], a[136:139], v[136:139]\n\t"
    "ds_read_b128 a[20:23], v10 offset:5120\n\t"
    "v_accvgpr_write_b32 a58, v72\n\t"
    "v_mfma_f32_16x16x32_bf16 v[136:139], a[28:31], a[152:155], v[136:139]\n\t"
    "v_accvgpr_write_b32 a59, v74\n\t"
    "v_cvt_pk_bf16_f32 v60, v60, v61\n\t"
    "v_mfma_f32_16x16x32_bf16 v[140:143], a[28:31], a[156:159], v[140:143]\n\t"
    "v_cvt_pk_bf16_f32 v62, v62, v63\n\t"
    "v_cvt_pk_bf16_f32 v76, v76, v77\n\t"
    "v_mfma_f32_16x16x32_bf16 v[140:143], a[24:27], a[140:143], v[140:143]\n\t"
    "v_cvt_pk_bf16_f32 v78, v78, v79\n\t"
    "v_accvgpr_write_b32 a60, v60\n\t"
    "v_accvgpr_write_b32 a61, v62\n\t"
    "v_accvgpr_write_b32 a62, v76\n\t"
    "v_accvgpr_write_b32 a63, v78\n\t"
    "s_waitcnt lgkmcnt(4)\n\t"
    "v_mfma_f32_16x16x32_bf16 v[16:19], a[0:3], a[32:35], 0\n\t"
    "ds_read_b128 a[24:27], v10 offset:6144\n\t"
    "v_cvt_pk_bf16_f32 v80, v80, v81\n\t"
    "v_mfma_f32_16x16x32_bf16 v[16:19], a[4:7], a[48:51], v[16:19]\n\t"
    "v_cvt_pk_bf16_f32 v82, v82, v83\n\t"
    "v_cvt_pk_bf16_f32 v96, v96, v97\n\t"
    "v_mfma_f32_16x16x32_bf16 v[20:23], a[4:7], a[52:55], 0\n\t"
    "v_cvt_pk_bf16_f32 v98, v98, v99\n\t"
    "v_accvgpr_write_b32 a64, v80\n\t"
    "v_mfma_f32_16x16x32_bf16 v[20:23], a[0:3], a[36:39], v[20:23]\n\t"
    "v_accvgpr_write_b32 a65, v82\n\t"
    "v_accvgpr_write_b32 a66, v96\n\t"
    "v_mfma_f32_16x16x32_bf16 v[24:27], a[0:3], a[40:43], 0\n\t"
    "ds_read_b128 a[28:31], v10 offset:7168\n\t"
    "v_accvgpr_write_b32 a67, v98\n\t"
    "v_mfma_f32_16x16x32_bf16 v[24:27], a[4:7], a[56:59], v[24:27]\n\t"
    "v_cvt_pk_bf16_f32 v84, v84, v85\n\t"
    "v_cvt_pk_bf16_f32 v86, v86, v87\n\t"
    "v_mfma_f32_16x16x32_bf16 v[28:31], a[4:7], a[60:63], 0\n\t"
    "v_cvt_pk_bf16_f32 v100, v100, v101\n\t"
    "v_cvt_pk_bf16_f32 v102, v102, v103\n\t"
    "v_mfma_f32_16x16x32_bf16 v[28:31], a[0:3], a[44:47], v[28:31]\n\t"
    "v_accvgpr_write_b32 a68, v84\n\t"
    "v_accvgpr_write_b32 a69, v86\n\t"
    "v_accvgpr_write_b32 a70, v100\n\t"
    "v_accvgpr_write_b32 a71, v102\n\t"
    "v_cvt_pk_bf16_f32 v88, v88, v89\n\t"
    "v_cvt_pk_bf16_f32 v90, v90, v91\n\t"
    "v_cvt_pk_bf16_f32 v104, v104, v105\n\t"
    "v_cvt_pk_bf16_f32 v106, v106, v107\n\t"
    "v_accvgpr_write_b32 a72, v88\n\t"
    "v_accvgpr_write_b32 a73, v90\n\t"
    "v_accvgpr_write_b32 a74, v104\n\t"
    "v_accvgpr_write_b32 a75, v106\n\t"
    "v_cvt_pk_bf16_f32 v92, v92, v93\n\t"
    "v_cvt_pk_bf16_f32 v94, v94, v95\n\t"
    "v_cvt_pk_bf16_f32 v108, v108, v109\n\t"
    "v_cvt_pk_bf16_f32 v110, v110, v111\n\t"
    "v_accvgpr_write_b32 a76, v92\n\t"
    "v_accvgpr_write_b32 a77, v94\n\t"
    "v_accvgpr_write_b32 a78, v108\n\t"
    "v_accvgpr_write_b32 a79, v110\n\t"
    "v_cvt_pk_bf16_f32 v112, v112, v113\n\t"
    "v_cvt_pk_bf16_f32 v114, v114, v115\n\t"
    "v_cvt_pk_bf16_f32 v128, v128, v129\n\t"
    "v_cvt_pk_bf16_f32 v130, v130, v131\n\t"
    "v_accvgpr_write_b32 a80, v112\n\t"
    "v_accvgpr_write_b32 a81, v114\n\t"
    "v_accvgpr_write_b32 a82, v128\n\t"
    "v_accvgpr_write_b32 a83, v130\n\t"
    "v_cvt_pk_bf16_f32 v116, v116, v117\n\t"
    "v_cvt_pk_bf16_f32 v118, v118, v119\n\t"
    "v_cvt_pk_bf16_f32 v132, v132, v133\n\t"
    "v_cvt_pk_bf16_f32 v134, v134, v135\n\t"
    "v_accvgpr_write_b32 a84, v116\n\t"
    "v_accvgpr_write_b32 a85, v118\n\t"
    "v_accvgpr_write_b32 a86, v132\n\t"
    "v_accvgpr_write_b32 a87, v134\n\t"
    "v_cvt_pk_bf16_f32 v120, v120, v121\n\t"
    "v_cvt_pk_bf16_f32 v122, v122, v123\n\t"
    "v_cvt_pk_bf16_f32 v136, v136, v137\n\t"
    "v_cvt_pk_bf16_f32 v138, v138, v139\n\t"
    "v_accvgpr_write_b32 a88, v120\n\t"
    "v_accvgpr_write_b32 a89, v122\n\t"
    "v_accvgpr_write_b32 a90, v136\n\t"
    "v_accvgpr_write_b32 a91, v138\n\t"
    "v_cvt_pk_bf16_f32 v124, v124, v125\n\t"
    "v_cvt_pk_bf16_f32 v126, v126, v127\n\t"
    "v_cvt_pk_bf16_f32 v140, v140, v141\n\t"
    "v_cvt_pk_bf16_f32 v142, v142, v143\n\t"
    "v_accvgpr_write_b32 a92, v124\n\t"
    "v_accvgpr_write_b32 a93, v126\n\t"
    "v_accvgpr_write_b32 a94, v140\n\t"
    "v_accvgpr_write_b32 a95, v142\n\t"
    "s_waitcnt lgkmcnt(4)\n\t"
    "v_mfma_f32_16x16x32_bf16 v[16:19], a[8:11], a[64:67], v[16:19]\n\t"
    "ds_read_b128 a[0:3], v10 offset:8192\n\t"
    "v_add_f32 v251, 1.0, v251\n\t"
    "v_mfma_f32_16x16x32_bf16 v[16:19], a[12:15], a[80:83], v[16:19]\n\t"
    "v_rcp_f32 v240, v240\n\t"
    "v_mfma_f32_16x16x32_bf16 v[20:23], a[12:15], a[84:87], v[20:23]\n\t"
    "v_rcp_f32 v241, v241\n\t"
    "v_mfma_f32_16x16x32_bf16 v[20:23], a[8:11], a[68:71], v[20:23]\n\t"
    "v_rcp_f32 v242, v242\n\t"
    "v_mfma_f32_16x16x32_bf16 v[24:27], a[8:11], a[72:75], v[24:27]\n\t"
    "ds_read_b128 a[4:7], v10 offset:9216\n\t"
    "v_mul_f32 v244, v148, v244\n\t"
    "v_mfma_f32_16x16x32_bf16 v[24:27], a[12:15], a[88:91], v[24:27]\n\t"
    "v_rcp_f32 v243, v243\n\t"
    "v_mfma_f32_16x16x32_bf16 v[28:31], a[12:15], a[92:95], v[28:31]\n\t"
    "v_rcp_f32 v248, v248\n\t"
    "v_mfma_f32_16x16x32_bf16 v[28:31], a[8:11], a[76:79], v[28:31]\n\t"
    "v_mul_f32 v245, v149, v245\n\t"
    "s_waitcnt lgkmcnt(4)\n\t"
    "v_mfma_f32_16x16x32_bf16 v[32:35], a[16:19], a[32:35], 0\n\t"
    "ds_read_b128 a[8:11], v10 offset:10240\n\t"
    "v_mul_f32 v246, v150, v246\n\t"
    "v_mfma_f32_16x16x32_bf16 v[32:35], a[20:23], a[48:51], v[32:35]\n\t"
    "v_rcp_f32 v249, v249\n\t"
    "v_mfma_f32_16x16x32_bf16 v[36:39], a[20:23], a[52:55], 0\n\t"
    "v_rcp_f32 v250, v250\n\t"
    "v_mfma_f32_16x16x32_bf16 v[36:39], a[16:19], a[36:39], v[36:39]\n\t"
    "v_rcp_f32 v251, v251\n\t"
    "v_mfma_f32_16x16x32_bf16 v[40:43], a[16:19], a[40:43], 0\n\t"
    "ds_read_b128 a[12:15], v10 offset:11264\n\t"
    "v_max3_f32 v136, v16, v17, v18\n\t"
    "v_mfma_f32_16x16x32_bf16 v[40:43], a[20:23], a[56:59], v[40:43]\n\t"
    "v_max3_f32 v137, v20, v21, v22\n\t"
    "v_max3_f32 v138, v24, v25, v26\n\t"
    "v_mfma_f32_16x16x32_bf16 v[44:47], a[20:23], a[60:63], 0\n\t"
    "v_max3_f32 v139, v28, v29, v30\n\t"
    "v_mul_f32 v240, v152, v240\n\t"
    "v_mfma_f32_16x16x32_bf16 v[44:47], a[16:19], a[44:47], v[44:47]\n\t"
    "v_mul_f32 v241, v153, v241\n\t"
    "s_waitcnt lgkmcnt(4)\n\t"
    "v_mfma_f32_16x16x32_bf16 v[32:35], a[24:27], a[64:67], v[32:35]\n\t"
    "ds_read_b128 a[16:19], v10 offset:12288\n\t"
    "v_mul_f32 v242, v154, v242\n\t"
    "v_mfma_f32_16x16x32_bf16 v[32:35], a[28:31], a[80:83], v[32:35]\n\t"
    "v_mul_f32 v243, v155, v243\n\t"
    "k_pga5: s_cbranch_execz k_pga9\n\t"
    "v_mul_f32 v248, v168, v248\n\t"
    "v_mfma_f32_16x16x32_bf16 v[36:39], a[28:31], a[84:87], v[36:39]\n\t"
    "v_mul_f32 v249, v169, v249\n\t"
    "v_mul_f32 v250, v170, v250\n\t"
    "v_mfma_f32_16x16x32_bf16 v[36:39], a[24:27], a[68:71], v[36:39]\n\t"
    "v_mul_f32 v251, v171, v251\n\t"
    "v_cvt_pk_bf16_f32 v152, v240, v241\n\t"
    "v_mfma_f32_16x16x32_bf16 v[40:43], a[24:27], a[72:75], v[40:43]\n\t"
    "ds_read_b128 a[20:23], v10 offset:13312\n\t"
    "v_cvt_pk_bf16_f32 v153, v242, v243\n\t"
    "v_mfma_f32_16x16x32_bf16 v[40:43], a[28:31], a[88:91], v[40:43]\n\t"
    "v_cvt_pk_bf16_f32 v154, v248, v249\n\t"
    "v_cvt_pk_bf16_f32 v155, v250, v251\n\t"
    "v_mfma_f32_16x16x32_bf16 v[44:47], a[28:31], a[92:95], v[44:47]\n\t"
    "buffer_store_dwordx4 v[152:155], %[vL16], %[srdWS], %[sHa] offen offset:2048 sc1\n\t"
    "v_mul_f32 v240, s62, v176\n\t"
    "v_mfma_f32_16x16x32_bf16 v[44:47], a[24:27], a[76:79], v[44:47]\n\t"
    "v_mul_f32 v241, s63, v177\n\t"
    "s_waitcnt lgkmcnt(4)\n\t"
    "v_mfma_f32_16x16x32_bf16 v[48:51], a[0:3], a[32:35], 0\n\t"
    "ds_read_b128 a[24:27], v10 offset:14336\n\t"
    "v_mul_f32 v242, s62, v178\n\t"
    "v_mfma_f32_16x16x32_bf16 v[48:51], a[4:7], a[48:51], v[48:51]\n\t"
    "v_mul_f32 v243, s63, v179\n\t"
    "v_mul_f32 v248, s62, v192\n\t"
    "v_mfma_f32_16x16x32_bf16 v[52:55], a[4:7], a[52:55], 0\n\t"
    "v_mul_f32 v249, s63, v193\n\t"
    "v_mul_f32 v250, s62, v194\n\t"
    "v_mfma_f32_16x16x32_bf16 v[52:55], a[0:3], a[36:39], v[52:55]\n\t"
    "v_max3_f32 v140, v19, v32, v33\n\t"
    "v_max3_f32 v141, v23, v36, v37\n\t"
    "v_mfma_f32_16x16x32_bf16 v[56:59], a[0:3], a[40:43], 0\n\t"
    "ds_read_b128 a[28:31], v10 offset:15360\n\t"
    "v_max3_f32 v142, v27, v40, v41\n\t"
    "v_mfma_f32_16x16x32_bf16 v[56:59], a[4:7], a[56:59], v[56:59]\n\t"
    "v_max3_f32 v143, v31, v44, v45\n\t"
    "v_max3_f32 v136, v136, v34, v35\n\t"
    "v_mfma_f32_16x16x32_bf16 v[60:63], a[4:7], a[60:63], 0\n\t"
    "v_max3_f32 v137, v137, v38, v39\n\t"
    "v_max3_f32 v138, v138, v42, v43\n\t"
    "v_mfma_f32_16x16x32_bf16 v[60:63], a[0:3], a[44:47], v[60:63]\n\t"
    "v_max3_f32 v139, v139, v46, v47\n\t"
    "s_waitcnt vmcnt(8) lgkmcnt(4)\n\t"
    "s_barrier\n\t"
    "v_mfma_f32_16x16x32_bf16 v[48:51], a[8:11], a[64:67], v[48:51]\n\t"
    "ds_read_b128 a[0:3], v10 offset:32832\n\t"
    "v_mul_f32 v251, s63, v195\n\t"
    "v_mfma_f32_16x16x32_bf16 v[48:51], a[12:15], a[80:83], v[48:51]\n\t"
    "s_add_u32 m0, %[sLds4], 98496\n\t"
    "s_add_u32 s65, %[sKV], 98304\n\t"
    "v_mfma_f32_16x16x32_bf16 v[52:55], a[12:15], a[84:87], v[52:55]\n\t"
    "buffer_load_dwordx4 %[vL16], %[srdWS], s65 offen offset:0 lds\n\t"
    "v_mul_f32 v247, v151, v247\n\t"
    "v_mfma_f32_16x16x32_bf16 v[52:55], a[8:11], a[68:71], v[52:55]\n\t"
    "v_exp_f32 v240, v240\n\t"
    "v_mfma_f32_16x16x32_bf16 v[56:59], a[8:11], a[72:75], v[56:59]\n\t"
    "ds_read_b128 a[4:7], v10 offset:33856\n\t"
    "v_mul_f32 v252, v164, v252\n\t"
    "v_mfma_f32_16x16x32_bf16 v[56:59], a[12:15], a[88:91], v[56:59]\n\t"
    "v_exp_f32 v241, v241\n\t"
    "v_mfma_f32_16x16x32_bf16 v[60:63], a[12:15], a[92:95], v[60:63]\n\t"
    "v_exp_f32 v242, v242\n\t"
    "v_mfma_f32_16x16x32_bf16 v[60:63], a[8:11], a[76:79], v[60:63]\n\t"
    "v_mul_f32 v253, v165, v253\n\t"
    "s_waitcnt lgkmcnt(4)\n\t"
    "v_mfma_f32_16x16x32_bf16 v[64:67], a[16:19], a[32:35], 0\n\t"
    "ds_read_b128 a[8:11], v10 offset:34880\n\t"
    "buffer_load_dwordx4 %[vL16], %[srdWS], s65 offen offset:1024 lds\n\t"
    "v_mfma_f32_16x16x32_bf16 v[64:67], a[20:23], a[48:51], v[64:67]\n\t"
    "v_exp_f32 v243, v243\n\t"
    "v_mfma_f32_16x16x32_bf16 v[68:71], a[20:23], a[52:55], 0\n\t"
    "v_exp_f32 v248, v248\n\t"
    "v_mfma_f32_16x16x32_bf16 v[68:71], a[16:19], a[36:39], v[68:71]\n\t"
    "v_exp_f32 v249, v249\n\t"
    "v_mfma_f32_16x16x32_bf16 v[72:75], a[16:19], a[40:43], 0\n\t"
    "ds_read_b128 a[12:15], v10 offset:35904\n\t"
    "v_max3_f32 v140, v140, v48, v49\n\t"
    "v_mfma_f32_16x16x32_bf16 v[72:75], a[20:23], a[56:59], v[72:75]\n\t"
    "v_exp_f32 v250, v250\n\t"
    "v_mfma_f32_16x16x32_bf16 v[76:79], a[20:23], a[60:63], 0\n\t"
    "buffer_load_dwordx4 %[vL16], %[srdWS], s65 offen offset:2048 lds\n\t"
    "v_max3_f32 v141, v141, v52, v53\n\t"
    "v_mfma_f32_16x16x32_bf16 v[76:79], a[16:19], a[44:47], v[76:79]\n\t"
    "v_max3_f32 v142, v142, v56, v57\n\t"
    "s_waitcnt lgkmcnt(4)\n\t"
    "v_mfma_f32_16x16x32_bf16 v[64:67], a[24:27], a[64:67], v[64:67]\n\t"
    "ds_read_b128 a[16:19], v10 offset:36928\n\t"
    "v_max3_f32 v143, v143, v60, v61\n\t"
    "v_mfma_f32_16x16x32_bf16 v[64:67], a[28:31], a[80:83], v[64:67]\n\t"
    "v_exp_f32 v251, v251\n\t"
    "v_mfma_f32_16x16x32_bf16 v[68:71], a[28:31], a[84:87], v[68:71]\n\t"
    "v_max3_f32 v136, v136, v50, v51\n\t"
    "v_max3_f32 v137, v137, v54, v55\n\t"
    "v_mfma_f32_16x16x32_bf16 v[68:71], a[24:27], a[68:71], v[68:71]\n\t"
    "v_max3_f32 v138, v138, v58, v59\n\t"
    "v_max3_f32 v139, v139, v62, v63\n\t"
    "v_mfma_f32_16x16x32_bf16 v[72:75], a[24:27], a[72:75], v[72:75]\n\t"
    "ds_read_b128 a[20:23], v10 offset:37952\n\t"
    "buffer_load_dwordx4 %[vL16], %[srdWS], s65 offen offset:3072 lds\n\t"
    "v_mfma_f32_16x16x32_bf16 v[72:75], a[28:31], a[88:91], v[72:75]\n\t"
    "s_add_u32 m0, %[sLds4], 114880\n\t"
    "s_add_u32 s66, %[sKV], 49152\n\t"
    "v_mfma_f32_16x16x32_bf16 v[76:79], a[28:31], a[92:95], v[76:79]\n\t"
    "v_add_f32 v240, 1.0, v240\n\t"
    "v_add_f32 v241, 1.0, v241\n\t"
    "v_mfma_f32_16x16x32_bf16 v[76:79], a[24:27], a[76:79], v[76:79]\n\t"
    "v_add_f32 v242, 1.0, v242\n\t"
    "v_max3_f32 v140, v140, v64, v65\n\t"
    "v_add_f32 v243, 1.0, v243\n\t"
    "v_pk_mul_f32 v[254:255], v[254:255], v[166:167]\n\t"
    "v_bfe_u32 v12, %[vL16], 4, 2\n\t"
    "v_max3_f32 v141, v141, v68, v69\n\t"
    "v_pk_add_f32 v[248:249], v[248:249], 1.0 op_sel_hi:[1,0]\n\t"
    "v_cvt_pk_bf16_f32 v148, v244, v245\n\t"
    "v_mov_b32 v13, 0x3f803f80\n\t"
    "v_max3_f32 v142, v142, v72, v73\n\t"
    "v_pk_add_f32 v[250:251], v[250:251], 1.0 op_sel_hi:[1,0]\n\t"
    "v_cvt_pk_bf16_f32 v149, v246, v247\n\t"
    "v_cmp_eq_u32 vcc, 0, v12\n\t"
    "s_nop 1\n\t"
    "v_cndmask_b32 v14, 0, v13, vcc\n\t"
    "v_max3_f32 v143, v143, v76, v77\n\t"
    "v_rcp_f32 v240, v240\n\t"
    "v_cvt_pk_bf16_f32 v150, v252, v253\n\t"
    "v_accvgpr_write_b32 a96, v14\n\t"
    "v_max3_f32 v136, v136, v66, v67\n\t"
    "v_rcp_f32 v241, v241\n\t"
    "v_cvt_pk_bf16_f32 v151, v254, v255\n\t"
    "v_accvgpr_write_b32 a97, v14\n\t"
    "v_max3_f32 v137, v137, v70, v71\n\t"
    "v_rcp_f32 v242, v242\n\t"
    "buffer_store_dwordx4 v[148:151], %[vL16], %[srdWS], %[sHa] offen offset:1024 sc1\n\t"
    "v_accvgpr_write_b32 a98, v14\n\t"
    "v_max3_f32 v138, v138, v74, v75\n\t"
    "v_rcp_f32 v243, v243\n\t"
    "v_pk_mul_f32 v[244:245], v[156:157], s[62:63]\n\t"
    "v_accvgpr_write_b32 a99, v14\n\t"
    "v_max3_f32 v139, v139, v78, v79\n\t"
    "v_rcp_f32 v248, v248\n\t"
    "v_pk_mul_f32 v[246:247], v[158:159], s[62:63]\n\t"
    "v_cmp_eq_u32 vcc, 1, v12\n\t"
    "s_nop 1\n\t"
    "v_cndmask_b32 v14, 0, v13, vcc\n\t"
    "v_max_f32 v136, v136, v140\n\t"
    "v_rcp_f32 v249, v249\n\t"
    "v_pk_mul_f32 v[252:253], v[172:173], s[62:63]\n\t"
    "v_accvgpr_write_b32 a100, v14\n\t"
    "v_max_f32 v137, v137, v141\n\t"
    "v_rcp_f32 v250, v250\n\t"
    "v_pk_mul_f32 v[254:255], v[174:175], s[62:63]\n\t"
    "v_accvgpr_write_b32 a101, v14\n\t"
    "v_max_f32 v138, v138, v142\n\t"
    "v_rcp_f32 v251, v251\n\t"
    "v_exp_f32 v244, v244\n\t"
    "v_accvgpr_write_b32 a102, v14\n\t"
    "v_max_f32 v139, v139, v143\n\t"
    "v_pk_mul_f32 v[240:241], v[240:241], v[176:177]\n\t"
    "v_exp_f32 v245, v245\n\t"
    "v_accvgpr_write_b32 a103, v14\n\t"
    "v_mov_b32 v140, v136\n\t"
    "v_pk_mul_f32 v[242:243], v[242:243], v[178:179]\n\t"
    "v_exp_f32 v246, v246\n\t"
    "v_cmp_eq_u32 vcc, 2, v12\n\t"
    "s_nop 1\n\t"
    "v_cndmask_b32 v14, 0, v13, vcc\n\t"
    "v_mov_b32 v141, v137\n\t"
    "v_pk_mul_f32 v[248:249], v[248:249], v[192:193]\n\t"
    "v_exp_f32 v247, v247\n\t"
    "v_accvgpr_write_b32 a104, v14\n\t"
    "v_mov_b32 v142, v138\n\t"
    "v_pk_mul_f32 v[250:251], v[250:251], v[194:195]\n\t"
    "v_exp_f32 v252, v252\n\t"
    "v_accvgpr_write_b32 a105, v14\n\t"
    "v_mov_b32 v143, v139\n\t"
    "v_cvt_pk_bf16_f32 v176, v240, v241\n\t"
    "v_exp_f32 v253, v253\n\t"
    "v_accvgpr_write_b32 a106, v14\n\t"
    "v_permlane16_swap_b32 v136, v140\n\t"
    "v_cvt_pk_bf16_f32 v177, v242, v243\n\t"
    "v_exp_f32 v254, v254\n\t"
    "v_accvgpr_write_b32 a107, v14\n\t"
    "v_permlane16_swap_b32 v137, v141\n\t"
    "v_cvt_pk_bf16_f32 v178, v248, v249\n\t"
    "v_exp_f32 v255, v255\n\t"
    "v_cmp_eq_u32 vcc, 3, v12\n\t"
    "s_nop 1\n\t"
    "v_cndmask_b32 v14, 0, v13, vcc\n\t"
    "v_permlane16_swap_b32 v138, v142\n\t"
    "v_cvt_pk_bf16_f32 v179, v250, v251\n\t"
    "v_pk_add_f32 v[244:245], v[244:245], 1.0 op_sel_hi:[1,0]\n\t"
    "v_accvgpr_write_b32 a108, v14\n\t"
    "v_permlane16_swap_b32 v139, v143\n\t"
    "buffer_store_dwordx4 v[176:179], %[vL16], %[srdWS], %[sHb] offen offset:0 sc1\n\t"
    "v_pk_add_f32 v[246:247], v[246:247], 1.0 op_sel_hi:[1,0]\n\t"
    "v_accvgpr_write_b32 a109, v14\n\t"
    "v_max_f32 v136, v136, v140\n\t"
    "v_pk_mul_f32 v[240:241], v[184:185], s[62:63]\n\t"
    "v_pk_add_f32 v[252:253], v[252:253], 1.0 op_sel_hi:[1,0]\n\t"
    "v_accvgpr_write_b32 a110, v14\n\t"
    "v_max_f32 v137, v137, v141\n\t"
    "v_pk_mul_f32 v[242:243], v[186:187], s[62:63]\n\t"
    "v_pk_add_f32 v[254:255], v[254:255], 1.0 op_sel_hi:[1,0]\n\t"
    "v_accvgpr_write_b32 a111, v14\n\t"
    "v_max_f32 v138, v138, v142\n\t"
    "v_pk_mul_f32 v[248:249], v[200:201], s[62:63]\n\t"
    "v_rcp_f32 v244, v244\n\t"
    "v_max_f32 v139, v139, v143\n\t"
    "v_pk_mul_f32 v[250:251], v[202:203], s[62:63]\n\t"
    "v_rcp_f32 v245, v245\n\t"
    "v_mov_b32 v140, v136\n\t"
    "v_exp_f32 v240, v240\n\t"
    "v_rcp_f32 v246, v246\n\t"
    "v_mov_b32 v141, v137\n\t"
    "v_exp_f32 v241, v241\n\t"
    "v_rcp_f32 v247, v247\n\t"
    "v_mov_b32 v142, v138\n\t"
    "v_exp_f32 v242, v242\n\t"
    "v_rcp_f32 v252, v252\n\t"
    "v_mov_b32 v143, v139\n\t"
    "v_exp_f32 v243, v243\n\t"
    "v_rcp_f32 v253, v253\n\t"
    "v_permlane32_swap_b32 v136, v140\n\t"
    "v_exp_f32 v248, v248\n\t"
    "v_rcp_f32 v254, v254\n\t"
    "v_permlane32_swap_b32 v137, v141\n\t"
    "v_exp_f32 v249, v249\n\t"
    "v_rcp_f32 v255, v255\n\t"
    "v_permlane32_swap_b32 v138, v142\n\t"
    "v_exp_f32 v250, v250\n\t"
    "v_pk_mul_f32 v[244:245], v[244:245], v[156:157]\n\t"
    "v_permlane32_swap_b32 v139, v143\n\t"
    "v_exp_f32 v251, v251\n\t"
    "v_pk_mul_f32 v[246:247], v[246:247], v[158:159]\n\t"
    "v_max_f32 v136, v136, v140\n\t"
    "v_pk_add_f32 v[240:241], v[240:241], 1.0 op_sel_hi:[1,0]\n\t"
    "v_pk_mul_f32 v[252:253], v[252:253], v[172:173]\n\t"
    "v_max_f32 v137, v137, v141\n\t"
    "v_pk_add_f32 v[242:243], v[242:243], 1.0 op_sel_hi:[1,0]\n\t"
    "v_pk_mul_f32 v[254:255], v[254:255], v[174:175]\n\t"
    "v_max_f32 v138, v138, v142\n\t"
    "v_pk_add_f32 v[248:249], v[248:249], 1.0 op_sel_hi:[1,0]\n\t"
    "v_cvt_pk_bf16_f32 v156, v244, v245\n\t"
    "v_max_f32 v139, v139, v143\n\t"
    "v_pk_add_f32 v[250:251], v[250:251], 1.0 op_sel_hi:[1,0]\n\t"
    "v_cvt_pk_bf16_f32 v157, v246, v247\n\t"
    "v_rcp_f32 v240, v240\n\t"
    "v_cvt_pk_bf16_f32 v158, v252, v253\n\t"
    "v_rcp_f32 v241, v241\n\t"
    "v_cvt_pk_bf16_f32 v159, v254, v255\n\t"
    "v_rcp_f32 v242, v242\n\t"
    "buffer_store_dwordx4 v[156:159], %[vL16], %[srdWS], %[sHa] offen offset:3072 sc1\n\t"
    "v_rcp_f32 v243, v243\n\t"
    "v_pk_mul_f32 v[244:245], v[180:181], s[62:63]\n\t"
    "v_rcp_f32 v248, v248\n\t"
    "v_pk_mul_f32 v[246:247], v[182:183], s[62:63]\n\t"
    "v_rcp_f32 v249, v249\n\t"
    "v_pk_mul_f32 v[252:253], v[196:197], s[62:63]\n\t"
    "v_rcp_f32 v250, v250\n\t"
    "v_pk_mul_f32 v[254:255], v[198:199], s[62:63]\n\t"
    "v_rcp_f32 v251, v251\n\t"
    "v_exp_f32 v244, v244\n\t"
    "v_pk_mul_f32 v[240:241], v[240:241], v[184:185]\n\t"
    "v_exp_f32 v245, v245\n\t"
    "v_pk_mul_f32 v[242:243], v[242:243], v[186:187]\n\t"
    "v_exp_f32 v246, v246\n\t"
    "v_pk_mul_f32 v[248:249], v[248:249], v[200:201]\n\t"
    "v_exp_f32 v247, v247\n\t"
    "v_pk_mul_f32 v[250:251], v[250:251], v[202:203]\n\t"
    "v_exp_f32 v252, v252\n\t"
    "v_cvt_pk_bf16_f32 v184, v240, v241\n\t"
    "v_exp_f32 v253, v253\n\t"
    "v_cvt_pk_bf16_f32 v185, v242, v243\n\t"
    "v_exp_f32 v254, v254\n\t"
    "v_cvt_pk_bf16_f32 v186, v248, v249\n\t"
    "v_exp_f32 v255, v255\n\t"
    "v_cvt_pk_bf16_f32 v187, v250, v251\n\t"
    "v_pk_add_f32 v[244:245], v[244:245], 1.0 op_sel_hi:[1,0]\n\t"
    "buffer_store_dwordx4 v[184:187], %[vL16], %[srdWS], %[sHb] offen offset:2048 sc1\n\t"
    "v_pk_add_f32 v[246:247], v[246:247], 1.0 op_sel_hi:[1,0]\n\t"
    "v_pk_add_f32 v[252:253], v[252:253], 1.0 op_sel_hi:[1,0]\n\t"
    "v_pk_add_f32 v[254:255], v[254:255], 1.0 op_sel_hi:[1,0]\n\t"
    "v_rcp_f32 v244, v244\n\t"
    "v_rcp_f32 v245, v245\n\t"
    "v_rcp_f32 v246, v246\n\t"
    "v_rcp_f32 v247, v247\n\t"
    "v_rcp_f32 v252, v252\n\t"
    "v_rcp_f32 v253, v253\n\t"
    "v_rcp_f32 v254, v254\n\t"
    "v_rcp_f32 v255, v255\n\t"
    "v_pk_mul_f32 v[244:245], v[244:245], v[180:181]\n\t"
    "v_pk_mul_f32 v[246:247], v[246:247], v[182:183]\n\t"
    "v_pk_mul_f32 v[252:253], v[252:253], v[196:197]\n\t"
    "v_pk_mul_f32 v[254:255], v[254:255], v[198:199]\n\t"
    "v_cvt_pk_bf16_f32 v180, v244, v245\n\t"
    "v_cvt_pk_bf16_f32 v181, v246, v247\n\t"
    "v_cvt_pk_bf16_f32 v182, v252, v253\n\t"
    "v_cvt_pk_bf16_f32 v183, v254, v255\n\t"
    "buffer_store_dwordx4 v[180:183], %[vL16], %[srdWS], %[sHb] offen offset:1024 sc1\n\t"
    "v_pk_mul_f32 v[244:245], v[188:189], s[62:63]\n\t"
    "v_pk_mul_f32 v[246:247], v[190:191], s[62:63]\n\t"
    "v_pk_mul_f32 v[252:253], v[204:205], s[62:63]\n\t"
    "v_pk_mul_f32 v[254:255], v[206:207], s[62:63]\n\t"
    "v_exp_f32 v244, v244\n\t"
    "v_exp_f32 v245, v245\n\t"
    "v_exp_f32 v246, v246\n\t"
    "v_exp_f32 v247, v247\n\t"
    "v_exp_f32 v252, v252\n\t"
    "v_exp_f32 v253, v253\n\t"
    "v_exp_f32 v254, v254\n\t"
    "v_exp_f32 v255, v255\n\t"
    "v_pk_add_f32 v[244:245], v[244:245], 1.0 op_sel_hi:[1,0]\n\t"
    "v_pk_add_f32 v[246:247], v[246:247], 1.0 op_sel_hi:[1,0]\n\t"
    "v_pk_add_f32 v[252:253], v[252:253], 1.0 op_sel_hi:[1,0]\n\t"
    "v_pk_add_f32 v[254:255], v[254:255], 1.0 op_sel_hi:[1,0]\n\t"
    "v_rcp_f32 v244, v244\n\t"
    "v_rcp_f32 v245, v245\n\t"
    "v_rcp_f32 v246, v246\n\t"
    "v_rcp_f32 v247, v247\n\t"
    "v_rcp_f32 v252, v252\n\t"
    "v_rcp_f32 v253, v253\n\t"
    "v_rcp_f32 v254, v254\n\t"
    "v_rcp_f32 v255, v255\n\t"
    "v_pk_mul_f32 v[244:245], v[244:245], v[188:189]\n\t"
    "v_pk_mul_f32 v[246:247], v[246:247], v[190:191]\n\t"
    "v_pk_mul_f32 v[252:253], v[252:253], v[204:205]\n\t"
    "v_pk_mul_f32 v[254:255], v[254:255], v[206:207]\n\t"
    "v_cvt_pk_bf16_f32 v188, v244, v245\n\t"
    "v_cvt_pk_bf16_f32 v189, v246, v247\n\t"
    "v_cvt_pk_bf16_f32 v190, v252, v253\n\t"
    "v_cvt_pk_bf16_f32 v191, v254, v255\n\t"
    "buffer_store_dwordx4 v[188:191], %[vL16], %[srdWS], %[sHb] offen offset:3072 sc1\n\t"
    "v_sub_f32 v208, 0, v136\n\t"
    "v_sub_f32 v209, 0, v136\n\t"
    "v_sub_f32 v210, 0, v136\n\t"
    "v_sub_f32 v211, 0, v136\n\t"
    "v_sub_f32 v212, 0, v137\n\t"
    "v_sub_f32 v213, 0, v137\n\t"
    "v_sub_f32 v214, 0, v137\n\t"
    "v_sub_f32 v215, 0, v137\n\t"
    "v_sub_f32 v216, 0, v138\n\t"
    "v_sub_f32 v217, 0, v138\n\t"
    "v_sub_f32 v218, 0, v138\n\t"
    "v_sub_f32 v219, 0, v138\n\t"
    "v_sub_f32 v220, 0, v139\n\t"
    "v_sub_f32 v221, 0, v139\n\t"
    "v_sub_f32 v222, 0, v139\n\t"
    "v_sub_f32 v223, 0, v139\n\t"
    "v_mov_b32 v228, 0\n\t"
    "v_mov_b32 v229, 0\n\t"
    "v_mov_b32 v230, 0\n\t"
    "v_mov_b32 v231, 0\n\t"
    "s_waitcnt lgkmcnt(4)\n\t"
    "v_mfma_f32_16x16x32_bf16 v[80:83], a[0:3], a[32:35], v[208:211]\n\t"
    "ds_read_b128 a[24:27], v10 offset:38976\n\t"
    "v_add_f32 v16, v208, v16\n\t"
    "v_mfma_f32_16x16x32_bf16 v[80:83], a[4:7], a[48:51], v[80:83]\n\t"
    "v_add_f32 v17, v208, v17\n\t"
    "v_exp_f32 v232, v16\n\t"
    "v_mfma_f32_16x16x32_bf16 v[84:87], a[4:7], a[52:55], v[212:215]\n\t"
    "buffer_load_dwordx4 %[vL16], %[srdWS], s66 offen offset:0 lds\n\t"
    "v_exp_f32 v233, v17\n\t"
    "v_mfma_f32_16x16x32_bf16 v[84:87], a[0:3], a[36:39], v[84:87]\n\t"
    "v_cvt_pk_bf16_f32 v144, v232, v233\n\t"
    "v_add_f32 v18, v208, v18\n\t"
    "v_mfma_f32_16x16x32_bf16 v[88:91], a[0:3], a[40:43], v[216:219]\n\t"
    "ds_read_b128 a[28:31], v10 offset:40000\n\t"
    "v_add_f32 v19, v208, v19\n\t"
    "v_mfma_f32_16x16x32_bf16 v[88:91], a[4:7], a[56:59], v[88:91]\n\t"
    "v_exp_f32 v234, v18\n\t"
    "v_mfma_f32_16x16x32_bf16 v[92:95], a[4:7], a[60:63], v[220:223]\n\t"
    "v_exp_f32 v235, v19\n\t"
    "v_mfma_f32_16x16x32_bf16 v[92:95], a[0:3], a[44:47], v[92:95]\n\t"
    "v_cvt_pk_bf16_f32 v145, v234, v235\n\t"
    "s_waitcnt lgkmcnt(4)\n\t"
    "v_mfma_f32_16x16x32_bf16 v[80:83], a[8:11], a[64:67], v[80:83]\n\t"
    "ds_read_b128 a[0:3], v10 offset:41024\n\t"
    "buffer_load_dwordx4 %[vL16], %[srdWS], s66 offen offset:1024 lds\n\t"
    "v_mfma_f32_16x16x32_bf16 v[80:83], a[12:15], a[80:83], v[80:83]\n\t"
    "v_add_f32 v32, v208, v32\n\t"
    "v_add_f32 v33, v208, v33\n\t"
    "v_mfma_f32_16x16x32_bf16 v[84:87], a[12:15], a[84:87], v[84:87]\n\t"
    "v_exp_f32 v236, v32\n\t"
    "v_mfma_f32_16x16x32_bf16 v[84:87], a[8:11], a[68:71], v[84:87]\n\t"
    "v_exp_f32 v237, v33\n\t"
    "v_mfma_f32_16x16x32_bf16 v[88:91], a[8:11], a[72:75], v[88:91]\n\t"
    "ds_read_b128 a[4:7], v10 offset:42048\n\t"
    "v_cvt_pk_bf16_f32 v146, v236, v237\n\t"
    "v_mfma_f32_16x16x32_bf16 v[88:91], a[12:15], a[88:91], v[88:91]\n\t"
    "v_add_f32 v34, v208, v34\n\t"
    "v_add_f32 v35, v208, v35\n\t"
    "v_mfma_f32_16x16x32_bf16 v[92:95], a[12:15], a[92:95], v[92:95]\n\t"
    "buffer_load_dwordx4 %[vL16], %[srdWS], s66 offen offset:2048 lds\n\t"
    "v_exp_f32 v238, v34\n\t"
    "v_mfma_f32_16x16x32_bf16 v[92:95], a[8:11], a[76:79], v[92:95]\n\t"
    "v_exp_f32 v239, v35\n\t"
    "s_waitcnt lgkmcnt(4)\n\t"
    "v_mfma_f32_16x16x32_bf16 v[96:99], a[16:19], a[32:35], v[208:211]\n\t"
    "ds_read_b128 a[8:11], v10 offset:43072\n\t"
    "v_cvt_pk_bf16_f32 v147, v238, v239\n\t"
    "v_mfma_f32_16x16x32_bf16 v[96:99], a[20:23], a[48:51], v[96:99]\n\t"
    "v_add_f32 v20, v212, v20\n\t"
    "v_add_f32 v21, v212, v21\n\t"
    "v_mfma_f32_16x16x32_bf16 v[100:103], a[20:23], a[52:55], v[212:215]\n\t"
    "v_exp_f32 v240, v20\n\t"
    "v_mfma_f32_16x16x32_bf16 v[100:103], a[16:19], a[36:39], v[100:103]\n\t"
    "v_exp_f32 v241, v21\n\t"
    "v_mfma_f32_16x16x32_bf16 v[104:107], a[16:19], a[40:43], v[216:219]\n\t"
    "ds_read_b128 a[12:15], v10 offset:44096\n\t"
    "buffer_load_dwordx4 %[vL16], %[srdWS], s66 offen offset:3072 lds\n\t"
    "v_mfma_f32_16x16x32_bf16 v[104:107], a[20:23], a[56:59], v[104:107]\n\t"
    "v_cvt_pk_bf16_f32 v148, v240, v241\n\t"
    "v_add_f32 v22, v212, v22\n\t"
    "v_mfma_f32_16x16x32_bf16 v[108:111], a[20:23], a[60:63], v[220:223]\n\t"
    "v_add_f32 v23, v212, v23\n\t"
    "v_exp_f32 v242, v22\n\t"
    "v_mfma_f32_16x16x32_bf16 v[108:111], a[16:19], a[44:47], v[108:111]\n\t"
    "v_exp_f32 v243, v23\n\t"
    "s_waitcnt lgkmcnt(4)\n\t"
    "v_mfma_f32_16x16x32_bf16 v[96:99], a[24:27], a[64:67], v[96:99]\n\t"
    "ds_read_b128 a[16:19], v10 offset:45120\n\t"
    "v_cvt_pk_bf16_f32 v149, v242, v243\n\t"
    "v_mfma_f32_16x16x32_bf16 v[96:99], a[28:31], a[80:83], v[96:99]\n\t"
    "v_add_f32 v36, v212, v36\n\t"
    "v_add_f32 v37, v212, v37\n\t"
    "v_mfma_f32_16x16x32_bf16 v[100:103], a[28:31], a[84:87], v[100:103]\n\t"
    "v_exp_f32 v232, v36\n\t"
    "v_mfma_f32_16x16x32_bf16 v[100:103], a[24:27], a[68:71], v[100:103]\n\t"
    "v_exp_f32 v233, v37\n\t"
    "v_mfma_f32_16x16x32_bf16 v[104:107], a[24:27], a[72:75], v[104:107]\n\t"
    "ds_read_b128 a[20:23], v10 offset:46144\n\t"
    "v_cvt_pk_bf16_f32 v150, v232, v233\n\t"
    "v_mfma_f32_16x16x32_bf16 v[104:107], a[28:31], a[88:91], v[104:107]\n\t"
    "v_add_f32 v38, v212, v38\n\t"
    "v_add_f32 v39, v212, v39\n\t"
    "v_mfma_f32_16x16x32_bf16 v[108:111], a[28:31], a[92:95], v[108:111]\n\t"
    "v_exp_f32 v234, v38\n\t"
    "v_mfma_f32_16x16x32_bf16 v[108:111], a[24:27], a[76:79], v[108:111]\n\t"
    "v_exp_f32 v235, v39\n\t"
    "s_waitcnt lgkmcnt(4)\n\t"
    "v_mfma_f32_16x16x32_bf16 v[112:115], a[0:3], a[32:35], v[208:211]\n\t"
    "ds_read_b128 a[24:27], v10 offset:47168\n\t"
    "v_cvt_pk_bf16_f32 v151, v234, v235\n\t"
    "v_mfma_f32_16x16x32_bf16 v[112:115], a[4:7], a[48:51], v[112:115]\n\t"
    "v_add_f32 v24, v216, v24\n\t"
    "v_add_f32 v25, v216, v25\n\t"
    "v_mfma_f32_16x16x32_bf16 v[116:119], a[4:7], a[52:55], v[212:215]\n\t"
    "v_exp_f32 v236, v24\n\t"
    "v_mfma_f32_16x16x32_bf16 v[116:119], a[0:3], a[36:39], v[116:119]\n\t"
    "v_exp_f32 v237, v25\n\t"
    "v_mfma_f32_16x16x32_bf16 v[120:123], a[0:3], a[40:43], v[216:219]\n\t"
    "ds_read_b128 a[28:31], v10 offset:48192\n\t"
    "v_cvt_pk_bf16_f32 v152, v236, v237\n\t"
    "v_mfma_f32_16x16x32_bf16 v[120:123], a[4:7], a[56:59], v[120:123]\n\t"
    "v_add_f32 v26, v216, v26\n\t"
    "v_add_f32 v27, v216, v27\n\t"
    "v_mfma_f32_16x16x32_bf16 v[124:127], a[4:7], a[60:63], v[220:223]\n\t"
    "v_exp_f32 v238, v26\n\t"
    "v_mfma_f32_16x16x32_bf16 v[124:127], a[0:3], a[44:47], v[124:127]\n\t"
    "v_exp_f32 v239, v27\n\t"
    "s_waitcnt vmcnt(8) lgkmcnt(4)\n\t"
    "s_barrier\n\t"
    "v_mfma_f32_16x16x32_bf16 v[112:115], a[8:11], a[64:67], v[112:115]\n\t"
    "ds_read_b128 a[0:3], v11 offset:16384\n\t"
    "v_cvt_pk_bf16_f32 v153, v238, v239\n\t"
    "v_mfma_f32_16x16x32_bf16 v[112:115], a[12:15], a[80:83], v[112:115]\n\t"
    "s_add_u32 m0, %[sLds4], 0\n\t"
    "s_add_u32 s65, %[sKV], 131072\n\t"
    "v_mfma_f32_16x16x32_bf16 v[116:119], a[12:15], a[84:87], v[116:119]\n\t"
    "buffer_load_dwordx4 %[vL16], %[srdWS], s65 offen offset:0 lds\n\t"
    "v_add_f32 v40, v216, v40\n\t"
    "v_mfma_f32_16x16x32_bf16 v[116:119], a[8:11], a[68:71], v[116:119]\n\t"
    "v_add_f32 v41, v216, v41\n\t"
    "v_exp_f32 v240, v40\n\t"
    "v_mfma_f32_16x16x32_bf16 v[120:123], a[8:11], a[72:75], v[120:123]\n\t"
    "ds_read_b128 a[4:7], v11 offset:17408\n\t"
    "v_exp_f32 v241, v41\n\t"
    "v_mfma_f32_16x16x32_bf16 v[120:123], a[12:15], a[88:91], v[120:123]\n\t"
    "v_cvt_pk_bf16_f32 v154, v240, v241\n\t"
    "v_add_f32 v42, v216, v42\n\t"
    "v_mfma_f32_16x16x32_bf16 v[124:127], a[12:15], a[92:95], v[124:127]\n\t"
    "v_add_f32 v43, v216, v43\n\t"
    "v_exp_f32 v242, v42\n\t"
    "v_mfma_f32_16x16x32_bf16 v[124:127], a[8:11], a[76:79], v[124:127]\n\t"
    "v_exp_f32 v243, v43\n\t"
    "s_waitcnt lgkmcnt(4)\n\t"
    "v_mfma_f32_16x16x32_bf16 v[128:131], a[16:19], a[32:35], v[208:211]\n\t"
    "ds_read_b128 a[8:11], v11 offset:18432\n\t"
    "buffer_load_dwordx4 %[vL16], %[srdWS], s65 offen offset:1024 lds\n\t"
    "v_mfma_f32_16x16x32_bf16 v[128:131], a[20:23], a[48:51], v[128:131]\n\t"
    "v_cvt_pk_bf16_f32 v155, v242, v243\n\t"
    "v_add_f32 v28, v220, v28\n\t"
    "v_mfma_f32_16x16x32_bf16 v[132:135], a[20:23], a[52:55], v[212:215]\n\t"
    "v_add_f32 v29, v220, v29\n\t"
    "v_exp_f32 v232, v28\n\t"
    "v_mfma_f32_16x16x32_bf16 v[132:135], a[16:19], a[36:39], v[132:135]\n\t"
    "v_exp_f32 v233, v29\n\t"
    "v_mfma_f32_16x16x32_bf16 v[136:139], a[16:19], a[40:43], v[216:219]\n\t"
    "ds_read_b128 a[12:15], v11 offset:19456\n\t"
    "v_cvt_pk_bf16_f32 v156, v232, v233\n\t"
    "v_mfma_f32_16x16x32_bf16 v[136:139], a[20:23], a[56:59], v[136:139]\n\t"
    "v_add_f32 v30, v220, v30\n\t"
    "v_add_f32 v31, v220, v31\n\t"
    "v_mfma_f32_16x16x32_bf16 v[140:143], a[20:23], a[60:63], v[220:223]\n\t"
    "buffer_load_dwordx4 %[vL16], %[srdWS], s65 offen offset:2048 lds\n\t"
    "v_exp_f32 v234, v30\n\t"
    "v_mfma_f32_16x16x32_bf16 v[140:143], a[16:19], a[44:47], v[140:143]\n\t"
    "v_exp_f32 v235, v31\n\t"
    "s_nop 0\n\t"
    "v_cvt_pk_bf16_f32 v157, v234, v235\n\t"
    "v_pk_add_f32 v[44:45], v[44:45], v[220:221] op_sel_hi:[1,0]\n\t"
    "v_exp_f32 v236, v44\n\t"
    "v_exp_f32 v237, v45\n\t"
    "s_nop 0\n\t"
    "v_cvt_pk_bf16_f32 v158, v236, v237\n\t"
    "v_pk_add_f32 v[46:47], v[46:47], v[220:221] op_sel_hi:[1,0]\n\t"
    "v_exp_f32 v238, v46\n\t"
    "v_exp_f32 v239, v47\n\t"
    "s_nop 0\n\t"
    "v_cvt_pk_bf16_f32 v159, v238, v239\n\t"
    "v_pk_add_f32 v[48:49], v[48:49], v[208:209] op_sel_hi:[1,0]\n\t"
    "v_exp_f32 v240, v48\n\t"
    "v_exp_f32 v241, v49\n\t"
    "s_nop 0\n\t"
    "v_cvt_pk_bf16_f32 v160, v240, v241\n\t"
    "v_pk_add_f32 v[50:51], v[50:51], v[208:209] op_sel_hi:[1,0]\n\t"
    "v_exp_f32 v242, v50\n\t"
    "v_exp_f32 v243, v51\n\t"
    "s_nop 0\n\t"
    "v_cvt_pk_bf16_f32 v161, v242, v243\n\t"
    "v_pk_add_f32 v[64:65], v[64:65], v[208:209] op_sel_hi:[1,0]\n\t"
    "v_exp_f32 v232, v64\n\t"
    "v_exp_f32 v233, v65\n\t"
    "s_nop 0\n\t"
    "v_cvt_pk_bf16_f32 v162, v232, v233\n\t"
    "v_pk_add_f32 v[66:67], v[66:67], v[208:209] op_sel_hi:[1,0]\n\t"
    "v_exp_f32 v234, v66\n\t"
    "v_exp_f32 v235, v67\n\t"
    "s_nop 0\n\t"
    "v_cvt_pk_bf16_f32 v163, v234, v235\n\t"
    "v_pk_add_f32 v[52:53], v[52:53], v[212:213] op_sel_hi:[1,0]\n\t"
    "v_exp_f32 v236, v52\n\t"
    "v_exp_f32 v237, v53\n\t"
    "s_nop 0\n\t"
    "v_cvt_pk_bf16_f32 v164, v236, v237\n\t"
    "v_pk_add_f32 v[54:55], v[54:55], v[212:213] op_sel_hi:[1,0]\n\t"
    "v_exp_f32 v238, v54\n\t"
    "v_exp_f32 v239, v55\n\t"
    "s_nop 0\n\t"
    "v_cvt_pk_bf16_f32 v165, v238, v239\n\t"
    "v_pk_add_f32 v[68:69], v[68:69], v[212:213] op_sel_hi:[1,0]\n\t"
    "v_exp_f32 v240, v68\n\t"
    "v_exp_f32 v241, v69\n\t"
    "s_nop 0\n\t"
    "v_cvt_pk_bf16_f32 v166, v240, v241\n\t"
    "v_pk_add_f32 v[70:71], v[70:71], v[212:213] op_sel_hi:[1,0]\n\t"
    "v_exp_f32 v242, v70\n\t"
    "v_exp_f32 v243, v71\n\t"
    "s_nop 0\n\t"
    "v_cvt_pk_bf16_f32 v167, v242, v243\n\t"
    "v_pk_add_f32 v[56:57], v[56:57], v[216:217] op_sel_hi:[1,0]\n\t"
    "v_exp_f32 v232, v56\n\t"
    "v_exp_f32 v233, v57\n\t"
    "s_nop 0\n\t"
    "v_cvt_pk_bf16_f32 v168, v232, v233\n\t"
    "v_pk_add_f32 v[58:59], v[58:59], v[216:217] op_sel_hi:[1,0]\n\t"
    "v_exp_f32 v234, v58\n\t"
    "v_exp_f32 v235, v59\n\t"
    "s_nop 0\n\t"
    "v_cvt_pk_bf16_f32 v169, v234, v235\n\t"
    "v_pk_add_f32 v[72:73], v[72:73], v[216:217] op_sel_hi:[1,0]\n\t"
    "v_exp_f32 v236, v72\n\t"
    "v_exp_f32 v237, v73\n\t"
    "s_nop 0\n\t"
    "v_cvt_pk_bf16_f32 v170, v236, v237\n\t"
    "v_pk_add_f32 v[74:75], v[74:75], v[216:217] op_sel_hi:[1,0]\n\t"
    "v_exp_f32 v238, v74\n\t"
    "v_exp_f32 v239, v75\n\t"
    "s_nop 0\n\t"
    "v_cvt_pk_bf16_f32 v171, v238, v239\n\t"
    "v_pk_add_f32 v[60:61], v[60:61], v[220:221] op_sel_hi:[1,0]\n\t"
    "v_exp_f32 v240, v60\n\t"
    "v_exp_f32 v241, v61\n\t"
    "s_nop 0\n\t"
    "v_cvt_pk_bf16_f32 v172, v240, v241\n\t"
    "v_pk_add_f32 v[62:63], v[62:63], v[220:221] op_sel_hi:[1,0]\n\t"
    "v_exp_f32 v242, v62\n\t"
    "v_exp_f32 v243, v63\n\t"
    "s_nop 0\n\t"
    "v_cvt_pk_bf16_f32 v173, v242, v243\n\t"
    "v_pk_add_f32 v[76:77], v[76:77], v[220:221] op_sel_hi:[1,0]\n\t"
    "v_exp_f32 v232, v76\n\t"
    "v_exp_f32 v233, v77\n\t"
    "s_nop 0\n\t"
    "v_cvt_pk_bf16_f32 v174, v232, v233\n\t"
    "v_pk_add_f32 v[78:79], v[78:79], v[220:221] op_sel_hi:[1,0]\n\t"
    "v_exp_f32 v234, v78\n\t"
    "v_exp_f32 v235, v79\n\t"
    "s_nop 0\n\t"
    "v_cvt_pk_bf16_f32 v175, v234, v235\n\t"
    "v_mfma_f32_16x16x32_bf16 v[224:227], a[96:99], v[144:147], v[228:231]\n\t"
    "v_mfma_f32_16x16x32_bf16 v[224:227], a[100:103], v[148:151], v[224:227]\n\t"
    "v_mfma_f32_16x16x32_bf16 v[224:227], a[104:107], v[152:155], v[224:227]\n\t"
    "v_mfma_f32_16x16x32_bf16 v[224:227], a[108:111], v[156:159], v[224:227]\n\t"
    "v_mfma_f32_16x16x32_bf16 v[224:227], a[96:99], v[160:163], v[224:227]\n\t"
    "buffer_load_dwordx4 %[vL16], %[srdWS], s65 offen offset:3072 lds\n\t"
    "s_add_u32 m0, %[sLds4], 16384\n\t"
    "s_add_u32 s66, %[sKV], 81920\n\t"
    "v_mfma_f32_16x16x32_bf16 v[224:227], a[100:103], v[164:167], v[224:227]\n\t"
    "v_mfma_f32_16x16x32_bf16 v[224:227], a[104:107], v[168:171], v[224:227]\n\t"
    "v_mfma_f32_16x16x32_bf16 v[224:227], a[108:111], v[172:175], v[224:227]\n\t"
    "s_waitcnt lgkmcnt(4)\n\t"
    "v_mfma_f32_16x16x32_bf16 v[128:131], a[24:27], a[64:67], v[128:131]\n\t"
    "ds_read_b128 a[16:19], v11 offset:20480\n\t"
    "v_mfma_f32_16x16x32_bf16 v[128:131], a[28:31], a[80:83], v[128:131]\n\t"
    "v_mfma_f32_16x16x32_bf16 v[132:135], a[28:31], a[84:87], v[132:135]\n\t"
    "buffer_load_dwordx4 %[vL16], %[srdWS], s66 offen offset:0 lds\n\t"
    "v_mfma_f32_16x16x32_bf16 v[132:135], a[24:27], a[68:71], v[132:135]\n\t"
    "v_mfma_f32_16x16x32_bf16 v[136:139], a[24:27], a[72:75], v[136:139]\n\t"
    "ds_read_b128 a[20:23], v11 offset:21504\n\t"
    "v_max3_f32 v232, v224, v225, v226\n\t"
    "v_mfma_f32_16x16x32_bf16 v[136:139], a[28:31], a[88:91], v[136:139]\n\t"
    "v_max_f32 v232, v232, v227\n\t"
    "v_mfma_f32_16x16x32_bf16 v[140:143], a[28:31], a[92:95], v[140:143]\n\t"
    "v_cmp_nge_f32 vcc, s64, v232\n\t"
    "s_getpc_b64 s[60:61]\n\t"
    "v_mfma_f32_16x16x32_bf16 v[140:143], a[24:27], a[76:79], v[140:143]\n\t"
    "s_cbranch_vccnz L_k1_fix0_%=\n\t"
    "s_waitcnt lgkmcnt(4)\n\t"
    "v_mfma_f32_16x16x32_bf16 a[128:131], a[0:3], v[144:147], 0\n\t"
    "ds_read_b128 a[24:27], v11 offset:22528\n\t"
    "buffer_load_dwordx4 %[vL16], %[srdWS], s66 offen offset:1024 lds\n\t"
    "v_mfma_f32_16x16x32_bf16 a[128:131], a[4:7], v[160:163], a[128:131]\n\t"
    "v_exp_f32 v232, v80\n\t"
    "v_mfma_f32_16x16x32_bf16 a[132:135], a[4:7], v[164:167], 0\n\t"
    "v_exp_f32 v233, v81\n\t"
    "v_mfma_f32_16x16x32_bf16 a[132:135], a[0:3], v[148:151], a[132:135]\n\t"
    "v_exp_f32 v234, v82\n\t"
    "v_mfma_f32_16x16x32_bf16 a[136:139], a[0:3], v[152:155], 0\n\t"
    "ds_read_b128 a[28:31], v11 offset:23552\n\t"
    "v_cvt_pk_bf16_f32 v176, v232, v233\n\t"
    "v_mfma_f32_16x16x32_bf16 a[136:139], a[4:7], v[168:171], a[136:139]\n\t"
    "v_exp_f32 v235, v83\n\t"
    "v_mfma_f32_16x16x32_bf16 a[140:143], a[4:7], v[172:175], 0\n\t"
    "buffer_load_dwordx4 %[vL16], %[srdWS], s66 offen offset:2048 lds\n\t"
    "v_cvt_pk_bf16_f32 v177, v234, v235\n\t"
    "v_mfma_f32_16x16x32_bf16 a[140:143], a[0:3], v[156:159], a[140:143]\n\t"
    "ds_read_b128 v[248:251], %[vX] offset:0\n\t"
    "s_waitcnt lgkmcnt(5)\n\t"
    "v_mfma_f32_16x16x32_bf16 a[144:147], a[8:11], v[144:147], 0\n\t"
    "ds_read_b128 a[0:3], v11 offset:24576\n\t"
    "ds_read_b128 v[252:255], %[vX] offset:1024\n\t"
    "v_mfma_f32_16x16x32_bf16 a[144:147], a[12:15], v[160:163], a[144:147]\n\t"
    "v_exp_f32 v236, v96\n\t"
    "v_mfma_f32_16x16x32_bf16 a[148:151], a[12:15], v[164:167], 0\n\t"
    "v_exp_f32 v237, v97\n\t"
    "v_mfma_f32_16x16x32_bf16 a[148:151], a[8:11], v[148:151], a[148:151]\n\t"
    "v_exp_f32 v238, v98\n\t"
    "v_mfma_f32_16x16x32_bf16 a[152:155], a[8:11], v[152:155], 0\n\t"
    "ds_read_b128 a[4:7], v11 offset:25600\n\t"
    "buffer_load_dwordx4 %[vL16], %[srdWS], s66 offen offset:3072 lds\n\t"
    "v_mfma_f32_16x16x32_bf16 a[152:155], a[12:15], v[168:171], a[152:155]\n\t"
    "k_pga6: s_cbranch_execz k_pga10\n\t"
    "v_exp_f32 v239, v99\n\t"
    "v_mfma_f32_16x16x32_bf16 a[156:159], a[12:15], v[172:175], 0\n\t"
    "v_exp_f32 v240, v84\n\t"
    "v_mfma_f32_16x16x32_bf16 a[156:159], a[8:11], v[156:159], a[156:159]\n\t"
    "v_lshlrev_b32 v8, 2, %[vL16]\n\t"
    "s_waitcnt lgkmcnt(6)\n\t"
    "v_mfma_f32_16x16x32_bf16 a[160:163], a[16:19], v[144:147], 0\n\t"
    "ds_read_b128 a[8:11], v11 offset:26624\n\t"
    "v_cvt_pk_bf16_f32 v178, v236, v237\n\t"
    "v_mfma_f32_16x16x32_bf16 a[160:163], a[20:23], v[160:163], a[160:163]\n\t"
    "v_exp_f32 v241, v85\n\t"
    "v_mfma_f32_16x16x32_bf16 a[164:167], a[20:23], v[164:167], 0\n\t"
    "buffer_load_dword v9, v8, %[srdP2], %[sP2] offen\n\t"
    "v_cvt_pk_bf16_f32 v179, v238, v239\n\t"
    "v_mfma_f32_16x16x32_bf16 a[164:167], a[16:19], v[148:151], a[164:167]\n\t"
    "v_exp_f32 v242, v86\n\t"
    "v_mfma_f32_16x16x32_bf16 a[168:171], a[16:19], v[152:155], 0\n\t"
    "ds_read_b128 a[12:15], v11 offset:27648\n\t"
    "v_cvt_pk_bf16_f32 v180, v240, v241\n\t"
    "v_mfma_f32_16x16x32_bf16 a[168:171], a[20:23], v[168:171], a[168:171]\n\t"
    "v_exp_f32 v243, v87\n\t"
    "v_mfma_f32_16x16x32_bf16 a[172:175], a[20:23], v[172:175], 0\n\t"
    "v_exp_f32 v232, v100\n\t"
    "v_mfma_f32_16x16x32_bf16 a[172:175], a[16:19], v[156:159], a[172:175]\n\t"
    "v_cvt_pk_bf16_f32 v181, v242, v243\n\t"
    "s_waitcnt lgkmcnt(6)\n\t"
    "v_mfma_f32_16x16x32_bf16 a[176:179], a[24:27], v[144:147], 0\n\t"
    "ds_read_b128 a[16:19], v11 offset:28672\n\t"
    "v_mfma_f32_16x16x32_bf16 a[176:179], a[28:31], v[160:163], a[176:179]\n\t"
    "v_exp_f32 v233, v101\n\t"
    "v_mfma_f32_16x16x32_bf16 a[180:183], a[28:31], v[164:167], 0\n\t"
    "v_exp_f32 v234, v102\n\t"
    "v_mfma_f32_16x16x32_bf16 a[180:183], a[24:27], v[148:151], a[180:183]\n\t"
    "v_exp_f32 v235, v103\n\t"
    "v_mfma_f32_16x16x32_bf16 a[184:187], a[24:27], v[152:155], 0\n\t"
    "ds_read_b128 a[20:23], v11 offset:29696\n\t"
    "v_cvt_pk_bf16_f32 v182, v232, v233\n\t"
    "v_mfma_f32_16x16x32_bf16 a[184:187], a[28:31], v[168:171], a[184:187]\n\t"
    "v_exp_f32 v236, v88\n\t"
    "v_mfma_f32_16x16x32_bf16 a[188:191], a[28:31], v[172:175], 0\n\t"
    "v_exp_f32 v237, v89\n\t"
    "v_mfma_f32_16x16x32_bf16 a[188:191], a[24:27], v[156:159], a[188:191]\n\t"
    "v_cvt_pk_bf16_f32 v183, v234, v235\n\t"
    "s_waitcnt lgkmcnt(4)\n\t"
    "v_mfma_f32_16x16x32_bf16 a[192:195], a[0:3], v[144:147], 0\n\t"
    "ds_read_b128 a[24:27], v11 offset:30720\n\t"
    "v_cvt_pk_bf16_f32 v184, v236, v237\n\t"
    "v_mfma_f32_16x16x32_bf16 a[192:195], a[4:7], v[160:163], a[192:195]\n\t"
    "v_exp_f32 v238, v90\n\t"
    "v_mfma_f32_16x16x32_bf16 a[196:199], a[4:7], v[164:167], 0\n\t"
    "v_exp_f32 v239, v91\n\t"
    "v_mfma_f32_16x16x32_bf16 a[196:199], a[0:3], v[148:151], a[196:199]\n\t"
    "v_exp_f32 v240, v104\n\t"
    "v_mfma_f32_16x16x32_bf16 a[200:203], a[0:3], v[152:155], 0\n\t"
    "ds_read_b128 a[28:31], v11 offset:31744\n\t"
    "v_cvt_pk_bf16_f32 v185, v238, v239\n\t"
    "v_mfma_f32_16x16x32_bf16 a[200:203], a[4:7], v[168:171], a[200:203]\n\t"
    "v_exp_f32 v241, v105\n\t"
    "v_mfma_f32_16x16x32_bf16 a[204:207], a[4:7], v[172:175], 0\n\t"
    "v_exp_f32 v242, v106\n\t"
    "v_mfma_f32_16x16x32_bf16 a[204:207], a[0:3], v[156:159], a[204:207]\n\t"
    "v_cvt_pk_bf16_f32 v186, v240, v241\n\t"
    "s_waitcnt lgkmcnt(4)\n\t"
    "v_mfma_f32_16x16x32_bf16 a[208:211], a[8:11], v[144:147], 0\n\t"
    "ds_read_b128 a[0:3], v11 offset:0\n\t"
    "v_mul_f32 v244, s62, v248\n\t"
    "v_mfma_f32_16x16x32_bf16 a[208:211], a[12:15], v[160:163], a[208:211]\n\t"
    "v_exp_f32 v243, v107\n\t"
    "v_mfma_f32_16x16x32_bf16 a[212:215], a[12:15], v[164:167], 0\n\t"
    "v_exp_f32 v232, v92\n\t"
    "v_mfma_f32_16x16x32_bf16 a[212:215], a[8:11], v[148:151], a[212:215]\n\t"
    "v_exp_f32 v233, v93\n\t"
    "v_mfma_f32_16x16x32_bf16 a[216:219], a[8:11], v[152:155], 0\n\t"
    "ds_read_b128 a[4:7], v11 offset:1024\n\t"
    "v_cvt_pk_bf16_f32 v187, v242, v243\n\t"
    "v_mfma_f32_16x16x32_bf16 a[216:219], a[12:15], v[168:171], a[216:219]\n\t"
    "v_exp_f32 v234, v94\n\t"
    "v_mfma_f32_16x16x32_bf16 a[220:223], a[12:15], v[172:175], 0\n\t"
    "v_exp_f32 v235, v95\n\t"
    "v_mfma_f32_16x16x32_bf16 a[220:223], a[8:11], v[156:159], a[220:223]\n\t"
    "v_cvt_pk_bf16_f32 v188, v232, v233\n\t"
    "s_waitcnt lgkmcnt(4)\n\t"
    "v_mfma_f32_16x16x32_bf16 a[224:227], a[16:19], v[144:147], 0\n\t"
    "ds_read_b128 a[8:11], v11 offset:2048\n\t"
    "v_cvt_pk_bf16_f32 v189, v234, v235\n\t"
    "v_mfma_f32_16x16x32_bf16 a[224:227], a[20:23], v[160:163], a[224:227]\n\t"
    "v_exp_f32 v236, v108\n\t"
    "v_mfma_f32_16x16x32_bf16 a[228:231], a[20:23], v[164:167], 0\n\t"
    "v_exp_f32 v237, v109\n\t"
    "v_mfma_f32_16x16x32_bf16 a[228:231], a[16:19], v[148:151], a[228:231]\n\t"
    "v_exp_f32 v238, v110\n\t"
    "v_mfma_f32_16x16x32_bf16 a[232:235], a[16:19], v[152:155], 0\n\t"
    "ds_read_b128 a[12:15], v11 offset:3072\n\t"
    "v_cvt_pk_bf16_f32 v190, v236, v237\n\t"
    "v_mfma_f32_16x16x32_bf16 a[232:235], a[20:23], v[168:171], a[232:235]\n\t"
    "v_exp_f32 v239, v111\n\t"
    "v_mfma_f32_16x16x32_bf16 a[236:239], a[20:23], v[172:175], 0\n\t"
    "v_exp_f32 v240, v112\n\t"
    "v_mfma_f32_16x16x32_bf16 a[236:239], a[16:19], v[156:159], a[236:239]\n\t"
    "v_cvt_pk_bf16_f32 v191, v238, v239\n\t"
    "v_mfma_f32_16x16x32_bf16 v[228:231], a[96:99], v[176:179], v[224:227]\n\t"
    "v_exp_f32 v241, v113\n\t"
    "v_mfma_f32_16x16x32_bf16 v[228:231], a[100:103], v[180:183], v[228:231]\n\t"
    "v_exp_f32 v242, v114\n\t"
    "v_mfma_f32_16x16x32_bf16 v[228:231], a[104:107], v[184:187], v[228:231]\n\t"
    "v_exp_f32 v243, v115\n\t"
    "v_mfma_f32_16x16x32_bf16 v[228:231], a[108:111], v[188:191], v[228:231]\n\t"
    "v_exp_f32 v232, v128\n\t"
    "s_waitcnt lgkmcnt(4)\n\t"
    "v_mfma_f32_16x16x32_bf16 a[240:243], a[24:27], v[144:147], 0\n\t"
    "ds_read_b128 a[16:19], v11 offset:4096\n\t"
    "v_cvt_pk_bf16_f32 v192, v240, v241\n\t"
    "v_mfma_f32_16x16x32_bf16 a[240:243], a[28:31], v[160:163], a[240:243]\n\t"
    "v_exp_f32 v233, v129\n\t"
    "v_mfma_f32_16x16x32_bf16 a[244:247], a[28:31], v[164:167], 0\n\t"
    "v_exp_f32 v234, v130\n\t"
    "v_mfma_f32_16x16x32_bf16 a[244:247], a[24:27], v[148:151], a[244:247]\n\t"
    "v_exp_f32 v235, v131\n\t"
    "v_mfma_f32_16x16x32_bf16 a[248:251], a[24:27], v[152:155], 0\n\t"
    "ds_read_b128 a[20:23], v11 offset:5120\n\t"
    "v_cvt_pk_bf16_f32 v193, v242, v243\n\t"
    "v_mfma_f32_16x16x32_bf16 a[248:251], a[28:31], v[168:171], a[248:251]\n\t"
    "v_exp_f32 v236, v116\n\t"
    "v_mfma_f32_16x16x32_bf16 a[252:255], a[28:31], v[172:175], 0\n\t"
    "v_exp_f32 v237, v117\n\t"
    "v_mfma_f32_16x16x32_bf16 a[252:255], a[24:27], v[156:159], a[252:255]\n\t"
    "v_cvt_pk_bf16_f32 v194, v232, v233\n\t"
    "s_waitcnt lgkmcnt(4)\n\t"
    "v_mfma_f32_16x16x32_bf16 v[16:19], a[0:3], a[32:35], v[208:211]\n\t"
    "ds_read_b128 a[24:27], v11 offset:6144\n\t"
    "v_cvt_pk_bf16_f32 v195, v234, v235\n\t"
    "v_mfma_f32_16x16x32_bf16 v[16:19], a[4:7], a[48:51], v[16:19]\n\t"
    "v_exp_f32 v238, v118\n\t"
    "v_mfma_f32_16x16x32_bf16 v[20:23], a[4:7], a[52:55], v[212:215]\n\t"
    "v_exp_f32 v239, v119\n\t"
    "v_mfma_f32_16x16x32_bf16 v[20:23], a[0:3], a[36:39], v[20:23]\n\t"
    "v_exp_f32 v240, v132\n\t"
    "v_mfma_f32_16x16x32_bf16 v[24:27], a[0:3], a[40:43], v[216:219]\n\t"
    "ds_read_b128 a[28:31], v11 offset:7168\n\t"
    "v_cvt_pk_bf16_f32 v196, v236, v237\n\t"
    "v_mfma_f32_16x16x32_bf16 v[24:27], a[4:7], a[56:59], v[24:27]\n\t"
    "v_exp_f32 v241, v133\n\t"
    "v_mfma_f32_16x16x32_bf16 v[28:31], a[4:7], a[60:63], v[220:223]\n\t"
    "v_exp_f32 v242, v134\n\t"
    "v_mfma_f32_16x16x32_bf16 v[28:31], a[0:3], a[44:47], v[28:31]\n\t"
    "v_cvt_pk_bf16_f32 v197, v238, v239\n\t"
    "s_waitcnt lgkmcnt(4)\n\t"
    "v_mfma_f32_16x16x32_bf16 v[16:19], a[8:11], a[64:67], v[16:19]\n\t"
    "ds_read_b128 a[0:3], v11 offset:8192\n\t"
    "v_cvt_pk_bf16_f32 v198, v240, v241\n\t"
    "v_mfma_f32_16x16x32_bf16 v[16:19], a[12:15], a[80:83], v[16:19]\n\t"
    "v_exp_f32 v243, v135\n\t"
    "v_mfma_f32_16x16x32_bf16 v[20:23], a[12:15], a[84:87], v[20:23]\n\t"
    "v_exp_f32 v232, v120\n\t"
    "v_mfma_f32_16x16x32_bf16 v[20:23], a[8:11], a[68:71], v[20:23]\n\t"
    "v_exp_f32 v233, v121\n\t"
    "v_mfma_f32_16x16x32_bf16 v[24:27], a[8:11], a[72:75], v[24:27]\n\t"
    "ds_read_b128 a[4:7], v11 offset:9216\n\t"
    "v_cvt_pk_bf16_f32 v199, v242, v243\n\t"
    "v_mfma_f32_16x16x32_bf16 v[24:27], a[12:15], a[88:91], v[24:27]\n\t"
    "v_exp_f32 v234, v122\n\t"
    "v_mfma_f32_16x16x32_bf16 v[28:31], a[12:15], a[92:95], v[28:31]\n\t"
    "v_exp_f32 v235, v123\n\t"
    "v_mfma_f32_16x16x32_bf16 v[28:31], a[8:11], a[76:79], v[28:31]\n\t"
    "v_cvt_pk_bf16_f32 v200, v232, v233\n\t"
    "s_waitcnt lgkmcnt(4)\n\t"
    "v_mfma_f32_16x16x32_bf16 v[32:35], a[16:19], a[32:35], v[208:211]\n\t"
    "ds_read_b128 a[8:11], v11 offset:10240\n\t"
    "v_cvt_pk_bf16_f32 v201, v234, v235\n\t"
    "v_mfma_f32_16x16x32_bf16 v[32:35], a[20:23], a[48:51], v[32:35]\n\t"
    "v_exp_f32 v236, v136\n\t"
    "v_mfma_f32_16x16x32_bf16 v[36:39], a[20:23], a[52:55], v[212:215]\n\t"
    "v_exp_f32 v237, v137\n\t"
    "v_mfma_f32_16x16x32_bf16 v[36:39], a[16:19], a[36:39], v[36:39]\n\t"
    "v_exp_f32 v238, v138\n\t"
    "v_mfma_f32_16x16x32_bf16 v[40:43], a[16:19], a[40:43], v[216:219]\n\t"
    "ds_read_b128 a[12:15], v11 offset:11264\n\t"
    "v_cvt_pk_bf16_f32 v202, v236, v237\n\t"
    "v_mfma_f32_16x16x32_bf16 v[40:43], a[20:23], a[56:59], v[40:43]\n\t"
    "v_exp_f32 v239, v139\n\t"
    "v_mfma_f32_16x16x32_bf16 v[44:47], a[20:23], a[60:63], v[220:223]\n\t"
    "v_exp_f32 v240, v124\n\t"
    "v_mfma_f32_16x16x32_bf16 v[44:47], a[16:19], a[44:47], v[44:47]\n\t"
    "v_cvt_pk_bf16_f32 v203, v238, v239\n\t"
    "s_waitcnt lgkmcnt(4)\n\t"
    "v_mfma_f32_16x16x32_bf16 v[32:35], a[24:27], a[64:67], v[32:35]\n\t"
    "ds_read_b128 a[16:19], v11 offset:12288\n\t"
    "v_mul_f32 v245, s63, v249\n\t"
    "v_mfma_f32_16x16x32_bf16 v[32:35], a[28:31], a[80:83], v[32:35]\n\t"
    "v_exp_f32 v241, v125\n\t"
    "v_mfma_f32_16x16x32_bf16 v[36:39], a[28:31], a[84:87], v[36:39]\n\t"
    "v_exp_f32 v242, v126\n\t"
    "v_mfma_f32_16x16x32_bf16 v[36:39], a[24:27], a[68:71], v[36:39]\n\t"
    "v_exp_f32 v243, v127\n\t"
    "v_mfma_f32_16x16x32_bf16 v[40:43], a[24:27], a[72:75], v[40:43]\n\t"
    "ds_read_b128 a[20:23], v11 offset:13312\n\t"
    "v_cvt_pk_bf16_f32 v204, v240, v241\n\t"
    "v_mfma_f32_16x16x32_bf16 v[40:43], a[28:31], a[88:91], v[40:43]\n\t"
    "v_exp_f32 v232, v140\n\t"
    "v_mfma_f32_16x16x32_bf16 v[44:47], a[28:31], a[92:95], v[44:47]\n\t"
    "v_exp_f32 v233, v141\n\t"
    "v_mfma_f32_16x16x32_bf16 v[44:47], a[24:27], a[76:79], v[44:47]\n\t"
    "v_cvt_pk_bf16_f32 v205, v242, v243\n\t"
    "s_waitcnt lgkmcnt(4)\n\t"
    "v_mfma_f32_16x16x32_bf16 v[48:51], a[0:3], a[32:35], v[208:211]\n\t"
    "ds_read_b128 a[24:27], v11 offset:14336\n\t"
    "v_cvt_pk_bf16_f32 v206, v232, v233\n\t"
    "v_mfma_f32_16x16x32_bf16 v[48:51], a[4:7], a[48:51], v[48:51]\n\t"
    "v_exp_f32 v234, v142\n\t"
    "v_mfma_f32_16x16x32_bf16 v[52:55], a[4:7], a[52:55], v[212:215]\n\t"
    "v_exp_f32 v235, v143\n\t"
    "v_mfma_f32_16x16x32_bf16 v[52:55], a[0:3], a[36:39], v[52:55]\n\t"
    "v_cvt_pk_bf16_f32 v207, v234, v235\n\t"
    "v_mul_f32 v246, s62, v250\n\t"
    "v_mfma_f32_16x16x32_bf16 v[56:59], a[0:3], a[40:43], v[216:219]\n\t"
    "ds_read_b128 a[28:31], v11 offset:15360\n\t"
    "v_mul_f32 v247, s63, v251\n\t"
    "v_mfma_f32_16x16x32_bf16 v[56:59], a[4:7], a[56:59], v[56:59]\n\t"
    "v_mul_f32 v12, s62, v252\n\t"
    "v_mul_f32 v13, s63, v253\n\t"
    "v_mfma_f32_16x16x32_bf16 v[60:63], a[4:7], a[60:63], v[220:223]\n\t"
    "v_mul_f32 v14, s62, v254\n\t"
    "v_mul_f32 v15, s63, v255\n\t"
    "v_mfma_f32_16x16x32_bf16 v[60:63], a[0:3], a[44:47], v[60:63]\n\t"
    "v_mfma_f32_16x16x32_bf16 v[228:231], a[96:99], v[192:195], v[228:231]\n\t"
    "v_exp_f32 v244, v244\n\t"
    "v_mfma_f32_16x16x32_bf16 v[228:231], a[100:103], v[196:199], v[228:231]\n\t"
    "v_exp_f32 v245, v245\n\t"
    "v_mfma_f32_16x16x32_bf16 v[228:231], a[104:107], v[200:203], v[228:231]\n\t"
    "v_exp_f32 v246, v246\n\t"
    "v_mfma_f32_16x16x32_bf16 v[228:231], a[108:111], v[204:207], v[228:231]\n\t"
    "v_exp_f32 v247, v247\n\t"
    "s_waitcnt vmcnt(9) lgkmcnt(4)\n\t"
    "s_barrier\n\t"
    "v_mfma_f32_16x16x32_bf16 v[48:51], a[8:11], a[64:67], v[48:51]\n\t"
    "ds_read_b128 a[0:3], v11 offset:49216\n\t"
    "v_mfma_f32_16x16x32_bf16 v[48:51], a[12:15], a[80:83], v[48:51]\n\t"
    "s_add_u32 m0, %[sLds4], 32832\n\t"
    "s_add_u32 s65, %[sKV], 163840\n\t"
    "v_mfma_f32_16x16x32_bf16 v[52:55], a[12:15], a[84:87], v[52:55]\n\t"
    "buffer_load_dwordx4 %[vL16], %[srdWS], s65 offen offset:0 lds\n\t"
    "v_mfma_f32_16x16x32_bf16 v[52:55], a[8:11], a[68:71], v[52:55]\n\t"
    "v_exp_f32 v12, v12\n\t"
    "v_mfma_f32_16x16x32_bf16 v[56:59], a[8:11], a[72:75], v[56:59]\n\t"
    "ds_read_b128 a[4:7], v11 offset:50240\n\t"
    "v_mfma_f32_16x16x32_bf16 v[56:59], a[12:15], a[88:91], v[56:59]\n\t"
    "v_exp_f32 v13, v13\n\t"
    "v_mfma_f32_16x16x32_bf16 v[60:63], a[12:15], a[92:95], v[60:63]\n\t"
    "v_exp_f32 v14, v14\n\t"
    "v_mfma_f32_16x16x32_bf16 v[60:63], a[8:11], a[76:79], v[60:63]\n\t"
    "s_waitcnt lgkmcnt(4)\n\t"
    "v_mfma_f32_16x16x32_bf16 v[64:67], a[16:19], a[32:35], v[208:211]\n\t"
    "ds_read_b128 a[8:11], v11 offset:51264\n\t"
    "buffer_load_dwordx4 %[vL16], %[srdWS], s65 offen offset:1024 lds\n\t"
    "v_mfma_f32_16x16x32_bf16 v[64:67], a[20:23], a[48:51], v[64:67]\n\t"
    "v_exp_f32 v15, v15\n\t"
    "v_mfma_f32_16x16x32_bf16 v[68:71], a[20:23], a[52:55], v[212:215]\n\t"
    "v_add_f32 v244, 1.0, v244\n\t"
    "v_add_f32 v245, 1.0, v245\n\t"
    "v_mfma_f32_16x16x32_bf16 v[68:71], a[16:19], a[36:39], v[68:71]\n\t"
    "v_add_f32 v246, 1.0, v246\n\t"
    "v_add_f32 v247, 1.0, v247\n\t"
    "v_mfma_f32_16x16x32_bf16 v[72:75], a[16:19], a[40:43], v[216:219]\n\t"
    "ds_read_b128 a[12:15], v11 offset:52288\n\t"
    "v_add_f32 v12, 1.0, v12\n\t"
    "v_mfma_f32_16x16x32_bf16 v[72:75], a[20:23], a[56:59], v[72:75]\n\t"
    "v_add_f32 v13, 1.0, v13\n\t"
    "v_add_f32 v14, 1.0, v14\n\t"
    "v_mfma_f32_16x16x32_bf16 v[76:79], a[20:23], a[60:63], v[220:223]\n\t"
    "buffer_load_dwordx4 %[vL16], %[srdWS], s65 offen offset:2048 lds\n\t"
    "v_add_f32 v15, 1.0, v15\n\t"
    "v_mfma_f32_16x16x32_bf16 v[76:79], a[16:19], a[44:47], v[76:79]\n\t"
    "s_waitcnt lgkmcnt(4)\n\t"
    "v_mfma_f32_16x16x32_bf16 v[64:67], a[24:27], a[64:67], v[64:67]\n\t"
    "ds_read_b128 a[16:19], v11 offset:53312\n\t"
    "v_mfma_f32_16x16x32_bf16 v[64:67], a[28:31], a[80:83], v[64:67]\n\t"
    "v_rcp_f32 v244, v244\n\t"
    "v_mfma_f32_16x16x32_bf16 v[68:71], a[28:31], a[84:87], v[68:71]\n\t"
    "v_rcp_f32 v245, v245\n\t"
    "v_mfma_f32_16x16x32_bf16 v[68:71], a[24:27], a[68:71], v[68:71]\n\t"
    "v_rcp_f32 v246, v246\n\t"
    "v_mfma_f32_16x16x32_bf16 v[72:75], a[24:27], a[72:75], v[72:75]\n\t"
    "ds_read_b128 a[20:23], v11 offset:54336\n\t"
    "v_max3_f32 v232, v228, v229, v230\n\t"
    "v_mfma_f32_16x16x32_bf16 v[72:75], a[28:31], a[88:91], v[72:75]\n\t"
    "v_max_f32 v232, v232, v231\n\t"
    "buffer_load_dwordx4 %[vL16], %[srdWS], s65 offen offset:3072 lds\n\t"
    "v_mfma_f32_16x16x32_bf16 v[76:79], a[28:31], a[92:95], v[76:79]\n\t"
    "v_cmp_nge_f32 vcc, s64, v232\n\t"
    "s_getpc_b64 s[60:61]\n\t"
    "v_mfma_f32_16x16x32_bf16 v[76:79], a[24:27], a[76:79], v[76:79]\n\t"
    "s_cbranch_vccnz L_k1_fix1_%=\n\t"
    "s_waitcnt lgkmcnt(4)\n\t"
    "v_mfma_f32_16x16x32_bf16 a[128:131], a[0:3], v[176:179], a[128:131]\n\t"
    "ds_read_b128 a[24:27], v11 offset:55360\n\t"
    "v_mfma_f32_16x16x32_bf16 a[128:131], a[4:7], v[192:195], a[128:131]\n\t"
    "s_add_u32 m0, %[sLds4], 49216\n\t"
    "s_add_u32 s66, %[sKV], 114688\n\t"
    "v_mfma_f32_16x16x32_bf16 a[132:135], a[4:7], v[196:199], a[132:135]\n\t"
    "v_exp_f32 v232, v16\n\t"
    "v_mfma_f32_16x16x32_bf16 a[132:135], a[0:3], v[180:183], a[132:135]\n\t"
    "buffer_load_dwordx4 %[vL16], %[srdWS], s66 offen offset:0 lds\n\t"
    "v_mfma_f32_16x16x32_bf16 a[136:139], a[0:3], v[184:187], a[136:139]\n\t"
    "ds_read_b128 a[28:31], v11 offset:56384\n\t"
    "v_mfma_f32_16x16x32_bf16 a[136:139], a[4:7], v[200:203], a[136:139]\n\t"
    "v_exp_f32 v233, v17\n\t"
    "v_mfma_f32_16x16x32_bf16 a[140:143], a[4:7], v[204:207], a[140:143]\n\t"
    "v_exp_f32 v234, v18\n\t"
    "v_mfma_f32_16x16x32_bf16 a[140:143], a[0:3], v[188:191], a[140:143]\n\t"
    "v_cvt_pk_bf16_f32 v144, v232, v233\n\t"
    "s_waitcnt lgkmcnt(4)\n\t"
    "v_mfma_f32_16x16x32_bf16 a[144:147], a[8:11], v[176:179], a[144:147]\n\t"
    "ds_read_b128 a[0:3], v11 offset:57408\n\t"
    "v_mfma_f32_16x16x32_bf16 a[144:147], a[12:15], v[192:195], a[144:147]\n\t"
    "buffer_load_dwordx4 %[vL16], %[srdWS], s66 offen offset:1024 lds\n\t"
    "v_mfma_f32_16x16x32_bf16 a[148:151], a[12:15], v[196:199], a[148:151]\n\t"
    "v_exp_f32 v235, v19\n\t"
    "v_mfma_f32_16x16x32_bf16 a[148:151], a[8:11], v[180:183], a[148:151]\n\t"
    "v_exp_f32 v236, v32\n\t"
    "v_mfma_f32_16x16x32_bf16 a[152:155], a[8:11], v[184:187], a[152:155]\n\t"
    "ds_read_b128 a[4:7], v11 offset:58432\n\t"
    "v_cvt_pk_bf16_f32 v145, v234, v235\n\t"
    "v_mfma_f32_16x16x32_bf16 a[152:155], a[12:15], v[200:203], a[152:155]\n\t"
    "v_exp_f32 v237, v33\n\t"
    "v_mfma_f32_16x16x32_bf16 a[156:159], a[12:15], v[204:207], a[156:159]\n\t"
    "v_exp_f32 v238, v34\n\t"
    "v_mfma_f32_16x16x32_bf16 a[156:159], a[8:11], v[188:191], a[156:159]\n\t"
    "buffer_load_dwordx4 %[vL16], %[srdWS], s66 offen offset:2048 lds\n\t"
    "s_waitcnt lgkmcnt(4)\n\t"
    "v_mfma_f32_16x16x32_bf16 a[160:163], a[16:19], v[176:179], a[160:163]\n\t"
    "ds_read_b128 a[8:11], v11 offset:59456\n\t"
    "v_cvt_pk_bf16_f32 v146, v236, v237\n\t"
    "v_mfma_f32_16x16x32_bf16 a[160:163], a[20:23], v[192:195], a[160:163]\n\t"
    "v_exp_f32 v239, v35\n\t"
    "v_mfma_f32_16x16x32_bf16 a[164:167], a[20:23], v[196:199], a[164:167]\n\t"
    "v_exp_f32 v240, v20\n\t"
    "v_mfma_f32_16x16x32_bf16 a[164:167], a[16:19], v[180:183], a[164:167]\n\t"
    "v_exp_f32 v241, v21\n\t"
    "v_mfma_f32_16x16x32_bf16 a[168:171], a[16:19], v[184:187], a[168:171]\n\t"
    "ds_read_b128 a[12:15], v11 offset:60480\n\t"
    "v_cvt_pk_bf16_f32 v147, v238, v239\n\t"
    "v_mfma_f32_16x16x32_bf16 a[168:171], a[20:23], v[200:203], a[168:171]\n\t"
    "buffer_load_dwordx4 %[vL16], %[srdWS], s66 offen offset:3072 lds\n\t"
    "s_add_u32 %[sP2], %[sP2], 4096\n\t"
    "v_mfma_f32_16x16x32_bf16 a[172:175], a[20:23], v[204:207], a[172:175]\n\t"
    "v_exp_f32 v242, v22\n\t"
    "v_mfma_f32_16x16x32_bf16 a[172:175], a[16:19], v[188:191], a[172:175]\n\t"
    "v_cvt_pk_bf16_f32 v148, v240, v241\n\t"
    "s_waitcnt lgkmcnt(4)\n\t"
    "v_mfma_f32_16x16x32_bf16 a[176:179], a[24:27], v[176:179], a[176:179]\n\t"
    "ds_read_b128 a[16:19], v11 offset:61504\n\t"
    "v_mfma_f32_16x16x32_bf16 a[176:179], a[28:31], v[192:195], a[176:179]\n\t"
    "v_exp_f32 v243, v23\n\t"
    "v_mfma_f32_16x16x32_bf16 a[180:183], a[28:31], v[196:199], a[180:183]\n\t"
    "v_exp_f32 v232, v36\n\t"
    "v_mfma_f32_16x16x32_bf16 a[180:183], a[24:27], v[180:183], a[180:183]\n\t"
    "buffer_load_dword v9, v8, %[srdP2], %[sP2] offen\n\t"
    "v_cvt_pk_bf16_f32 v149, v242, v243\n\t"
    "v_mfma_f32_16x16x32_bf16 a[184:187], a[24:27], v[184:187], a[184:187]\n\t"
    "ds_read_b128 a[20:23], v11 offset:62528\n\t"
    "v_mfma_f32_16x16x32_bf16 a[184:187], a[28:31], v[200:203], a[184:187]\n\t"
    "v_exp_f32 v233, v37\n\t"
    "v_mfma_f32_16x16x32_bf16 a[188:191], a[28:31], v[204:207], a[188:191]\n\t"
    "v_exp_f32 v234, v38\n\t"
    "v_mfma_f32_16x16x32_bf16 a[188:191], a[24:27], v[188:191], a[188:191]\n\t"
    "v_cvt_pk_bf16_f32 v150, v232, v233\n\t"
    "s_waitcnt lgkmcnt(4)\n\t"
    "v_mfma_f32_16x16x32_bf16 a[192:195], a[0:3], v[176:179], a[192:195]\n\t"
    "ds_read_b128 a[24:27], v11 offset:63552\n\t"
    "v_mfma_f32_16x16x32_bf16 a[192:195], a[4:7], v[192:195], a[192:195]\n\t"
    "v_exp_f32 v235, v39\n\t"
    "v_mfma_f32_16x16x32_bf16 a[196:199], a[4:7], v[196:199], a[196:199]\n\t"
    "v_exp_f32 v236, v24\n\t"
    "v_mfma_f32_16x16x32_bf16 a[196:199], a[0:3], v[180:183], a[196:199]\n\t"
    "v_exp_f32 v237, v25\n\t"
    "v_mfma_f32_16x16x32_bf16 a[200:203], a[0:3], v[184:187], a[200:203]\n\t"
    "ds_read_b128 a[28:31], v11 offset:64576\n\t"
    "v_cvt_pk_bf16_f32 v151, v234, v235\n\t"
    "v_mfma_f32_16x16x32_bf16 a[200:203], a[4:7], v[200:203], a[200:203]\n\t"
    "v_exp_f32 v238, v26\n\t"
    "v_mfma_f32_16x16x32_bf16 a[204:207], a[4:7], v[204:207], a[204:207]\n\t"
    "v_exp_f32 v239, v27\n\t"
    "v_mfma_f32_16x16x32_bf16 a[204:207], a[0:3], v[188:191], a[204:207]\n\t"
    "v_cvt_pk_bf16_f32 v152, v236, v237\n\t"
    "s_waitcnt lgkmcnt(4)\n\t"
    "v_mfma_f32_16x16x32_bf16 a[208:211], a[8:11], v[176:179], a[208:211]\n\t"
    "ds_read_b128 a[0:3], v11 offset:32832\n\t"
    "v_cvt_pk_bf16_f32 v153, v238, v239\n\t"
    "v_mfma_f32_16x16x32_bf16 a[208:211], a[12:15], v[192:195], a[208:211]\n\t"
    "v_exp_f32 v240, v40\n\t"
    "v_mfma_f32_16x16x32_bf16 a[212:215], a[12:15], v[196:199], a[212:215]\n\t"
    "v_exp_f32 v241, v41\n\t"
    "v_mfma_f32_16x16x32_bf16 a[212:215], a[8:11], v[180:183], a[212:215]\n\t"
    "v_exp_f32 v242, v42\n\t"
    "v_mfma_f32_16x16x32_bf16 a[216:219], a[8:11], v[184:187], a[216:219]\n\t"
    "ds_read_b128 a[4:7], v11 offset:33856\n\t"
    "v_cvt_pk_bf16_f32 v154, v240, v241\n\t"
    "v_mfma_f32_16x16x32_bf16 a[216:219], a[12:15], v[200:203], a[216:219]\n\t"
    "v_exp_f32 v243, v43\n\t"
    "v_mfma_f32_16x16x32_bf16 a[220:223], a[12:15], v[204:207], a[220:223]\n\t"
    "v_exp_f32 v232, v28\n\t"
    "v_mfma_f32_16x16x32_bf16 a[220:223], a[8:11], v[188:191], a[220:223]\n\t"
    "v_cvt_pk_bf16_f32 v155, v242, v243\n\t"
    "s_waitcnt lgkmcnt(4)\n\t"
    "v_mfma_f32_16x16x32_bf16 a[224:227], a[16:19], v[176:179], a[224:227]\n\t"
    "ds_read_b128 a[8:11], v11 offset:34880\n\t"
    "v_mfma_f32_16x16x32_bf16 a[224:227], a[20:23], v[192:195], a[224:227]\n\t"
    "v_exp_f32 v233, v29\n\t"
    "v_mfma_f32_16x16x32_bf16 a[228:231], a[20:23], v[196:199], a[228:231]\n\t"
    "v_exp_f32 v234, v30\n\t"
    "v_mfma_f32_16x16x32_bf16 a[228:231], a[16:19], v[180:183], a[228:231]\n\t"
    "v_exp_f32 v235, v31\n\t"
    "v_mfma_f32_16x16x32_bf16 a[232:235], a[16:19], v[184:187], a[232:235]\n\t"
    "ds_read_b128 a[12:15], v11 offset:35904\n\t"
    "v_cvt_pk_bf16_f32 v156, v232, v233\n\t"
    "v_mfma_f32_16x16x32_bf16 a[232:235], a[20:23], v[200:203], a[232:235]\n\t"
    "v_exp_f32 v236, v44\n\t"
    "v_mfma_f32_16x16x32_bf16 a[236:239], a[20:23], v[204:207], a[236:239]\n\t"
    "v_exp_f32 v237, v45\n\t"
    "v_mfma_f32_16x16x32_bf16 a[236:239], a[16:19], v[188:191], a[236:239]\n\t"
    "v_cvt_pk_bf16_f32 v157, v234, v235\n\t"
    "s_waitcnt lgkmcnt(4)\n\t"
    "v_mfma_f32_16x16x32_bf16 a[240:243], a[24:27], v[176:179], a[240:243]\n\t"
    "ds_read_b128 a[16:19], v11 offset:36928\n\t"
    "v_cvt_pk_bf16_f32 v158, v236, v237\n\t"
    "v_mfma_f32_16x16x32_bf16 a[240:243], a[28:31], v[192:195], a[240:243]\n\t"
    "v_exp_f32 v238, v46\n\t"
    "v_mfma_f32_16x16x32_bf16 a[244:247], a[28:31], v[196:199], a[244:247]\n\t"
    "v_exp_f32 v239, v47\n\t"
    "v_mfma_f32_16x16x32_bf16 a[244:247], a[24:27], v[180:183], a[244:247]\n\t"
    "v_exp_f32 v240, v48\n\t"
    "v_mfma_f32_16x16x32_bf16 a[248:251], a[24:27], v[184:187], a[248:251]\n\t"
    "ds_read_b128 a[20:23], v11 offset:37952\n\t"
    "v_cvt_pk_bf16_f32 v159, v238, v239\n\t"
    "v_mfma_f32_16x16x32_bf16 a[248:251], a[28:31], v[200:203], a[248:251]\n\t"
    "v_exp_f32 v241, v49\n\t"
    "v_mfma_f32_16x16x32_bf16 a[252:255], a[28:31], v[204:207], a[252:255]\n\t"
    "v_exp_f32 v242, v50\n\t"
    "v_mfma_f32_16x16x32_bf16 a[252:255], a[24:27], v[188:191], a[252:255]\n\t"
    "v_cvt_pk_bf16_f32 v160, v240, v241\n\t"
    "v_mfma_f32_16x16x32_bf16 v[224:227], a[96:99], v[144:147], v[228:231]\n\t"
    "v_exp_f32 v243, v51\n\t"
    "v_mfma_f32_16x16x32_bf16 v[224:227], a[100:103], v[148:151], v[224:227]\n\t"
    "v_exp_f32 v232, v64\n\t"
    "v_mfma_f32_16x16x32_bf16 v[224:227], a[104:107], v[152:155], v[224:227]\n\t"
    "v_exp_f32 v233, v65\n\t"
    "v_mfma_f32_16x16x32_bf16 v[224:227], a[108:111], v[156:159], v[224:227]\n\t"
    "v_exp_f32 v234, v66\n\t"
    "s_waitcnt lgkmcnt(4)\n\t"
    "v_mfma_f32_16x16x32_bf16 v[80:83], a[0:3], a[32:35], v[208:211]\n\t"
    "ds_read_b128 a[24:27], v11 offset:38976\n\t"
    "v_cvt_pk_bf16_f32 v161, v242, v243\n\t"
    "v_mfma_f32_16x16x32_bf16 v[80:83], a[4:7], a[48:51], v[80:83]\n\t"
    "v_exp_f32 v235, v67\n\t"
    "v_mfma_f32_16x16x32_bf16 v[84:87], a[4:7], a[52:55], v[212:215]\n\t"
    "v_exp_f32 v236, v52\n\t"
    "v_mfma_f32_16x16x32_bf16 v[84:87], a[0:3], a[36:39], v[84:87]\n\t"
    "v_exp_f32 v237, v53\n\t"
    "v_mfma_f32_16x16x32_bf16 v[88:91], a[0:3], a[40:43], v[216:219]\n\t"
    "ds_read_b128 a[28:31], v11 offset:40000\n\t"
    "v_cvt_pk_bf16_f32 v162, v232, v233\n\t"
    "v_mfma_f32_16x16x32_bf16 v[88:91], a[4:7], a[56:59], v[88:91]\n\t"
    "v_exp_f32 v238, v54\n\t"
    "v_mfma_f32_16x16x32_bf16 v[92:95], a[4:7], a[60:63], v[220:223]\n\t"
    "v_exp_f32 v239, v55\n\t"
    "v_mfma_f32_16x16x32_bf16 v[92:95], a[0:3], a[44:47], v[92:95]\n\t"
    "v_cvt_pk_bf16_f32 v163, v234, v235\n\t"
    "s_waitcnt lgkmcnt(4)\n\t"
    "v_mfma_f32_16x16x32_bf16 v[80:83], a[8:11], a[64:67], v[80:83]\n\t"
    "ds_read_b128 a[0:3], v11 offset:41024\n\t"
    "v_cvt_pk_bf16_f32 v164, v236, v237\n\t"
    "v_mfma_f32_16x16x32_bf16 v[80:83], a[12:15], a[80:83], v[80:83]\n\t"
    "v_exp_f32 v240, v68\n\t"
    "v_mfma_f32_16x16x32_bf16 v[84:87], a[12:15], a[84:87], v[84:87]\n\t"
    "v_exp_f32 v241, v69\n\t"
    "v_mfma_f32_16x16x32_bf16 v[84:87], a[8:11], a[68:71], v[84:87]\n\t"
    "v_exp_f32 v242, v70\n\t"
    "v_mfma_f32_16x16x32_bf16 v[88:91], a[8:11], a[72:75], v[88:91]\n\t"
    "ds_read_b128 a[4:7], v11 offset:42048\n\t"
    "v_cvt_pk_bf16_f32 v165, v238, v239\n\t"
    "v_mfma_f32_16x16x32_bf16 v[88:91], a[12:15], a[88:91], v[88:91]\n\t"
    "v_exp_f32 v243, v71\n\t"
    "v_mfma_f32_16x16x32_bf16 v[92:95], a[12:15], a[92:95], v[92:95]\n\t"
    "v_exp_f32 v232, v56\n\t"
    "v_mfma_f32_16x16x32_bf16 v[92:95], a[8:11], a[76:79], v[92:95]\n\t"
    "v_cvt_pk_bf16_f32 v166, v240, v241\n\t"
    "s_waitcnt lgkmcnt(4)\n\t"
    "v_mfma_f32_16x16x32_bf16 v[96:99], a[16:19], a[32:35], v[208:211]\n\t"
    "ds_read_b128 a[8:11], v11 offset:43072\n\t"
    "v_cvt_pk_bf16_f32 v167, v242, v243\n\t"
    "v_mfma_f32_16x16x32_bf16 v[96:99], a[20:23], a[48:51], v[96:99]\n\t"
    "v_exp_f32 v233, v57\n\t"
    "v_mfma_f32_16x16x32_bf16 v[100:103], a[20:23], a[52:55], v[212:215]\n\t"
    "v_exp_f32 v234, v58\n\t"
    "v_mfma_f32_16x16x32_bf16 v[100:103], a[16:19], a[36:39], v[100:103]\n\t"
    "v_exp_f32 v235, v59\n\t"
    "v_mfma_f32_16x16x32_bf16 v[104:107], a[16:19], a[40:43], v[216:219]\n\t"
    "ds_read_b128 a[12:15], v11 offset:44096\n\t"
    "v_cvt_pk_bf16_f32 v168, v232, v233\n\t"
    "v_mfma_f32_16x16x32_bf16 v[104:107], a[20:23], a[56:59], v[104:107]\n\t"
    "v_exp_f32 v236, v72\n\t"
    "v_mfma_f32_16x16x32_bf16 v[108:111], a[20:23], a[60:63], v[220:223]\n\t"
    "v_exp_f32 v237, v73\n\t"
    "v_mfma_f32_16x16x32_bf16 v[108:111], a[16:19], a[44:47], v[108:111]\n\t"
    "v_cvt_pk_bf16_f32 v169, v234, v235\n\t"
    "s_waitcnt lgkmcnt(4)\n\t"
    "v_mfma_f32_16x16x32_bf16 v[96:99], a[24:27], a[64:67], v[96:99]\n\t"
    "ds_read_b128 a[16:19], v11 offset:45120\n\t"
    "v_cvt_pk_bf16_f32 v170, v236, v237\n\t"
    "v_mfma_f32_16x16x32_bf16 v[96:99], a[28:31], a[80:83], v[96:99]\n\t"
    "v_exp_f32 v238, v74\n\t"
    "v_mfma_f32_16x16x32_bf16 v[100:103], a[28:31], a[84:87], v[100:103]\n\t"
    "v_exp_f32 v239, v75\n\t"
    "v_mfma_f32_16x16x32_bf16 v[100:103], a[24:27], a[68:71], v[100:103]\n\t"
    "v_exp_f32 v240, v60\n\t"
    "v_mfma_f32_16x16x32_bf16 v[104:107], a[24:27], a[72:75], v[104:107]\n\t"
    "ds_read_b128 a[20:23], v11 offset:46144\n\t"
    "v_cvt_pk_bf16_f32 v171, v238, v239\n\t"
    "v_mfma_f32_16x16x32_bf16 v[104:107], a[28:31], a[88:91], v[104:107]\n\t"
    "v_exp_f32 v241, v61\n\t"
    "v_mfma_f32_16x16x32_bf16 v[108:111], a[28:31], a[92:95], v[108:111]\n\t"
    "v_exp_f32 v242, v62\n\t"
    "v_mfma_f32_16x16x32_bf16 v[108:111], a[24:27], a[76:79], v[108:111]\n\t"
    "v_cvt_pk_bf16_f32 v172, v240, v241\n\t"
    "s_waitcnt lgkmcnt(4)\n\t"
    "v_mfma_f32_16x16x32_bf16 v[112:115], a[0:3], a[32:35], v[208:211]\n\t"
    "ds_read_b128 a[24:27], v11 offset:47168\n\t"
    "v_mfma_f32_16x16x32_bf16 v[112:115], a[4:7], a[48:51], v[112:115]\n\t"
    "v_exp_f32 v243, v63\n\t"
    "v_mfma_f32_16x16x32_bf16 v[116:119], a[4:7], a[52:55], v[212:215]\n\t"
    "v_exp_f32 v232, v76\n\t"
    "v_mfma_f32_16x16x32_bf16 v[116:119], a[0:3], a[36:39], v[116:119]\n\t"
    "v_exp_f32 v233, v77\n\t"
    "v_mfma_f32_16x16x32_bf16 v[120:123], a[0:3], a[40:43], v[216:219]\n\t"
    "ds_read_b128 a[28:31], v11 offset:48192\n\t"
    "v_cvt_pk_bf16_f32 v173, v242, v243\n\t"
    "v_mfma_f32_16x16x32_bf16 v[120:123], a[4:7], a[56:59], v[120:123]\n\t"
    "v_exp_f32 v234, v78\n\t"
    "v_mfma_f32_16x16x32_bf16 v[124:127], a[4:7], a[60:63], v[220:223]\n\t"
    "v_exp_f32 v235, v79\n\t"
    "v_mfma_f32_16x16x32_bf16 v[124:127], a[0:3], a[44:47], v[124:127]\n\t"
    "v_cvt_pk_bf16_f32 v174, v232, v233\n\t"
    "s_waitcnt vmcnt(10) lgkmcnt(4)\n\t"
    "s_barrier\n\t"
    "v_mfma_f32_16x16x32_bf16 v[112:115], a[8:11], a[64:67], v[112:115]\n\t"
    "ds_read_b128 a[0:3], v10 offset:16384\n\t"
    "v_cvt_pk_bf16_f32 v175, v234, v235\n\t"
    "v_mfma_f32_16x16x32_bf16 v[112:115], a[12:15], a[80:83], v[112:115]\n\t"
    "s_add_u32 m0, %[sLds4], 65664\n\t"
    "s_add_u32 s65, %[sKV], 196608\n\t"
    "v_mfma_f32_16x16x32_bf16 v[116:119], a[12:15], a[84:87], v[116:119]\n\t"
    "buffer_load_dwordx4 %[vL16], %[srdWS], s65 offen offset:0 lds\n\t"
    "v_mfma_f32_16x16x32_bf16 v[116:119], a[8:11], a[68:71], v[116:119]\n\t"
    "v_rcp_f32 v247, v247\n\t"
    "v_mfma_f32_16x16x32_bf16 v[120:123], a[8:11], a[72:75], v[120:123]\n\t"
    "ds_read_b128 a[4:7], v10 offset:17408\n\t"
    "v_mfma_f32_16x16x32_bf16 v[120:123], a[12:15], a[88:91], v[120:123]\n\t"
    "v_rcp_f32 v12, v12\n\t"
    "v_mfma_f32_16x16x32_bf16 v[124:127], a[12:15], a[92:95], v[124:127]\n\t"
    "v_rcp_f32 v13, v13\n\t"
    "v_mfma_f32_16x16x32_bf16 v[124:127], a[8:11], a[76:79], v[124:127]\n\t"
    "v_mfma_f32_16x16x32_bf16 v[224:227], a[96:99], v[160:163], v[224:227]\n\t"
    "buffer_load_dwordx4 %[vL16], %[srdWS], s65 offen offset:1024 lds\n\t"
    "v_mfma_f32_16x16x32_bf16 v[224:227], a[100:103], v[164:167], v[224:227]\n\t"
    "v_rcp_f32 v14, v14\n\t"
    "v_mfma_f32_16x16x32_bf16 v[224:227], a[104:107], v[168:171], v[224:227]\n\t"
    "v_rcp_f32 v15, v15\n\t"
    "v_mfma_f32_16x16x32_bf16 v[224:227], a[108:111], v[172:175], v[224:227]\n\t"
    "v_mul_f32 v244, v248, v244\n\t"
    "v_mul_f32 v245, v249, v245\n\t"
    "s_waitcnt lgkmcnt(4)\n\t"
    "v_mfma_f32_16x16x32_bf16 v[128:131], a[16:19], a[32:35], v[208:211]\n\t"
    "ds_read_b128 a[8:11], v10 offset:18432\n\t"
    "v_mul_f32 v246, v250, v246\n\t"
    "v_mfma_f32_16x16x32_bf16 v[128:131], a[20:23], a[48:51], v[128:131]\n\t"
    "v_mul_f32 v247, v251, v247\n\t"
    "v_mul_f32 v12, v252, v12\n\t"
    "v_mfma_f32_16x16x32_bf16 v[132:135], a[20:23], a[52:55], v[212:215]\n\t"
    "buffer_load_dwordx4 %[vL16], %[srdWS], s65 offen offset:2048 lds\n\t"
    "v_mul_f32 v13, v253, v13\n\t"
    "v_mfma_f32_16x16x32_bf16 v[132:135], a[16:19], a[36:39], v[132:135]\n\t"
    "v_mul_f32 v14, v254, v14\n\t"
    "v_mul_f32 v15, v255, v15\n\t"
    "v_mfma_f32_16x16x32_bf16 v[136:139], a[16:19], a[40:43], v[216:219]\n\t"
    "ds_read_b128 a[12:15], v10 offset:19456\n\t"
    "v_cvt_pk_bf16_f32 v248, v244, v245\n\t"
    "v_mfma_f32_16x16x32_bf16 v[136:139], a[20:23], a[56:59], v[136:139]\n\t"
    "v_cvt_pk_bf16_f32 v249, v246, v247\n\t"
    "v_cvt_pk_bf16_f32 v250, v12, v13\n\t"
    "v_mfma_f32_16x16x32_bf16 v[140:143], a[20:23], a[60:63], v[220:223]\n\t"
    "v_cvt_pk_bf16_f32 v251, v14, v15\n\t"
    "buffer_store_dwordx4 v[248:251], %[vL16], %[srdWS], %[sHc] offen offset:0 sc1\n\t"
    "v_mfma_f32_16x16x32_bf16 v[140:143], a[16:19], a[44:47], v[140:143]\n\t"
    "s_nop 0\n\t"
    "ds_read_b128 v[248:251], %[vX] offset:2048\n\t"
    "s_waitcnt lgkmcnt(5)\n\t"
    "v_mfma_f32_16x16x32_bf16 v[128:131], a[24:27], a[64:67], v[128:131]\n\t"
    "ds_read_b128 a[16:19], v10 offset:20480\n\t"
    "buffer_load_dwordx4 %[vL16], %[srdWS], s65 offen offset:3072 lds\n\t"
    "v_mfma_f32_16x16x32_bf16 v[128:131], a[28:31], a[80:83], v[128:131]\n\t"
    "s_add_u32 m0, %[sLds4], 82048\n\t"
    "s_add_u32 s66, %[sKV], 147456\n\t"
    "v_mfma_f32_16x16x32_bf16 v[132:135], a[28:31], a[84:87], v[132:135]\n\t"
    "ds_read_b128 v[252:255], %[vX] offset:3072\n\t"
    "v_mfma_f32_16x16x32_bf16 v[132:135], a[24:27], a[68:71], v[132:135]\n\t"
    "v_mfma_f32_16x16x32_bf16 v[136:139], a[24:27], a[72:75], v[136:139]\n\t"
    "ds_read_b128 a[20:23], v10 offset:21504\n\t"
    "v_max3_f32 v232, v224, v225, v226\n\t"
    "v_mfma_f32_16x16x32_bf16 v[136:139], a[28:31], a[88:91], v[136:139]\n\t"
    "v_max_f32 v232, v232, v227\n\t"
    "v_mfma_f32_16x16x32_bf16 v[140:143], a[28:31], a[92:95], v[140:143]\n\t"
    "k_pga7: s_cbranch_execz k_pga11\n\t"
    "v_cmp_nge_f32 vcc, s64, v232\n\t"
    "s_getpc_b64 s[60:61]\n\t"
    "v_mfma_f32_16x16x32_bf16 v[140:143], a[24:27], a[76:79], v[140:143]\n\t"
    "s_cbranch_vccnz L_k1_fix0_%=\n\t"
    "s_waitcnt lgkmcnt(6)\n\t"
    "v_mfma_f32_16x16x32_bf16 a[128:131], a[0:3], v[144:147], a[128:131]\n\t"
    "ds_read_b128 a[24:27], v10 offset:22528\n\t"
    "buffer_load_dwordx4 %[vL16], %[srdWS], s66 offen offset:0 lds\n\t"
    "v_mfma_f32_16x16x32_bf16 a[128:131], a[4:7], v[160:163], a[128:131]\n\t"
    "v_exp_f32 v232, v80\n\t"
    "v_mfma_f32_16x16x32_bf16 a[132:135], a[4:7], v[164:167], a[132:135]\n\t"
    "v_exp_f32 v233, v81\n\t"
    "v_mfma_f32_16x16x32_bf16 a[132:135], a[0:3], v[148:151], a[132:135]\n\t"
    "v_exp_f32 v234, v82\n\t"
    "v_mfma_f32_16x16x32_bf16 a[136:139], a[0:3], v[152:155], a[136:139]\n\t"
    "ds_read_b128 a[28:31], v10 offset:23552\n\t"
    "v_cvt_pk_bf16_f32 v176, v232, v233\n\t"
    "v_mfma_f32_16x16x32_bf16 a[136:139], a[4:7], v[168:171], a[136:139]\n\t"
    "v_exp_f32 v235, v83\n\t"
    "v_mfma_f32_16x16x32_bf16 a[140:143], a[4:7], v[172:175], a[140:143]\n\t"
    "buffer_load_dwordx4 %[vL16], %[srdWS], s66 offen offset:1024 lds\n\t"
    "v_cvt_pk_bf16_f32 v177, v234, v235\n\t"
    "v_mfma_f32_16x16x32_bf16 a[140:143], a[0:3], v[156:159], a[140:143]\n\t"
    "s_waitcnt lgkmcnt(6)\n\t"
    "v_mfma_f32_16x16x32_bf16 a[144:147], a[8:11], v[144:147], a[144:147]\n\t"
    "ds_read_b128 a[0:3], v10 offset:24576\n\t"
    "v_mfma_f32_16x16x32_bf16 a[144:147], a[12:15], v[160:163], a[144:147]\n\t"
    "v_exp_f32 v236, v96\n\t"
    "v_mfma_f32_16x16x32_bf16 a[148:151], a[12:15], v[164:167], a[148:151]\n\t"
    "v_exp_f32 v237, v97\n\t"
    "v_mfma_f32_16x16x32_bf16 a[148:151], a[8:11], v[148:151], a[148:151]\n\t"
    "v_exp_f32 v238, v98\n\t"
    "v_mfma_f32_16x16x32_bf16 a[152:155], a[8:11], v[152:155], a[152:155]\n\t"
    "ds_read_b128 a[4:7], v10 offset:25600\n\t"
    "buffer_load_dwordx4 %[vL16], %[srdWS], s66 offen offset:2048 lds\n\t"
    "v_mfma_f32_16x16x32_bf16 a[152:155], a[12:15], v[168:171], a[152:155]\n\t"
    "v_exp_f32 v239, v99\n\t"
    "v_mfma_f32_16x16x32_bf16 a[156:159], a[12:15], v[172:175], a[156:159]\n\t"
    "v_exp_f32 v240, v84\n\t"
    "v_mfma_f32_16x16x32_bf16 a[156:159], a[8:11], v[156:159], a[156:159]\n\t"
    "v_cvt_pk_bf16_f32 v178, v236, v237\n\t"
    "s_waitcnt lgkmcnt(4)\n\t"
    "v_mfma_f32_16x16x32_bf16 a[160:163], a[16:19], v[144:147], a[160:163]\n\t"
    "ds_read_b128 a[8:11], v10 offset:26624\n\t"
    "v_cvt_pk_bf16_f32 v179, v238, v239\n\t"
    "v_mfma_f32_16x16x32_bf16 a[160:163], a[20:23], v[160:163], a[160:163]\n\t"
    "v_exp_f32 v241, v85\n\t"
    "v_mfma_f32_16x16x32_bf16 a[164:167], a[20:23], v[164:167], a[164:167]\n\t"
    "buffer_load_dwordx4 %[vL16], %[srdWS], s66 offen offset:3072 lds\n\t"
    "s_add_u32 %[sP2], %[sP2], 4096\n\t"
    "v_mfma_f32_16x16x32_bf16 a[164:167], a[16:19], v[148:151], a[164:167]\n\t"
    "v_exp_f32 v242, v86\n\t"
    "v_mfma_f32_16x16x32_bf16 a[168:171], a[16:19], v[152:155], a[168:171]\n\t"
    "ds_read_b128 a[12:15], v10 offset:27648\n\t"
    "v_cvt_pk_bf16_f32 v180, v240, v241\n\t"
    "v_mfma_f32_16x16x32_bf16 a[168:171], a[20:23], v[168:171], a[168:171]\n\t"
    "v_exp_f32 v243, v87\n\t"
    "v_mfma_f32_16x16x32_bf16 a[172:175], a[20:23], v[172:175], a[172:175]\n\t"
    "v_exp_f32 v232, v100\n\t"
    "v_mfma_f32_16x16x32_bf16 a[172:175], a[16:19], v[156:159], a[172:175]\n\t"
    "v_cvt_pk_bf16_f32 v181, v242, v243\n\t"
    "s_waitcnt lgkmcnt(4)\n\t"
    "v_mfma_f32_16x16x32_bf16 a[176:179], a[24:27], v[144:147], a[176:179]\n\t"
    "ds_read_b128 a[16:19], v10 offset:28672\n\t"
    "buffer_load_dword v9, v8, %[srdP2], %[sP2] offen\n\t"
    "v_mfma_f32_16x16x32_bf16 a[176:179], a[28:31], v[160:163], a[176:179]\n\t"
    "v_exp_f32 v233, v101\n\t"
    "v_mfma_f32_16x16x32_bf16 a[180:183], a[28:31], v[164:167], a[180:183]\n\t"
    "v_exp_f32 v234, v102\n\t"
    "v_mfma_f32_16x16x32_bf16 a[180:183], a[24:27], v[148:151], a[180:183]\n\t"
    "v_exp_f32 v235, v103\n\t"
    "v_mfma_f32_16x16x32_bf16 a[184:187], a[24:27], v[152:155], a[184:187]\n\t"
    "ds_read_b128 a[20:23], v10 offset:29696\n\t"
    "v_cvt_pk_bf16_f32 v182, v232, v233\n\t"
    "v_mfma_f32_16x16x32_bf16 a[184:187], a[28:31], v[168:171], a[184:187]\n\t"
    "v_exp_f32 v236, v88\n\t"
    "v_mfma_f32_16x16x32_bf16 a[188:191], a[28:31], v[172:175], a[188:191]\n\t"
    "v_exp_f32 v237, v89\n\t"
    "v_mfma_f32_16x16x32_bf16 a[188:191], a[24:27], v[156:159], a[188:191]\n\t"
    "v_cvt_pk_bf16_f32 v183, v234, v235\n\t"
    "s_waitcnt lgkmcnt(4)\n\t"
    "v_mfma_f32_16x16x32_bf16 a[192:195], a[0:3], v[144:147], a[192:195]\n\t"
    "ds_read_b128 a[24:27], v10 offset:30720\n\t"
    "v_cvt_pk_bf16_f32 v184, v236, v237\n\t"
    "v_mfma_f32_16x16x32_bf16 a[192:195], a[4:7], v[160:163], a[192:195]\n\t"
    "v_exp_f32 v238, v90\n\t"
    "v_mfma_f32_16x16x32_bf16 a[196:199], a[4:7], v[164:167], a[196:199]\n\t"
    "v_exp_f32 v239, v91\n\t"
    "v_mfma_f32_16x16x32_bf16 a[196:199], a[0:3], v[148:151], a[196:199]\n\t"
    "v_exp_f32 v240, v104\n\t"
    "v_mfma_f32_16x16x32_bf16 a[200:203], a[0:3], v[152:155], a[200:203]\n\t"
    "ds_read_b128 a[28:31], v10 offset:31744\n\t"
    "v_cvt_pk_bf16_f32 v185, v238, v239\n\t"
    "v_mfma_f32_16x16x32_bf16 a[200:203], a[4:7], v[168:171], a[200:203]\n\t"
    "v_exp_f32 v241, v105\n\t"
    "v_mfma_f32_16x16x32_bf16 a[204:207], a[4:7], v[172:175], a[204:207]\n\t"
    "v_exp_f32 v242, v106\n\t"
    "v_mfma_f32_16x16x32_bf16 a[204:207], a[0:3], v[156:159], a[204:207]\n\t"
    "v_cvt_pk_bf16_f32 v186, v240, v241\n\t"
    "s_waitcnt lgkmcnt(4)\n\t"
    "v_mfma_f32_16x16x32_bf16 a[208:211], a[8:11], v[144:147], a[208:211]\n\t"
    "ds_read_b128 a[0:3], v10 offset:0\n\t"
    "v_mul_f32 v244, s62, v248\n\t"
    "v_mfma_f32_16x16x32_bf16 a[208:211], a[12:15], v[160:163], a[208:211]\n\t"
    "v_exp_f32 v243, v107\n\t"
    "v_mfma_f32_16x16x32_bf16 a[212:215], a[12:15], v[164:167], a[212:215]\n\t"
    "v_exp_f32 v232, v92\n\t"
    "v_mfma_f32_16x16x32_bf16 a[212:215], a[8:11], v[148:151], a[212:215]\n\t"
    "v_exp_f32 v233, v93\n\t"
    "v_mfma_f32_16x16x32_bf16 a[216:219], a[8:11], v[152:155], a[216:219]\n\t"
    "ds_read_b128 a[4:7], v10 offset:1024\n\t"
    "v_cvt_pk_bf16_f32 v187, v242, v243\n\t"
    "v_mfma_f32_16x16x32_bf16 a[216:219], a[12:15], v[168:171], a[216:219]\n\t"
    "v_exp_f32 v234, v94\n\t"
    "v_mfma_f32_16x16x32_bf16 a[220:223], a[12:15], v[172:175], a[220:223]\n\t"
    "v_exp_f32 v235, v95\n\t"
    "v_mfma_f32_16x16x32_bf16 a[220:223], a[8:11], v[156:159], a[220:223]\n\t"
    "v_cvt_pk_bf16_f32 v188, v232, v233\n\t"
    "s_waitcnt lgkmcnt(4)\n\t"
    "v_mfma_f32_16x16x32_bf16 a[224:227], a[16:19], v[144:147], a[224:227]\n\t"
    "ds_read_b128 a[8:11], v10 offset:2048\n\t"
    "v_cvt_pk_bf16_f32 v189, v234, v235\n\t"
    "v_mfma_f32_16x16x32_bf16 a[224:227], a[20:23], v[160:163], a[224:227]\n\t"
    "v_exp_f32 v236, v108\n\t"
    "v_mfma_f32_16x16x32_bf16 a[228:231], a[20:23], v[164:167], a[228:231]\n\t"
    "v_exp_f32 v237, v109\n\t"
    "v_mfma_f32_16x16x32_bf16 a[228:231], a[16:19], v[148:151], a[228:231]\n\t"
    "v_exp_f32 v238, v110\n\t"
    "v_mfma_f32_16x16x32_bf16 a[232:235], a[16:19], v[152:155], a[232:235]\n\t"
    "ds_read_b128 a[12:15], v10 offset:3072\n\t"
    "v_cvt_pk_bf16_f32 v190, v236, v237\n\t"
    "v_mfma_f32_16x16x32_bf16 a[232:235], a[20:23], v[168:171], a[232:235]\n\t"
    "v_exp_f32 v239, v111\n\t"
    "v_mfma_f32_16x16x32_bf16 a[236:239], a[20:23], v[172:175], a[236:239]\n\t"
    "v_exp_f32 v240, v112\n\t"
    "v_mfma_f32_16x16x32_bf16 a[236:239], a[16:19], v[156:159], a[236:239]\n\t"
    "v_cvt_pk_bf16_f32 v191, v238, v239\n\t"
    "v_mfma_f32_16x16x32_bf16 v[228:231], a[96:99], v[176:179], v[224:227]\n\t"
    "v_exp_f32 v241, v113\n\t"
    "v_mfma_f32_16x16x32_bf16 v[228:231], a[100:103], v[180:183], v[228:231]\n\t"
    "v_exp_f32 v242, v114\n\t"
    "v_mfma_f32_16x16x32_bf16 v[228:231], a[104:107], v[184:187], v[228:231]\n\t"
    "v_exp_f32 v243, v115\n\t"
    "v_mfma_f32_16x16x32_bf16 v[228:231], a[108:111], v[188:191], v[228:231]\n\t"
    "v_exp_f32 v232, v128\n\t"
    "s_waitcnt lgkmcnt(4)\n\t"
    "v_mfma_f32_16x16x32_bf16 a[240:243], a[24:27], v[144:147], a[240:243]\n\t"
    "ds_read_b128 a[16:19], v10 offset:4096\n\t"
    "v_cvt_pk_bf16_f32 v192, v240, v241\n\t"
    "v_mfma_f32_16x16x32_bf16 a[240:243], a[28:31], v[160:163], a[240:243]\n\t"
    "v_exp_f32 v233, v129\n\t"
    "v_mfma_f32_16x16x32_bf16 a[244:247], a[28:31], v[164:167], a[244:247]\n\t"
    "v_exp_f32 v234, v130\n\t"
    "v_mfma_f32_16x16x32_bf16 a[244:247], a[24:27], v[148:151], a[244:247]\n\t"
    "v_exp_f32 v235, v131\n\t"
    "v_mfma_f32_16x16x32_bf16 a[248:251], a[24:27], v[152:155], a[248:251]\n\t"
    "ds_read_b128 a[20:23], v10 offset:5120\n\t"
    "v_cvt_pk_bf16_f32 v193, v242, v243\n\t"
    "v_mfma_f32_16x16x32_bf16 a[248:251], a[28:31], v[168:171], a[248:251]\n\t"
    "v_exp_f32 v236, v116\n\t"
    "v_mfma_f32_16x16x32_bf16 a[252:255], a[28:31], v[172:175], a[252:255]\n\t"
    "v_exp_f32 v237, v117\n\t"
    "v_mfma_f32_16x16x32_bf16 a[252:255], a[24:27], v[156:159], a[252:255]\n\t"
    "v_cvt_pk_bf16_f32 v194, v232, v233\n\t"
    "s_waitcnt lgkmcnt(4)\n\t"
    "v_mfma_f32_16x16x32_bf16 v[16:19], a[0:3], a[32:35], v[208:211]\n\t"
    "ds_read_b128 a[24:27], v10 offset:6144\n\t"
    "v_cvt_pk_bf16_f32 v195, v234, v235\n\t"
    "v_mfma_f32_16x16x32_bf16 v[16:19], a[4:7], a[48:51], v[16:19]\n\t"
    "v_exp_f32 v238, v118\n\t"
    "v_mfma_f32_16x16x32_bf16 v[20:23], a[4:7], a[52:55], v[212:215]\n\t"
    "v_exp_f32 v239, v119\n\t"
    "v_mfma_f32_16x16x32_bf16 v[20:23], a[0:3], a[36:39], v[20:23]\n\t"
    "v_exp_f32 v240, v132\n\t"
    "v_mfma_f32_16x16x32_bf16 v[24:27], a[0:3], a[40:43], v[216:219]\n\t"
    "ds_read_b128 a[28:31], v10 offset:7168\n\t"
    "v_cvt_pk_bf16_f32 v196, v236, v237\n\t"
    "v_mfma_f32_16x16x32_bf16 v[24:27], a[4:7], a[56:59], v[24:27]\n\t"
    "v_exp_f32 v241, v133\n\t"
    "v_mfma_f32_16x16x32_bf16 v[28:31], a[4:7], a[60:63], v[220:223]\n\t"
    "v_exp_f32 v242, v134\n\t"
    "v_mfma_f32_16x16x32_bf16 v[28:31], a[0:3], a[44:47], v[28:31]\n\t"
    "v_cvt_pk_bf16_f32 v197, v238, v239\n\t"
    "s_waitcnt lgkmcnt(4)\n\t"
    "v_mfma_f32_16x16x32_bf16 v[16:19], a[8:11], a[64:67], v[16:19]\n\t"
    "ds_read_b128 a[0:3], v10 offset:8192\n\t"
    "v_cvt_pk_bf16_f32 v198, v240, v241\n\t"
    "v_mfma_f32_16x16x32_bf16 v[16:19], a[12:15], a[80:83], v[16:19]\n\t"
    "v_exp_f32 v243, v135\n\t"
    "v_mfma_f32_16x16x32_bf16 v[20:23], a[12:15], a[84:87], v[20:23]\n\t"
    "v_exp_f32 v232, v120\n\t"
    "v_mfma_f32_16x16x32_bf16 v[20:23], a[8:11], a[68:71], v[20:23]\n\t"
    "v_exp_f32 v233, v121\n\t"
    "v_mfma_f32_16x16x32_bf16 v[24:27], a[8:11], a[72:75], v[24:27]\n\t"
    "ds_read_b128 a[4:7], v10 offset:9216\n\t"
    "v_cvt_pk_bf16_f32 v199, v242, v243\n\t"
    "v_mfma_f32_16x16x32_bf16 v[24:27], a[12:15], a[88:91], v[24:27]\n\t"
    "v_exp_f32 v234, v122\n\t"
    "v_mfma_f32_16x16x32_bf16 v[28:31], a[12:15], a[92:95], v[28:31]\n\t"
    "v_exp_f32 v235, v123\n\t"
    "v_mfma_f32_16x16x32_bf16 v[28:31], a[8:11], a[76:79], v[28:31]\n\t"
    "v_cvt_pk_bf16_f32 v200, v232, v233\n\t"
    "s_waitcnt lgkmcnt(4)\n\t"
    "v_mfma_f32_16x16x32_bf16 v[32:35], a[16:19], a[32:35], v[208:211]\n\t"
    "ds_read_b128 a[8:11], v10 offset:10240\n\t"
    "v_cvt_pk_bf16_f32 v201, v234, v235\n\t"
    "v_mfma_f32_16x16x32_bf16 v[32:35], a[20:23], a[48:51], v[32:35]\n\t"
    "v_exp_f32 v236, v136\n\t"
    "v_mfma_f32_16x16x32_bf16 v[36:39], a[20:23], a[52:55], v[212:215]\n\t"
    "v_exp_f32 v237, v137\n\t"
    "v_mfma_f32_16x16x32_bf16 v[36:39], a[16:19], a[36:39], v[36:39]\n\t"
    "v_exp_f32 v238, v138\n\t"
    "v_mfma_f32_16x16x32_bf16 v[40:43], a[16:19], a[40:43], v[216:219]\n\t"
    "ds_read_b128 a[12:15], v10 offset:11264\n\t"
    "v_cvt_pk_bf16_f32 v202, v236, v237\n\t"
    "v_mfma_f32_16x16x32_bf16 v[40:43], a[20:23], a[56:59], v[40:43]\n\t"
    "v_exp_f32 v239, v139\n\t"
    "v_mfma_f32_16x16x32_bf16 v[44:47], a[20:23], a[60:63], v[220:223]\n\t"
    "v_exp_f32 v240, v124\n\t"
    "v_mfma_f32_16x16x32_bf16 v[44:47], a[16:19], a[44:47], v[44:47]\n\t"
    "v_cvt_pk_bf16_f32 v203, v238, v239\n\t"
    "s_waitcnt lgkmcnt(4)\n\t"
    "v_mfma_f32_16x16x32_bf16 v[32:35], a[24:27], a[64:67], v[32:35]\n\t"
    "ds_read_b128 a[16:19], v10 offset:12288\n\t"
    "v_mul_f32 v245, s63, v249\n\t"
    "v_mfma_f32_16x16x32_bf16 v[32:35], a[28:31], a[80:83], v[32:35]\n\t"
    "v_exp_f32 v241, v125\n\t"
    "v_mfma_f32_16x16x32_bf16 v[36:39], a[28:31], a[84:87], v[36:39]\n\t"
    "v_exp_f32 v242, v126\n\t"
    "v_mfma_f32_16x16x32_bf16 v[36:39], a[24:27], a[68:71], v[36:39]\n\t"
    "v_exp_f32 v243, v127\n\t"
    "v_mfma_f32_16x16x32_bf16 v[40:43], a[24:27], a[72:75], v[40:43]\n\t"
    "ds_read_b128 a[20:23], v10 offset:13312\n\t"
    "v_cvt_pk_bf16_f32 v204, v240, v241\n\t"
    "v_mfma_f32_16x16x32_bf16 v[40:43], a[28:31], a[88:91], v[40:43]\n\t"
    "v_exp_f32 v232, v140\n\t"
    "v_mfma_f32_16x16x32_bf16 v[44:47], a[28:31], a[92:95], v[44:47]\n\t"
    "v_exp_f32 v233, v141\n\t"
    "v_mfma_f32_16x16x32_bf16 v[44:47], a[24:27], a[76:79], v[44:47]\n\t"
    "v_cvt_pk_bf16_f32 v205, v242, v243\n\t"
    "s_waitcnt lgkmcnt(4)\n\t"
    "v_mfma_f32_16x16x32_bf16 v[48:51], a[0:3], a[32:35], v[208:211]\n\t"
    "ds_read_b128 a[24:27], v10 offset:14336\n\t"
    "v_cvt_pk_bf16_f32 v206, v232, v233\n\t"
    "v_mfma_f32_16x16x32_bf16 v[48:51], a[4:7], a[48:51], v[48:51]\n\t"
    "v_exp_f32 v234, v142\n\t"
    "v_mfma_f32_16x16x32_bf16 v[52:55], a[4:7], a[52:55], v[212:215]\n\t"
    "v_exp_f32 v235, v143\n\t"
    "v_mfma_f32_16x16x32_bf16 v[52:55], a[0:3], a[36:39], v[52:55]\n\t"
    "v_cvt_pk_bf16_f32 v207, v234, v235\n\t"
    "v_mul_f32 v246, s62, v250\n\t"
    "v_mfma_f32_16x16x32_bf16 v[56:59], a[0:3], a[40:43], v[216:219]\n\t"
    "ds_read_b128 a[28:31], v10 offset:15360\n\t"
    "v_mul_f32 v247, s63, v251\n\t"
    "v_mfma_f32_16x16x32_bf16 v[56:59], a[4:7], a[56:59], v[56:59]\n\t"
    "v_mul_f32 v12, s62, v252\n\t"
    "v_mul_f32 v13, s63, v253\n\t"
    "v_mfma_f32_16x16x32_bf16 v[60:63], a[4:7], a[60:63], v[220:223]\n\t"
    "v_mul_f32 v14, s62, v254\n\t"
    "v_mul_f32 v15, s63, v255\n\t"
    "v_mfma_f32_16x16x32_bf16 v[60:63], a[0:3], a[44:47], v[60:63]\n\t"
    "v_mfma_f32_16x16x32_bf16 v[228:231], a[96:99], v[192:195], v[228:231]\n\t"
    "v_exp_f32 v244, v244\n\t"
    "v_mfma_f32_16x16x32_bf16 v[228:231], a[100:103], v[196:199], v[228:231]\n\t"
    "v_exp_f32 v245, v245\n\t"
    "v_mfma_f32_16x16x32_bf16 v[228:231], a[104:107], v[200:203], v[228:231]\n\t"
    "v_exp_f32 v246, v246\n\t"
    "v_mfma_f32_16x16x32_bf16 v[228:231], a[108:111], v[204:207], v[228:231]\n\t"
    "v_exp_f32 v247, v247\n\t"
    "s_waitcnt vmcnt(10) lgkmcnt(4)\n\t"
    "s_barrier\n\t"
    "v_mfma_f32_16x16x32_bf16 v[48:51], a[8:11], a[64:67], v[48:51]\n\t"
    "ds_read_b128 a[0:3], v10 offset:49216\n\t"
    "v_mfma_f32_16x16x32_bf16 v[48:51], a[12:15], a[80:83], v[48:51]\n\t"
    "s_add_u32 m0, %[sLds4], 98496\n\t"
    "s_add_u32 s65, %[sKV], 229376\n\t"
    "v_mfma_f32_16x16x32_bf16 v[52:55], a[12:15], a[84:87], v[52:55]\n\t"
    "buffer_load_dwordx4 %[vL16], %[srdWS], s65 offen offset:0 lds\n\t"
    "v_mfma_f32_16x16x32_bf16 v[52:55], a[8:11], a[68:71], v[52:55]\n\t"
    "v_exp_f32 v12, v12\n\t"
    "v_mfma_f32_16x16x32_bf16 v[56:59], a[8:11], a[72:75], v[56:59]\n\t"
    "ds_read_b128 a[4:7], v10 offset:50240\n\t"
    "v_mfma_f32_16x16x32_bf16 v[56:59], a[12:15], a[88:91], v[56:59]\n\t"
    "v_exp_f32 v13, v13\n\t"
    "v_mfma_f32_16x16x32_bf16 v[60:63], a[12:15], a[92:95], v[60:63]\n\t"
    "v_exp_f32 v14, v14\n\t"
    "v_mfma_f32_16x16x32_bf16 v[60:63], a[8:11], a[76:79], v[60:63]\n\t"
    "s_waitcnt lgkmcnt(4)\n\t"
    "v_mfma_f32_16x16x32_bf16 v[64:67], a[16:19], a[32:35], v[208:211]\n\t"
    "ds_read_b128 a[8:11], v10 offset:51264\n\t"
    "buffer_load_dwordx4 %[vL16], %[srdWS], s65 offen offset:1024 lds\n\t"
    "v_mfma_f32_16x16x32_bf16 v[64:67], a[20:23], a[48:51], v[64:67]\n\t"
    "v_exp_f32 v15, v15\n\t"
    "v_mfma_f32_16x16x32_bf16 v[68:71], a[20:23], a[52:55], v[212:215]\n\t"
    "v_add_f32 v244, 1.0, v244\n\t"
    "v_add_f32 v245, 1.0, v245\n\t"
    "v_mfma_f32_16x16x32_bf16 v[68:71], a[16:19], a[36:39], v[68:71]\n\t"
    "v_add_f32 v246, 1.0, v246\n\t"
    "v_add_f32 v247, 1.0, v247\n\t"
    "v_mfma_f32_16x16x32_bf16 v[72:75], a[16:19], a[40:43], v[216:219]\n\t"
    "ds_read_b128 a[12:15], v10 offset:52288\n\t"
    "v_add_f32 v12, 1.0, v12\n\t"
    "v_mfma_f32_16x16x32_bf16 v[72:75], a[20:23], a[56:59], v[72:75]\n\t"
    "v_add_f32 v13, 1.0, v13\n\t"
    "v_add_f32 v14, 1.0, v14\n\t"
    "v_mfma_f32_16x16x32_bf16 v[76:79], a[20:23], a[60:63], v[220:223]\n\t"
    "buffer_load_dwordx4 %[vL16], %[srdWS], s65 offen offset:2048 lds\n\t"
    "v_add_f32 v15, 1.0, v15\n\t"
    "v_mfma_f32_16x16x32_bf16 v[76:79], a[16:19], a[44:47], v[76:79]\n\t"
    "s_waitcnt lgkmcnt(4)\n\t"
    "v_mfma_f32_16x16x32_bf16 v[64:67], a[24:27], a[64:67], v[64:67]\n\t"
    "ds_read_b128 a[16:19], v10 offset:53312\n\t"
    "v_mfma_f32_16x16x32_bf16 v[64:67], a[28:31], a[80:83], v[64:67]\n\t"
    "v_rcp_f32 v244, v244\n\t"
    "v_mfma_f32_16x16x32_bf16 v[68:71], a[28:31], a[84:87], v[68:71]\n\t"
    "v_rcp_f32 v245, v245\n\t"
    "v_mfma_f32_16x16x32_bf16 v[68:71], a[24:27], a[68:71], v[68:71]\n\t"
    "v_rcp_f32 v246, v246\n\t"
    "v_mfma_f32_16x16x32_bf16 v[72:75], a[24:27], a[72:75], v[72:75]\n\t"
    "ds_read_b128 a[20:23], v10 offset:54336\n\t"
    "v_max3_f32 v232, v228, v229, v230\n\t"
    "v_mfma_f32_16x16x32_bf16 v[72:75], a[28:31], a[88:91], v[72:75]\n\t"
    "v_max_f32 v232, v232, v231\n\t"
    "buffer_load_dwordx4 %[vL16], %[srdWS], s65 offen offset:3072 lds\n\t"
    "v_mfma_f32_16x16x32_bf16 v[76:79], a[28:31], a[92:95], v[76:79]\n\t"
    "v_cmp_nge_f32 vcc, s64, v232\n\t"
    "s_getpc_b64 s[60:61]\n\t"
    "v_mfma_f32_16x16x32_bf16 v[76:79], a[24:27], a[76:79], v[76:79]\n\t"
    "s_cbranch_vccnz L_k1_fix1_%=\n\t"
    "s_waitcnt lgkmcnt(4)\n\t"
    "v_mfma_f32_16x16x32_bf16 a[128:131], a[0:3], v[176:179], a[128:131]\n\t"
    "ds_read_b128 a[24:27], v10 offset:55360\n\t"
    "v_mfma_f32_16x16x32_bf16 a[128:131], a[4:7], v[192:195], a[128:131]\n\t"
    "s_add_u32 m0, %[sLds4], 114880\n\t"
    "s_add_u32 s66, %[sKV], 180224\n\t"
    "v_mfma_f32_16x16x32_bf16 a[132:135], a[4:7], v[196:199], a[132:135]\n\t"
    "v_exp_f32 v232, v16\n\t"
    "v_mfma_f32_16x16x32_bf16 a[132:135], a[0:3], v[180:183], a[132:135]\n\t"
    "buffer_load_dwordx4 %[vL16], %[srdWS], s66 offen offset:0 lds\n\t"
    "v_mfma_f32_16x16x32_bf16 a[136:139], a[0:3], v[184:187], a[136:139]\n\t"
    "ds_read_b128 a[28:31], v10 offset:56384\n\t"
    "v_mfma_f32_16x16x32_bf16 a[136:139], a[4:7], v[200:203], a[136:139]\n\t"
    "v_exp_f32 v233, v17\n\t"
    "v_mfma_f32_16x16x32_bf16 a[140:143], a[4:7], v[204:207], a[140:143]\n\t"
    "v_exp_f32 v234, v18\n\t"
    "v_mfma_f32_16x16x32_bf16 a[140:143], a[0:3], v[188:191], a[140:143]\n\t"
    "v_cvt_pk_bf16_f32 v144, v232, v233\n\t"
    "s_waitcnt lgkmcnt(4)\n\t"
    "v_mfma_f32_16x16x32_bf16 a[144:147], a[8:11], v[176:179], a[144:147]\n\t"
    "ds_read_b128 a[0:3], v10 offset:57408\n\t"
    "v_mfma_f32_16x16x32_bf16 a[144:147], a[12:15], v[192:195], a[144:147]\n\t"
    "buffer_load_dwordx4 %[vL16], %[srdWS], s66 offen offset:1024 lds\n\t"
    "v_mfma_f32_16x16x32_bf16 a[148:151], a[12:15], v[196:199], a[148:151]\n\t"
    "v_exp_f32 v235, v19\n\t"
    "v_mfma_f32_16x16x32_bf16 a[148:151], a[8:11], v[180:183], a[148:151]\n\t"
    "v_exp_f32 v236, v32\n\t"
    "v_mfma_f32_16x16x32_bf16 a[152:155], a[8:11], v[184:187], a[152:155]\n\t"
    "ds_read_b128 a[4:7], v10 offset:58432\n\t"
    "v_cvt_pk_bf16_f32 v145, v234, v235\n\t"
    "v_mfma_f32_16x16x32_bf16 a[152:155], a[12:15], v[200:203], a[152:155]\n\t"
    "v_exp_f32 v237, v33\n\t"
    "v_mfma_f32_16x16x32_bf16 a[156:159], a[12:15], v[204:207], a[156:159]\n\t"
    "v_exp_f32 v238, v34\n\t"
    "v_mfma_f32_16x16x32_bf16 a[156:159], a[8:11], v[188:191], a[156:159]\n\t"
    "buffer_load_dwordx4 %[vL16], %[srdWS], s66 offen offset:2048 lds\n\t"
    "s_waitcnt lgkmcnt(4)\n\t"
    "v_mfma_f32_16x16x32_bf16 a[160:163], a[16:19], v[176:179], a[160:163]\n\t"
    "ds_read_b128 a[8:11], v10 offset:59456\n\t"
    "v_cvt_pk_bf16_f32 v146, v236, v237\n\t"
    "v_mfma_f32_16x16x32_bf16 a[160:163], a[20:23], v[192:195], a[160:163]\n\t"
    "v_exp_f32 v239, v35\n\t"
    "v_mfma_f32_16x16x32_bf16 a[164:167], a[20:23], v[196:199], a[164:167]\n\t"
    "v_exp_f32 v240, v20\n\t"
    "v_mfma_f32_16x16x32_bf16 a[164:167], a[16:19], v[180:183], a[164:167]\n\t"
    "v_exp_f32 v241, v21\n\t"
    "v_mfma_f32_16x16x32_bf16 a[168:171], a[16:19], v[184:187], a[168:171]\n\t"
    "ds_read_b128 a[12:15], v10 offset:60480\n\t"
    "v_cvt_pk_bf16_f32 v147, v238, v239\n\t"
    "v_mfma_f32_16x16x32_bf16 a[168:171], a[20:23], v[200:203], a[168:171]\n\t"
    "buffer_load_dwordx4 %[vL16], %[srdWS], s66 offen offset:3072 lds\n\t"
    "s_add_u32 %[sP2], %[sP2], 4096\n\t"
    "v_mfma_f32_16x16x32_bf16 a[172:175], a[20:23], v[204:207], a[172:175]\n\t"
    "v_exp_f32 v242, v22\n\t"
    "v_mfma_f32_16x16x32_bf16 a[172:175], a[16:19], v[188:191], a[172:175]\n\t"
    "v_cvt_pk_bf16_f32 v148, v240, v241\n\t"
    "s_waitcnt lgkmcnt(4)\n\t"
    "v_mfma_f32_16x16x32_bf16 a[176:179], a[24:27], v[176:179], a[176:179]\n\t"
    "ds_read_b128 a[16:19], v10 offset:61504\n\t"
    "v_mfma_f32_16x16x32_bf16 a[176:179], a[28:31], v[192:195], a[176:179]\n\t"
    "v_exp_f32 v243, v23\n\t"
    "v_mfma_f32_16x16x32_bf16 a[180:183], a[28:31], v[196:199], a[180:183]\n\t"
    "v_exp_f32 v232, v36\n\t"
    "v_mfma_f32_16x16x32_bf16 a[180:183], a[24:27], v[180:183], a[180:183]\n\t"
    "buffer_load_dword v9, v8, %[srdP2], %[sP2] offen\n\t"
    "v_cvt_pk_bf16_f32 v149, v242, v243\n\t"
    "v_mfma_f32_16x16x32_bf16 a[184:187], a[24:27], v[184:187], a[184:187]\n\t"
    "ds_read_b128 a[20:23], v10 offset:62528\n\t"
    "v_mfma_f32_16x16x32_bf16 a[184:187], a[28:31], v[200:203], a[184:187]\n\t"
    "v_exp_f32 v233, v37\n\t"
    "v_mfma_f32_16x16x32_bf16 a[188:191], a[28:31], v[204:207], a[188:191]\n\t"
    "v_exp_f32 v234, v38\n\t"
    "v_mfma_f32_16x16x32_bf16 a[188:191], a[24:27], v[188:191], a[188:191]\n\t"
    "v_cvt_pk_bf16_f32 v150, v232, v233\n\t"
    "s_waitcnt lgkmcnt(4)\n\t"
    "v_mfma_f32_16x16x32_bf16 a[192:195], a[0:3], v[176:179], a[192:195]\n\t"
    "ds_read_b128 a[24:27], v10 offset:63552\n\t"
    "v_mfma_f32_16x16x32_bf16 a[192:195], a[4:7], v[192:195], a[192:195]\n\t"
    "v_exp_f32 v235, v39\n\t"
    "v_mfma_f32_16x16x32_bf16 a[196:199], a[4:7], v[196:199], a[196:199]\n\t"
    "v_exp_f32 v236, v24\n\t"
    "v_mfma_f32_16x16x32_bf16 a[196:199], a[0:3], v[180:183], a[196:199]\n\t"
    "v_exp_f32 v237, v25\n\t"
    "v_mfma_f32_16x16x32_bf16 a[200:203], a[0:3], v[184:187], a[200:203]\n\t"
    "ds_read_b128 a[28:31], v10 offset:64576\n\t"
    "v_cvt_pk_bf16_f32 v151, v234, v235\n\t"
    "v_mfma_f32_16x16x32_bf16 a[200:203], a[4:7], v[200:203], a[200:203]\n\t"
    "v_exp_f32 v238, v26\n\t"
    "v_mfma_f32_16x16x32_bf16 a[204:207], a[4:7], v[204:207], a[204:207]\n\t"
    "v_exp_f32 v239, v27\n\t"
    "v_mfma_f32_16x16x32_bf16 a[204:207], a[0:3], v[188:191], a[204:207]\n\t"
    "v_cvt_pk_bf16_f32 v152, v236, v237\n\t"
    "s_waitcnt lgkmcnt(4)\n\t"
    "v_mfma_f32_16x16x32_bf16 a[208:211], a[8:11], v[176:179], a[208:211]\n\t"
    "ds_read_b128 a[0:3], v10 offset:32832\n\t"
    "v_cvt_pk_bf16_f32 v153, v238, v239\n\t"
    "v_mfma_f32_16x16x32_bf16 a[208:211], a[12:15], v[192:195], a[208:211]\n\t"
    "v_exp_f32 v240, v40\n\t"
    "v_mfma_f32_16x16x32_bf16 a[212:215], a[12:15], v[196:199], a[212:215]\n\t"
    "v_exp_f32 v241, v41\n\t"
    "v_mfma_f32_16x16x32_bf16 a[212:215], a[8:11], v[180:183], a[212:215]\n\t"
    "v_exp_f32 v242, v42\n\t"
    "v_mfma_f32_16x16x32_bf16 a[216:219], a[8:11], v[184:187], a[216:219]\n\t"
    "ds_read_b128 a[4:7], v10 offset:33856\n\t"
    "v_cvt_pk_bf16_f32 v154, v240, v241\n\t"
    "v_mfma_f32_16x16x32_bf16 a[216:219], a[12:15], v[200:203], a[216:219]\n\t"
    "v_exp_f32 v243, v43\n\t"
    "v_mfma_f32_16x16x32_bf16 a[220:223], a[12:15], v[204:207], a[220:223]\n\t"
    "v_exp_f32 v232, v28\n\t"
    "v_mfma_f32_16x16x32_bf16 a[220:223], a[8:11], v[188:191], a[220:223]\n\t"
    "v_cvt_pk_bf16_f32 v155, v242, v243\n\t"
    "s_waitcnt lgkmcnt(4)\n\t"
    "v_mfma_f32_16x16x32_bf16 a[224:227], a[16:19], v[176:179], a[224:227]\n\t"
    "ds_read_b128 a[8:11], v10 offset:34880\n\t"
    "v_mfma_f32_16x16x32_bf16 a[224:227], a[20:23], v[192:195], a[224:227]\n\t"
    "v_exp_f32 v233, v29\n\t"
    "v_mfma_f32_16x16x32_bf16 a[228:231], a[20:23], v[196:199], a[228:231]\n\t"
    "v_exp_f32 v234, v30\n\t"
    "v_mfma_f32_16x16x32_bf16 a[228:231], a[16:19], v[180:183], a[228:231]\n\t"
    "v_exp_f32 v235, v31\n\t"
    "v_mfma_f32_16x16x32_bf16 a[232:235], a[16:19], v[184:187], a[232:235]\n\t"
    "ds_read_b128 a[12:15], v10 offset:35904\n\t"
    "v_cvt_pk_bf16_f32 v156, v232, v233\n\t"
    "v_mfma_f32_16x16x32_bf16 a[232:235], a[20:23], v[200:203], a[232:235]\n\t"
    "v_exp_f32 v236, v44\n\t"
    "v_mfma_f32_16x16x32_bf16 a[236:239], a[20:23], v[204:207], a[236:239]\n\t"
    "v_exp_f32 v237, v45\n\t"
    "v_mfma_f32_16x16x32_bf16 a[236:239], a[16:19], v[188:191], a[236:239]\n\t"
    "v_cvt_pk_bf16_f32 v157, v234, v235\n\t"
    "s_waitcnt lgkmcnt(4)\n\t"
    "v_mfma_f32_16x16x32_bf16 a[240:243], a[24:27], v[176:179], a[240:243]\n\t"
    "ds_read_b128 a[16:19], v10 offset:36928\n\t"
    "v_cvt_pk_bf16_f32 v158, v236, v237\n\t"
    "v_mfma_f32_16x16x32_bf16 a[240:243], a[28:31], v[192:195], a[240:243]\n\t"
    "v_exp_f32 v238, v46\n\t"
    "v_mfma_f32_16x16x32_bf16 a[244:247], a[28:31], v[196:199], a[244:247]\n\t"
    "v_exp_f32 v239, v47\n\t"
    "v_mfma_f32_16x16x32_bf16 a[244:247], a[24:27], v[180:183], a[244:247]\n\t"
    "v_exp_f32 v240, v48\n\t"
    "v_mfma_f32_16x16x32_bf16 a[248:251], a[24:27], v[184:187], a[248:251]\n\t"
    "ds_read_b128 a[20:23], v10 offset:37952\n\t"
    "v_cvt_pk_bf16_f32 v159, v238, v239\n\t"
    "v_mfma_f32_16x16x32_bf16 a[248:251], a[28:31], v[200:203], a[248:251]\n\t"
    "v_exp_f32 v241, v49\n\t"
    "v_mfma_f32_16x16x32_bf16 a[252:255], a[28:31], v[204:207], a[252:255]\n\t"
    "v_exp_f32 v242, v50\n\t"
    "v_mfma_f32_16x16x32_bf16 a[252:255], a[24:27], v[188:191], a[252:255]\n\t"
    "v_cvt_pk_bf16_f32 v160, v240, v241\n\t"
    "v_mfma_f32_16x16x32_bf16 v[224:227], a[96:99], v[144:147], v[228:231]\n\t"
    "v_exp_f32 v243, v51\n\t"
    "v_mfma_f32_16x16x32_bf16 v[224:227], a[100:103], v[148:151], v[224:227]\n\t"
    "v_exp_f32 v232, v64\n\t"
    "v_mfma_f32_16x16x32_bf16 v[224:227], a[104:107], v[152:155], v[224:227]\n\t"
    "v_exp_f32 v233, v65\n\t"
    "v_mfma_f32_16x16x32_bf16 v[224:227], a[108:111], v[156:159], v[224:227]\n\t"
    "v_exp_f32 v234, v66\n\t"
    "s_waitcnt lgkmcnt(4)\n\t"
    "v_mfma_f32_16x16x32_bf16 v[80:83], a[0:3], a[32:35], v[208:211]\n\t"
    "ds_read_b128 a[24:27], v10 offset:38976\n\t"
    "v_cvt_pk_bf16_f32 v161, v242, v243\n\t"
    "v_mfma_f32_16x16x32_bf16 v[80:83], a[4:7], a[48:51], v[80:83]\n\t"
    "v_exp_f32 v235, v67\n\t"
    "v_mfma_f32_16x16x32_bf16 v[84:87], a[4:7], a[52:55], v[212:215]\n\t"
    "v_exp_f32 v236, v52\n\t"
    "v_mfma_f32_16x16x32_bf16 v[84:87], a[0:3], a[36:39], v[84:87]\n\t"
    "v_exp_f32 v237, v53\n\t"
    "v_mfma_f32_16x16x32_bf16 v[88:91], a[0:3], a[40:43], v[216:219]\n\t"
    "ds_read_b128 a[28:31], v10 offset:40000\n\t"
    "v_cvt_pk_bf16_f32 v162, v232, v233\n\t"
    "v_mfma_f32_16x16x32_bf16 v[88:91], a[4:7], a[56:59], v[88:91]\n\t"
    "v_exp_f32 v238, v54\n\t"
    "v_mfma_f32_16x16x32_bf16 v[92:95], a[4:7], a[60:63], v[220:223]\n\t"
    "v_exp_f32 v239, v55\n\t"
    "v_mfma_f32_16x16x32_bf16 v[92:95], a[0:3], a[44:47], v[92:95]\n\t"
    "v_cvt_pk_bf16_f32 v163, v234, v235\n\t"
    "s_waitcnt lgkmcnt(4)\n\t"
    "v_mfma_f32_16x16x32_bf16 v[80:83], a[8:11], a[64:67], v[80:83]\n\t"
    "ds_read_b128 a[0:3], v10 offset:41024\n\t"
    "v_cvt_pk_bf16_f32 v164, v236, v237\n\t"
    "v_mfma_f32_16x16x32_bf16 v[80:83], a[12:15], a[80:83], v[80:83]\n\t"
    "v_exp_f32 v240, v68\n\t"
    "v_mfma_f32_16x16x32_bf16 v[84:87], a[12:15], a[84:87], v[84:87]\n\t"
    "v_exp_f32 v241, v69\n\t"
    "v_mfma_f32_16x16x32_bf16 v[84:87], a[8:11], a[68:71], v[84:87]\n\t"
    "v_exp_f32 v242, v70\n\t"
    "v_mfma_f32_16x16x32_bf16 v[88:91], a[8:11], a[72:75], v[88:91]\n\t"
    "ds_read_b128 a[4:7], v10 offset:42048\n\t"
    "v_cvt_pk_bf16_f32 v165, v238, v239\n\t"
    "v_mfma_f32_16x16x32_bf16 v[88:91], a[12:15], a[88:91], v[88:91]\n\t"
    "v_exp_f32 v243, v71\n\t"
    "v_mfma_f32_16x16x32_bf16 v[92:95], a[12:15], a[92:95], v[92:95]\n\t"
    "v_exp_f32 v232, v56\n\t"
    "v_mfma_f32_16x16x32_bf16 v[92:95], a[8:11], a[76:79], v[92:95]\n\t"
    "v_cvt_pk_bf16_f32 v166, v240, v241\n\t"
    "s_waitcnt lgkmcnt(4)\n\t"
    "v_mfma_f32_16x16x32_bf16 v[96:99], a[16:19], a[32:35], v[208:211]\n\t"
    "ds_read_b128 a[8:11], v10 offset:43072\n\t"
    "v_cvt_pk_bf16_f32 v167, v242, v243\n\t"
    "v_mfma_f32_16x16x32_bf16 v[96:99], a[20:23], a[48:51], v[96:99]\n\t"
    "v_exp_f32 v233, v57\n\t"
    "v_mfma_f32_16x16x32_bf16 v[100:103], a[20:23], a[52:55], v[212:215]\n\t"
    "v_exp_f32 v234, v58\n\t"
    "v_mfma_f32_16x16x32_bf16 v[100:103], a[16:19], a[36:39], v[100:103]\n\t"
    "v_exp_f32 v235, v59\n\t"
    "v_mfma_f32_16x16x32_bf16 v[104:107], a[16:19], a[40:43], v[216:219]\n\t"
    "ds_read_b128 a[12:15], v10 offset:44096\n\t"
    "v_cvt_pk_bf16_f32 v168, v232, v233\n\t"
    "v_mfma_f32_16x16x32_bf16 v[104:107], a[20:23], a[56:59], v[104:107]\n\t"
    "v_exp_f32 v236, v72\n\t"
    "v_mfma_f32_16x16x32_bf16 v[108:111], a[20:23], a[60:63], v[220:223]\n\t"
    "v_exp_f32 v237, v73\n\t"
    "v_mfma_f32_16x16x32_bf16 v[108:111], a[16:19], a[44:47], v[108:111]\n\t"
    "v_cvt_pk_bf16_f32 v169, v234, v235\n\t"
    "s_waitcnt lgkmcnt(4)\n\t"
    "v_mfma_f32_16x16x32_bf16 v[96:99], a[24:27], a[64:67], v[96:99]\n\t"
    "ds_read_b128 a[16:19], v10 offset:45120\n\t"
    "v_cvt_pk_bf16_f32 v170, v236, v237\n\t"
    "v_mfma_f32_16x16x32_bf16 v[96:99], a[28:31], a[80:83], v[96:99]\n\t"
    "v_exp_f32 v238, v74\n\t"
    "v_mfma_f32_16x16x32_bf16 v[100:103], a[28:31], a[84:87], v[100:103]\n\t"
    "v_exp_f32 v239, v75\n\t"
    "v_mfma_f32_16x16x32_bf16 v[100:103], a[24:27], a[68:71], v[100:103]\n\t"
    "v_exp_f32 v240, v60\n\t"
    "v_mfma_f32_16x16x32_bf16 v[104:107], a[24:27], a[72:75], v[104:107]\n\t"
    "ds_read_b128 a[20:23], v10 offset:46144\n\t"
    "v_cvt_pk_bf16_f32 v171, v238, v239\n\t"
    "v_mfma_f32_16x16x32_bf16 v[104:107], a[28:31], a[88:91], v[104:107]\n\t"
    "v_exp_f32 v241, v61\n\t"
    "v_mfma_f32_16x16x32_bf16 v[108:111], a[28:31], a[92:95], v[108:111]\n\t"
    "v_exp_f32 v242, v62\n\t"
    "v_mfma_f32_16x16x32_bf16 v[108:111], a[24:27], a[76:79], v[108:111]\n\t"
    "v_cvt_pk_bf16_f32 v172, v240, v241\n\t"
    "s_waitcnt lgkmcnt(4)\n\t"
    "v_mfma_f32_16x16x32_bf16 v[112:115], a[0:3], a[32:35], v[208:211]\n\t"
    "ds_read_b128 a[24:27], v10 offset:47168\n\t"
    "v_mfma_f32_16x16x32_bf16 v[112:115], a[4:7], a[48:51], v[112:115]\n\t"
    "v_exp_f32 v243, v63\n\t"
    "v_mfma_f32_16x16x32_bf16 v[116:119], a[4:7], a[52:55], v[212:215]\n\t"
    "v_exp_f32 v232, v76\n\t"
    "v_mfma_f32_16x16x32_bf16 v[116:119], a[0:3], a[36:39], v[116:119]\n\t"
    "v_exp_f32 v233, v77\n\t"
    "v_mfma_f32_16x16x32_bf16 v[120:123], a[0:3], a[40:43], v[216:219]\n\t"
    "ds_read_b128 a[28:31], v10 offset:48192\n\t"
    "v_cvt_pk_bf16_f32 v173, v242, v243\n\t"
    "v_mfma_f32_16x16x32_bf16 v[120:123], a[4:7], a[56:59], v[120:123]\n\t"
    "v_exp_f32 v234, v78\n\t"
    "v_mfma_f32_16x16x32_bf16 v[124:127], a[4:7], a[60:63], v[220:223]\n\t"
    "v_exp_f32 v235, v79\n\t"
    "v_mfma_f32_16x16x32_bf16 v[124:127], a[0:3], a[44:47], v[124:127]\n\t"
    "v_cvt_pk_bf16_f32 v174, v232, v233\n\t"
    "s_waitcnt vmcnt(10) lgkmcnt(4)\n\t"
    "s_barrier\n\t"
    "v_mfma_f32_16x16x32_bf16 v[112:115], a[8:11], a[64:67], v[112:115]\n\t"
    "ds_read_b128 a[0:3], v11 offset:16384\n\t"
    "v_cvt_pk_bf16_f32 v175, v234, v235\n\t"
    "v_mfma_f32_16x16x32_bf16 v[112:115], a[12:15], a[80:83], v[112:115]\n\t"
    "s_add_u32 m0, %[sLds4], 16384\n\t"
    "s_add_u32 s66, %[sKV], 212992\n\t"
    "v_mfma_f32_16x16x32_bf16 v[116:119], a[12:15], a[84:87], v[116:119]\n\t"
    "buffer_load_dwordx4 %[vL16], %[srdWS], s66 offen offset:0 lds\n\t"
    "v_mfma_f32_16x16x32_bf16 v[116:119], a[8:11], a[68:71], v[116:119]\n\t"
    "v_rcp_f32 v247, v247\n\t"
    "v_mfma_f32_16x16x32_bf16 v[120:123], a[8:11], a[72:75], v[120:123]\n\t"
    "ds_read_b128 a[4:7], v11 offset:17408\n\t"
    "v_mfma_f32_16x16x32_bf16 v[120:123], a[12:15], a[88:91], v[120:123]\n\t"
    "v_rcp_f32 v12, v12\n\t"
    "v_mfma_f32_16x16x32_bf16 v[124:127], a[12:15], a[92:95], v[124:127]\n\t"
    "v_rcp_f32 v13, v13\n\t"
    "v_mfma_f32_16x16x32_bf16 v[124:127], a[8:11], a[76:79], v[124:127]\n\t"
    "v_mfma_f32_16x16x32_bf16 v[224:227], a[96:99], v[160:163], v[224:227]\n\t"
    "buffer_load_dwordx4 %[vL16], %[srdWS], s66 offen offset:1024 lds\n\t"
    "v_mfma_f32_16x16x32_bf16 v[224:227], a[100:103], v[164:167], v[224:227]\n\t"
    "v_rcp_f32 v14, v14\n\t"
    "v_mfma_f32_16x16x32_bf16 v[224:227], a[104:107], v[168:171], v[224:227]\n\t"
    "v_rcp_f32 v15, v15\n\t"
    "v_mfma_f32_16x16x32_bf16 v[224:227], a[108:111], v[172:175], v[224:227]\n\t"
    "v_mul_f32 v244, v248, v244\n\t"
    "v_mul_f32 v245, v249, v245\n\t"
    "s_waitcnt lgkmcnt(4)\n\t"
    "v_mfma_f32_16x16x32_bf16 v[128:131], a[16:19], a[32:35], v[208:211]\n\t"
    "ds_read_b128 a[8:11], v11 offset:18432\n\t"
    "v_mul_f32 v246, v250, v246\n\t"
    "v_mfma_f32_16x16x32_bf16 v[128:131], a[20:23], a[48:51], v[128:131]\n\t"
    "v_mul_f32 v247, v251, v247\n\t"
    "v_mul_f32 v12, v252, v12\n\t"
    "k_pga8: s_cbranch_execz k_pga_back\n\t"
    "v_mfma_f32_16x16x32_bf16 v[132:135], a[20:23], a[52:55], v[212:215]\n\t"
    "buffer_load_dwordx4 %[vL16], %[srdWS], s66 offen offset:2048 lds\n\t"
    "v_mul_f32 v13, v253, v13\n\t"
    "v_mfma_f32_16x16x32_bf16 v[132:135], a[16:19], a[36:39], v[132:135]\n\t"
    "v_mul_f32 v14, v254, v14\n\t"
    "v_mul_f32 v15, v255, v15\n\t"
    "v_mfma_f32_16x16x32_bf16 v[136:139], a[16:19], a[40:43], v[216:219]\n\t"
    "ds_read_b128 a[12:15], v11 offset:19456\n\t"
    "v_cvt_pk_bf16_f32 v248, v244, v245\n\t"
    "v_mfma_f32_16x16x32_bf16 v[136:139], a[20:23], a[56:59], v[136:139]\n\t"
    "v_cvt_pk_bf16_f32 v249, v246, v247\n\t"
    "v_cvt_pk_bf16_f32 v250, v12, v13\n\t"
    "v_mfma_f32_16x16x32_bf16 v[140:143], a[20:23], a[60:63], v[220:223]\n\t"
    "v_cvt_pk_bf16_f32 v251, v14, v15\n\t"
    "buffer_store_dwordx4 v[248:251], %[vL16], %[srdWS], %[sHc] offen offset:1024 sc1\n\t"
    "v_mfma_f32_16x16x32_bf16 v[140:143], a[16:19], a[44:47], v[140:143]\n\t"
    "s_nop 0\n\t"
    "ds_read_b128 v[248:251], %[vX] offset:4096\n\t"
    "s_waitcnt lgkmcnt(5)\n\t"
    "v_mfma_f32_16x16x32_bf16 v[128:131], a[24:27], a[64:67], v[128:131]\n\t"
    "ds_read_b128 a[16:19], v11 offset:20480\n\t"
    "buffer_load_dwordx4 %[vL16], %[srdWS], s66 offen offset:3072 lds\n\t"
    "v_mfma_f32_16x16x32_bf16 v[128:131], a[28:31], a[80:83], v[128:131]\n\t"
    "ds_read_b128 v[252:255], %[vX] offset:5120\n\t"
    "v_mfma_f32_16x16x32_bf16 v[132:135], a[28:31], a[84:87], v[132:135]\n\t"
    "v_mfma_f32_16x16x32_bf16 v[132:135], a[24:27], a[68:71], v[132:135]\n\t"
    "v_mfma_f32_16x16x32_bf16 v[136:139], a[24:27], a[72:75], v[136:139]\n\t"
    "ds_read_b128 a[20:23], v11 offset:21504\n\t"
    "v_max3_f32 v232, v224, v225, v226\n\t"
    "v_mfma_f32_16x16x32_bf16 v[136:139], a[28:31], a[88:91], v[136:139]\n\t"
    "v_max_f32 v232, v232, v227\n\t"
    "v_mfma_f32_16x16x32_bf16 v[140:143], a[28:31], a[92:95], v[140:143]\n\t"
    "v_cmp_nge_f32 vcc, s64, v232\n\t"
    "s_getpc_b64 s[60:61]\n\t"
    "v_mfma_f32_16x16x32_bf16 v[140:143], a[24:27], a[76:79], v[140:143]\n\t"
    "s_cbranch_vccnz L_k1_fix0_%=\n\t"
    "s_waitcnt lgkmcnt(6)\n\t"
    "v_mfma_f32_16x16x32_bf16 a[128:131], a[0:3], v[144:147], a[128:131]\n\t"
    "ds_read_b128 a[24:27], v11 offset:22528\n\t"
    "v_mfma_f32_16x16x32_bf16 a[128:131], a[4:7], v[160:163], a[128:131]\n\t"
    "v_exp_f32 v232, v80\n\t"
    "v_mfma_f32_16x16x32_bf16 a[132:135], a[4:7], v[164:167], a[132:135]\n\t"
    "v_exp_f32 v233, v81\n\t"
    "v_mfma_f32_16x16x32_bf16 a[132:135], a[0:3], v[148:151], a[132:135]\n\t"
    "v_exp_f32 v234, v82\n\t"
    "v_mfma_f32_16x16x32_bf16 a[136:139], a[0:3], v[152:155], a[136:139]\n\t"
    "ds_read_b128 a[28:31], v11 offset:23552\n\t"
    "v_cvt_pk_bf16_f32 v176, v232, v233\n\t"
    "v_mfma_f32_16x16x32_bf16 a[136:139], a[4:7], v[168:171], a[136:139]\n\t"
    "v_exp_f32 v235, v83\n\t"
    "v_mfma_f32_16x16x32_bf16 a[140:143], a[4:7], v[172:175], a[140:143]\n\t"
    "v_exp_f32 v236, v96\n\t"
    "v_mfma_f32_16x16x32_bf16 a[140:143], a[0:3], v[156:159], a[140:143]\n\t"
    "v_cvt_pk_bf16_f32 v177, v234, v235\n\t"
    "s_waitcnt lgkmcnt(6)\n\t"
    "v_mfma_f32_16x16x32_bf16 a[144:147], a[8:11], v[144:147], a[144:147]\n\t"
    "ds_read_b128 a[0:3], v11 offset:24576\n\t"
    "v_mfma_f32_16x16x32_bf16 a[144:147], a[12:15], v[160:163], a[144:147]\n\t"
    "v_exp_f32 v237, v97\n\t"
    "v_mfma_f32_16x16x32_bf16 a[148:151], a[12:15], v[164:167], a[148:151]\n\t"
    "v_exp_f32 v238, v98\n\t"
    "v_mfma_f32_16x16x32_bf16 a[148:151], a[8:11], v[148:151], a[148:151]\n\t"
    "v_exp_f32 v239, v99\n\t"
    "v_mfma_f32_16x16x32_bf16 a[152:155], a[8:11], v[152:155], a[152:155]\n\t"
    "ds_read_b128 a[4:7], v11 offset:25600\n\t"
    "v_cvt_pk_bf16_f32 v178, v236, v237\n\t"
    "v_mfma_f32_16x16x32_bf16 a[152:155], a[12:15], v[168:171], a[152:155]\n\t"
    "v_exp_f32 v240, v84\n\t"
    "v_mfma_f32_16x16x32_bf16 a[156:159], a[12:15], v[172:175], a[156:159]\n\t"
    "v_exp_f32 v241, v85\n\t"
    "v_mfma_f32_16x16x32_bf16 a[156:159], a[8:11], v[156:159], a[156:159]\n\t"
    "v_cvt_pk_bf16_f32 v179, v238, v239\n\t"
    "s_waitcnt lgkmcnt(4)\n\t"
    "v_mfma_f32_16x16x32_bf16 a[160:163], a[16:19], v[144:147], a[160:163]\n\t"
    "ds_read_b128 a[8:11], v11 offset:26624\n\t"
    "v_cvt_pk_bf16_f32 v180, v240, v241\n\t"
    "v_mfma_f32_16x16x32_bf16 a[160:163], a[20:23], v[160:163], a[160:163]\n\t"
    "v_exp_f32 v242, v86\n\t"
    "v_mfma_f32_16x16x32_bf16 a[164:167], a[20:23], v[164:167], a[164:167]\n\t"
    "v_exp_f32 v243, v87\n\t"
    "v_mfma_f32_16x16x32_bf16 a[164:167], a[16:19], v[148:151], a[164:167]\n\t"
    "v_exp_f32 v232, v100\n\t"
    "v_mfma_f32_16x16x32_bf16 a[168:171], a[16:19], v[152:155], a[168:171]\n\t"
    "ds_read_b128 a[12:15], v11 offset:27648\n\t"
    "v_cvt_pk_bf16_f32 v181, v242, v243\n\t"
    "v_mfma_f32_16x16x32_bf16 a[168:171], a[20:23], v[168:171], a[168:171]\n\t"
    "v_exp_f32 v233, v101\n\t"
    "v_mfma_f32_16x16x32_bf16 a[172:175], a[20:23], v[172:175], a[172:175]\n\t"
    "v_exp_f32 v234, v102\n\t"
    "v_mfma_f32_16x16x32_bf16 a[172:175], a[16:19], v[156:159], a[172:175]\n\t"
    "v_cvt_pk_bf16_f32 v182, v232, v233\n\t"
    "s_waitcnt lgkmcnt(4)\n\t"
    "v_mfma_f32_16x16x32_bf16 a[176:179], a[24:27], v[144:147], a[176:179]\n\t"
    "ds_read_b128 a[16:19], v11 offset:28672\n\t"
    "v_mul_f32 v244, s62, v248\n\t"
    "v_mfma_f32_16x16x32_bf16 a[176:179], a[28:31], v[160:163], a[176:179]\n\t"
    "v_exp_f32 v235, v103\n\t"
    "v_mfma_f32_16x16x32_bf16 a[180:183], a[28:31], v[164:167], a[180:183]\n\t"
    "v_exp_f32 v236, v88\n\t"
    "v_mfma_f32_16x16x32_bf16 a[180:183], a[24:27], v[148:151], a[180:183]\n\t"
    "v_exp_f32 v237, v89\n\t"
    "v_mfma_f32_16x16x32_bf16 a[184:187], a[24:27], v[152:155], a[184:187]\n\t"
    "ds_read_b128 a[20:23], v11 offset:29696\n\t"
    "v_cvt_pk_bf16_f32 v183, v234, v235\n\t"
    "v_mfma_f32_16x16x32_bf16 a[184:187], a[28:31], v[168:171], a[184:187]\n\t"
    "v_exp_f32 v238, v90\n\t"
    "v_mfma_f32_16x16x32_bf16 a[188:191], a[28:31], v[172:175], a[188:191]\n\t"
    "v_exp_f32 v239, v91\n\t"
    "v_mfma_f32_16x16x32_bf16 a[188:191], a[24:27], v[156:159], a[188:191]\n\t"
    "v_cvt_pk_bf16_f32 v184, v236, v237\n\t"
    "s_waitcnt lgkmcnt(4)\n\t"
    "v_mfma_f32_16x16x32_bf16 a[192:195], a[0:3], v[144:147], a[192:195]\n\t"
    "ds_read_b128 a[24:27], v11 offset:30720\n\t"
    "v_cvt_pk_bf16_f32 v185, v238, v239\n\t"
    "v_mfma_f32_16x16x32_bf16 a[192:195], a[4:7], v[160:163], a[192:195]\n\t"
    "v_exp_f32 v240, v104\n\t"
    "v_mfma_f32_16x16x32_bf16 a[196:199], a[4:7], v[164:167], a[196:199]\n\t"
    "v_exp_f32 v241, v105\n\t"
    "v_mfma_f32_16x16x32_bf16 a[196:199], a[0:3], v[148:151], a[196:199]\n\t"
    "v_exp_f32 v242, v106\n\t"
    "v_mfma_f32_16x16x32_bf16 a[200:203], a[0:3], v[152:155], a[200:203]\n\t"
    "ds_read_b128 a[28:31], v11 offset:31744\n\t"
    "v_cvt_pk_bf16_f32 v186, v240, v241\n\t"
    "v_mfma_f32_16x16x32_bf16 a[200:203], a[4:7], v[168:171], a[200:203]\n\t"
    "v_exp_f32 v243, v107\n\t"
    "v_mfma_f32_16x16x32_bf16 a[204:207], a[4:7], v[172:175], a[204:207]\n\t"
    "v_exp_f32 v232, v92\n\t"
    "v_mfma_f32_16x16x32_bf16 a[204:207], a[0:3], v[156:159], a[204:207]\n\t"
    "v_cvt_pk_bf16_f32 v187, v242, v243\n\t"
    "s_waitcnt lgkmcnt(4)\n\t"
    "v_mfma_f32_16x16x32_bf16 a[208:211], a[8:11], v[144:147], a[208:211]\n\t"
    "ds_read_b128 a[0:3], v11 offset:0\n\t"
    "v_mul_f32 v245, s63, v249\n\t"
    "v_mfma_f32_16x16x32_bf16 a[208:211], a[12:15], v[160:163], a[208:211]\n\t"
    "v_exp_f32 v233, v93\n\t"
    "v_mfma_f32_16x16x32_bf16 a[212:215], a[12:15], v[164:167], a[212:215]\n\t"
    "v_exp_f32 v234, v94\n\t"
    "v_mfma_f32_16x16x32_bf16 a[212:215], a[8:11], v[148:151], a[212:215]\n\t"
    "v_exp_f32 v235, v95\n\t"
    "v_mfma_f32_16x16x32_bf16 a[216:219], a[8:11], v[152:155], a[216:219]\n\t"
    "ds_read_b128 a[4:7], v11 offset:1024\n\t"
    "v_cvt_pk_bf16_f32 v188, v232, v233\n\t"
    "v_mfma_f32_16x16x32_bf16 a[216:219], a[12:15], v[168:171], a[216:219]\n\t"
    "v_exp_f32 v236, v108\n\t"
    "v_mfma_f32_16x16x32_bf16 a[220:223], a[12:15], v[172:175], a[220:223]\n\t"
    "v_exp_f32 v237, v109\n\t"
    "v_mfma_f32_16x16x32_bf16 a[220:223], a[8:11], v[156:159], a[220:223]\n\t"
    "v_cvt_pk_bf16_f32 v189, v234, v235\n\t"
    "s_waitcnt lgkmcnt(4)\n\t"
    "v_mfma_f32_16x16x32_bf16 a[224:227], a[16:19], v[144:147], a[224:227]\n\t"
    "ds_read_b128 a[8:11], v11 offset:2048\n\t"
    "v_cvt_pk_bf16_f32 v190, v236, v237\n\t"
    "v_mfma_f32_16x16x32_bf16 a[224:227], a[20:23], v[160:163], a[224:227]\n\t"
    "v_exp_f32 v238, v110\n\t"
    "v_mfma_f32_16x16x32_bf16 a[228:231], a[20:23], v[164:167], a[228:231]\n\t"
    "v_exp_f32 v239, v111\n\t"
    "v_mfma_f32_16x16x32_bf16 a[228:231], a[16:19], v[148:151], a[228:231]\n\t"
    "v_exp_f32 v240, v112\n\t"
    "v_mfma_f32_16x16x32_bf16 a[232:235], a[16:19], v[152:155], a[232:235]\n\t"
    "ds_read_b128 a[12:15], v11 offset:3072\n\t"
    "v_cvt_pk_bf16_f32 v191, v238, v239\n\t"
    "v_mfma_f32_16x16x32_bf16 a[232:235], a[20:23], v[168:171], a[232:235]\n\t"
    "v_exp_f32 v241, v113\n\t"
    "v_mfma_f32_16x16x32_bf16 a[236:239], a[20:23], v[172:175], a[236:239]\n\t"
    "v_exp_f32 v242, v114\n\t"
    "v_mfma_f32_16x16x32_bf16 a[236:239], a[16:19], v[156:159], a[236:239]\n\t"
    "v_cvt_pk_bf16_f32 v192, v240, v241\n\t"
    "v_mfma_f32_16x16x32_bf16 v[228:231], a[96:99], v[176:179], v[224:227]\n\t"
    "v_exp_f32 v243, v115\n\t"
    "v_mfma_f32_16x16x32_bf16 v[228:231], a[100:103], v[180:183], v[228:231]\n\t"
    "v_exp_f32 v232, v128\n\t"
    "v_mfma_f32_16x16x32_bf16 v[228:231], a[104:107], v[184:187], v[228:231]\n\t"
    "v_exp_f32 v233, v129\n\t"
    "v_mfma_f32_16x16x32_bf16 v[228:231], a[108:111], v[188:191], v[228:231]\n\t"
    "v_exp_f32 v234, v130\n\t"
    "s_waitcnt lgkmcnt(4)\n\t"
    "v_mfma_f32_16x16x32_bf16 a[240:243], a[24:27], v[144:147], a[240:243]\n\t"
    "ds_read_b128 a[16:19], v11 offset:4096\n\t"
    "v_cvt_pk_bf16_f32 v193, v242, v243\n\t"
    "v_mfma_f32_16x16x32_bf16 a[240:243], a[28:31], v[160:163], a[240:243]\n\t"
    "v_exp_f32 v235, v131\n\t"
    "v_mfma_f32_16x16x32_bf16 a[244:247], a[28:31], v[164:167], a[244:247]\n\t"
    "v_exp_f32 v236, v116\n\t"
    "v_mfma_f32_16x16x32_bf16 a[244:247], a[24:27], v[148:151], a[244:247]\n\t"
    "v_exp_f32 v237, v117\n\t"
    "v_mfma_f32_16x16x32_bf16 a[248:251], a[24:27], v[152:155], a[248:251]\n\t"
    "ds_read_b128 a[20:23], v11 offset:5120\n\t"
    "v_cvt_pk_bf16_f32 v194, v232, v233\n\t"
    "v_mfma_f32_16x16x32_bf16 a[248:251], a[28:31], v[168:171], a[248:251]\n\t"
    "v_exp_f32 v238, v118\n\t"
    "v_mfma_f32_16x16x32_bf16 a[252:255], a[28:31], v[172:175], a[252:255]\n\t"
    "v_exp_f32 v239, v119\n\t"
    "v_mfma_f32_16x16x32_bf16 a[252:255], a[24:27], v[156:159], a[252:255]\n\t"
    "v_cvt_pk_bf16_f32 v195, v234, v235\n\t"
    "s_waitcnt lgkmcnt(4)\n\t"
    "v_mfma_f32_16x16x32_bf16 v[16:19], a[0:3], a[32:35], v[208:211]\n\t"
    "ds_read_b128 a[24:27], v11 offset:6144\n\t"
    "v_cvt_pk_bf16_f32 v196, v236, v237\n\t"
    "v_mfma_f32_16x16x32_bf16 v[16:19], a[4:7], a[48:51], v[16:19]\n\t"
    "v_exp_f32 v240, v132\n\t"
    "v_mfma_f32_16x16x32_bf16 v[20:23], a[4:7], a[52:55], v[212:215]\n\t"
    "v_exp_f32 v241, v133\n\t"
    "v_mfma_f32_16x16x32_bf16 v[20:23], a[0:3], a[36:39], v[20:23]\n\t"
    "v_exp_f32 v242, v134\n\t"
    "v_mfma_f32_16x16x32_bf16 v[24:27], a[0:3], a[40:43], v[216:219]\n\t"
    "ds_read_b128 a[28:31], v11 offset:7168\n\t"
    "v_cvt_pk_bf16_f32 v197, v238, v239\n\t"
    "v_mfma_f32_16x16x32_bf16 v[24:27], a[4:7], a[56:59], v[24:27]\n\t"
    "v_exp_f32 v243, v135\n\t"
    "v_mfma_f32_16x16x32_bf16 v[28:31], a[4:7], a[60:63], v[220:223]\n\t"
    "v_exp_f32 v232, v120\n\t"
    "v_mfma_f32_16x16x32_bf16 v[28:31], a[0:3], a[44:47], v[28:31]\n\t"
    "v_cvt_pk_bf16_f32 v198, v240, v241\n\t"
    "s_waitcnt lgkmcnt(4)\n\t"
    "v_mfma_f32_16x16x32_bf16 v[16:19], a[8:11], a[64:67], v[16:19]\n\t"
    "ds_read_b128 a[0:3], v11 offset:8192\n\t"
    "v_cvt_pk_bf16_f32 v199, v242, v243\n\t"
    "v_mfma_f32_16x16x32_bf16 v[16:19], a[12:15], a[80:83], v[16:19]\n\t"
    "v_exp_f32 v233, v121\n\t"
    "v_mfma_f32_16x16x32_bf16 v[20:23], a[12:15], a[84:87], v[20:23]\n\t"
    "v_exp_f32 v234, v122\n\t"
    "v_mfma_f32_16x16x32_bf16 v[20:23], a[8:11], a[68:71], v[20:23]\n\t"
    "v_exp_f32 v235, v123\n\t"
    "v_mfma_f32_16x16x32_bf16 v[24:27], a[8:11], a[72:75], v[24:27]\n\t"
    "ds_read_b128 a[4:7], v11 offset:9216\n\t"
    "v_cvt_pk_bf16_f32 v200, v232, v233\n\t"
    "v_mfma_f32_16x16x32_bf16 v[24:27], a[12:15], a[88:91], v[24:27]\n\t"
    "v_exp_f32 v236, v136\n\t"
    "v_mfma_f32_16x16x32_bf16 v[28:31], a[12:15], a[92:95], v[28:31]\n\t"
    "v_exp_f32 v237, v137\n\t"
    "v_mfma_f32_16x16x32_bf16 v[28:31], a[8:11], a[76:79], v[28:31]\n\t"
    "v_cvt_pk_bf16_f32 v201, v234, v235\n\t"
    "s_waitcnt lgkmcnt(4)\n\t"
    "v_mfma_f32_16x16x32_bf16 v[32:35], a[16:19], a[32:35], v[208:211]\n\t"
    "ds_read_b128 a[8:11], v11 offset:10240\n\t"
    "v_cvt_pk_bf16_f32 v202, v236, v237\n\t"
    "v_mfma_f32_16x16x32_bf16 v[32:35], a[20:23], a[48:51], v[32:35]\n\t"
    "v_exp_f32 v238, v138\n\t"
    "v_mfma_f32_16x16x32_bf16 v[36:39], a[20:23], a[52:55], v[212:215]\n\t"
    "v_exp_f32 v239, v139\n\t"
    "v_mfma_f32_16x16x32_bf16 v[36:39], a[16:19], a[36:39], v[36:39]\n\t"
    "v_exp_f32 v240, v124\n\t"
    "v_mfma_f32_16x16x32_bf16 v[40:43], a[16:19], a[40:43], v[216:219]\n\t"
    "ds_read_b128 a[12:15], v11 offset:11264\n\t"
    "v_cvt_pk_bf16_f32 v203, v238, v239\n\t"
    "v_mfma_f32_16x16x32_bf16 v[40:43], a[20:23], a[56:59], v[40:43]\n\t"
    "v_exp_f32 v241, v125\n\t"
    "v_mfma_f32_16x16x32_bf16 v[44:47], a[20:23], a[60:63], v[220:223]\n\t"
    "v_exp_f32 v242, v126\n\t"
    "v_mfma_f32_16x16x32_bf16 v[44:47], a[16:19], a[44:47], v[44:47]\n\t"
    "v_cvt_pk_bf16_f32 v204, v240, v241\n\t"
    "s_waitcnt lgkmcnt(4)\n\t"
    "v_mfma_f32_16x16x32_bf16 v[32:35], a[24:27], a[64:67], v[32:35]\n\t"
    "ds_read_b128 a[16:19], v11 offset:12288\n\t"
    "v_mul_f32 v246, s62, v250\n\t"
    "v_mfma_f32_16x16x32_bf16 v[32:35], a[28:31], a[80:83], v[32:35]\n\t"
    "v_exp_f32 v243, v127\n\t"
    "v_mfma_f32_16x16x32_bf16 v[36:39], a[28:31], a[84:87], v[36:39]\n\t"
    "v_exp_f32 v232, v140\n\t"
    "v_mfma_f32_16x16x32_bf16 v[36:39], a[24:27], a[68:71], v[36:39]\n\t"
    "v_exp_f32 v233, v141\n\t"
    "v_mfma_f32_16x16x32_bf16 v[40:43], a[24:27], a[72:75], v[40:43]\n\t"
    "ds_read_b128 a[20:23], v11 offset:13312\n\t"
    "v_cvt_pk_bf16_f32 v205, v242, v243\n\t"
    "v_mfma_f32_16x16x32_bf16 v[40:43], a[28:31], a[88:91], v[40:43]\n\t"
    "v_exp_f32 v234, v142\n\t"
    "v_mfma_f32_16x16x32_bf16 v[44:47], a[28:31], a[92:95], v[44:47]\n\t"
    "v_exp_f32 v235, v143\n\t"
    "v_mfma_f32_16x16x32_bf16 v[44:47], a[24:27], a[76:79], v[44:47]\n\t"
    "v_cvt_pk_bf16_f32 v206, v232, v233\n\t"
    "s_waitcnt lgkmcnt(4)\n\t"
    "v_mfma_f32_16x16x32_bf16 v[48:51], a[0:3], a[32:35], v[208:211]\n\t"
    "ds_read_b128 a[24:27], v11 offset:14336\n\t"
    "v_cvt_pk_bf16_f32 v207, v234, v235\n\t"
    "v_mfma_f32_16x16x32_bf16 v[48:51], a[4:7], a[48:51], v[48:51]\n\t"
    "v_mul_f32 v247, s63, v251\n\t"
    "v_mul_f32 v12, s62, v252\n\t"
    "v_mfma_f32_16x16x32_bf16 v[52:55], a[4:7], a[52:55], v[212:215]\n\t"
    "v_mul_f32 v13, s63, v253\n\t"
    "v_mul_f32 v14, s62, v254\n\t"
    "v_mfma_f32_16x16x32_bf16 v[52:55], a[0:3], a[36:39], v[52:55]\n\t"
    "v_mul_f32 v15, s63, v255\n\t"
    "v_mfma_f32_16x16x32_bf16 v[56:59], a[0:3], a[40:43], v[216:219]\n\t"
    "ds_read_b128 a[28:31], v11 offset:15360\n\t"
    "v_mfma_f32_16x16x32_bf16 v[56:59], a[4:7], a[56:59], v[56:59]\n\t"
    "v_exp_f32 v244, v244\n\t"
    "v_mfma_f32_16x16x32_bf16 v[60:63], a[4:7], a[60:63], v[220:223]\n\t"
    "v_exp_f32 v245, v245\n\t"
    "v_mfma_f32_16x16x32_bf16 v[60:63], a[0:3], a[44:47], v[60:63]\n\t"
    "v_mfma_f32_16x16x32_bf16 v[228:231], a[96:99], v[192:195], v[228:231]\n\t"
    "v_exp_f32 v246, v246\n\t"
    "v_mfma_f32_16x16x32_bf16 v[228:231], a[100:103], v[196:199], v[228:231]\n\t"
    "v_exp_f32 v247, v247\n\t"
    "v_mfma_f32_16x16x32_bf16 v[228:231], a[104:107], v[200:203], v[228:231]\n\t"
    "v_exp_f32 v12, v12\n\t"
    "v_mfma_f32_16x16x32_bf16 v[228:231], a[108:111], v[204:207], v[228:231]\n\t"
    "v_exp_f32 v13, v13\n\t"
    "s_waitcnt vmcnt(5) lgkmcnt(4)\n\t"
    "s_barrier\n\t"
    "v_mfma_f32_16x16x32_bf16 v[48:51], a[8:11], a[64:67], v[48:51]\n\t"
    "ds_read_b128 a[0:3], v11 offset:49216\n\t"
    "v_mfma_f32_16x16x32_bf16 v[48:51], a[12:15], a[80:83], v[48:51]\n\t"
    "s_add_u32 m0, %[sLds4], 49216\n\t"
    "s_add_u32 s66, %[sKV], 245760\n\t"
    "v_mfma_f32_16x16x32_bf16 v[52:55], a[12:15], a[84:87], v[52:55]\n\t"
    "buffer_load_dwordx4 %[vL16], %[srdWS], s66 offen offset:0 lds\n\t"
    "v_mfma_f32_16x16x32_bf16 v[52:55], a[8:11], a[68:71], v[52:55]\n\t"
    "v_exp_f32 v14, v14\n\t"
    "v_mfma_f32_16x16x32_bf16 v[56:59], a[8:11], a[72:75], v[56:59]\n\t"
    "ds_read_b128 a[4:7], v11 offset:50240\n\t"
    "v_mfma_f32_16x16x32_bf16 v[56:59], a[12:15], a[88:91], v[56:59]\n\t"
    "v_exp_f32 v15, v15\n\t"
    "v_mfma_f32_16x16x32_bf16 v[60:63], a[12:15], a[92:95], v[60:63]\n\t"
    "v_add_f32 v244, 1.0, v244\n\t"
    "v_add_f32 v245, 1.0, v245\n\t"
    "v_mfma_f32_16x16x32_bf16 v[60:63], a[8:11], a[76:79], v[60:63]\n\t"
    "v_add_f32 v246, 1.0, v246\n\t"
    "s_waitcnt lgkmcnt(4)\n\t"
    "v_mfma_f32_16x16x32_bf16 v[64:67], a[16:19], a[32:35], v[208:211]\n\t"
    "ds_read_b128 a[8:11], v11 offset:51264\n\t"
    "buffer_load_dwordx4 %[vL16], %[srdWS], s66 offen offset:1024 lds\n\t"
    "v_mfma_f32_16x16x32_bf16 v[64:67], a[20:23], a[48:51], v[64:67]\n\t"
    "v_add_f32 v247, 1.0, v247\n\t"
    "v_add_f32 v12, 1.0, v12\n\t"
    "v_mfma_f32_16x16x32_bf16 v[68:71], a[20:23], a[52:55], v[212:215]\n\t"
    "v_add_f32 v13, 1.0, v13\n\t"
    "v_add_f32 v14, 1.0, v14\n\t"
    "v_mfma_f32_16x16x32_bf16 v[68:71], a[16:19], a[36:39], v[68:71]\n\t"
    "v_add_f32 v15, 1.0, v15\n\t"
    "v_mfma_f32_16x16x32_bf16 v[72:75], a[16:19], a[40:43], v[216:219]\n\t"
    "ds_read_b128 a[12:15], v11 offset:52288\n\t"
    "v_mfma_f32_16x16x32_bf16 v[72:75], a[20:23], a[56:59], v[72:75]\n\t"
    "v_rcp_f32 v244, v244\n\t"
    "v_mfma_f32_16x16x32_bf16 v[76:79], a[20:23], a[60:63], v[220:223]\n\t"
    "buffer_load_dwordx4 %[vL16], %[srdWS], s66 offen offset:2048 lds\n\t"
    "v_mfma_f32_16x16x32_bf16 v[76:79], a[16:19], a[44:47], v[76:79]\n\t"
    "s_waitcnt lgkmcnt(4)\n\t"
    "v_mfma_f32_16x16x32_bf16 v[64:67], a[24:27], a[64:67], v[64:67]\n\t"
    "ds_read_b128 a[16:19], v11 offset:53312\n\t"
    "v_mfma_f32_16x16x32_bf16 v[64:67], a[28:31], a[80:83], v[64:67]\n\t"
    "v_rcp_f32 v245, v245\n\t"
    "v_mfma_f32_16x16x32_bf16 v[68:71], a[28:31], a[84:87], v[68:71]\n\t"
    "v_rcp_f32 v246, v246\n\t"
    "v_mfma_f32_16x16x32_bf16 v[68:71], a[24:27], a[68:71], v[68:71]\n\t"
    "v_rcp_f32 v247, v247\n\t"
    "v_mfma_f32_16x16x32_bf16 v[72:75], a[24:27], a[72:75], v[72:75]\n\t"
    "ds_read_b128 a[20:23], v11 offset:54336\n\t"
    "v_max3_f32 v232, v228, v229, v230\n\t"
    "v_mfma_f32_16x16x32_bf16 v[72:75], a[28:31], a[88:91], v[72:75]\n\t"
    "v_max_f32 v232, v232, v231\n\t"
    "buffer_load_dwordx4 %[vL16], %[srdWS], s66 offen offset:3072 lds\n\t"
    "v_mfma_f32_16x16x32_bf16 v[76:79], a[28:31], a[92:95], v[76:79]\n\t"
    "v_cmp_nge_f32 vcc, s64, v232\n\t"
    "s_getpc_b64 s[60:61]\n\t"
    "v_mfma_f32_16x16x32_bf16 v[76:79], a[24:27], a[76:79], v[76:79]\n\t"
    "s_cbranch_vccnz L_k1_fix1_%=\n\t"
    "s_waitcnt lgkmcnt(4)\n\t"
    "v_mfma_f32_16x16x32_bf16 a[128:131], a[0:3], v[176:179], a[128:131]\n\t"
    "ds_read_b128 a[24:27], v11 offset:55360\n\t"
    "v_mfma_f32_16x16x32_bf16 a[128:131], a[4:7], v[192:195], a[128:131]\n\t"
    "v_exp_f32 v232, v16\n\t"
    "v_mfma_f32_16x16x32_bf16 a[132:135], a[4:7], v[196:199], a[132:135]\n\t"
    "v_exp_f32 v233, v17\n\t"
    "v_mfma_f32_16x16x32_bf16 a[132:135], a[0:3], v[180:183], a[132:135]\n\t"
    "v_exp_f32 v234, v18\n\t"
    "v_mfma_f32_16x16x32_bf16 a[136:139], a[0:3], v[184:187], a[136:139]\n\t"
    "ds_read_b128 a[28:31], v11 offset:56384\n\t"
    "v_cvt_pk_bf16_f32 v144, v232, v233\n\t"
    "v_mfma_f32_16x16x32_bf16 a[136:139], a[4:7], v[200:203], a[136:139]\n\t"
    "v_exp_f32 v235, v19\n\t"
    "v_mfma_f32_16x16x32_bf16 a[140:143], a[4:7], v[204:207], a[140:143]\n\t"
    "v_exp_f32 v236, v32\n\t"
    "v_mfma_f32_16x16x32_bf16 a[140:143], a[0:3], v[188:191], a[140:143]\n\t"
    "v_cvt_pk_bf16_f32 v145, v234, v235\n\t"
    "s_waitcnt lgkmcnt(4)\n\t"
    "v_mfma_f32_16x16x32_bf16 a[144:147], a[8:11], v[176:179], a[144:147]\n\t"
    "ds_read_b128 a[0:3], v11 offset:57408\n\t"
    "v_mfma_f32_16x16x32_bf16 a[144:147], a[12:15], v[192:195], a[144:147]\n\t"
    "v_exp_f32 v237, v33\n\t"
    "v_mfma_f32_16x16x32_bf16 a[148:151], a[12:15], v[196:199], a[148:151]\n\t"
    "v_exp_f32 v238, v34\n\t"
    "v_mfma_f32_16x16x32_bf16 a[148:151], a[8:11], v[180:183], a[148:151]\n\t"
    "v_exp_f32 v239, v35\n\t"
    "v_mfma_f32_16x16x32_bf16 a[152:155], a[8:11], v[184:187], a[152:155]\n\t"
    "ds_read_b128 a[4:7], v11 offset:58432\n\t"
    "v_cvt_pk_bf16_f32 v146, v236, v237\n\t"
    "v_mfma_f32_16x16x32_bf16 a[152:155], a[12:15], v[200:203], a[152:155]\n\t"
    "v_exp_f32 v240, v20\n\t"
    "v_mfma_f32_16x16x32_bf16 a[156:159], a[12:15], v[204:207], a[156:159]\n\t"
    "v_exp_f32 v241, v21\n\t"
    "v_mfma_f32_16x16x32_bf16 a[156:159], a[8:11], v[188:191], a[156:159]\n\t"
    "v_cvt_pk_bf16_f32 v147, v238, v239\n\t"
    "s_waitcnt lgkmcnt(4)\n\t"
    "v_mfma_f32_16x16x32_bf16 a[160:163], a[16:19], v[176:179], a[160:163]\n\t"
    "ds_read_b128 a[8:11], v11 offset:59456\n\t"
    "v_cvt_pk_bf16_f32 v148, v240, v241\n\t"
    "v_mfma_f32_16x16x32_bf16 a[160:163], a[20:23], v[192:195], a[160:163]\n\t"
    "v_exp_f32 v242, v22\n\t"
    "v_mfma_f32_16x16x32_bf16 a[164:167], a[20:23], v[196:199], a[164:167]\n\t"
    "v_exp_f32 v243, v23\n\t"
    "v_mfma_f32_16x16x32_bf16 a[164:167], a[16:19], v[180:183], a[164:167]\n\t"
    "v_exp_f32 v232, v36\n\t"
    "v_mfma_f32_16x16x32_bf16 a[168:171], a[16:19], v[184:187], a[168:171]\n\t"
    "ds_read_b128 a[12:15], v11 offset:60480\n\t"
    "v_cvt_pk_bf16_f32 v149, v242, v243\n\t"
    "v_mfma_f32_16x16x32_bf16 a[168:171], a[20:23], v[200:203], a[168:171]\n\t"
    "v_exp_f32 v233, v37\n\t"
    "v_mfma_f32_16x16x32_bf16 a[172:175], a[20:23], v[204:207], a[172:175]\n\t"
    "v_exp_f32 v234, v38\n\t"
    "v_mfma_f32_16x16x32_bf16 a[172:175], a[16:19], v[188:191], a[172:175]\n\t"
    "v_cvt_pk_bf16_f32 v150, v232, v233\n\t"
    "s_waitcnt lgkmcnt(4)\n\t"
    "v_mfma_f32_16x16x32_bf16 a[176:179], a[24:27], v[176:179], a[176:179]\n\t"
    "ds_read_b128 a[16:19], v11 offset:61504\n\t"
    "v_mfma_f32_16x16x32_bf16 a[176:179], a[28:31], v[192:195], a[176:179]\n\t"
    "v_exp_f32 v235, v39\n\t"
    "v_mfma_f32_16x16x32_bf16 a[180:183], a[28:31], v[196:199], a[180:183]\n\t"
    "v_exp_f32 v236, v24\n\t"
    "v_mfma_f32_16x16x32_bf16 a[180:183], a[24:27], v[180:183], a[180:183]\n\t"
    "v_exp_f32 v237, v25\n\t"
    "v_mfma_f32_16x16x32_bf16 a[184:187], a[24:27], v[184:187], a[184:187]\n\t"
    "ds_read_b128 a[20:23], v11 offset:62528\n\t"
    "v_cvt_pk_bf16_f32 v151, v234, v235\n\t"
    "v_mfma_f32_16x16x32_bf16 a[184:187], a[28:31], v[200:203], a[184:187]\n\t"
    "v_exp_f32 v238, v26\n\t"
    "v_mfma_f32_16x16x32_bf16 a[188:191], a[28:31], v[204:207], a[188:191]\n\t"
    "v_exp_f32 v239, v27\n\t"
    "v_mfma_f32_16x16x32_bf16 a[188:191], a[24:27], v[188:191], a[188:191]\n\t"
    "v_cvt_pk_bf16_f32 v152, v236, v237\n\t"
    "s_waitcnt lgkmcnt(4)\n\t"
    "v_mfma_f32_16x16x32_bf16 a[192:195], a[0:3], v[176:179], a[192:195]\n\t"
    "ds_read_b128 a[24:27], v11 offset:63552\n\t"
    "v_cvt_pk_bf16_f32 v153, v238, v239\n\t"
    "v_mfma_f32_16x16x32_bf16 a[192:195], a[4:7], v[192:195], a[192:195]\n\t"
    "v_exp_f32 v240, v40\n\t"
    "v_mfma_f32_16x16x32_bf16 a[196:199], a[4:7], v[196:199], a[196:199]\n\t"
    "v_exp_f32 v241, v41\n\t"
    "v_mfma_f32_16x16x32_bf16 a[196:199], a[0:3], v[180:183], a[196:199]\n\t"
    "v_exp_f32 v242, v42\n\t"
    "v_mfma_f32_16x16x32_bf16 a[200:203], a[0:3], v[184:187], a[200:203]\n\t"
    "ds_read_b128 a[28:31], v11 offset:64576\n\t"
    "v_cvt_pk_bf16_f32 v154, v240, v241\n\t"
    "v_mfma_f32_16x16x32_bf16 a[200:203], a[4:7], v[200:203], a[200:203]\n\t"
    "v_exp_f32 v243, v43\n\t"
    "v_mfma_f32_16x16x32_bf16 a[204:207], a[4:7], v[204:207], a[204:207]\n\t"
    "v_exp_f32 v232, v28\n\t"
    "v_mfma_f32_16x16x32_bf16 a[204:207], a[0:3], v[188:191], a[204:207]\n\t"
    "v_cvt_pk_bf16_f32 v155, v242, v243\n\t"
    "s_waitcnt lgkmcnt(4)\n\t"
    "v_mfma_f32_16x16x32_bf16 a[208:211], a[8:11], v[176:179], a[208:211]\n\t"
    "ds_read_b128 a[0:3], v11 offset:32832\n\t"
    "v_mfma_f32_16x16x32_bf16 a[208:211], a[12:15], v[192:195], a[208:211]\n\t"
    "v_exp_f32 v233, v29\n\t"
    "v_mfma_f32_16x16x32_bf16 a[212:215], a[12:15], v[196:199], a[212:215]\n\t"
    "v_exp_f32 v234, v30\n\t"
    "v_mfma_f32_16x16x32_bf16 a[212:215], a[8:11], v[180:183], a[212:215]\n\t"
    "v_exp_f32 v235, v31\n\t"
    "v_mfma_f32_16x16x32_bf16 a[216:219], a[8:11], v[184:187], a[216:219]\n\t"
    "ds_read_b128 a[4:7], v11 offset:33856\n\t"
    "v_cvt_pk_bf16_f32 v156, v232, v233\n\t"
    "v_mfma_f32_16x16x32_bf16 a[216:219], a[12:15], v[200:203], a[216:219]\n\t"
    "v_exp_f32 v236, v44\n\t"
    "v_mfma_f32_16x16x32_bf16 a[220:223], a[12:15], v[204:207], a[220:223]\n\t"
    "v_exp_f32 v237, v45\n\t"
    "v_mfma_f32_16x16x32_bf16 a[220:223], a[8:11], v[188:191], a[220:223]\n\t"
    "v_cvt_pk_bf16_f32 v157, v234, v235\n\t"
    "s_waitcnt lgkmcnt(4)\n\t"
    "v_mfma_f32_16x16x32_bf16 a[224:227], a[16:19], v[176:179], a[224:227]\n\t"
    "ds_read_b128 a[8:11], v11 offset:34880\n\t"
    "v_cvt_pk_bf16_f32 v158, v236, v237\n\t"
    "v_mfma_f32_16x16x32_bf16 a[224:227], a[20:23], v[192:195], a[224:227]\n\t"
    "v_exp_f32 v238, v46\n\t"
    "v_mfma_f32_16x16x32_bf16 a[228:231], a[20:23], v[196:199], a[228:231]\n\t"
    "v_exp_f32 v239, v47\n\t"
    "v_mfma_f32_16x16x32_bf16 a[228:231], a[16:19], v[180:183], a[228:231]\n\t"
    "v_exp_f32 v240, v48\n\t"
    "v_mfma_f32_16x16x32_bf16 a[232:235], a[16:19], v[184:187], a[232:235]\n\t"
    "ds_read_b128 a[12:15], v11 offset:35904\n\t"
    "v_cvt_pk_bf16_f32 v159, v238, v239\n\t"
    "v_mfma_f32_16x16x32_bf16 a[232:235], a[20:23], v[200:203], a[232:235]\n\t"
    "v_exp_f32 v241, v49\n\t"
    "v_mfma_f32_16x16x32_bf16 a[236:239], a[20:23], v[204:207], a[236:239]\n\t"
    "v_exp_f32 v242, v50\n\t"
    "v_mfma_f32_16x16x32_bf16 a[236:239], a[16:19], v[188:191], a[236:239]\n\t"
    "v_cvt_pk_bf16_f32 v160, v240, v241\n\t"
    "v_mfma_f32_16x16x32_bf16 v[224:227], a[96:99], v[144:147], v[228:231]\n\t"
    "v_exp_f32 v243, v51\n\t"
    "v_mfma_f32_16x16x32_bf16 v[224:227], a[100:103], v[148:151], v[224:227]\n\t"
    "v_exp_f32 v232, v64\n\t"
    "v_mfma_f32_16x16x32_bf16 v[224:227], a[104:107], v[152:155], v[224:227]\n\t"
    "v_exp_f32 v233, v65\n\t"
    "v_mfma_f32_16x16x32_bf16 v[224:227], a[108:111], v[156:159], v[224:227]\n\t"
    "v_exp_f32 v234, v66\n\t"
    "s_waitcnt lgkmcnt(4)\n\t"
    "v_mfma_f32_16x16x32_bf16 a[240:243], a[24:27], v[176:179], a[240:243]\n\t"
    "ds_read_b128 a[16:19], v11 offset:36928\n\t"
    "v_cvt_pk_bf16_f32 v161, v242, v243\n\t"
    "v_mfma_f32_16x16x32_bf16 a[240:243], a[28:31], v[192:195], a[240:243]\n\t"
    "v_exp_f32 v235, v67\n\t"
    "v_mfma_f32_16x16x32_bf16 a[244:247], a[28:31], v[196:199], a[244:247]\n\t"
    "v_exp_f32 v236, v52\n\t"
    "v_mfma_f32_16x16x32_bf16 a[244:247], a[24:27], v[180:183], a[244:247]\n\t"
    "v_exp_f32 v237, v53\n\t"
    "v_mfma_f32_16x16x32_bf16 a[248:251], a[24:27], v[184:187], a[248:251]\n\t"
    "ds_read_b128 a[20:23], v11 offset:37952\n\t"
    "v_cvt_pk_bf16_f32 v162, v232, v233\n\t"
    "v_mfma_f32_16x16x32_bf16 a[248:251], a[28:31], v[200:203], a[248:251]\n\t"
    "v_exp_f32 v238, v54\n\t"
    "v_mfma_f32_16x16x32_bf16 a[252:255], a[28:31], v[204:207], a[252:255]\n\t"
    "v_exp_f32 v239, v55\n\t"
    "v_mfma_f32_16x16x32_bf16 a[252:255], a[24:27], v[188:191], a[252:255]\n\t"
    "v_cvt_pk_bf16_f32 v163, v234, v235\n\t"
    "s_waitcnt lgkmcnt(4)\n\t"
    "v_mfma_f32_16x16x32_bf16 v[80:83], a[0:3], a[32:35], v[208:211]\n\t"
    "ds_read_b128 a[24:27], v11 offset:38976\n\t"
    "v_cvt_pk_bf16_f32 v164, v236, v237\n\t"
    "v_mfma_f32_16x16x32_bf16 v[80:83], a[4:7], a[48:51], v[80:83]\n\t"
    "v_exp_f32 v240, v68\n\t"
    "v_mfma_f32_16x16x32_bf16 v[84:87], a[4:7], a[52:55], v[212:215]\n\t"
    "v_exp_f32 v241, v69\n\t"
    "v_mfma_f32_16x16x32_bf16 v[84:87], a[0:3], a[36:39], v[84:87]\n\t"
    "v_exp_f32 v242, v70\n\t"
    "v_mfma_f32_16x16x32_bf16 v[88:91], a[0:3], a[40:43], v[216:219]\n\t"
    "ds_read_b128 a[28:31], v11 offset:40000\n\t"
    "v_cvt_pk_bf16_f32 v165, v238, v239\n\t"
    "v_mfma_f32_16x16x32_bf16 v[88:91], a[4:7], a[56:59], v[88:91]\n\t"
    "v_exp_f32 v243, v71\n\t"
    "v_mfma_f32_16x16x32_bf16 v[92:95], a[4:7], a[60:63], v[220:223]\n\t"
    "v_exp_f32 v232, v56\n\t"
    "v_mfma_f32_16x16x32_bf16 v[92:95], a[0:3], a[44:47], v[92:95]\n\t"
    "v_cvt_pk_bf16_f32 v166, v240, v241\n\t"
    "s_waitcnt lgkmcnt(4)\n\t"
    "v_mfma_f32_16x16x32_bf16 v[80:83], a[8:11], a[64:67], v[80:83]\n\t"
    "ds_read_b128 a[0:3], v11 offset:41024\n\t"
    "v_cvt_pk_bf16_f32 v167, v242, v243\n\t"
    "v_mfma_f32_16x16x32_bf16 v[80:83], a[12:15], a[80:83], v[80:83]\n\t"
    "v_exp_f32 v233, v57\n\t"
    "v_mfma_f32_16x16x32_bf16 v[84:87], a[12:15], a[84:87], v[84:87]\n\t"
    "v_exp_f32 v234, v58\n\t"
    "v_mfma_f32_16x16x32_bf16 v[84:87], a[8:11], a[68:71], v[84:87]\n\t"
    "v_exp_f32 v235, v59\n\t"
    "v_mfma_f32_16x16x32_bf16 v[88:91], a[8:11], a[72:75], v[88:91]\n\t"
    "ds_read_b128 a[4:7], v11 offset:42048\n\t"
    "v_cvt_pk_bf16_f32 v168, v232, v233\n\t"
    "v_mfma_f32_16x16x32_bf16 v[88:91], a[12:15], a[88:91], v[88:91]\n\t"
    "v_exp_f32 v236, v72\n\t"
    "v_mfma_f32_16x16x32_bf16 v[92:95], a[12:15], a[92:95], v[92:95]\n\t"
    "v_exp_f32 v237, v73\n\t"
    "v_mfma_f32_16x16x32_bf16 v[92:95], a[8:11], a[76:79], v[92:95]\n\t"
    "v_cvt_pk_bf16_f32 v169, v234, v235\n\t"
    "s_waitcnt lgkmcnt(4)\n\t"
    "v_mfma_f32_16x16x32_bf16 v[96:99], a[16:19], a[32:35], v[208:211]\n\t"
    "ds_read_b128 a[8:11], v11 offset:43072\n\t"
    "v_cvt_pk_bf16_f32 v170, v236, v237\n\t"
    "v_mfma_f32_16x16x32_bf16 v[96:99], a[20:23], a[48:51], v[96:99]\n\t"
    "v_exp_f32 v238, v74\n\t"
    "v_mfma_f32_16x16x32_bf16 v[100:103], a[20:23], a[52:55], v[212:215]\n\t"
    "v_exp_f32 v239, v75\n\t"
    "v_mfma_f32_16x16x32_bf16 v[100:103], a[16:19], a[36:39], v[100:103]\n\t"
    "v_exp_f32 v240, v60\n\t"
    "v_mfma_f32_16x16x32_bf16 v[104:107], a[16:19], a[40:43], v[216:219]\n\t"
    "ds_read_b128 a[12:15], v11 offset:44096\n\t"
    "v_cvt_pk_bf16_f32 v171, v238, v239\n\t"
    "v_mfma_f32_16x16x32_bf16 v[104:107], a[20:23], a[56:59], v[104:107]\n\t"
    "v_exp_f32 v241, v61\n\t"
    "v_mfma_f32_16x16x32_bf16 v[108:111], a[20:23], a[60:63], v[220:223]\n\t"
    "v_exp_f32 v242, v62\n\t"
    "v_mfma_f32_16x16x32_bf16 v[108:111], a[16:19], a[44:47], v[108:111]\n\t"
    "v_cvt_pk_bf16_f32 v172, v240, v241\n\t"
    "s_waitcnt lgkmcnt(4)\n\t"
    "v_mfma_f32_16x16x32_bf16 v[96:99], a[24:27], a[64:67], v[96:99]\n\t"
    "ds_read_b128 a[16:19], v11 offset:45120\n\t"
    "v_mfma_f32_16x16x32_bf16 v[96:99], a[28:31], a[80:83], v[96:99]\n\t"
    "v_exp_f32 v243, v63\n\t"
    "v_mfma_f32_16x16x32_bf16 v[100:103], a[28:31], a[84:87], v[100:103]\n\t"
    "v_exp_f32 v232, v76\n\t"
    "v_mfma_f32_16x16x32_bf16 v[100:103], a[24:27], a[68:71], v[100:103]\n\t"
    "v_exp_f32 v233, v77\n\t"
    "v_mfma_f32_16x16x32_bf16 v[104:107], a[24:27], a[72:75], v[104:107]\n\t"
    "ds_read_b128 a[20:23], v11 offset:46144\n\t"
    "v_cvt_pk_bf16_f32 v173, v242, v243\n\t"
    "v_mfma_f32_16x16x32_bf16 v[104:107], a[28:31], a[88:91], v[104:107]\n\t"
    "v_exp_f32 v234, v78\n\t"
    "v_mfma_f32_16x16x32_bf16 v[108:111], a[28:31], a[92:95], v[108:111]\n\t"
    "v_exp_f32 v235, v79\n\t"
    "v_mfma_f32_16x16x32_bf16 v[108:111], a[24:27], a[76:79], v[108:111]\n\t"
    "v_cvt_pk_bf16_f32 v174, v232, v233\n\t"
    "s_waitcnt lgkmcnt(4)\n\t"
    "v_mfma_f32_16x16x32_bf16 v[112:115], a[0:3], a[32:35], v[208:211]\n\t"
    "ds_read_b128 a[24:27], v11 offset:47168\n\t"
    "v_cvt_pk_bf16_f32 v175, v234, v235\n\t"
    "v_mfma_f32_16x16x32_bf16 v[112:115], a[4:7], a[48:51], v[112:115]\n\t"
    "v_rcp_f32 v12, v12\n\t"
    "v_mfma_f32_16x16x32_bf16 v[116:119], a[4:7], a[52:55], v[212:215]\n\t"
    "v_rcp_f32 v13, v13\n\t"
    "v_mfma_f32_16x16x32_bf16 v[116:119], a[0:3], a[36:39], v[116:119]\n\t"
    "v_rcp_f32 v14, v14\n\t"
    "v_mfma_f32_16x16x32_bf16 v[120:123], a[0:3], a[40:43], v[216:219]\n\t"
    "ds_read_b128 a[28:31], v11 offset:48192\n\t"
    "v_mfma_f32_16x16x32_bf16 v[120:123], a[4:7], a[56:59], v[120:123]\n\t"
    "v_rcp_f32 v15, v15\n\t"
    "v_mfma_f32_16x16x32_bf16 v[124:127], a[4:7], a[60:63], v[220:223]\n\t"
    "v_mul_f32 v244, v248, v244\n\t"
    "v_mul_f32 v245, v249, v245\n\t"
    "v_mfma_f32_16x16x32_bf16 v[124:127], a[0:3], a[44:47], v[124:127]\n\t"
    "v_mul_f32 v246, v250, v246\n\t"
    "v_mfma_f32_16x16x32_bf16 v[224:227], a[96:99], v[160:163], v[224:227]\n\t"
    "v_mul_f32 v247, v251, v247\n\t"
    "v_mul_f32 v12, v252, v12\n\t"
    "v_mfma_f32_16x16x32_bf16 v[224:227], a[100:103], v[164:167], v[224:227]\n\t"
    "v_mul_f32 v13, v253, v13\n\t"
    "v_mul_f32 v14, v254, v14\n\t"
    "v_mfma_f32_16x16x32_bf16 v[224:227], a[104:107], v[168:171], v[224:227]\n\t"
    "v_mul_f32 v15, v255, v15\n\t"
    "v_cvt_pk_bf16_f32 v248, v244, v245\n\t"
    "v_mfma_f32_16x16x32_bf16 v[224:227], a[108:111], v[172:175], v[224:227]\n\t"
    "v_cvt_pk_bf16_f32 v249, v246, v247\n\t"
    "k_pga9: s_cbranch_execz k_pga_back\n\t"
    "v_cvt_pk_bf16_f32 v250, v12, v13\n\t"
    "s_waitcnt vmcnt(4) lgkmcnt(4)\n\t"
    "s_barrier\n\t"
    "v_mfma_f32_16x16x32_bf16 v[112:115], a[8:11], a[64:67], v[112:115]\n\t"
    "ds_read_b128 a[0:3], v10 offset:16384\n\t"
    "v_cvt_pk_bf16_f32 v251, v14, v15\n\t"
    "v_mfma_f32_16x16x32_bf16 v[112:115], a[12:15], a[80:83], v[112:115]\n\t"
    "buffer_store_dwordx4 v[248:251], %[vL16], %[srdWS], %[sHc] offen offset:2048 sc1\n\t"
    "s_nop 1\n\t"
    "v_accvgpr_read_b32 v248, a112\n\t"
    "v_mfma_f32_16x16x32_bf16 v[116:119], a[12:15], a[84:87], v[116:119]\n\t"
    "v_accvgpr_read_b32 v249, a113\n\t"
    "v_accvgpr_read_b32 v250, a114\n\t"
    "v_mfma_f32_16x16x32_bf16 v[116:119], a[8:11], a[68:71], v[116:119]\n\t"
    "v_accvgpr_read_b32 v251, a115\n\t"
    "v_accvgpr_read_b32 v252, a116\n\t"
    "v_mfma_f32_16x16x32_bf16 v[120:123], a[8:11], a[72:75], v[120:123]\n\t"
    "ds_read_b128 a[4:7], v10 offset:17408\n\t"
    "v_accvgpr_read_b32 v253, a117\n\t"
    "v_mfma_f32_16x16x32_bf16 v[120:123], a[12:15], a[88:91], v[120:123]\n\t"
    "v_accvgpr_read_b32 v254, a118\n\t"
    "v_accvgpr_read_b32 v255, a119\n\t"
    "v_mfma_f32_16x16x32_bf16 v[124:127], a[12:15], a[92:95], v[124:127]\n\t"
    "v_mul_f32 v244, s62, v248\n\t"
    "v_mul_f32 v245, s63, v249\n\t"
    "v_mfma_f32_16x16x32_bf16 v[124:127], a[8:11], a[76:79], v[124:127]\n\t"
    "v_mul_f32 v246, s62, v250\n\t"
    "s_waitcnt lgkmcnt(4)\n\t"
    "v_mfma_f32_16x16x32_bf16 v[128:131], a[16:19], a[32:35], v[208:211]\n\t"
    "ds_read_b128 a[8:11], v10 offset:18432\n\t"
    "v_mul_f32 v247, s63, v251\n\t"
    "v_mfma_f32_16x16x32_bf16 v[128:131], a[20:23], a[48:51], v[128:131]\n\t"
    "v_mul_f32 v12, s62, v252\n\t"
    "v_mul_f32 v13, s63, v253\n\t"
    "v_mfma_f32_16x16x32_bf16 v[132:135], a[20:23], a[52:55], v[212:215]\n\t"
    "v_mul_f32 v14, s62, v254\n\t"
    "v_mul_f32 v15, s63, v255\n\t"
    "v_mfma_f32_16x16x32_bf16 v[132:135], a[16:19], a[36:39], v[132:135]\n\t"
    "v_exp_f32 v244, v244\n\t"
    "v_mfma_f32_16x16x32_bf16 v[136:139], a[16:19], a[40:43], v[216:219]\n\t"
    "ds_read_b128 a[12:15], v10 offset:19456\n\t"
    "v_mfma_f32_16x16x32_bf16 v[136:139], a[20:23], a[56:59], v[136:139]\n\t"
    "v_exp_f32 v245, v245\n\t"
    "v_mfma_f32_16x16x32_bf16 v[140:143], a[20:23], a[60:63], v[220:223]\n\t"
    "v_exp_f32 v246, v246\n\t"
    "v_mfma_f32_16x16x32_bf16 v[140:143], a[16:19], a[44:47], v[140:143]\n\t"
    "s_waitcnt lgkmcnt(4)\n\t"
    "v_mfma_f32_16x16x32_bf16 v[128:131], a[24:27], a[64:67], v[128:131]\n\t"
    "ds_read_b128 a[16:19], v10 offset:20480\n\t"
    "v_mfma_f32_16x16x32_bf16 v[128:131], a[28:31], a[80:83], v[128:131]\n\t"
    "v_exp_f32 v247, v247\n\t"
    "v_mfma_f32_16x16x32_bf16 v[132:135], a[28:31], a[84:87], v[132:135]\n\t"
    "v_exp_f32 v12, v12\n\t"
    "v_mfma_f32_16x16x32_bf16 v[132:135], a[24:27], a[68:71], v[132:135]\n\t"
    "v_exp_f32 v13, v13\n\t"
    "v_mfma_f32_16x16x32_bf16 v[136:139], a[24:27], a[72:75], v[136:139]\n\t"
    "ds_read_b128 a[20:23], v10 offset:21504\n\t"
    "v_max3_f32 v232, v224, v225, v226\n\t"
    "v_mfma_f32_16x16x32_bf16 v[136:139], a[28:31], a[88:91], v[136:139]\n\t"
    "v_max_f32 v232, v232, v227\n\t"
    "v_mfma_f32_16x16x32_bf16 v[140:143], a[28:31], a[92:95], v[140:143]\n\t"
    "v_cmp_nge_f32 vcc, s64, v232\n\t"
    "s_getpc_b64 s[60:61]\n\t"
    "v_mfma_f32_16x16x32_bf16 v[140:143], a[24:27], a[76:79], v[140:143]\n\t"
    "s_cbranch_vccnz L_k1_fix0_%=\n\t"
    "v_exp_f32 v14, v14\n\t"
    "v_exp_f32 v15, v15\n\t"
    "v_pk_add_f32 v[244:245], v[244:245], 1.0 op_sel_hi:[1,0]\n\t"
    "v_pk_add_f32 v[246:247], v[246:247], 1.0 op_sel_hi:[1,0]\n\t"
    "v_pk_add_f32 v[12:13], v[12:13], 1.0 op_sel_hi:[1,0]\n\t"
    "v_pk_add_f32 v[14:15], v[14:15], 1.0 op_sel_hi:[1,0]\n\t"
    "v_rcp_f32 v244, v244\n\t"
    "v_rcp_f32 v245, v245\n\t"
    "v_rcp_f32 v246, v246\n\t"
    "v_rcp_f32 v247, v247\n\t"
    "v_rcp_f32 v12, v12\n\t"
    "v_rcp_f32 v13, v13\n\t"
    "v_rcp_f32 v14, v14\n\t"
    "v_rcp_f32 v15, v15\n\t"
    "v_pk_mul_f32 v[244:245], v[244:245], v[248:249]\n\t"
    "v_pk_mul_f32 v[246:247], v[246:247], v[250:251]\n\t"
    "v_pk_mul_f32 v[12:13], v[12:13], v[252:253]\n\t"
    "v_pk_mul_f32 v[14:15], v[14:15], v[254:255]\n\t"
    "v_cvt_pk_bf16_f32 v248, v244, v245\n\t"
    "v_cvt_pk_bf16_f32 v249, v246, v247\n\t"
    "v_cvt_pk_bf16_f32 v250, v12, v13\n\t"
    "v_cvt_pk_bf16_f32 v251, v14, v15\n\t"
    "buffer_store_dwordx4 v[248:251], %[vL16], %[srdWS], %[sHc] offen offset:3072 sc1\n\t"
    "s_waitcnt lgkmcnt(4)\n\t"
    "v_mfma_f32_16x16x32_bf16 a[128:131], a[0:3], v[144:147], a[128:131]\n\t"
    "ds_read_b128 a[24:27], v10 offset:22528\n\t"
    "v_exp_f32 v232, v80\n\t"
    "v_mfma_f32_16x16x32_bf16 a[128:131], a[4:7], v[160:163], a[128:131]\n\t"
    "v_exp_f32 v233, v81\n\t"
    "v_mfma_f32_16x16x32_bf16 a[132:135], a[4:7], v[164:167], a[132:135]\n\t"
    "v_exp_f32 v234, v82\n\t"
    "v_mfma_f32_16x16x32_bf16 a[132:135], a[0:3], v[148:151], a[132:135]\n\t"
    "v_exp_f32 v235, v83\n\t"
    "v_mfma_f32_16x16x32_bf16 a[136:139], a[0:3], v[152:155], a[136:139]\n\t"
    "ds_read_b128 a[28:31], v10 offset:23552\n\t"
    "v_cvt_pk_bf16_f32 v176, v232, v233\n\t"
    "v_mfma_f32_16x16x32_bf16 a[136:139], a[4:7], v[168:171], a[136:139]\n\t"
    "v_exp_f32 v236, v96\n\t"
    "v_mfma_f32_16x16x32_bf16 a[140:143], a[4:7], v[172:175], a[140:143]\n\t"
    "v_exp_f32 v237, v97\n\t"
    "v_mfma_f32_16x16x32_bf16 a[140:143], a[0:3], v[156:159], a[140:143]\n\t"
    "v_cvt_pk_bf16_f32 v177, v234, v235\n\t"
    "s_waitcnt lgkmcnt(4)\n\t"
    "v_mfma_f32_16x16x32_bf16 a[144:147], a[8:11], v[144:147], a[144:147]\n\t"
    "ds_read_b128 a[0:3], v10 offset:24576\n\t"
    "v_cvt_pk_bf16_f32 v178, v236, v237\n\t"
    "v_mfma_f32_16x16x32_bf16 a[144:147], a[12:15], v[160:163], a[144:147]\n\t"
    "v_exp_f32 v238, v98\n\t"
    "v_mfma_f32_16x16x32_bf16 a[148:151], a[12:15], v[164:167], a[148:151]\n\t"
    "v_exp_f32 v239, v99\n\t"
    "v_mfma_f32_16x16x32_bf16 a[148:151], a[8:11], v[148:151], a[148:151]\n\t"
    "v_exp_f32 v240, v84\n\t"
    "v_mfma_f32_16x16x32_bf16 a[152:155], a[8:11], v[152:155], a[152:155]\n\t"
    "ds_read_b128 a[4:7], v10 offset:25600\n\t"
    "v_cvt_pk_bf16_f32 v179, v238, v239\n\t"
    "v_mfma_f32_16x16x32_bf16 a[152:155], a[12:15], v[168:171], a[152:155]\n\t"
    "v_exp_f32 v241, v85\n\t"
    "v_mfma_f32_16x16x32_bf16 a[156:159], a[12:15], v[172:175], a[156:159]\n\t"
    "v_exp_f32 v242, v86\n\t"
    "v_mfma_f32_16x16x32_bf16 a[156:159], a[8:11], v[156:159], a[156:159]\n\t"
    "v_cvt_pk_bf16_f32 v180, v240, v241\n\t"
    "s_waitcnt lgkmcnt(4)\n\t"
    "v_mfma_f32_16x16x32_bf16 a[160:163], a[16:19], v[144:147], a[160:163]\n\t"
    "ds_read_b128 a[8:11], v10 offset:26624\n\t"
    "v_exp_f32 v243, v87\n\t"
    "v_mfma_f32_16x16x32_bf16 a[160:163], a[20:23], v[160:163], a[160:163]\n\t"
    "v_exp_f32 v232, v100\n\t"
    "v_mfma_f32_16x16x32_bf16 a[164:167], a[20:23], v[164:167], a[164:167]\n\t"
    "v_exp_f32 v233, v101\n\t"
    "v_mfma_f32_16x16x32_bf16 a[164:167], a[16:19], v[148:151], a[164:167]\n\t"
    "v_exp_f32 v234, v102\n\t"
    "v_mfma_f32_16x16x32_bf16 a[168:171], a[16:19], v[152:155], a[168:171]\n\t"
    "ds_read_b128 a[12:15], v10 offset:27648\n\t"
    "v_cvt_pk_bf16_f32 v181, v242, v243\n\t"
    "v_mfma_f32_16x16x32_bf16 a[168:171], a[20:23], v[168:171], a[168:171]\n\t"
    "v_exp_f32 v235, v103\n\t"
    "v_mfma_f32_16x16x32_bf16 a[172:175], a[20:23], v[172:175], a[172:175]\n\t"
    "v_exp_f32 v236, v88\n\t"
    "v_mfma_f32_16x16x32_bf16 a[172:175], a[16:19], v[156:159], a[172:175]\n\t"
    "v_cvt_pk_bf16_f32 v182, v232, v233\n\t"
    "s_waitcnt lgkmcnt(4)\n\t"
    "v_mfma_f32_16x16x32_bf16 a[176:179], a[24:27], v[144:147], a[176:179]\n\t"
    "ds_read_b128 a[16:19], v10 offset:28672\n\t"
    "v_cvt_pk_bf16_f32 v183, v234, v235\n\t"
    "v_mfma_f32_16x16x32_bf16 a[176:179], a[28:31], v[160:163], a[176:179]\n\t"
    "v_exp_f32 v237, v89\n\t"
    "v_mfma_f32_16x16x32_bf16 a[180:183], a[28:31], v[164:167], a[180:183]\n\t"
    "v_exp_f32 v238, v90\n\t"
    "v_mfma_f32_16x16x32_bf16 a[180:183], a[24:27], v[148:151], a[180:183]\n\t"
    "v_exp_f32 v239, v91\n\t"
    "v_mfma_f32_16x16x32_bf16 a[184:187], a[24:27], v[152:155], a[184:187]\n\t"
    "ds_read_b128 a[20:23], v10 offset:29696\n\t"
    "v_cvt_pk_bf16_f32 v184, v236, v237\n\t"
    "v_mfma_f32_16x16x32_bf16 a[184:187], a[28:31], v[168:171], a[184:187]\n\t"
    "v_exp_f32 v240, v104\n\t"
    "v_mfma_f32_16x16x32_bf16 a[188:191], a[28:31], v[172:175], a[188:191]\n\t"
    "v_exp_f32 v241, v105\n\t"
    "v_mfma_f32_16x16x32_bf16 a[188:191], a[24:27], v[156:159], a[188:191]\n\t"
    "v_cvt_pk_bf16_f32 v185, v238, v239\n\t"
    "s_waitcnt lgkmcnt(4)\n\t"
    "v_mfma_f32_16x16x32_bf16 a[192:195], a[0:3], v[144:147], a[192:195]\n\t"
    "ds_read_b128 a[24:27], v10 offset:30720\n\t"
    "v_cvt_pk_bf16_f32 v186, v240, v241\n\t"
    "v_mfma_f32_16x16x32_bf16 a[192:195], a[4:7], v[160:163], a[192:195]\n\t"
    "v_exp_f32 v242, v106\n\t"
    "v_mfma_f32_16x16x32_bf16 a[196:199], a[4:7], v[164:167], a[196:199]\n\t"
    "v_exp_f32 v243, v107\n\t"
    "v_mfma_f32_16x16x32_bf16 a[196:199], a[0:3], v[148:151], a[196:199]\n\t"
    "v_exp_f32 v232, v92\n\t"
    "v_mfma_f32_16x16x32_bf16 a[200:203], a[0:3], v[152:155], a[200:203]\n\t"
    "ds_read_b128 a[28:31], v10 offset:31744\n\t"
    "v_cvt_pk_bf16_f32 v187, v242, v243\n\t"
    "v_mfma_f32_16x16x32_bf16 a[200:203], a[4:7], v[168:171], a[200:203]\n\t"
    "v_exp_f32 v233, v93\n\t"
    "v_mfma_f32_16x16x32_bf16 a[204:207], a[4:7], v[172:175], a[204:207]\n\t"
    "v_exp_f32 v234, v94\n\t"
    "v_mfma_f32_16x16x32_bf16 a[204:207], a[0:3], v[156:159], a[204:207]\n\t"
    "v_cvt_pk_bf16_f32 v188, v232, v233\n\t"
    "s_waitcnt vmcnt(0) lgkmcnt(4)\n\t"
    "v_mov_b32 v12, 1\n\t"
    "s_mov_b64 exec, 1\n\t"
    "buffer_atomic_add v12, off, %[srdWS], %[sSig] offset:128\n\t"
    "s_mov_b64 exec, -1\n\t"
    "s_barrier\n\t"
    "v_mfma_f32_16x16x32_bf16 a[208:211], a[8:11], v[144:147], a[208:211]\n\t"
    "ds_read_b128 a[0:3], v10 offset:49216\n\t"
    "v_exp_f32 v235, v95\n\t"
    "v_mfma_f32_16x16x32_bf16 a[208:211], a[12:15], v[160:163], a[208:211]\n\t"
    "v_exp_f32 v236, v108\n\t"
    "v_mfma_f32_16x16x32_bf16 a[212:215], a[12:15], v[164:167], a[212:215]\n\t"
    "v_exp_f32 v237, v109\n\t"
    "v_mfma_f32_16x16x32_bf16 a[212:215], a[8:11], v[148:151], a[212:215]\n\t"
    "v_exp_f32 v238, v110\n\t"
    "v_mfma_f32_16x16x32_bf16 a[216:219], a[8:11], v[152:155], a[216:219]\n\t"
    "ds_read_b128 a[4:7], v10 offset:50240\n\t"
    "v_cvt_pk_bf16_f32 v189, v234, v235\n\t"
    "v_mfma_f32_16x16x32_bf16 a[216:219], a[12:15], v[168:171], a[216:219]\n\t"
    "v_exp_f32 v239, v111\n\t"
    "v_mfma_f32_16x16x32_bf16 a[220:223], a[12:15], v[172:175], a[220:223]\n\t"
    "v_exp_f32 v240, v112\n\t"
    "v_mfma_f32_16x16x32_bf16 a[220:223], a[8:11], v[156:159], a[220:223]\n\t"
    "v_cvt_pk_bf16_f32 v190, v236, v237\n\t"
    "s_waitcnt lgkmcnt(4)\n\t"
    "v_mfma_f32_16x16x32_bf16 a[224:227], a[16:19], v[144:147], a[224:227]\n\t"
    "ds_read_b128 a[8:11], v10 offset:51264\n\t"
    "v_cvt_pk_bf16_f32 v191, v238, v239\n\t"
    "v_mfma_f32_16x16x32_bf16 a[224:227], a[20:23], v[160:163], a[224:227]\n\t"
    "v_exp_f32 v241, v113\n\t"
    "v_mfma_f32_16x16x32_bf16 a[228:231], a[20:23], v[164:167], a[228:231]\n\t"
    "v_exp_f32 v242, v114\n\t"
    "v_mfma_f32_16x16x32_bf16 a[228:231], a[16:19], v[148:151], a[228:231]\n\t"
    "v_exp_f32 v243, v115\n\t"
    "v_mfma_f32_16x16x32_bf16 a[232:235], a[16:19], v[152:155], a[232:235]\n\t"
    "ds_read_b128 a[12:15], v10 offset:52288\n\t"
    "v_cvt_pk_bf16_f32 v192, v240, v241\n\t"
    "v_mfma_f32_16x16x32_bf16 a[232:235], a[20:23], v[168:171], a[232:235]\n\t"
    "v_exp_f32 v232, v128\n\t"
    "v_mfma_f32_16x16x32_bf16 a[236:239], a[20:23], v[172:175], a[236:239]\n\t"
    "v_exp_f32 v233, v129\n\t"
    "v_mfma_f32_16x16x32_bf16 a[236:239], a[16:19], v[156:159], a[236:239]\n\t"
    "v_cvt_pk_bf16_f32 v193, v242, v243\n\t"
    "v_cvt_pk_bf16_f32 v194, v232, v233\n\t"
    "v_exp_f32 v234, v130\n\t"
    "v_exp_f32 v235, v131\n\t"
    "s_nop 0\n\t"
    "v_cvt_pk_bf16_f32 v195, v234, v235\n\t"
    "v_exp_f32 v236, v116\n\t"
    "v_exp_f32 v237, v117\n\t"
    "s_nop 0\n\t"
    "v_cvt_pk_bf16_f32 v196, v236, v237\n\t"
    "v_exp_f32 v238, v118\n\t"
    "v_exp_f32 v239, v119\n\t"
    "s_nop 0\n\t"
    "v_cvt_pk_bf16_f32 v197, v238, v239\n\t"
    "v_exp_f32 v240, v132\n\t"
    "v_exp_f32 v241, v133\n\t"
    "s_nop 0\n\t"
    "v_cvt_pk_bf16_f32 v198, v240, v241\n\t"
    "v_exp_f32 v242, v134\n\t"
    "v_exp_f32 v243, v135\n\t"
    "s_nop 0\n\t"
    "v_cvt_pk_bf16_f32 v199, v242, v243\n\t"
    "v_exp_f32 v232, v120\n\t"
    "v_exp_f32 v233, v121\n\t"
    "s_nop 0\n\t"
    "v_cvt_pk_bf16_f32 v200, v232, v233\n\t"
    "v_exp_f32 v234, v122\n\t"
    "v_exp_f32 v235, v123\n\t"
    "s_nop 0\n\t"
    "v_cvt_pk_bf16_f32 v201, v234, v235\n\t"
    "v_exp_f32 v236, v136\n\t"
    "v_exp_f32 v237, v137\n\t"
    "s_nop 0\n\t"
    "v_cvt_pk_bf16_f32 v202, v236, v237\n\t"
    "v_exp_f32 v238, v138\n\t"
    "v_exp_f32 v239, v139\n\t"
    "s_nop 0\n\t"
    "v_cvt_pk_bf16_f32 v203, v238, v239\n\t"
    "v_exp_f32 v240, v124\n\t"
    "v_exp_f32 v241, v125\n\t"
    "s_nop 0\n\t"
    "v_cvt_pk_bf16_f32 v204, v240, v241\n\t"
    "v_exp_f32 v242, v126\n\t"
    "v_exp_f32 v243, v127\n\t"
    "s_nop 0\n\t"
    "v_cvt_pk_bf16_f32 v205, v242, v243\n\t"
    "v_exp_f32 v232, v140\n\t"
    "v_exp_f32 v233, v141\n\t"
    "s_nop 0\n\t"
    "v_cvt_pk_bf16_f32 v206, v232, v233\n\t"
    "v_exp_f32 v234, v142\n\t"
    "v_exp_f32 v235, v143\n\t"
    "s_nop 0\n\t"
    "v_cvt_pk_bf16_f32 v207, v234, v235\n\t"
    "v_mfma_f32_16x16x32_bf16 v[228:231], a[96:99], v[176:179], v[224:227]\n\t"
    "v_mfma_f32_16x16x32_bf16 v[228:231], a[100:103], v[180:183], v[228:231]\n\t"
    "v_mfma_f32_16x16x32_bf16 v[228:231], a[104:107], v[184:187], v[228:231]\n\t"
    "v_mfma_f32_16x16x32_bf16 v[228:231], a[108:111], v[188:191], v[228:231]\n\t"
    "v_mfma_f32_16x16x32_bf16 v[228:231], a[96:99], v[192:195], v[228:231]\n\t"
    "v_mfma_f32_16x16x32_bf16 v[228:231], a[100:103], v[196:199], v[228:231]\n\t"
    "v_mfma_f32_16x16x32_bf16 v[228:231], a[104:107], v[200:203], v[228:231]\n\t"
    "v_mfma_f32_16x16x32_bf16 v[228:231], a[108:111], v[204:207], v[228:231]\n\t"
    "s_waitcnt lgkmcnt(4)\n\t"
    "v_mfma_f32_16x16x32_bf16 a[240:243], a[24:27], v[144:147], a[240:243]\n\t"
    "ds_read_b128 a[16:19], v10 offset:53312\n\t"
    "v_mfma_f32_16x16x32_bf16 a[240:243], a[28:31], v[160:163], a[240:243]\n\t"
    "v_mfma_f32_16x16x32_bf16 a[244:247], a[28:31], v[164:167], a[244:247]\n\t"
    "v_mfma_f32_16x16x32_bf16 a[244:247], a[24:27], v[148:151], a[244:247]\n\t"
    "v_mfma_f32_16x16x32_bf16 a[248:251], a[24:27], v[152:155], a[248:251]\n\t"
    "ds_read_b128 a[20:23], v10 offset:54336\n\t"
    "v_max3_f32 v232, v228, v229, v230\n\t"
    "v_mfma_f32_16x16x32_bf16 a[248:251], a[28:31], v[168:171], a[248:251]\n\t"
    "v_max_f32 v232, v232, v231\n\t"
    "v_mfma_f32_16x16x32_bf16 a[252:255], a[28:31], v[172:175], a[252:255]\n\t"
    "v_cmp_nge_f32 vcc, s64, v232\n\t"
    "s_getpc_b64 s[60:61]\n\t"
    "v_mfma_f32_16x16x32_bf16 a[252:255], a[24:27], v[156:159], a[252:255]\n\t"
    "s_cbranch_vccnz L_k1_fix1_%=\n\t"
    "s_add_u32 s65, %[sO], 0\n\t"
    "s_add_u32 s66, %[sO], 4096\n\t"
    "s_add_u32 s60, %[sO], 8192\n\t"
    "s_add_u32 s61, %[sO], 12288\n\t"
    "s_waitcnt lgkmcnt(4)\n\t"
    "v_mfma_f32_16x16x32_bf16 a[128:131], a[0:3], v[176:179], a[128:131]\n\t"
    "ds_read_b128 a[24:27], v10 offset:55360\n\t"
    "v_rcp_f32 v248, v228\n\t"
    "v_mfma_f32_16x16x32_bf16 a[128:131], a[4:7], v[192:195], a[128:131]\n\t"
    "v_rcp_f32 v250, v229\n\t"
    "v_mfma_f32_16x16x32_bf16 a[132:135], a[4:7], v[196:199], a[132:135]\n\t"
    "v_rcp_f32 v252, v230\n\t"
    "v_mfma_f32_16x16x32_bf16 a[132:135], a[0:3], v[180:183], a[132:135]\n\t"
    "v_rcp_f32 v254, v231\n\t"
    "v_mfma_f32_16x16x32_bf16 a[136:139], a[0:3], v[184:187], a[136:139]\n\t"
    "ds_read_b128 a[28:31], v10 offset:56384\n\t"
    "v_mfma_f32_16x16x32_bf16 a[136:139], a[4:7], v[200:203], a[136:139]\n\t"
    "v_mfma_f32_16x16x32_bf16 a[140:143], a[4:7], v[204:207], a[140:143]\n\t"
    "v_mfma_f32_16x16x32_bf16 a[140:143], a[0:3], v[188:191], a[140:143]\n\t"
    "s_waitcnt lgkmcnt(4)\n\t"
    "v_mfma_f32_16x16x32_bf16 a[144:147], a[8:11], v[176:179], a[144:147]\n\t"
    "ds_read_b128 a[0:3], v10 offset:57408\n\t"
    "v_mfma_f32_16x16x32_bf16 a[144:147], a[12:15], v[192:195], a[144:147]\n\t"
    "v_mfma_f32_16x16x32_bf16 a[148:151], a[12:15], v[196:199], a[148:151]\n\t"
    "v_mfma_f32_16x16x32_bf16 a[148:151], a[8:11], v[180:183], a[148:151]\n\t"
    "v_mfma_f32_16x16x32_bf16 a[152:155], a[8:11], v[184:187], a[152:155]\n\t"
    "ds_read_b128 a[4:7], v10 offset:58432\n\t"
    "v_mfma_f32_16x16x32_bf16 a[152:155], a[12:15], v[200:203], a[152:155]\n\t"
    "v_mfma_f32_16x16x32_bf16 a[156:159], a[12:15], v[204:207], a[156:159]\n\t"
    "v_mfma_f32_16x16x32_bf16 a[156:159], a[8:11], v[188:191], a[156:159]\n\t"
    "s_waitcnt lgkmcnt(4)\n\t"
    "v_mfma_f32_16x16x32_bf16 a[160:163], a[16:19], v[176:179], a[160:163]\n\t"
    "ds_read_b128 a[8:11], v10 offset:59456\n\t"
    "v_mfma_f32_16x16x32_bf16 a[160:163], a[20:23], v[192:195], a[160:163]\n\t"
    "v_mfma_f32_16x16x32_bf16 a[164:167], a[20:23], v[196:199], a[164:167]\n\t"
    "v_mfma_f32_16x16x32_bf16 a[164:167], a[16:19], v[180:183], a[164:167]\n\t"
    "v_accvgpr_read_b32 v16, a128\n\t"
    "v_accvgpr_read_b32 v17, a129\n\t"
    "v_mfma_f32_16x16x32_bf16 a[168:171], a[16:19], v[184:187], a[168:171]\n\t"
    "ds_read_b128 a[12:15], v10 offset:60480\n\t"
    "v_accvgpr_read_b32 v18, a130\n\t"
    "v_mfma_f32_16x16x32_bf16 a[168:171], a[20:23], v[200:203], a[168:171]\n\t"
    "v_accvgpr_read_b32 v19, a131\n\t"
    "v_accvgpr_read_b32 v20, a144\n\t"
    "v_mfma_f32_16x16x32_bf16 a[172:175], a[20:23], v[204:207], a[172:175]\n\t"
    "v_accvgpr_read_b32 v21, a145\n\t"
    "v_accvgpr_read_b32 v22, a146\n\t"
    "v_mfma_f32_16x16x32_bf16 a[172:175], a[16:19], v[188:191], a[172:175]\n\t"
    "v_accvgpr_read_b32 v23, a147\n\t"
    "s_waitcnt lgkmcnt(4)\n\t"
    "v_mfma_f32_16x16x32_bf16 a[176:179], a[24:27], v[176:179], a[176:179]\n\t"
    "ds_read_b128 a[16:19], v10 offset:61504\n\t"
    "v_mul_f32 v16, v248, v16\n\t"
    "v_mfma_f32_16x16x32_bf16 a[176:179], a[28:31], v[192:195], a[176:179]\n\t"
    "v_mul_f32 v17, v248, v17\n\t"
    "v_mul_f32 v18, v248, v18\n\t"
    "v_mfma_f32_16x16x32_bf16 a[180:183], a[28:31], v[196:199], a[180:183]\n\t"
    "v_mul_f32 v19, v248, v19\n\t"
    "v_mul_f32 v20, v248, v20\n\t"
    "v_mfma_f32_16x16x32_bf16 a[180:183], a[24:27], v[180:183], a[180:183]\n\t"
    "v_mul_f32 v21, v248, v21\n\t"
    "v_mul_f32 v22, v248, v22\n\t"
    "v_mfma_f32_16x16x32_bf16 a[184:187], a[24:27], v[184:187], a[184:187]\n\t"
    "ds_read_b128 a[20:23], v10 offset:62528\n\t"
    "v_mul_f32 v23, v248, v23\n\t"
    "v_mfma_f32_16x16x32_bf16 a[184:187], a[28:31], v[200:203], a[184:187]\n\t"
    "v_cvt_pk_bf16_f32 v24, v16, v17\n\t"
    "v_cvt_pk_bf16_f32 v25, v18, v19\n\t"
    "v_mfma_f32_16x16x32_bf16 a[188:191], a[28:31], v[204:207], a[188:191]\n\t"
    "v_cvt_pk_bf16_f32 v26, v20, v21\n\t"
    "v_cvt_pk_bf16_f32 v27, v22, v23\n\t"
    "v_mfma_f32_16x16x32_bf16 a[188:191], a[24:27], v[188:191], a[188:191]\n\t"
    "buffer_store_dwordx4 v[24:27], %[vL16], %[srdWS], s65 offen offset:0 sc1\n\t"
    "s_waitcnt lgkmcnt(4)\n\t"
    "v_mfma_f32_16x16x32_bf16 a[192:195], a[0:3], v[176:179], a[192:195]\n\t"
    "ds_read_b128 a[24:27], v10 offset:63552\n\t"
    "v_accvgpr_read_b32 v40, a136\n\t"
    "v_mfma_f32_16x16x32_bf16 a[192:195], a[4:7], v[192:195], a[192:195]\n\t"
    "v_accvgpr_read_b32 v41, a137\n\t"
    "v_accvgpr_read_b32 v42, a138\n\t"
    "v_mfma_f32_16x16x32_bf16 a[196:199], a[4:7], v[196:199], a[196:199]\n\t"
    "v_accvgpr_read_b32 v43, a139\n\t"
    "v_accvgpr_read_b32 v44, a152\n\t"
    "v_mfma_f32_16x16x32_bf16 a[196:199], a[0:3], v[180:183], a[196:199]\n\t"
    "v_accvgpr_read_b32 v45, a153\n\t"
    "v_accvgpr_read_b32 v46, a154\n\t"
    "v_mfma_f32_16x16x32_bf16 a[200:203], a[0:3], v[184:187], a[200:203]\n\t"
    "ds_read_b128 a[28:31], v10 offset:64576\n\t"
    "v_accvgpr_read_b32 v47, a155\n\t"
    "v_mfma_f32_16x16x32_bf16 a[200:203], a[4:7], v[200:203], a[200:203]\n\t"
    "v_mul_f32 v40, v252, v40\n\t"
    "v_mul_f32 v41, v252, v41\n\t"
    "v_mfma_f32_16x16x32_bf16 a[204:207], a[4:7], v[204:207], a[204:207]\n\t"
    "v_mul_f32 v42, v252, v42\n\t"
    "v_mul_f32 v43, v252, v43\n\t"
    "v_mfma_f32_16x16x32_bf16 a[204:207], a[0:3], v[188:191], a[204:207]\n\t"
    "v_mul_f32 v44, v252, v44\n\t"
    "s_waitcnt lgkmcnt(4)\n\t"
    "v_mfma_f32_16x16x32_bf16 a[208:211], a[8:11], v[176:179], a[208:211]\n\t"
    "v_mul_f32 v45, v252, v45\n\t"
    "v_mul_f32 v46, v252, v46\n\t"
    "v_mfma_f32_16x16x32_bf16 a[208:211], a[12:15], v[192:195], a[208:211]\n\t"
    "v_mul_f32 v47, v252, v47\n\t"
    "v_cvt_pk_bf16_f32 v48, v40, v41\n\t"
    "v_mfma_f32_16x16x32_bf16 a[212:215], a[12:15], v[196:199], a[212:215]\n\t"
    "v_cvt_pk_bf16_f32 v49, v42, v43\n\t"
    "v_cvt_pk_bf16_f32 v50, v44, v45\n\t"
    "v_mfma_f32_16x16x32_bf16 a[212:215], a[8:11], v[180:183], a[212:215]\n\t"
    "v_cvt_pk_bf16_f32 v51, v46, v47\n\t"
    "buffer_store_dwordx4 v[48:51], %[vL16], %[srdWS], s65 offen offset:2048 sc1\n\t"
    "v_mfma_f32_16x16x32_bf16 a[216:219], a[8:11], v[184:187], a[216:219]\n\t"
    "v_accvgpr_read_b32 v64, a160\n\t"
    "v_accvgpr_read_b32 v65, a161\n\t"
    "v_mfma_f32_16x16x32_bf16 a[216:219], a[12:15], v[200:203], a[216:219]\n\t"
    "v_accvgpr_read_b32 v66, a162\n\t"
    "v_accvgpr_read_b32 v67, a163\n\t"
    "v_mfma_f32_16x16x32_bf16 a[220:223], a[12:15], v[204:207], a[220:223]\n\t"
    "v_accvgpr_read_b32 v68, a176\n\t"
    "v_accvgpr_read_b32 v69, a177\n\t"
    "v_mfma_f32_16x16x32_bf16 a[220:223], a[8:11], v[188:191], a[220:223]\n\t"
    "v_accvgpr_read_b32 v70, a178\n\t"
    "s_waitcnt lgkmcnt(2)\n\t"
    "v_mfma_f32_16x16x32_bf16 a[224:227], a[16:19], v[176:179], a[224:227]\n\t"
    "v_accvgpr_read_b32 v71, a179\n\t"
    "v_mul_f32 v64, v248, v64\n\t"
    "v_mfma_f32_16x16x32_bf16 a[224:227], a[20:23], v[192:195], a[224:227]\n\t"
    "v_mul_f32 v65, v248, v65\n\t"
    "v_mul_f32 v66, v248, v66\n\t"
    "v_mfma_f32_16x16x32_bf16 a[228:231], a[20:23], v[196:199], a[228:231]\n\t"
    "v_mul_f32 v67, v248, v67\n\t"
    "v_mul_f32 v68, v248, v68\n\t"
    "v_mfma_f32_16x16x32_bf16 a[228:231], a[16:19], v[180:183], a[228:231]\n\t"
    "v_mul_f32 v69, v248, v69\n\t"
    "v_mul_f32 v70, v248, v70\n\t"
    "v_mfma_f32_16x16x32_bf16 a[232:235], a[16:19], v[184:187], a[232:235]\n\t"
    "v_mul_f32 v71, v248, v71\n\t"
    "v_cvt_pk_bf16_f32 v72, v64, v65\n\t"
    "v_mfma_f32_16x16x32_bf16 a[232:235], a[20:23], v[200:203], a[232:235]\n\t"
    "v_cvt_pk_bf16_f32 v73, v66, v67\n\t"
    "v_cvt_pk_bf16_f32 v74, v68, v69\n\t"
    "v_mfma_f32_16x16x32_bf16 a[236:239], a[20:23], v[204:207], a[236:239]\n\t"
    "v_cvt_pk_bf16_f32 v75, v70, v71\n\t"
    "buffer_store_dwordx4 v[72:75], %[vL16], %[srdWS], s66 offen offset:0 sc1\n\t"
    "v_mfma_f32_16x16x32_bf16 a[236:239], a[16:19], v[188:191], a[236:239]\n\t"
    "v_accvgpr_read_b32 v88, a168\n\t"
    "s_waitcnt lgkmcnt(0)\n\t"
    "v_mfma_f32_16x16x32_bf16 a[240:243], a[24:27], v[176:179], a[240:243]\n\t"
    "v_accvgpr_read_b32 v89, a169\n\t"
    "v_accvgpr_read_b32 v90, a170\n\t"
    "v_mfma_f32_16x16x32_bf16 a[240:243], a[28:31], v[192:195], a[240:243]\n\t"
    "v_accvgpr_read_b32 v91, a171\n\t"
    "v_accvgpr_read_b32 v92, a184\n\t"
    "v_mfma_f32_16x16x32_bf16 a[244:247], a[28:31], v[196:199], a[244:247]\n\t"
    "v_accvgpr_read_b32 v93, a185\n\t"
    "v_accvgpr_read_b32 v94, a186\n\t"
    "v_mfma_f32_16x16x32_bf16 a[244:247], a[24:27], v[180:183], a[244:247]\n\t"
    "v_accvgpr_read_b32 v95, a187\n\t"
    "v_mul_f32 v88, v252, v88\n\t"
    "v_mfma_f32_16x16x32_bf16 a[248:251], a[24:27], v[184:187], a[248:251]\n\t"
    "v_mul_f32 v89, v252, v89\n\t"
    "v_mul_f32 v90, v252, v90\n\t"
    "v_mfma_f32_16x16x32_bf16 a[248:251], a[28:31], v[200:203], a[248:251]\n\t"
    "v_mul_f32 v91, v252, v91\n\t"
    "v_mul_f32 v92, v252, v92\n\t"
    "v_mfma_f32_16x16x32_bf16 a[252:255], a[28:31], v[204:207], a[252:255]\n\t"
    "v_mul_f32 v93, v252, v93\n\t"
    "v_mul_f32 v94, v252, v94\n\t"
    "v_mfma_f32_16x16x32_bf16 a[252:255], a[24:27], v[188:191], a[252:255]\n\t"
    "v_mul_f32 v95, v252, v95\n\t"
    "v_cvt_pk_bf16_f32 v96, v88, v89\n\t"
    "v_accvgpr_read_b32 v28, a132\n\t"
    "v_cvt_pk_bf16_f32 v97, v90, v91\n\t"
    "v_accvgpr_read_b32 v29, a133\n\t"
    "v_cvt_pk_bf16_f32 v98, v92, v93\n\t"
    "v_accvgpr_read_b32 v30, a134\n\t"
    "v_cvt_pk_bf16_f32 v99, v94, v95\n\t"
    "v_accvgpr_read_b32 v31, a135\n\t"
    "buffer_store_dwordx4 v[96:99], %[vL16], %[srdWS], s66 offen offset:2048 sc1\n\t"
    "v_accvgpr_read_b32 v32, a148\n\t"
    "v_accvgpr_read_b32 v16, a192\n\t"
    "v_accvgpr_read_b32 v33, a149\n\t"
    "v_accvgpr_read_b32 v17, a193\n\t"
    "v_accvgpr_read_b32 v34, a150\n\t"
    "v_accvgpr_read_b32 v18, a194\n\t"
    "v_accvgpr_read_b32 v35, a151\n\t"
    "v_accvgpr_read_b32 v19, a195\n\t"
    "v_pk_mul_f32 v[28:29], v[28:29], v[250:251] op_sel_hi:[1,0]\n\t"
    "v_accvgpr_read_b32 v20, a208\n\t"
    "v_pk_mul_f32 v[30:31], v[30:31], v[250:251] op_sel_hi:[1,0]\n\t"
    "v_accvgpr_read_b32 v21, a209\n\t"
    "v_pk_mul_f32 v[32:33], v[32:33], v[250:251] op_sel_hi:[1,0]\n\t"
    "v_accvgpr_read_b32 v22, a210\n\t"
    "v_pk_mul_f32 v[34:35], v[34:35], v[250:251] op_sel_hi:[1,0]\n\t"
    "v_accvgpr_read_b32 v23, a211\n\t"
    "v_cvt_pk_bf16_f32 v36, v28, v29\n\t"
    "v_pk_mul_f32 v[16:17], v[16:17], v[248:249] op_sel_hi:[1,0]\n\t"
    "v_cvt_pk_bf16_f32 v37, v30, v31\n\t"
    "v_pk_mul_f32 v[18:19], v[18:19], v[248:249] op_sel_hi:[1,0]\n\t"
    "v_cvt_pk_bf16_f32 v38, v32, v33\n\t"
    "v_pk_mul_f32 v[20:21], v[20:21], v[248:249] op_sel_hi:[1,0]\n\t"
    "v_cvt_pk_bf16_f32 v39, v34, v35\n\t"
    "v_pk_mul_f32 v[22:23], v[22:23], v[248:249] op_sel_hi:[1,0]\n\t"
    "buffer_store_dwordx4 v[36:39], %[vL16], %[srdWS], s65 offen offset:1024 sc1\n\t"
    "v_cvt_pk_bf16_f32 v24, v16, v17\n\t"
    "v_accvgpr_read_b32 v52, a140\n\t"
    "v_cvt_pk_bf16_f32 v25, v18, v19\n\t"
    "v_accvgpr_read_b32 v53, a141\n\t"
    "v_cvt_pk_bf16_f32 v26, v20, v21\n\t"
    "v_accvgpr_read_b32 v54, a142\n\t"
    "v_cvt_pk_bf16_f32 v27, v22, v23\n\t"
    "v_accvgpr_read_b32 v55, a143\n\t"
    "buffer_store_dwordx4 v[24:27], %[vL16], %[srdWS], s60 offen offset:0 sc1\n\t"
    "v_accvgpr_read_b32 v56, a156\n\t"
    "v_accvgpr_read_b32 v40, a200\n\t"
    "v_accvgpr_read_b32 v57, a157\n\t"
    "v_accvgpr_read_b32 v41, a201\n\t"
    "v_accvgpr_read_b32 v58, a158\n\t"
    "v_accvgpr_read_b32 v42, a202\n\t"
    "v_accvgpr_read_b32 v59, a159\n\t"
    "v_accvgpr_read_b32 v43, a203\n\t"
    "v_pk_mul_f32 v[52:53], v[52:53], v[254:255] op_sel_hi:[1,0]\n\t"
    "v_accvgpr_read_b32 v44, a216\n\t"
    "v_pk_mul_f32 v[54:55], v[54:55], v[254:255] op_sel_hi:[1,0]\n\t"
    "v_accvgpr_read_b32 v45, a217\n\t"
    "v_pk_mul_f32 v[56:57], v[56:57], v[254:255] op_sel_hi:[1,0]\n\t"
    "v_accvgpr_read_b32 v46, a218\n\t"
    "v_pk_mul_f32 v[58:59], v[58:59], v[254:255] op_sel_hi:[1,0]\n\t"
    "v_accvgpr_read_b32 v47, a219\n\t"
    "v_cvt_pk_bf16_f32 v60, v52, v53\n\t"
    "v_pk_mul_f32 v[40:41], v[40:41], v[252:253] op_sel_hi:[1,0]\n\t"
    "v_cvt_pk_bf16_f32 v61, v54, v55\n\t"
    "v_pk_mul_f32 v[42:43], v[42:43], v[252:253] op_sel_hi:[1,0]\n\t"
    "v_cvt_pk_bf16_f32 v62, v56, v57\n\t"
    "v_pk_mul_f32 v[44:45], v[44:45], v[252:253] op_sel_hi:[1,0]\n\t"
    "v_cvt_pk_bf16_f32 v63, v58, v59\n\t"
    "v_pk_mul_f32 v[46:47], v[46:47], v[252:253] op_sel_hi:[1,0]\n\t"
    "buffer_store_dwordx4 v[60:63], %[vL16], %[srdWS], s65 offen offset:3072 sc1\n\t"
    "v_cvt_pk_bf16_f32 v48, v40, v41\n\t"
    "v_accvgpr_read_b32 v76, a164\n\t"
    "v_cvt_pk_bf16_f32 v49, v42, v43\n\t"
    "v_accvgpr_read_b32 v77, a165\n\t"
    "v_cvt_pk_bf16_f32 v50, v44, v45\n\t"
    "v_accvgpr_read_b32 v78, a166\n\t"
    "v_cvt_pk_bf16_f32 v51, v46, v47\n\t"
    "v_accvgpr_read_b32 v79, a167\n\t"
    "buffer_store_dwordx4 v[48:51], %[vL16], %[srdWS], s60 offen offset:2048 sc1\n\t"
    "v_accvgpr_read_b32 v80, a180\n\t"
    "v_accvgpr_read_b32 v64, a224\n\t"
    "v_accvgpr_read_b32 v81, a181\n\t"
    "v_accvgpr_read_b32 v65, a225\n\t"
    "v_accvgpr_read_b32 v82, a182\n\t"
    "v_accvgpr_read_b32 v66, a226\n\t"
    "v_accvgpr_read_b32 v83, a183\n\t"
    "v_accvgpr_read_b32 v67, a227\n\t"
    "v_pk_mul_f32 v[76:77], v[76:77], v[250:251] op_sel_hi:[1,0]\n\t"
    "v_accvgpr_read_b32 v68, a240\n\t"
    "v_pk_mul_f32 v[78:79], v[78:79], v[250:251] op_sel_hi:[1,0]\n\t"
    "v_accvgpr_read_b32 v69, a241\n\t"
    "v_pk_mul_f32 v[80:81], v[80:81], v[250:251] op_sel_hi:[1,0]\n\t"
    "v_accvgpr_read_b32 v70, a242\n\t"
    "v_pk_mul_f32 v[82:83], v[82:83], v[250:251] op_sel_hi:[1,0]\n\t"
    "v_accvgpr_read_b32 v71, a243\n\t"
    "v_cvt_pk_bf16_f32 v84, v76, v77\n\t"
    "v_pk_mul_f32 v[64:65], v[64:65], v[248:249] op_sel_hi:[1,0]\n\t"
    "v_cvt_pk_bf16_f32 v85, v78, v79\n\t"
    "v_pk_mul_f32 v[66:67], v[66:67], v[248:249] op_sel_hi:[1,0]\n\t"
    "v_cvt_pk_bf16_f32 v86, v80, v81\n\t"
    "v_pk_mul_f32 v[68:69], v[68:69], v[248:249] op_sel_hi:[1,0]\n\t"
    "v_cvt_pk_bf16_f32 v87, v82, v83\n\t"
    "v_pk_mul_f32 v[70:71], v[70:71], v[248:249] op_sel_hi:[1,0]\n\t"
    "buffer_store_dwordx4 v[84:87], %[vL16], %[srdWS], s66 offen offset:1024 sc1\n\t"
    "v_cvt_pk_bf16_f32 v72, v64, v65\n\t"
    "v_accvgpr_read_b32 v100, a172\n\t"
    "v_cvt_pk_bf16_f32 v73, v66, v67\n\t"
    "v_accvgpr_read_b32 v101, a173\n\t"
    "v_cvt_pk_bf16_f32 v74, v68, v69\n\t"
    "v_accvgpr_read_b32 v102, a174\n\t"
    "v_cvt_pk_bf16_f32 v75, v70, v71\n\t"
    "v_accvgpr_read_b32 v103, a175\n\t"
    "buffer_store_dwordx4 v[72:75], %[vL16], %[srdWS], s61 offen offset:0 sc1\n\t"
    "v_accvgpr_read_b32 v104, a188\n\t"
    "v_accvgpr_read_b32 v88, a232\n\t"
    "v_accvgpr_read_b32 v105, a189\n\t"
    "v_accvgpr_read_b32 v89, a233\n\t"
    "v_accvgpr_read_b32 v106, a190\n\t"
    "v_accvgpr_read_b32 v90, a234\n\t"
    "v_accvgpr_read_b32 v107, a191\n\t"
    "v_accvgpr_read_b32 v91, a235\n\t"
    "v_pk_mul_f32 v[100:101], v[100:101], v[254:255] op_sel_hi:[1,0]\n\t"
    "v_accvgpr_read_b32 v92, a248\n\t"
    "v_pk_mul_f32 v[102:103], v[102:103], v[254:255] op_sel_hi:[1,0]\n\t"
    "v_accvgpr_read_b32 v93, a249\n\t"
    "v_pk_mul_f32 v[104:105], v[104:105], v[254:255] op_sel_hi:[1,0]\n\t"
    "v_accvgpr_read_b32 v94, a250\n\t"
    "k_pga10: s_cbranch_execz k_pga_back\n\t"
    "v_pk_mul_f32 v[106:107], v[106:107], v[254:255] op_sel_hi:[1,0]\n\t"
    "v_accvgpr_read_b32 v95, a251\n\t"
    "v_cvt_pk_bf16_f32 v108, v100, v101\n\t"
    "v_pk_mul_f32 v[88:89], v[88:89], v[252:253] op_sel_hi:[1,0]\n\t"
    "v_cvt_pk_bf16_f32 v109, v102, v103\n\t"
    "v_pk_mul_f32 v[90:91], v[90:91], v[252:253] op_sel_hi:[1,0]\n\t"
    "v_cvt_pk_bf16_f32 v110, v104, v105\n\t"
    "v_pk_mul_f32 v[92:93], v[92:93], v[252:253] op_sel_hi:[1,0]\n\t"
    "v_cvt_pk_bf16_f32 v111, v106, v107\n\t"
    "v_pk_mul_f32 v[94:95], v[94:95], v[252:253] op_sel_hi:[1,0]\n\t"
    "buffer_store_dwordx4 v[108:111], %[vL16], %[srdWS], s66 offen offset:3072 sc1\n\t"
    "v_cvt_pk_bf16_f32 v96, v88, v89\n\t"
    "v_accvgpr_read_b32 v28, a196\n\t"
    "v_cvt_pk_bf16_f32 v97, v90, v91\n\t"
    "v_accvgpr_read_b32 v29, a197\n\t"
    "v_cvt_pk_bf16_f32 v98, v92, v93\n\t"
    "v_accvgpr_read_b32 v30, a198\n\t"
    "v_cvt_pk_bf16_f32 v99, v94, v95\n\t"
    "v_accvgpr_read_b32 v31, a199\n\t"
    "buffer_store_dwordx4 v[96:99], %[vL16], %[srdWS], s61 offen offset:2048 sc1\n\t"
    "v_accvgpr_read_b32 v32, a212\n\t"
    "v_accvgpr_read_b32 v33, a213\n\t"
    "v_accvgpr_read_b32 v34, a214\n\t"
    "v_accvgpr_read_b32 v35, a215\n\t"
    "v_pk_mul_f32 v[28:29], v[28:29], v[250:251] op_sel_hi:[1,0]\n\t"
    "v_pk_mul_f32 v[30:31], v[30:31], v[250:251] op_sel_hi:[1,0]\n\t"
    "v_pk_mul_f32 v[32:33], v[32:33], v[250:251] op_sel_hi:[1,0]\n\t"
    "v_pk_mul_f32 v[34:35], v[34:35], v[250:251] op_sel_hi:[1,0]\n\t"
    "v_cvt_pk_bf16_f32 v36, v28, v29\n\t"
    "v_cvt_pk_bf16_f32 v37, v30, v31\n\t"
    "v_cvt_pk_bf16_f32 v38, v32, v33\n\t"
    "v_cvt_pk_bf16_f32 v39, v34, v35\n\t"
    "buffer_store_dwordx4 v[36:39], %[vL16], %[srdWS], s60 offen offset:1024 sc1\n\t"
    "v_accvgpr_read_b32 v52, a204\n\t"
    "v_accvgpr_read_b32 v53, a205\n\t"
    "v_accvgpr_read_b32 v54, a206\n\t"
    "v_accvgpr_read_b32 v55, a207\n\t"
    "v_accvgpr_read_b32 v56, a220\n\t"
    "v_accvgpr_read_b32 v57, a221\n\t"
    "v_accvgpr_read_b32 v58, a222\n\t"
    "v_accvgpr_read_b32 v59, a223\n\t"
    "v_pk_mul_f32 v[52:53], v[52:53], v[254:255] op_sel_hi:[1,0]\n\t"
    "v_pk_mul_f32 v[54:55], v[54:55], v[254:255] op_sel_hi:[1,0]\n\t"
    "v_pk_mul_f32 v[56:57], v[56:57], v[254:255] op_sel_hi:[1,0]\n\t"
    "v_pk_mul_f32 v[58:59], v[58:59], v[254:255] op_sel_hi:[1,0]\n\t"
    "v_cvt_pk_bf16_f32 v60, v52, v53\n\t"
    "v_cvt_pk_bf16_f32 v61, v54, v55\n\t"
    "v_cvt_pk_bf16_f32 v62, v56, v57\n\t"
    "v_cvt_pk_bf16_f32 v63, v58, v59\n\t"
    "buffer_store_dwordx4 v[60:63], %[vL16], %[srdWS], s60 offen offset:3072 sc1\n\t"
    "v_accvgpr_read_b32 v76, a228\n\t"
    "v_accvgpr_read_b32 v77, a229\n\t"
    "v_accvgpr_read_b32 v78, a230\n\t"
    "v_accvgpr_read_b32 v79, a231\n\t"
    "v_accvgpr_read_b32 v80, a244\n\t"
    "v_accvgpr_read_b32 v81, a245\n\t"
    "v_accvgpr_read_b32 v82, a246\n\t"
    "v_accvgpr_read_b32 v83, a247\n\t"
    "v_pk_mul_f32 v[76:77], v[76:77], v[250:251] op_sel_hi:[1,0]\n\t"
    "v_pk_mul_f32 v[78:79], v[78:79], v[250:251] op_sel_hi:[1,0]\n\t"
    "v_pk_mul_f32 v[80:81], v[80:81], v[250:251] op_sel_hi:[1,0]\n\t"
    "v_pk_mul_f32 v[82:83], v[82:83], v[250:251] op_sel_hi:[1,0]\n\t"
    "v_cvt_pk_bf16_f32 v84, v76, v77\n\t"
    "v_cvt_pk_bf16_f32 v85, v78, v79\n\t"
    "v_cvt_pk_bf16_f32 v86, v80, v81\n\t"
    "v_cvt_pk_bf16_f32 v87, v82, v83\n\t"
    "buffer_store_dwordx4 v[84:87], %[vL16], %[srdWS], s61 offen offset:1024 sc1\n\t"
    "v_accvgpr_read_b32 v100, a236\n\t"
    "v_accvgpr_read_b32 v101, a237\n\t"
    "v_accvgpr_read_b32 v102, a238\n\t"
    "v_accvgpr_read_b32 v103, a239\n\t"
    "v_accvgpr_read_b32 v104, a252\n\t"
    "v_accvgpr_read_b32 v105, a253\n\t"
    "v_accvgpr_read_b32 v106, a254\n\t"
    "v_accvgpr_read_b32 v107, a255\n\t"
    "v_pk_mul_f32 v[100:101], v[100:101], v[254:255] op_sel_hi:[1,0]\n\t"
    "v_pk_mul_f32 v[102:103], v[102:103], v[254:255] op_sel_hi:[1,0]\n\t"
    "v_pk_mul_f32 v[104:105], v[104:105], v[254:255] op_sel_hi:[1,0]\n\t"
    "v_pk_mul_f32 v[106:107], v[106:107], v[254:255] op_sel_hi:[1,0]\n\t"
    "v_cvt_pk_bf16_f32 v108, v100, v101\n\t"
    "v_cvt_pk_bf16_f32 v109, v102, v103\n\t"
    "v_cvt_pk_bf16_f32 v110, v104, v105\n\t"
    "v_cvt_pk_bf16_f32 v111, v106, v107\n\t"
    "buffer_store_dwordx4 v[108:111], %[vL16], %[srdWS], s61 offen offset:3072 sc1\n\t"
    "s_branch L_k1_end_%=\n\t"
    "L_k1_fix0_%=:\n\t"
    "s_nop 15\n\t"
    "v_max3_f32 v144, v16, v17, v18\n\t"
    "v_max3_f32 v145, v20, v21, v22\n\t"
    "v_max3_f32 v146, v24, v25, v26\n\t"
    "v_max3_f32 v147, v28, v29, v30\n\t"
    "v_max3_f32 v148, v19, v32, v33\n\t"
    "v_max3_f32 v149, v23, v36, v37\n\t"
    "v_max3_f32 v150, v27, v40, v41\n\t"
    "v_max3_f32 v151, v31, v44, v45\n\t"
    "v_max3_f32 v144, v144, v34, v35\n\t"
    "v_max3_f32 v145, v145, v38, v39\n\t"
    "v_max3_f32 v146, v146, v42, v43\n\t"
    "v_max3_f32 v147, v147, v46, v47\n\t"
    "v_max3_f32 v148, v148, v48, v49\n\t"
    "v_max3_f32 v149, v149, v52, v53\n\t"
    "v_max3_f32 v150, v150, v56, v57\n\t"
    "v_max3_f32 v151, v151, v60, v61\n\t"
    "v_max3_f32 v144, v144, v50, v51\n\t"
    "v_max3_f32 v145, v145, v54, v55\n\t"
    "v_max3_f32 v146, v146, v58, v59\n\t"
    "v_max3_f32 v147, v147, v62, v63\n\t"
    "v_max3_f32 v148, v148, v64, v65\n\t"
    "v_max3_f32 v149, v149, v68, v69\n\t"
    "v_max3_f32 v150, v150, v72, v73\n\t"
    "v_max3_f32 v151, v151, v76, v77\n\t"
    "v_max3_f32 v144, v144, v66, v67\n\t"
    "v_max3_f32 v145, v145, v70, v71\n\t"
    "v_max3_f32 v146, v146, v74, v75\n\t"
    "v_max3_f32 v147, v147, v78, v79\n\t"
    "v_max_f32 v144, v144, v148\n\t"
    "v_max_f32 v145, v145, v149\n\t"
    "v_max_f32 v146, v146, v150\n\t"
    "v_max_f32 v147, v147, v151\n\t"
    "v_mov_b32 v148, v144\n\t"
    "v_mov_b32 v149, v145\n\t"
    "v_mov_b32 v150, v146\n\t"
    "v_mov_b32 v151, v147\n\t"
    "v_permlane16_swap_b32 v144, v148\n\t"
    "v_permlane16_swap_b32 v145, v149\n\t"
    "v_permlane16_swap_b32 v146, v150\n\t"
    "v_permlane16_swap_b32 v147, v151\n\t"
    "v_max_f32 v144, v144, v148\n\t"
    "v_max_f32 v145, v145, v149\n\t"
    "v_max_f32 v146, v146, v150\n\t"
    "v_max_f32 v147, v147, v151\n\t"
    "v_mov_b32 v148, v144\n\t"
    "v_mov_b32 v149, v145\n\t"
    "v_mov_b32 v150, v146\n\t"
    "v_mov_b32 v151, v147\n\t"
    "v_permlane32_swap_b32 v144, v148\n\t"
    "v_permlane32_swap_b32 v145, v149\n\t"
    "v_permlane32_swap_b32 v146, v150\n\t"
    "v_permlane32_swap_b32 v147, v151\n\t"
    "v_max_f32 v144, v144, v148\n\t"
    "v_max_f32 v145, v145, v149\n\t"
    "v_max_f32 v146, v146, v150\n\t"
    "v_max_f32 v147, v147, v151\n\t"
    "v_max_f32 v144, 0, v144\n\t"
    "v_max_f32 v145, 0, v145\n\t"
    "v_max_f32 v146, 0, v146\n\t"
    "v_max_f32 v147, 0, v147\n\t"
    "v_exp_f32 v148, -v144\n\t"
    "v_exp_f32 v149, -v145\n\t"
    "v_exp_f32 v150, -v146\n\t"
    "v_exp_f32 v151, -v147\n\t"
    "v_sub_f32 v208, v208, v144\n\t"
    "v_sub_f32 v209, v209, v144\n\t"
    "v_sub_f32 v210, v210, v144\n\t"
    "v_sub_f32 v211, v211, v144\n\t"
    "v_sub_f32 v212, v212, v145\n\t"
    "v_sub_f32 v213, v213, v145\n\t"
    "v_sub_f32 v214, v214, v145\n\t"
    "v_sub_f32 v215, v215, v145\n\t"
    "v_sub_f32 v216, v216, v146\n\t"
    "v_sub_f32 v217, v217, v146\n\t"
    "v_sub_f32 v218, v218, v146\n\t"
    "v_sub_f32 v219, v219, v146\n\t"
    "v_sub_f32 v220, v220, v147\n\t"
    "v_sub_f32 v221, v221, v147\n\t"
    "v_sub_f32 v222, v222, v147\n\t"
    "v_sub_f32 v223, v223, v147\n\t"
    "v_sub_f32 v16, v16, v144\n\t"
    "v_sub_f32 v17, v17, v144\n\t"
    "v_sub_f32 v18, v18, v144\n\t"
    "v_sub_f32 v19, v19, v144\n\t"
    "v_sub_f32 v20, v20, v145\n\t"
    "v_sub_f32 v21, v21, v145\n\t"
    "v_sub_f32 v22, v22, v145\n\t"
    "v_sub_f32 v23, v23, v145\n\t"
    "v_sub_f32 v24, v24, v146\n\t"
    "v_sub_f32 v25, v25, v146\n\t"
    "v_sub_f32 v26, v26, v146\n\t"
    "v_sub_f32 v27, v27, v146\n\t"
    "v_sub_f32 v28, v28, v147\n\t"
    "v_sub_f32 v29, v29, v147\n\t"
    "v_sub_f32 v30, v30, v147\n\t"
    "v_sub_f32 v31, v31, v147\n\t"
    "v_sub_f32 v32, v32, v144\n\t"
    "v_sub_f32 v33, v33, v144\n\t"
    "v_sub_f32 v34, v34, v144\n\t"
    "v_sub_f32 v35, v35, v144\n\t"
    "v_sub_f32 v36, v36, v145\n\t"
    "v_sub_f32 v37, v37, v145\n\t"
    "v_sub_f32 v38, v38, v145\n\t"
    "v_sub_f32 v39, v39, v145\n\t"
    "v_sub_f32 v40, v40, v146\n\t"
    "v_sub_f32 v41, v41, v146\n\t"
    "v_sub_f32 v42, v42, v146\n\t"
    "v_sub_f32 v43, v43, v146\n\t"
    "v_sub_f32 v44, v44, v147\n\t"
    "v_sub_f32 v45, v45, v147\n\t"
    "v_sub_f32 v46, v46, v147\n\t"
    "v_sub_f32 v47, v47, v147\n\t"
    "v_sub_f32 v48, v48, v144\n\t"
    "v_sub_f32 v49, v49, v144\n\t"
    "v_sub_f32 v50, v50, v144\n\t"
    "v_sub_f32 v51, v51, v144\n\t"
    "v_sub_f32 v52, v52, v145\n\t"
    "v_sub_f32 v53, v53, v145\n\t"
    "v_sub_f32 v54, v54, v145\n\t"
    "v_sub_f32 v55, v55, v145\n\t"
    "v_sub_f32 v56, v56, v146\n\t"
    "v_sub_f32 v57, v57, v146\n\t"
    "v_sub_f32 v58, v58, v146\n\t"
    "v_sub_f32 v59, v59, v146\n\t"
    "v_sub_f32 v60, v60, v147\n\t"
    "v_sub_f32 v61, v61, v147\n\t"
    "v_sub_f32 v62, v62, v147\n\t"
    "v_sub_f32 v63, v63, v147\n\t"
    "v_sub_f32 v64, v64, v144\n\t"
    "v_sub_f32 v65, v65, v144\n\t"
    "v_sub_f32 v66, v66, v144\n\t"
    "v_sub_f32 v67, v67, v144\n\t"
    "v_sub_f32 v68, v68, v145\n\t"
    "v_sub_f32 v69, v69, v145\n\t"
    "v_sub_f32 v70, v70, v145\n\t"
    "v_sub_f32 v71, v71, v145\n\t"
    "v_sub_f32 v72, v72, v146\n\t"
    "v_sub_f32 v73, v73, v146\n\t"
    "v_sub_f32 v74, v74, v146\n\t"
    "v_sub_f32 v75, v75, v146\n\t"
    "v_sub_f32 v76, v76, v147\n\t"
    "v_sub_f32 v77, v77, v147\n\t"
    "v_sub_f32 v78, v78, v147\n\t"
    "v_sub_f32 v79, v79, v147\n\t"
    "v_sub_f32 v80, v80, v144\n\t"
    "v_sub_f32 v81, v81, v144\n\t"
    "v_sub_f32 v82, v82, v144\n\t"
    "v_sub_f32 v83, v83, v144\n\t"
    "v_sub_f32 v84, v84, v145\n\t"
    "v_sub_f32 v85, v85, v145\n\t"
    "v_sub_f32 v86, v86, v145\n\t"
    "v_sub_f32 v87, v87, v145\n\t"
    "v_sub_f32 v88, v88, v146\n\t"
    "v_sub_f32 v89, v89, v146\n\t"
    "v_sub_f32 v90, v90, v146\n\t"
    "v_sub_f32 v91, v91, v146\n\t"
    "v_sub_f32 v92, v92, v147\n\t"
    "v_sub_f32 v93, v93, v147\n\t"
    "v_sub_f32 v94, v94, v147\n\t"
    "v_sub_f32 v95, v95, v147\n\t"
    "v_sub_f32 v96, v96, v144\n\t"
    "v_sub_f32 v97, v97, v144\n\t"
    "v_sub_f32 v98, v98, v144\n\t"
    "v_sub_f32 v99, v99, v144\n\t"
    "v_sub_f32 v100, v100, v145\n\t"
    "v_sub_f32 v101, v101, v145\n\t"
    "v_sub_f32 v102, v102, v145\n\t"
    "v_sub_f32 v103, v103, v145\n\t"
    "v_sub_f32 v104, v104, v146\n\t"
    "v_sub_f32 v105, v105, v146\n\t"
    "v_sub_f32 v106, v106, v146\n\t"
    "v_sub_f32 v107, v107, v146\n\t"
    "v_sub_f32 v108, v108, v147\n\t"
    "v_sub_f32 v109, v109, v147\n\t"
    "v_sub_f32 v110, v110, v147\n\t"
    "v_sub_f32 v111, v111, v147\n\t"
    "v_sub_f32 v112, v112, v144\n\t"
    "v_sub_f32 v113, v113, v144\n\t"
    "v_sub_f32 v114, v114, v144\n\t"
    "v_sub_f32 v115, v115, v144\n\t"
    "v_sub_f32 v116, v116, v145\n\t"
    "v_sub_f32 v117, v117, v145\n\t"
    "v_sub_f32 v118, v118, v145\n\t"
    "v_sub_f32 v119, v119, v145\n\t"
    "v_sub_f32 v120, v120, v146\n\t"
    "v_sub_f32 v121, v121, v146\n\t"
    "v_sub_f32 v122, v122, v146\n\t"
    "v_sub_f32 v123, v123, v146\n\t"
    "v_sub_f32 v124, v124, v147\n\t"
    "v_sub_f32 v125, v125, v147\n\t"
    "v_sub_f32 v126, v126, v147\n\t"
    "v_sub_f32 v127, v127, v147\n\t"
    "v_sub_f32 v128, v128, v144\n\t"
    "v_sub_f32 v129, v129, v144\n\t"
    "v_sub_f32 v130, v130, v144\n\t"
    "v_sub_f32 v131, v131, v144\n\t"
    "v_sub_f32 v132, v132, v145\n\t"
    "v_sub_f32 v133, v133, v145\n\t"
    "v_sub_f32 v134, v134, v145\n\t"
    "v_sub_f32 v135, v135, v145\n\t"
    "v_sub_f32 v136, v136, v146\n\t"
    "v_sub_f32 v137, v137, v146\n\t"
    "v_sub_f32 v138, v138, v146\n\t"
    "v_sub_f32 v139, v139, v146\n\t"
    "v_sub_f32 v140, v140, v147\n\t"
    "v_sub_f32 v141, v141, v147\n\t"
    "v_sub_f32 v142, v142, v147\n\t"
    "v_sub_f32 v143, v143, v147\n\t"
    "v_mul_f32 v228, v228, v148\n\t"
    "v_mul_f32 v229, v229, v149\n\t"
    "v_mul_f32 v230, v230, v150\n\t"
    "v_mul_f32 v231, v231, v151\n\t"
    "v_accvgpr_read_b32 v232, a128\n\t"
    "v_accvgpr_read_b32 v233, a129\n\t"
    "v_accvgpr_read_b32 v234, a130\n\t"
    "v_accvgpr_read_b32 v235, a131\n\t"
    "v_mul_f32 v232, v232, v148\n\t"
    "v_mul_f32 v233, v233, v148\n\t"
    "v_mul_f32 v234, v234, v148\n\t"
    "v_mul_f32 v235, v235, v148\n\t"
    "v_accvgpr_write_b32 a128, v232\n\t"
    "v_accvgpr_write_b32 a129, v233\n\t"
    "v_accvgpr_write_b32 a130, v234\n\t"
    "v_accvgpr_write_b32 a131, v235\n\t"
    "v_accvgpr_read_b32 v232, a132\n\t"
    "v_accvgpr_read_b32 v233, a133\n\t"
    "v_accvgpr_read_b32 v234, a134\n\t"
    "v_accvgpr_read_b32 v235, a135\n\t"
    "v_mul_f32 v232, v232, v149\n\t"
    "v_mul_f32 v233, v233, v149\n\t"
    "v_mul_f32 v234, v234, v149\n\t"
    "v_mul_f32 v235, v235, v149\n\t"
    "v_accvgpr_write_b32 a132, v232\n\t"
    "v_accvgpr_write_b32 a133, v233\n\t"
    "v_accvgpr_write_b32 a134, v234\n\t"
    "v_accvgpr_write_b32 a135, v235\n\t"
    "v_accvgpr_read_b32 v232, a136\n\t"
    "v_accvgpr_read_b32 v233, a137\n\t"
    "v_accvgpr_read_b32 v234, a138\n\t"
    "v_accvgpr_read_b32 v235, a139\n\t"
    "v_mul_f32 v232, v232, v150\n\t"
    "v_mul_f32 v233, v233, v150\n\t"
    "v_mul_f32 v234, v234, v150\n\t"
    "v_mul_f32 v235, v235, v150\n\t"
    "v_accvgpr_write_b32 a136, v232\n\t"
    "v_accvgpr_write_b32 a137, v233\n\t"
    "v_accvgpr_write_b32 a138, v234\n\t"
    "v_accvgpr_write_b32 a139, v235\n\t"
    "v_accvgpr_read_b32 v232, a140\n\t"
    "v_accvgpr_read_b32 v233, a141\n\t"
    "v_accvgpr_read_b32 v234, a142\n\t"
    "v_accvgpr_read_b32 v235, a143\n\t"
    "v_mul_f32 v232, v232, v151\n\t"
    "v_mul_f32 v233, v233, v151\n\t"
    "v_mul_f32 v234, v234, v151\n\t"
    "v_mul_f32 v235, v235, v151\n\t"
    "v_accvgpr_write_b32 a140, v232\n\t"
    "v_accvgpr_write_b32 a141, v233\n\t"
    "v_accvgpr_write_b32 a142, v234\n\t"
    "v_accvgpr_write_b32 a143, v235\n\t"
    "v_accvgpr_read_b32 v232, a144\n\t"
    "v_accvgpr_read_b32 v233, a145\n\t"
    "v_accvgpr_read_b32 v234, a146\n\t"
    "v_accvgpr_read_b32 v235, a147\n\t"
    "v_mul_f32 v232, v232, v148\n\t"
    "v_mul_f32 v233, v233, v148\n\t"
    "v_mul_f32 v234, v234, v148\n\t"
    "v_mul_f32 v235, v235, v148\n\t"
    "v_accvgpr_write_b32 a144, v232\n\t"
    "v_accvgpr_write_b32 a145, v233\n\t"
    "v_accvgpr_write_b32 a146, v234\n\t"
    "v_accvgpr_write_b32 a147, v235\n\t"
    "v_accvgpr_read_b32 v232, a148\n\t"
    "v_accvgpr_read_b32 v233, a149\n\t"
    "v_accvgpr_read_b32 v234, a150\n\t"
    "v_accvgpr_read_b32 v235, a151\n\t"
    "v_mul_f32 v232, v232, v149\n\t"
    "v_mul_f32 v233, v233, v149\n\t"
    "v_mul_f32 v234, v234, v149\n\t"
    "v_mul_f32 v235, v235, v149\n\t"
    "v_accvgpr_write_b32 a148, v232\n\t"
    "v_accvgpr_write_b32 a149, v233\n\t"
    "v_accvgpr_write_b32 a150, v234\n\t"
    "v_accvgpr_write_b32 a151, v235\n\t"
    "v_accvgpr_read_b32 v232, a152\n\t"
    "v_accvgpr_read_b32 v233, a153\n\t"
    "v_accvgpr_read_b32 v234, a154\n\t"
    "v_accvgpr_read_b32 v235, a155\n\t"
    "v_mul_f32 v232, v232, v150\n\t"
    "v_mul_f32 v233, v233, v150\n\t"
    "v_mul_f32 v234, v234, v150\n\t"
    "v_mul_f32 v235, v235, v150\n\t"
    "v_accvgpr_write_b32 a152, v232\n\t"
    "v_accvgpr_write_b32 a153, v233\n\t"
    "v_accvgpr_write_b32 a154, v234\n\t"
    "v_accvgpr_write_b32 a155, v235\n\t"
    "v_accvgpr_read_b32 v232, a156\n\t"
    "v_accvgpr_read_b32 v233, a157\n\t"
    "v_accvgpr_read_b32 v234, a158\n\t"
    "v_accvgpr_read_b32 v235, a159\n\t"
    "v_mul_f32 v232, v232, v151\n\t"
    "v_mul_f32 v233, v233, v151\n\t"
    "v_mul_f32 v234, v234, v151\n\t"
    "v_mul_f32 v235, v235, v151\n\t"
    "v_accvgpr_write_b32 a156, v232\n\t"
    "v_accvgpr_write_b32 a157, v233\n\t"
    "v_accvgpr_write_b32 a158, v234\n\t"
    "v_accvgpr_write_b32 a159, v235\n\t"
    "v_accvgpr_read_b32 v232, a160\n\t"
    "v_accvgpr_read_b32 v233, a161\n\t"
    "v_accvgpr_read_b32 v234, a162\n\t"
    "v_accvgpr_read_b32 v235, a163\n\t"
    "v_mul_f32 v232, v232, v148\n\t"
    "v_mul_f32 v233, v233, v148\n\t"
    "v_mul_f32 v234, v234, v148\n\t"
    "v_mul_f32 v235, v235, v148\n\t"
    "v_accvgpr_write_b32 a160, v232\n\t"
    "v_accvgpr_write_b32 a161, v233\n\t"
    "v_accvgpr_write_b32 a162, v234\n\t"
    "v_accvgpr_write_b32 a163, v235\n\t"
    "v_accvgpr_read_b32 v232, a164\n\t"
    "v_accvgpr_read_b32 v233, a165\n\t"
    "v_accvgpr_read_b32 v234, a166\n\t"
    "v_accvgpr_read_b32 v235, a167\n\t"
    "v_mul_f32 v232, v232, v149\n\t"
    "v_mul_f32 v233, v233, v149\n\t"
    "v_mul_f32 v234, v234, v149\n\t"
    "v_mul_f32 v235, v235, v149\n\t"
    "v_accvgpr_write_b32 a164, v232\n\t"
    "v_accvgpr_write_b32 a165, v233\n\t"
    "v_accvgpr_write_b32 a166, v234\n\t"
    "v_accvgpr_write_b32 a167, v235\n\t"
    "v_accvgpr_read_b32 v232, a168\n\t"
    "v_accvgpr_read_b32 v233, a169\n\t"
    "v_accvgpr_read_b32 v234, a170\n\t"
    "v_accvgpr_read_b32 v235, a171\n\t"
    "v_mul_f32 v232, v232, v150\n\t"
    "v_mul_f32 v233, v233, v150\n\t"
    "v_mul_f32 v234, v234, v150\n\t"
    "v_mul_f32 v235, v235, v150\n\t"
    "v_accvgpr_write_b32 a168, v232\n\t"
    "v_accvgpr_write_b32 a169, v233\n\t"
    "v_accvgpr_write_b32 a170, v234\n\t"
    "v_accvgpr_write_b32 a171, v235\n\t"
    "v_accvgpr_read_b32 v232, a172\n\t"
    "v_accvgpr_read_b32 v233, a173\n\t"
    "v_accvgpr_read_b32 v234, a174\n\t"
    "v_accvgpr_read_b32 v235, a175\n\t"
    "v_mul_f32 v232, v232, v151\n\t"
    "v_mul_f32 v233, v233, v151\n\t"
    "v_mul_f32 v234, v234, v151\n\t"
    "v_mul_f32 v235, v235, v151\n\t"
    "v_accvgpr_write_b32 a172, v232\n\t"
    "v_accvgpr_write_b32 a173, v233\n\t"
    "v_accvgpr_write_b32 a174, v234\n\t"
    "v_accvgpr_write_b32 a175, v235\n\t"
    "v_accvgpr_read_b32 v232, a176\n\t"
    "v_accvgpr_read_b32 v233, a177\n\t"
    "v_accvgpr_read_b32 v234, a178\n\t"
    "v_accvgpr_read_b32 v235, a179\n\t"
    "v_mul_f32 v232, v232, v148\n\t"
    "v_mul_f32 v233, v233, v148\n\t"
    "v_mul_f32 v234, v234, v148\n\t"
    "v_mul_f32 v235, v235, v148\n\t"
    "v_accvgpr_write_b32 a176, v232\n\t"
    "v_accvgpr_write_b32 a177, v233\n\t"
    "v_accvgpr_write_b32 a178, v234\n\t"
    "v_accvgpr_write_b32 a179, v235\n\t"
    "v_accvgpr_read_b32 v232, a180\n\t"
    "v_accvgpr_read_b32 v233, a181\n\t"
    "v_accvgpr_read_b32 v234, a182\n\t"
    "v_accvgpr_read_b32 v235, a183\n\t"
    "v_mul_f32 v232, v232, v149\n\t"
    "v_mul_f32 v233, v233, v149\n\t"
    "v_mul_f32 v234, v234, v149\n\t"
    "v_mul_f32 v235, v235, v149\n\t"
    "v_accvgpr_write_b32 a180, v232\n\t"
    "v_accvgpr_write_b32 a181, v233\n\t"
    "v_accvgpr_write_b32 a182, v234\n\t"
    "v_accvgpr_write_b32 a183, v235\n\t"
    "v_accvgpr_read_b32 v232, a184\n\t"
    "v_accvgpr_read_b32 v233, a185\n\t"
    "v_accvgpr_read_b32 v234, a186\n\t"
    "v_accvgpr_read_b32 v235, a187\n\t"
    "v_mul_f32 v232, v232, v150\n\t"
    "v_mul_f32 v233, v233, v150\n\t"
    "v_mul_f32 v234, v234, v150\n\t"
    "v_mul_f32 v235, v235, v150\n\t"
    "v_accvgpr_write_b32 a184, v232\n\t"
    "v_accvgpr_write_b32 a185, v233\n\t"
    "v_accvgpr_write_b32 a186, v234\n\t"
    "v_accvgpr_write_b32 a187, v235\n\t"
    "v_accvgpr_read_b32 v232, a188\n\t"
    "v_accvgpr_read_b32 v233, a189\n\t"
    "v_accvgpr_read_b32 v234, a190\n\t"
    "v_accvgpr_read_b32 v235, a191\n\t"
    "v_mul_f32 v232, v232, v151\n\t"
    "v_mul_f32 v233, v233, v151\n\t"
    "v_mul_f32 v234, v234, v151\n\t"
    "v_mul_f32 v235, v235, v151\n\t"
    "v_accvgpr_write_b32 a188, v232\n\t"
    "v_accvgpr_write_b32 a189, v233\n\t"
    "v_accvgpr_write_b32 a190, v234\n\t"
    "v_accvgpr_write_b32 a191, v235\n\t"
    "v_accvgpr_read_b32 v232, a192\n\t"
    "v_accvgpr_read_b32 v233, a193\n\t"
    "v_accvgpr_read_b32 v234, a194\n\t"
    "v_accvgpr_read_b32 v235, a195\n\t"
    "v_mul_f32 v232, v232, v148\n\t"
    "v_mul_f32 v233, v233, v148\n\t"
    "v_mul_f32 v234, v234, v148\n\t"
    "v_mul_f32 v235, v235, v148\n\t"
    "v_accvgpr_write_b32 a192, v232\n\t"
    "v_accvgpr_write_b32 a193, v233\n\t"
    "v_accvgpr_write_b32 a194, v234\n\t"
    "v_accvgpr_write_b32 a195, v235\n\t"
    "v_accvgpr_read_b32 v232, a196\n\t"
    "v_accvgpr_read_b32 v233, a197\n\t"
    "v_accvgpr_read_b32 v234, a198\n\t"
    "v_accvgpr_read_b32 v235, a199\n\t"
    "v_mul_f32 v232, v232, v149\n\t"
    "v_mul_f32 v233, v233, v149\n\t"
    "v_mul_f32 v234, v234, v149\n\t"
    "v_mul_f32 v235, v235, v149\n\t"
    "v_accvgpr_write_b32 a196, v232\n\t"
    "v_accvgpr_write_b32 a197, v233\n\t"
    "v_accvgpr_write_b32 a198, v234\n\t"
    "v_accvgpr_write_b32 a199, v235\n\t"
    "v_accvgpr_read_b32 v232, a200\n\t"
    "v_accvgpr_read_b32 v233, a201\n\t"
    "v_accvgpr_read_b32 v234, a202\n\t"
    "v_accvgpr_read_b32 v235, a203\n\t"
    "v_mul_f32 v232, v232, v150\n\t"
    "v_mul_f32 v233, v233, v150\n\t"
    "v_mul_f32 v234, v234, v150\n\t"
    "v_mul_f32 v235, v235, v150\n\t"
    "v_accvgpr_write_b32 a200, v232\n\t"
    "v_accvgpr_write_b32 a201, v233\n\t"
    "v_accvgpr_write_b32 a202, v234\n\t"
    "v_accvgpr_write_b32 a203, v235\n\t"
    "v_accvgpr_read_b32 v232, a204\n\t"
    "v_accvgpr_read_b32 v233, a205\n\t"
    "v_accvgpr_read_b32 v234, a206\n\t"
    "v_accvgpr_read_b32 v235, a207\n\t"
    "v_mul_f32 v232, v232, v151\n\t"
    "v_mul_f32 v233, v233, v151\n\t"
    "v_mul_f32 v234, v234, v151\n\t"
    "v_mul_f32 v235, v235, v151\n\t"
    "v_accvgpr_write_b32 a204, v232\n\t"
    "v_accvgpr_write_b32 a205, v233\n\t"
    "v_accvgpr_write_b32 a206, v234\n\t"
    "v_accvgpr_write_b32 a207, v235\n\t"
    "v_accvgpr_read_b32 v232, a208\n\t"
    "v_accvgpr_read_b32 v233, a209\n\t"
    "v_accvgpr_read_b32 v234, a210\n\t"
    "v_accvgpr_read_b32 v235, a211\n\t"
    "v_mul_f32 v232, v232, v148\n\t"
    "v_mul_f32 v233, v233, v148\n\t"
    "v_mul_f32 v234, v234, v148\n\t"
    "v_mul_f32 v235, v235, v148\n\t"
    "v_accvgpr_write_b32 a208, v232\n\t"
    "v_accvgpr_write_b32 a209, v233\n\t"
    "v_accvgpr_write_b32 a210, v234\n\t"
    "v_accvgpr_write_b32 a211, v235\n\t"
    "v_accvgpr_read_b32 v232, a212\n\t"
    "v_accvgpr_read_b32 v233, a213\n\t"
    "v_accvgpr_read_b32 v234, a214\n\t"
    "v_accvgpr_read_b32 v235, a215\n\t"
    "v_mul_f32 v232, v232, v149\n\t"
    "v_mul_f32 v233, v233, v149\n\t"
    "v_mul_f32 v234, v234, v149\n\t"
    "v_mul_f32 v235, v235, v149\n\t"
    "v_accvgpr_write_b32 a212, v232\n\t"
    "v_accvgpr_write_b32 a213, v233\n\t"
    "v_accvgpr_write_b32 a214, v234\n\t"
    "v_accvgpr_write_b32 a215, v235\n\t"
    "v_accvgpr_read_b32 v232, a216\n\t"
    "v_accvgpr_read_b32 v233, a217\n\t"
    "v_accvgpr_read_b32 v234, a218\n\t"
    "v_accvgpr_read_b32 v235, a219\n\t"
    "v_mul_f32 v232, v232, v150\n\t"
    "v_mul_f32 v233, v233, v150\n\t"
    "v_mul_f32 v234, v234, v150\n\t"
    "v_mul_f32 v235, v235, v150\n\t"
    "v_accvgpr_write_b32 a216, v232\n\t"
    "v_accvgpr_write_b32 a217, v233\n\t"
    "v_accvgpr_write_b32 a218, v234\n\t"
    "v_accvgpr_write_b32 a219, v235\n\t"
    "v_accvgpr_read_b32 v232, a220\n\t"
    "v_accvgpr_read_b32 v233, a221\n\t"
    "v_accvgpr_read_b32 v234, a222\n\t"
    "v_accvgpr_read_b32 v235, a223\n\t"
    "v_mul_f32 v232, v232, v151\n\t"
    "v_mul_f32 v233, v233, v151\n\t"
    "v_mul_f32 v234, v234, v151\n\t"
    "v_mul_f32 v235, v235, v151\n\t"
    "v_accvgpr_write_b32 a220, v232\n\t"
    "v_accvgpr_write_b32 a221, v233\n\t"
    "v_accvgpr_write_b32 a222, v234\n\t"
    "v_accvgpr_write_b32 a223, v235\n\t"
    "v_accvgpr_read_b32 v232, a224\n\t"
    "v_accvgpr_read_b32 v233, a225\n\t"
    "v_accvgpr_read_b32 v234, a226\n\t"
    "v_accvgpr_read_b32 v235, a227\n\t"
    "v_mul_f32 v232, v232, v148\n\t"
    "v_mul_f32 v233, v233, v148\n\t"
    "v_mul_f32 v234, v234, v148\n\t"
    "v_mul_f32 v235, v235, v148\n\t"
    "v_accvgpr_write_b32 a224, v232\n\t"
    "v_accvgpr_write_b32 a225, v233\n\t"
    "v_accvgpr_write_b32 a226, v234\n\t"
    "v_accvgpr_write_b32 a227, v235\n\t"
    "v_accvgpr_read_b32 v232, a228\n\t"
    "v_accvgpr_read_b32 v233, a229\n\t"
    "v_accvgpr_read_b32 v234, a230\n\t"
    "v_accvgpr_read_b32 v235, a231\n\t"
    "v_mul_f32 v232, v232, v149\n\t"
    "v_mul_f32 v233, v233, v149\n\t"
    "v_mul_f32 v234, v234, v149\n\t"
    "v_mul_f32 v235, v235, v149\n\t"
    "v_accvgpr_write_b32 a228, v232\n\t"
    "v_accvgpr_write_b32 a229, v233\n\t"
    "v_accvgpr_write_b32 a230, v234\n\t"
    "v_accvgpr_write_b32 a231, v235\n\t"
    "v_accvgpr_read_b32 v232, a232\n\t"
    "v_accvgpr_read_b32 v233, a233\n\t"
    "v_accvgpr_read_b32 v234, a234\n\t"
    "v_accvgpr_read_b32 v235, a235\n\t"
    "v_mul_f32 v232, v232, v150\n\t"
    "v_mul_f32 v233, v233, v150\n\t"
    "v_mul_f32 v234, v234, v150\n\t"
    "v_mul_f32 v235, v235, v150\n\t"
    "v_accvgpr_write_b32 a232, v232\n\t"
    "v_accvgpr_write_b32 a233, v233\n\t"
    "v_accvgpr_write_b32 a234, v234\n\t"
    "v_accvgpr_write_b32 a235, v235\n\t"
    "v_accvgpr_read_b32 v232, a236\n\t"
    "v_accvgpr_read_b32 v233, a237\n\t"
    "v_accvgpr_read_b32 v234, a238\n\t"
    "v_accvgpr_read_b32 v235, a239\n\t"
    "v_mul_f32 v232, v232, v151\n\t"
    "v_mul_f32 v233, v233, v151\n\t"
    "v_mul_f32 v234, v234, v151\n\t"
    "v_mul_f32 v235, v235, v151\n\t"
    "v_accvgpr_write_b32 a236, v232\n\t"
    "v_accvgpr_write_b32 a237, v233\n\t"
    "v_accvgpr_write_b32 a238, v234\n\t"
    "v_accvgpr_write_b32 a239, v235\n\t"
    "v_accvgpr_read_b32 v232, a240\n\t"
    "v_accvgpr_read_b32 v233, a241\n\t"
    "v_accvgpr_read_b32 v234, a242\n\t"
    "v_accvgpr_read_b32 v235, a243\n\t"
    "v_mul_f32 v232, v232, v148\n\t"
    "v_mul_f32 v233, v233, v148\n\t"
    "v_mul_f32 v234, v234, v148\n\t"
    "v_mul_f32 v235, v235, v148\n\t"
    "v_accvgpr_write_b32 a240, v232\n\t"
    "v_accvgpr_write_b32 a241, v233\n\t"
    "v_accvgpr_write_b32 a242, v234\n\t"
    "v_accvgpr_write_b32 a243, v235\n\t"
    "v_accvgpr_read_b32 v232, a244\n\t"
    "v_accvgpr_read_b32 v233, a245\n\t"
    "v_accvgpr_read_b32 v234, a246\n\t"
    "v_accvgpr_read_b32 v235, a247\n\t"
    "v_mul_f32 v232, v232, v149\n\t"
    "v_mul_f32 v233, v233, v149\n\t"
    "v_mul_f32 v234, v234, v149\n\t"
    "v_mul_f32 v235, v235, v149\n\t"
    "v_accvgpr_write_b32 a244, v232\n\t"
    "v_accvgpr_write_b32 a245, v233\n\t"
    "v_accvgpr_write_b32 a246, v234\n\t"
    "v_accvgpr_write_b32 a247, v235\n\t"
    "v_accvgpr_read_b32 v232, a248\n\t"
    "v_accvgpr_read_b32 v233, a249\n\t"
    "v_accvgpr_read_b32 v234, a250\n\t"
    "v_accvgpr_read_b32 v235, a251\n\t"
    "v_mul_f32 v232, v232, v150\n\t"
    "v_mul_f32 v233, v233, v150\n\t"
    "v_mul_f32 v234, v234, v150\n\t"
    "v_mul_f32 v235, v235, v150\n\t"
    "v_accvgpr_write_b32 a248, v232\n\t"
    "v_accvgpr_write_b32 a249, v233\n\t"
    "v_accvgpr_write_b32 a250, v234\n\t"
    "v_accvgpr_write_b32 a251, v235\n\t"
    "v_accvgpr_read_b32 v232, a252\n\t"
    "v_accvgpr_read_b32 v233, a253\n\t"
    "v_accvgpr_read_b32 v234, a254\n\t"
    "v_accvgpr_read_b32 v235, a255\n\t"
    "v_mul_f32 v232, v232, v151\n\t"
    "v_mul_f32 v233, v233, v151\n\t"
    "v_mul_f32 v234, v234, v151\n\t"
    "v_mul_f32 v235, v235, v151\n\t"
    "v_accvgpr_write_b32 a252, v232\n\t"
    "v_accvgpr_write_b32 a253, v233\n\t"
    "v_accvgpr_write_b32 a254, v234\n\t"
    "v_accvgpr_write_b32 a255, v235\n\t"
    "v_exp_f32 v232, v16\n\t"
    "v_exp_f32 v233, v17\n\t"
    "s_nop 0\n\t"
    "v_cvt_pk_bf16_f32 v144, v232, v233\n\t"
    "v_exp_f32 v234, v18\n\t"
    "v_exp_f32 v235, v19\n\t"
    "s_nop 0\n\t"
    "v_cvt_pk_bf16_f32 v145, v234, v235\n\t"
    "v_exp_f32 v236, v32\n\t"
    "v_exp_f32 v237, v33\n\t"
    "s_nop 0\n\t"
    "v_cvt_pk_bf16_f32 v146, v236, v237\n\t"
    "v_exp_f32 v238, v34\n\t"
    "v_exp_f32 v239, v35\n\t"
    "s_nop 0\n\t"
    "v_cvt_pk_bf16_f32 v147, v238, v239\n\t"
    "v_exp_f32 v240, v20\n\t"
    "v_exp_f32 v241, v21\n\t"
    "s_nop 0\n\t"
    "v_cvt_pk_bf16_f32 v148, v240, v241\n\t"
    "v_exp_f32 v242, v22\n\t"
    "v_exp_f32 v243, v23\n\t"
    "s_nop 0\n\t"
    "v_cvt_pk_bf16_f32 v149, v242, v243\n\t"
    "v_exp_f32 v232, v36\n\t"
    "v_exp_f32 v233, v37\n\t"
    "s_nop 0\n\t"
    "v_cvt_pk_bf16_f32 v150, v232, v233\n\t"
    "v_exp_f32 v234, v38\n\t"
    "v_exp_f32 v235, v39\n\t"
    "s_nop 0\n\t"
    "v_cvt_pk_bf16_f32 v151, v234, v235\n\t"
    "v_exp_f32 v236, v24\n\t"
    "v_exp_f32 v237, v25\n\t"
    "s_nop 0\n\t"
    "v_cvt_pk_bf16_f32 v152, v236, v237\n\t"
    "v_exp_f32 v238, v26\n\t"
    "v_exp_f32 v239, v27\n\t"
    "s_nop 0\n\t"
    "v_cvt_pk_bf16_f32 v153, v238, v239\n\t"
    "v_exp_f32 v240, v40\n\t"
    "v_exp_f32 v241, v41\n\t"
    "s_nop 0\n\t"
    "v_cvt_pk_bf16_f32 v154, v240, v241\n\t"
    "v_exp_f32 v242, v42\n\t"
    "v_exp_f32 v243, v43\n\t"
    "s_nop 0\n\t"
    "v_cvt_pk_bf16_f32 v155, v242, v243\n\t"
    "v_exp_f32 v232, v28\n\t"
    "v_exp_f32 v233, v29\n\t"
    "s_nop 0\n\t"
    "v_cvt_pk_bf16_f32 v156, v232, v233\n\t"
    "v_exp_f32 v234, v30\n\t"
    "v_exp_f32 v235, v31\n\t"
    "s_nop 0\n\t"
    "v_cvt_pk_bf16_f32 v157, v234, v235\n\t"
    "v_exp_f32 v236, v44\n\t"
    "v_exp_f32 v237, v45\n\t"
    "s_nop 0\n\t"
    "v_cvt_pk_bf16_f32 v158, v236, v237\n\t"
    "v_exp_f32 v238, v46\n\t"
    "v_exp_f32 v239, v47\n\t"
    "s_nop 0\n\t"
    "v_cvt_pk_bf16_f32 v159, v238, v239\n\t"
    "v_exp_f32 v240, v48\n\t"
    "v_exp_f32 v241, v49\n\t"
    "s_nop 0\n\t"
    "v_cvt_pk_bf16_f32 v160, v240, v241\n\t"
    "v_exp_f32 v242, v50\n\t"
    "v_exp_f32 v243, v51\n\t"
    "s_nop 0\n\t"
    "v_cvt_pk_bf16_f32 v161, v242, v243\n\t"
    "v_exp_f32 v232, v64\n\t"
    "v_exp_f32 v233, v65\n\t"
    "s_nop 0\n\t"
    "v_cvt_pk_bf16_f32 v162, v232, v233\n\t"
    "v_exp_f32 v234, v66\n\t"
    "v_exp_f32 v235, v67\n\t"
    "s_nop 0\n\t"
    "v_cvt_pk_bf16_f32 v163, v234, v235\n\t"
    "v_exp_f32 v236, v52\n\t"
    "v_exp_f32 v237, v53\n\t"
    "s_nop 0\n\t"
    "v_cvt_pk_bf16_f32 v164, v236, v237\n\t"
    "v_exp_f32 v238, v54\n\t"
    "v_exp_f32 v239, v55\n\t"
    "s_nop 0\n\t"
    "v_cvt_pk_bf16_f32 v165, v238, v239\n\t"
    "v_exp_f32 v240, v68\n\t"
    "v_exp_f32 v241, v69\n\t"
    "s_nop 0\n\t"
    "v_cvt_pk_bf16_f32 v166, v240, v241\n\t"
    "v_exp_f32 v242, v70\n\t"
    "v_exp_f32 v243, v71\n\t"
    "s_nop 0\n\t"
    "v_cvt_pk_bf16_f32 v167, v242, v243\n\t"
    "v_exp_f32 v232, v56\n\t"
    "v_exp_f32 v233, v57\n\t"
    "s_nop 0\n\t"
    "v_cvt_pk_bf16_f32 v168, v232, v233\n\t"
    "v_exp_f32 v234, v58\n\t"
    "v_exp_f32 v235, v59\n\t"
    "s_nop 0\n\t"
    "v_cvt_pk_bf16_f32 v169, v234, v235\n\t"
    "v_exp_f32 v236, v72\n\t"
    "v_exp_f32 v237, v73\n\t"
    "s_nop 0\n\t"
    "v_cvt_pk_bf16_f32 v170, v236, v237\n\t"
    "v_exp_f32 v238, v74\n\t"
    "v_exp_f32 v239, v75\n\t"
    "s_nop 0\n\t"
    "v_cvt_pk_bf16_f32 v171, v238, v239\n\t"
    "v_exp_f32 v240, v60\n\t"
    "v_exp_f32 v241, v61\n\t"
    "s_nop 0\n\t"
    "v_cvt_pk_bf16_f32 v172, v240, v241\n\t"
    "v_exp_f32 v242, v62\n\t"
    "v_exp_f32 v243, v63\n\t"
    "s_nop 0\n\t"
    "v_cvt_pk_bf16_f32 v173, v242, v243\n\t"
    "v_exp_f32 v232, v76\n\t"
    "v_exp_f32 v233, v77\n\t"
    "s_nop 0\n\t"
    "v_cvt_pk_bf16_f32 v174, v232, v233\n\t"
    "v_exp_f32 v234, v78\n\t"
    "v_exp_f32 v235, v79\n\t"
    "s_nop 0\n\t"
    "v_cvt_pk_bf16_f32 v175, v234, v235\n\t"
    "v_mfma_f32_16x16x32_bf16 v[224:227], a[96:99], v[144:147], v[228:231]\n\t"
    "v_mfma_f32_16x16x32_bf16 v[224:227], a[100:103], v[148:151], v[224:227]\n\t"
    "v_mfma_f32_16x16x32_bf16 v[224:227], a[104:107], v[152:155], v[224:227]\n\t"
    "v_mfma_f32_16x16x32_bf16 v[224:227], a[108:111], v[156:159], v[224:227]\n\t"
    "v_mfma_f32_16x16x32_bf16 v[224:227], a[96:99], v[160:163], v[224:227]\n\t"
    "v_mfma_f32_16x16x32_bf16 v[224:227], a[100:103], v[164:167], v[224:227]\n\t"
    "v_mfma_f32_16x16x32_bf16 v[224:227], a[104:107], v[168:171], v[224:227]\n\t"
    "v_mfma_f32_16x16x32_bf16 v[224:227], a[108:111], v[172:175], v[224:227]\n\t"
    "s_nop 15\n\t"
    "s_add_u32 s60, s60, 12\n\t"
    "s_addc_u32 s61, s61, 0\n\t"
    "s_setpc_b64 s[60:61]\n\t"
    "L_k1_fix1_%=:\n\t"
    "s_nop 15\n\t"
    "v_max3_f32 v176, v80, v81, v82\n\t"
    "v_max3_f32 v177, v84, v85, v86\n\t"
    "v_max3_f32 v178, v88, v89, v90\n\t"
    "v_max3_f32 v179, v92, v93, v94\n\t"
    "v_max3_f32 v180, v83, v96, v97\n\t"
    "v_max3_f32 v181, v87, v100, v101\n\t"
    "v_max3_f32 v182, v91, v104, v105\n\t"
    "v_max3_f32 v183, v95, v108, v109\n\t"
    "v_max3_f32 v176, v176, v98, v99\n\t"
    "v_max3_f32 v177, v177, v102, v103\n\t"
    "v_max3_f32 v178, v178, v106, v107\n\t"
    "v_max3_f32 v179, v179, v110, v111\n\t"
    "v_max3_f32 v180, v180, v112, v113\n\t"
    "v_max3_f32 v181, v181, v116, v117\n\t"
    "v_max3_f32 v182, v182, v120, v121\n\t"
    "v_max3_f32 v183, v183, v124, v125\n\t"
    "v_max3_f32 v176, v176, v114, v115\n\t"
    "v_max3_f32 v177, v177, v118, v119\n\t"
    "v_max3_f32 v178, v178, v122, v123\n\t"
    "v_max3_f32 v179, v179, v126, v127\n\t"
    "v_max3_f32 v180, v180, v128, v129\n\t"
    "v_max3_f32 v181, v181, v132, v133\n\t"
    "v_max3_f32 v182, v182, v136, v137\n\t"
    "v_max3_f32 v183, v183, v140, v141\n\t"
    "v_max3_f32 v176, v176, v130, v131\n\t"
    "v_max3_f32 v177, v177, v134, v135\n\t"
    "v_max3_f32 v178, v178, v138, v139\n\t"
    "v_max3_f32 v179, v179, v142, v143\n\t"
    "v_max_f32 v176, v176, v180\n\t"
    "v_max_f32 v177, v177, v181\n\t"
    "v_max_f32 v178, v178, v182\n\t"
    "v_max_f32 v179, v179, v183\n\t"
    "v_mov_b32 v180, v176\n\t"
    "v_mov_b32 v181, v177\n\t"
    "v_mov_b32 v182, v178\n\t"
    "v_mov_b32 v183, v179\n\t"
    "v_permlane16_swap_b32 v176, v180\n\t"
    "v_permlane16_swap_b32 v177, v181\n\t"
    "v_permlane16_swap_b32 v178, v182\n\t"
    "v_permlane16_swap_b32 v179, v183\n\t"
    "v_max_f32 v176, v176, v180\n\t"
    "v_max_f32 v177, v177, v181\n\t"
    "v_max_f32 v178, v178, v182\n\t"
    "v_max_f32 v179, v179, v183\n\t"
    "v_mov_b32 v180, v176\n\t"
    "v_mov_b32 v181, v177\n\t"
    "v_mov_b32 v182, v178\n\t"
    "v_mov_b32 v183, v179\n\t"
    "v_permlane32_swap_b32 v176, v180\n\t"
    "v_permlane32_swap_b32 v177, v181\n\t"
    "v_permlane32_swap_b32 v178, v182\n\t"
    "v_permlane32_swap_b32 v179, v183\n\t"
    "v_max_f32 v176, v176, v180\n\t"
    "v_max_f32 v177, v177, v181\n\t"
    "v_max_f32 v178, v178, v182\n\t"
    "v_max_f32 v179, v179, v183\n\t"
    "v_max_f32 v176, 0, v176\n\t"
    "v_max_f32 v177, 0, v177\n\t"
    "v_max_f32 v178, 0, v178\n\t"
    "v_max_f32 v179, 0, v179\n\t"
    "v_exp_f32 v180, -v176\n\t"
    "v_exp_f32 v181, -v177\n\t"
    "v_exp_f32 v182, -v178\n\t"
    "v_exp_f32 v183, -v179\n\t"
    "v_sub_f32 v208, v208, v176\n\t"
    "v_sub_f32 v209, v209, v176\n\t"
    "v_sub_f32 v210, v210, v176\n\t"
    "v_sub_f32 v211, v211, v176\n\t"
    "v_sub_f32 v212, v212, v177\n\t"
    "v_sub_f32 v213, v213, v177\n\t"
    "v_sub_f32 v214, v214, v177\n\t"
    "v_sub_f32 v215, v215, v177\n\t"
    "v_sub_f32 v216, v216, v178\n\t"
    "v_sub_f32 v217, v217, v178\n\t"
    "v_sub_f32 v218, v218, v178\n\t"
    "v_sub_f32 v219, v219, v178\n\t"
    "v_sub_f32 v220, v220, v179\n\t"
    "v_sub_f32 v221, v221, v179\n\t"
    "v_sub_f32 v222, v222, v179\n\t"
    "v_sub_f32 v223, v223, v179\n\t"
    "v_sub_f32 v80, v80, v176\n\t"
    "v_sub_f32 v81, v81, v176\n\t"
    "v_sub_f32 v82, v82, v176\n\t"
    "v_sub_f32 v83, v83, v176\n\t"
    "v_sub_f32 v84, v84, v177\n\t"
    "v_sub_f32 v85, v85, v177\n\t"
    "v_sub_f32 v86, v86, v177\n\t"
    "v_sub_f32 v87, v87, v177\n\t"
    "v_sub_f32 v88, v88, v178\n\t"
    "v_sub_f32 v89, v89, v178\n\t"
    "v_sub_f32 v90, v90, v178\n\t"
    "v_sub_f32 v91, v91, v178\n\t"
    "v_sub_f32 v92, v92, v179\n\t"
    "v_sub_f32 v93, v93, v179\n\t"
    "v_sub_f32 v94, v94, v179\n\t"
    "v_sub_f32 v95, v95, v179\n\t"
    "v_sub_f32 v96, v96, v176\n\t"
    "v_sub_f32 v97, v97, v176\n\t"
    "v_sub_f32 v98, v98, v176\n\t"
    "v_sub_f32 v99, v99, v176\n\t"
    "v_sub_f32 v100, v100, v177\n\t"
    "v_sub_f32 v101, v101, v177\n\t"
    "v_sub_f32 v102, v102, v177\n\t"
    "v_sub_f32 v103, v103, v177\n\t"
    "v_sub_f32 v104, v104, v178\n\t"
    "v_sub_f32 v105, v105, v178\n\t"
    "v_sub_f32 v106, v106, v178\n\t"
    "v_sub_f32 v107, v107, v178\n\t"
    "v_sub_f32 v108, v108, v179\n\t"
    "v_sub_f32 v109, v109, v179\n\t"
    "v_sub_f32 v110, v110, v179\n\t"
    "v_sub_f32 v111, v111, v179\n\t"
    "v_sub_f32 v112, v112, v176\n\t"
    "v_sub_f32 v113, v113, v176\n\t"
    "v_sub_f32 v114, v114, v176\n\t"
    "v_sub_f32 v115, v115, v176\n\t"
    "v_sub_f32 v116, v116, v177\n\t"
    "v_sub_f32 v117, v117, v177\n\t"
    "v_sub_f32 v118, v118, v177\n\t"
    "v_sub_f32 v119, v119, v177\n\t"
    "v_sub_f32 v120, v120, v178\n\t"
    "v_sub_f32 v121, v121, v178\n\t"
    "v_sub_f32 v122, v122, v178\n\t"
    "v_sub_f32 v123, v123, v178\n\t"
    "v_sub_f32 v124, v124, v179\n\t"
    "v_sub_f32 v125, v125, v179\n\t"
    "v_sub_f32 v126, v126, v179\n\t"
    "v_sub_f32 v127, v127, v179\n\t"
    "v_sub_f32 v128, v128, v176\n\t"
    "v_sub_f32 v129, v129, v176\n\t"
    "v_sub_f32 v130, v130, v176\n\t"
    "v_sub_f32 v131, v131, v176\n\t"
    "v_sub_f32 v132, v132, v177\n\t"
    "v_sub_f32 v133, v133, v177\n\t"
    "v_sub_f32 v134, v134, v177\n\t"
    "v_sub_f32 v135, v135, v177\n\t"
    "v_sub_f32 v136, v136, v178\n\t"
    "v_sub_f32 v137, v137, v178\n\t"
    "v_sub_f32 v138, v138, v178\n\t"
    "v_sub_f32 v139, v139, v178\n\t"
    "v_sub_f32 v140, v140, v179\n\t"
    "v_sub_f32 v141, v141, v179\n\t"
    "v_sub_f32 v142, v142, v179\n\t"
    "v_sub_f32 v143, v143, v179\n\t"
    "v_sub_f32 v16, v16, v176\n\t"
    "v_sub_f32 v17, v17, v176\n\t"
    "v_sub_f32 v18, v18, v176\n\t"
    "v_sub_f32 v19, v19, v176\n\t"
    "v_sub_f32 v20, v20, v177\n\t"
    "v_sub_f32 v21, v21, v177\n\t"
    "v_sub_f32 v22, v22, v177\n\t"
    "v_sub_f32 v23, v23, v177\n\t"
    "v_sub_f32 v24, v24, v178\n\t"
    "v_sub_f32 v25, v25, v178\n\t"
    "v_sub_f32 v26, v26, v178\n\t"
    "v_sub_f32 v27, v27, v178\n\t"
    "v_sub_f32 v28, v28, v179\n\t"
    "v_sub_f32 v29, v29, v179\n\t"
    "v_sub_f32 v30, v30, v179\n\t"
    "v_sub_f32 v31, v31, v179\n\t"
    "v_sub_f32 v32, v32, v176\n\t"
    "v_sub_f32 v33, v33, v176\n\t"
    "v_sub_f32 v34, v34, v176\n\t"
    "v_sub_f32 v35, v35, v176\n\t"
    "v_sub_f32 v36, v36, v177\n\t"
    "v_sub_f32 v37, v37, v177\n\t"
    "v_sub_f32 v38, v38, v177\n\t"
    "v_sub_f32 v39, v39, v177\n\t"
    "v_sub_f32 v40, v40, v178\n\t"
    "v_sub_f32 v41, v41, v178\n\t"
    "v_sub_f32 v42, v42, v178\n\t"
    "v_sub_f32 v43, v43, v178\n\t"
    "v_sub_f32 v44, v44, v179\n\t"
    "v_sub_f32 v45, v45, v179\n\t"
    "v_sub_f32 v46, v46, v179\n\t"
    "v_sub_f32 v47, v47, v179\n\t"
    "v_sub_f32 v48, v48, v176\n\t"
    "v_sub_f32 v49, v49, v176\n\t"
    "v_sub_f32 v50, v50, v176\n\t"
    "v_sub_f32 v51, v51, v176\n\t"
    "v_sub_f32 v52, v52, v177\n\t"
    "v_sub_f32 v53, v53, v177\n\t"
    "v_sub_f32 v54, v54, v177\n\t"
    "v_sub_f32 v55, v55, v177\n\t"
    "v_sub_f32 v56, v56, v178\n\t"
    "v_sub_f32 v57, v57, v178\n\t"
    "v_sub_f32 v58, v58, v178\n\t"
    "v_sub_f32 v59, v59, v178\n\t"
    "v_sub_f32 v60, v60, v179\n\t"
    "v_sub_f32 v61, v61, v179\n\t"
    "v_sub_f32 v62, v62, v179\n\t"
    "v_sub_f32 v63, v63, v179\n\t"
    "v_sub_f32 v64, v64, v176\n\t"
    "v_sub_f32 v65, v65, v176\n\t"
    "v_sub_f32 v66, v66, v176\n\t"
    "v_sub_f32 v67, v67, v176\n\t"
    "v_sub_f32 v68, v68, v177\n\t"
    "v_sub_f32 v69, v69, v177\n\t"
    "v_sub_f32 v70, v70, v177\n\t"
    "v_sub_f32 v71, v71, v177\n\t"
    "v_sub_f32 v72, v72, v178\n\t"
    "v_sub_f32 v73, v73, v178\n\t"
    "v_sub_f32 v74, v74, v178\n\t"
    "v_sub_f32 v75, v75, v178\n\t"
    "v_sub_f32 v76, v76, v179\n\t"
    "v_sub_f32 v77, v77, v179\n\t"
    "v_sub_f32 v78, v78, v179\n\t"
    "v_sub_f32 v79, v79, v179\n\t"
    "v_mul_f32 v224, v224, v180\n\t"
    "v_mul_f32 v225, v225, v181\n\t"
    "v_mul_f32 v226, v226, v182\n\t"
    "v_mul_f32 v227, v227, v183\n\t"
    "v_accvgpr_read_b32 v232, a128\n\t"
    "v_accvgpr_read_b32 v233, a129\n\t"
    "v_accvgpr_read_b32 v234, a130\n\t"
    "v_accvgpr_read_b32 v235, a131\n\t"
    "v_mul_f32 v232, v232, v180\n\t"
    "v_mul_f32 v233, v233, v180\n\t"
    "v_mul_f32 v234, v234, v180\n\t"
    "v_mul_f32 v235, v235, v180\n\t"
    "v_accvgpr_write_b32 a128, v232\n\t"
    "v_accvgpr_write_b32 a129, v233\n\t"
    "v_accvgpr_write_b32 a130, v234\n\t"
    "v_accvgpr_write_b32 a131, v235\n\t"
    "v_accvgpr_read_b32 v232, a132\n\t"
    "v_accvgpr_read_b32 v233, a133\n\t"
    "v_accvgpr_read_b32 v234, a134\n\t"
    "v_accvgpr_read_b32 v235, a135\n\t"
    "v_mul_f32 v232, v232, v181\n\t"
    "v_mul_f32 v233, v233, v181\n\t"
    "v_mul_f32 v234, v234, v181\n\t"
    "v_mul_f32 v235, v235, v181\n\t"
    "v_accvgpr_write_b32 a132, v232\n\t"
    "v_accvgpr_write_b32 a133, v233\n\t"
    "v_accvgpr_write_b32 a134, v234\n\t"
    "v_accvgpr_write_b32 a135, v235\n\t"
    "v_accvgpr_read_b32 v232, a136\n\t"
    "v_accvgpr_read_b32 v233, a137\n\t"
    "v_accvgpr_read_b32 v234, a138\n\t"
    "v_accvgpr_read_b32 v235, a139\n\t"
    "v_mul_f32 v232, v232, v182\n\t"
    "v_mul_f32 v233, v233, v182\n\t"
    "v_mul_f32 v234, v234, v182\n\t"
    "v_mul_f32 v235, v235, v182\n\t"
    "v_accvgpr_write_b32 a136, v232\n\t"
    "v_accvgpr_write_b32 a137, v233\n\t"
    "v_accvgpr_write_b32 a138, v234\n\t"
    "v_accvgpr_write_b32 a139, v235\n\t"
    "v_accvgpr_read_b32 v232, a140\n\t"
    "v_accvgpr_read_b32 v233, a141\n\t"
    "v_accvgpr_read_b32 v234, a142\n\t"
    "v_accvgpr_read_b32 v235, a143\n\t"
    "v_mul_f32 v232, v232, v183\n\t"
    "v_mul_f32 v233, v233, v183\n\t"
    "v_mul_f32 v234, v234, v183\n\t"
    "v_mul_f32 v235, v235, v183\n\t"
    "v_accvgpr_write_b32 a140, v232\n\t"
    "v_accvgpr_write_b32 a141, v233\n\t"
    "v_accvgpr_write_b32 a142, v234\n\t"
    "v_accvgpr_write_b32 a143, v235\n\t"
    "v_accvgpr_read_b32 v232, a144\n\t"
    "v_accvgpr_read_b32 v233, a145\n\t"
    "v_accvgpr_read_b32 v234, a146\n\t"
    "v_accvgpr_read_b32 v235, a147\n\t"
    "v_mul_f32 v232, v232, v180\n\t"
    "v_mul_f32 v233, v233, v180\n\t"
    "v_mul_f32 v234, v234, v180\n\t"
    "v_mul_f32 v235, v235, v180\n\t"
    "v_accvgpr_write_b32 a144, v232\n\t"
    "v_accvgpr_write_b32 a145, v233\n\t"
    "v_accvgpr_write_b32 a146, v234\n\t"
    "v_accvgpr_write_b32 a147, v235\n\t"
    "v_accvgpr_read_b32 v232, a148\n\t"
    "v_accvgpr_read_b32 v233, a149\n\t"
    "v_accvgpr_read_b32 v234, a150\n\t"
    "v_accvgpr_read_b32 v235, a151\n\t"
    "v_mul_f32 v232, v232, v181\n\t"
    "v_mul_f32 v233, v233, v181\n\t"
    "v_mul_f32 v234, v234, v181\n\t"
    "v_mul_f32 v235, v235, v181\n\t"
    "v_accvgpr_write_b32 a148, v232\n\t"
    "v_accvgpr_write_b32 a149, v233\n\t"
    "v_accvgpr_write_b32 a150, v234\n\t"
    "v_accvgpr_write_b32 a151, v235\n\t"
    "v_accvgpr_read_b32 v232, a152\n\t"
    "v_accvgpr_read_b32 v233, a153\n\t"
    "v_accvgpr_read_b32 v234, a154\n\t"
    "v_accvgpr_read_b32 v235, a155\n\t"
    "v_mul_f32 v232, v232, v182\n\t"
    "v_mul_f32 v233, v233, v182\n\t"
    "v_mul_f32 v234, v234, v182\n\t"
    "v_mul_f32 v235, v235, v182\n\t"
    "v_accvgpr_write_b32 a152, v232\n\t"
    "v_accvgpr_write_b32 a153, v233\n\t"
    "v_accvgpr_write_b32 a154, v234\n\t"
    "v_accvgpr_write_b32 a155, v235\n\t"
    "v_accvgpr_read_b32 v232, a156\n\t"
    "v_accvgpr_read_b32 v233, a157\n\t"
    "v_accvgpr_read_b32 v234, a158\n\t"
    "v_accvgpr_read_b32 v235, a159\n\t"
    "v_mul_f32 v232, v232, v183\n\t"
    "v_mul_f32 v233, v233, v183\n\t"
    "v_mul_f32 v234, v234, v183\n\t"
    "v_mul_f32 v235, v235, v183\n\t"
    "v_accvgpr_write_b32 a156, v232\n\t"
    "v_accvgpr_write_b32 a157, v233\n\t"
    "v_accvgpr_write_b32 a158, v234\n\t"
    "v_accvgpr_write_b32 a159, v235\n\t"
    "v_accvgpr_read_b32 v232, a160\n\t"
    "v_accvgpr_read_b32 v233, a161\n\t"
    "v_accvgpr_read_b32 v234, a162\n\t"
    "v_accvgpr_read_b32 v235, a163\n\t"
    "v_mul_f32 v232, v232, v180\n\t"
    "v_mul_f32 v233, v233, v180\n\t"
    "v_mul_f32 v234, v234, v180\n\t"
    "v_mul_f32 v235, v235, v180\n\t"
    "v_accvgpr_write_b32 a160, v232\n\t"
    "v_accvgpr_write_b32 a161, v233\n\t"
    "v_accvgpr_write_b32 a162, v234\n\t"
    "v_accvgpr_write_b32 a163, v235\n\t"
    "v_accvgpr_read_b32 v232, a164\n\t"
    "v_accvgpr_read_b32 v233, a165\n\t"
    "v_accvgpr_read_b32 v234, a166\n\t"
    "v_accvgpr_read_b32 v235, a167\n\t"
    "v_mul_f32 v232, v232, v181\n\t"
    "v_mul_f32 v233, v233, v181\n\t"
    "v_mul_f32 v234, v234, v181\n\t"
    "v_mul_f32 v235, v235, v181\n\t"
    "v_accvgpr_write_b32 a164, v232\n\t"
    "v_accvgpr_write_b32 a165, v233\n\t"
    "v_accvgpr_write_b32 a166, v234\n\t"
    "v_accvgpr_write_b32 a167, v235\n\t"
    "v_accvgpr_read_b32 v232, a168\n\t"
    "v_accvgpr_read_b32 v233, a169\n\t"
    "v_accvgpr_read_b32 v234, a170\n\t"
    "v_accvgpr_read_b32 v235, a171\n\t"
    "v_mul_f32 v232, v232, v182\n\t"
    "v_mul_f32 v233, v233, v182\n\t"
    "v_mul_f32 v234, v234, v182\n\t"
    "v_mul_f32 v235, v235, v182\n\t"
    "v_accvgpr_write_b32 a168, v232\n\t"
    "v_accvgpr_write_b32 a169, v233\n\t"
    "v_accvgpr_write_b32 a170, v234\n\t"
    "v_accvgpr_write_b32 a171, v235\n\t"
    "v_accvgpr_read_b32 v232, a172\n\t"
    "v_accvgpr_read_b32 v233, a173\n\t"
    "v_accvgpr_read_b32 v234, a174\n\t"
    "v_accvgpr_read_b32 v235, a175\n\t"
    "v_mul_f32 v232, v232, v183\n\t"
    "v_mul_f32 v233, v233, v183\n\t"
    "v_mul_f32 v234, v234, v183\n\t"
    "v_mul_f32 v235, v235, v183\n\t"
    "v_accvgpr_write_b32 a172, v232\n\t"
    "v_accvgpr_write_b32 a173, v233\n\t"
    "v_accvgpr_write_b32 a174, v234\n\t"
    "v_accvgpr_write_b32 a175, v235\n\t"
    "v_accvgpr_read_b32 v232, a176\n\t"
    "v_accvgpr_read_b32 v233, a177\n\t"
    "v_accvgpr_read_b32 v234, a178\n\t"
    "v_accvgpr_read_b32 v235, a179\n\t"
    "v_mul_f32 v232, v232, v180\n\t"
    "v_mul_f32 v233, v233, v180\n\t"
    "v_mul_f32 v234, v234, v180\n\t"
    "v_mul_f32 v235, v235, v180\n\t"
    "v_accvgpr_write_b32 a176, v232\n\t"
    "v_accvgpr_write_b32 a177, v233\n\t"
    "v_accvgpr_write_b32 a178, v234\n\t"
    "v_accvgpr_write_b32 a179, v235\n\t"
    "v_accvgpr_read_b32 v232, a180\n\t"
    "v_accvgpr_read_b32 v233, a181\n\t"
    "v_accvgpr_read_b32 v234, a182\n\t"
    "v_accvgpr_read_b32 v235, a183\n\t"
    "v_mul_f32 v232, v232, v181\n\t"
    "v_mul_f32 v233, v233, v181\n\t"
    "v_mul_f32 v234, v234, v181\n\t"
    "v_mul_f32 v235, v235, v181\n\t"
    "v_accvgpr_write_b32 a180, v232\n\t"
    "v_accvgpr_write_b32 a181, v233\n\t"
    "v_accvgpr_write_b32 a182, v234\n\t"
    "v_accvgpr_write_b32 a183, v235\n\t"
    "v_accvgpr_read_b32 v232, a184\n\t"
    "v_accvgpr_read_b32 v233, a185\n\t"
    "v_accvgpr_read_b32 v234, a186\n\t"
    "v_accvgpr_read_b32 v235, a187\n\t"
    "v_mul_f32 v232, v232, v182\n\t"
    "v_mul_f32 v233, v233, v182\n\t"
    "v_mul_f32 v234, v234, v182\n\t"
    "v_mul_f32 v235, v235, v182\n\t"
    "v_accvgpr_write_b32 a184, v232\n\t"
    "v_accvgpr_write_b32 a185, v233\n\t"
    "v_accvgpr_write_b32 a186, v234\n\t"
    "v_accvgpr_write_b32 a187, v235\n\t"
    "v_accvgpr_read_b32 v232, a188\n\t"
    "v_accvgpr_read_b32 v233, a189\n\t"
    "v_accvgpr_read_b32 v234, a190\n\t"
    "v_accvgpr_read_b32 v235, a191\n\t"
    "v_mul_f32 v232, v232, v183\n\t"
    "v_mul_f32 v233, v233, v183\n\t"
    "v_mul_f32 v234, v234, v183\n\t"
    "v_mul_f32 v235, v235, v183\n\t"
    "v_accvgpr_write_b32 a188, v232\n\t"
    "v_accvgpr_write_b32 a189, v233\n\t"
    "v_accvgpr_write_b32 a190, v234\n\t"
    "v_accvgpr_write_b32 a191, v235\n\t"
    "v_accvgpr_read_b32 v232, a192\n\t"
    "v_accvgpr_read_b32 v233, a193\n\t"
    "v_accvgpr_read_b32 v234, a194\n\t"
    "v_accvgpr_read_b32 v235, a195\n\t"
    "v_mul_f32 v232, v232, v180\n\t"
    "v_mul_f32 v233, v233, v180\n\t"
    "v_mul_f32 v234, v234, v180\n\t"
    "v_mul_f32 v235, v235, v180\n\t"
    "v_accvgpr_write_b32 a192, v232\n\t"
    "v_accvgpr_write_b32 a193, v233\n\t"
    "v_accvgpr_write_b32 a194, v234\n\t"
    "v_accvgpr_write_b32 a195, v235\n\t"
    "v_accvgpr_read_b32 v232, a196\n\t"
    "v_accvgpr_read_b32 v233, a197\n\t"
    "v_accvgpr_read_b32 v234, a198\n\t"
    "v_accvgpr_read_b32 v235, a199\n\t"
    "v_mul_f32 v232, v232, v181\n\t"
    "v_mul_f32 v233, v233, v181\n\t"
    "v_mul_f32 v234, v234, v181\n\t"
    "v_mul_f32 v235, v235, v181\n\t"
    "v_accvgpr_write_b32 a196, v232\n\t"
    "v_accvgpr_write_b32 a197, v233\n\t"
    "v_accvgpr_write_b32 a198, v234\n\t"
    "v_accvgpr_write_b32 a199, v235\n\t"
    "v_accvgpr_read_b32 v232, a200\n\t"
    "v_accvgpr_read_b32 v233, a201\n\t"
    "v_accvgpr_read_b32 v234, a202\n\t"
    "v_accvgpr_read_b32 v235, a203\n\t"
    "v_mul_f32 v232, v232, v182\n\t"
    "v_mul_f32 v233, v233, v182\n\t"
    "v_mul_f32 v234, v234, v182\n\t"
    "v_mul_f32 v235, v235, v182\n\t"
    "v_accvgpr_write_b32 a200, v232\n\t"
    "v_accvgpr_write_b32 a201, v233\n\t"
    "v_accvgpr_write_b32 a202, v234\n\t"
    "v_accvgpr_write_b32 a203, v235\n\t"
    "v_accvgpr_read_b32 v232, a204\n\t"
    "v_accvgpr_read_b32 v233, a205\n\t"
    "v_accvgpr_read_b32 v234, a206\n\t"
    "v_accvgpr_read_b32 v235, a207\n\t"
    "v_mul_f32 v232, v232, v183\n\t"
    "v_mul_f32 v233, v233, v183\n\t"
    "v_mul_f32 v234, v234, v183\n\t"
    "v_mul_f32 v235, v235, v183\n\t"
    "v_accvgpr_write_b32 a204, v232\n\t"
    "v_accvgpr_write_b32 a205, v233\n\t"
    "v_accvgpr_write_b32 a206, v234\n\t"
    "v_accvgpr_write_b32 a207, v235\n\t"
    "v_accvgpr_read_b32 v232, a208\n\t"
    "v_accvgpr_read_b32 v233, a209\n\t"
    "v_accvgpr_read_b32 v234, a210\n\t"
    "v_accvgpr_read_b32 v235, a211\n\t"
    "v_mul_f32 v232, v232, v180\n\t"
    "v_mul_f32 v233, v233, v180\n\t"
    "v_mul_f32 v234, v234, v180\n\t"
    "v_mul_f32 v235, v235, v180\n\t"
    "v_accvgpr_write_b32 a208, v232\n\t"
    "v_accvgpr_write_b32 a209, v233\n\t"
    "v_accvgpr_write_b32 a210, v234\n\t"
    "v_accvgpr_write_b32 a211, v235\n\t"
    "v_accvgpr_read_b32 v232, a212\n\t"
    "v_accvgpr_read_b32 v233, a213\n\t"
    "v_accvgpr_read_b32 v234, a214\n\t"
    "v_accvgpr_read_b32 v235, a215\n\t"
    "v_mul_f32 v232, v232, v181\n\t"
    "v_mul_f32 v233, v233, v181\n\t"
    "v_mul_f32 v234, v234, v181\n\t"
    "v_mul_f32 v235, v235, v181\n\t"
    "v_accvgpr_write_b32 a212, v232\n\t"
    "v_accvgpr_write_b32 a213, v233\n\t"
    "v_accvgpr_write_b32 a214, v234\n\t"
    "v_accvgpr_write_b32 a215, v235\n\t"
    "v_accvgpr_read_b32 v232, a216\n\t"
    "v_accvgpr_read_b32 v233, a217\n\t"
    "v_accvgpr_read_b32 v234, a218\n\t"
    "v_accvgpr_read_b32 v235, a219\n\t"
    "v_mul_f32 v232, v232, v182\n\t"
    "v_mul_f32 v233, v233, v182\n\t"
    "v_mul_f32 v234, v234, v182\n\t"
    "v_mul_f32 v235, v235, v182\n\t"
    "v_accvgpr_write_b32 a216, v232\n\t"
    "v_accvgpr_write_b32 a217, v233\n\t"
    "v_accvgpr_write_b32 a218, v234\n\t"
    "v_accvgpr_write_b32 a219, v235\n\t"
    "v_accvgpr_read_b32 v232, a220\n\t"
    "v_accvgpr_read_b32 v233, a221\n\t"
    "v_accvgpr_read_b32 v234, a222\n\t"
    "v_accvgpr_read_b32 v235, a223\n\t"
    "v_mul_f32 v232, v232, v183\n\t"
    "v_mul_f32 v233, v233, v183\n\t"
    "v_mul_f32 v234, v234, v183\n\t"
    "v_mul_f32 v235, v235, v183\n\t"
    "v_accvgpr_write_b32 a220, v232\n\t"
    "v_accvgpr_write_b32 a221, v233\n\t"
    "v_accvgpr_write_b32 a222, v234\n\t"
    "v_accvgpr_write_b32 a223, v235\n\t"
    "v_accvgpr_read_b32 v232, a224\n\t"
    "v_accvgpr_read_b32 v233, a225\n\t"
    "v_accvgpr_read_b32 v234, a226\n\t"
    "v_accvgpr_read_b32 v235, a227\n\t"
    "v_mul_f32 v232, v232, v180\n\t"
    "v_mul_f32 v233, v233, v180\n\t"
    "v_mul_f32 v234, v234, v180\n\t"
    "v_mul_f32 v235, v235, v180\n\t"
    "v_accvgpr_write_b32 a224, v232\n\t"
    "v_accvgpr_write_b32 a225, v233\n\t"
    "v_accvgpr_write_b32 a226, v234\n\t"
    "v_accvgpr_write_b32 a227, v235\n\t"
    "v_accvgpr_read_b32 v232, a228\n\t"
    "v_accvgpr_read_b32 v233, a229\n\t"
    "v_accvgpr_read_b32 v234, a230\n\t"
    "v_accvgpr_read_b32 v235, a231\n\t"
    "v_mul_f32 v232, v232, v181\n\t"
    "v_mul_f32 v233, v233, v181\n\t"
    "v_mul_f32 v234, v234, v181\n\t"
    "v_mul_f32 v235, v235, v181\n\t"
    "v_accvgpr_write_b32 a228, v232\n\t"
    "v_accvgpr_write_b32 a229, v233\n\t"
    "v_accvgpr_write_b32 a230, v234\n\t"
    "v_accvgpr_write_b32 a231, v235\n\t"
    "v_accvgpr_read_b32 v232, a232\n\t"
    "v_accvgpr_read_b32 v233, a233\n\t"
    "v_accvgpr_read_b32 v234, a234\n\t"
    "v_accvgpr_read_b32 v235, a235\n\t"
    "v_mul_f32 v232, v232, v182\n\t"
    "v_mul_f32 v233, v233, v182\n\t"
    "v_mul_f32 v234, v234, v182\n\t"
    "v_mul_f32 v235, v235, v182\n\t"
    "v_accvgpr_write_b32 a232, v232\n\t"
    "v_accvgpr_write_b32 a233, v233\n\t"
    "v_accvgpr_write_b32 a234, v234\n\t"
    "v_accvgpr_write_b32 a235, v235\n\t"
    "v_accvgpr_read_b32 v232, a236\n\t"
    "v_accvgpr_read_b32 v233, a237\n\t"
    "v_accvgpr_read_b32 v234, a238\n\t"
    "v_accvgpr_read_b32 v235, a239\n\t"
    "v_mul_f32 v232, v232, v183\n\t"
    "v_mul_f32 v233, v233, v183\n\t"
    "v_mul_f32 v234, v234, v183\n\t"
    "v_mul_f32 v235, v235, v183\n\t"
    "v_accvgpr_write_b32 a236, v232\n\t"
    "v_accvgpr_write_b32 a237, v233\n\t"
    "v_accvgpr_write_b32 a238, v234\n\t"
    "v_accvgpr_write_b32 a239, v235\n\t"
    "v_accvgpr_read_b32 v232, a240\n\t"
    "v_accvgpr_read_b32 v233, a241\n\t"
    "v_accvgpr_read_b32 v234, a242\n\t"
    "v_accvgpr_read_b32 v235, a243\n\t"
    "v_mul_f32 v232, v232, v180\n\t"
    "v_mul_f32 v233, v233, v180\n\t"
    "v_mul_f32 v234, v234, v180\n\t"
    "v_mul_f32 v235, v235, v180\n\t"
    "v_accvgpr_write_b32 a240, v232\n\t"
    "v_accvgpr_write_b32 a241, v233\n\t"
    "v_accvgpr_write_b32 a242, v234\n\t"
    "v_accvgpr_write_b32 a243, v235\n\t"
    "v_accvgpr_read_b32 v232, a244\n\t"
    "v_accvgpr_read_b32 v233, a245\n\t"
    "v_accvgpr_read_b32 v234, a246\n\t"
    "v_accvgpr_read_b32 v235, a247\n\t"
    "v_mul_f32 v232, v232, v181\n\t"
    "v_mul_f32 v233, v233, v181\n\t"
    "v_mul_f32 v234, v234, v181\n\t"
    "v_mul_f32 v235, v235, v181\n\t"
    "v_accvgpr_write_b32 a244, v232\n\t"
    "v_accvgpr_write_b32 a245, v233\n\t"
    "v_accvgpr_write_b32 a246, v234\n\t"
    "v_accvgpr_write_b32 a247, v235\n\t"
    "v_accvgpr_read_b32 v232, a248\n\t"
    "v_accvgpr_read_b32 v233, a249\n\t"
    "v_accvgpr_read_b32 v234, a250\n\t"
    "v_accvgpr_read_b32 v235, a251\n\t"
    "v_mul_f32 v232, v232, v182\n\t"
    "v_mul_f32 v233, v233, v182\n\t"
    "v_mul_f32 v234, v234, v182\n\t"
    "v_mul_f32 v235, v235, v182\n\t"
    "v_accvgpr_write_b32 a248, v232\n\t"
    "v_accvgpr_write_b32 a249, v233\n\t"
    "v_accvgpr_write_b32 a250, v234\n\t"
    "v_accvgpr_write_b32 a251, v235\n\t"
    "v_accvgpr_read_b32 v232, a252\n\t"
    "v_accvgpr_read_b32 v233, a253\n\t"
    "v_accvgpr_read_b32 v234, a254\n\t"
    "v_accvgpr_read_b32 v235, a255\n\t"
    "v_mul_f32 v232, v232, v183\n\t"
    "v_mul_f32 v233, v233, v183\n\t"
    "v_mul_f32 v234, v234, v183\n\t"
    "v_mul_f32 v235, v235, v183\n\t"
    "v_accvgpr_write_b32 a252, v232\n\t"
    "v_accvgpr_write_b32 a253, v233\n\t"
    "v_accvgpr_write_b32 a254, v234\n\t"
    "v_accvgpr_write_b32 a255, v235\n\t"
    "v_exp_f32 v232, v80\n\t"
    "v_exp_f32 v233, v81\n\t"
    "s_nop 0\n\t"
    "v_cvt_pk_bf16_f32 v176, v232, v233\n\t"
    "v_exp_f32 v234, v82\n\t"
    "v_exp_f32 v235, v83\n\t"
    "s_nop 0\n\t"
    "v_cvt_pk_bf16_f32 v177, v234, v235\n\t"
    "v_exp_f32 v236, v96\n\t"
    "v_exp_f32 v237, v97\n\t"
    "s_nop 0\n\t"
    "v_cvt_pk_bf16_f32 v178, v236, v237\n\t"
    "v_exp_f32 v238, v98\n\t"
    "v_exp_f32 v239, v99\n\t"
    "s_nop 0\n\t"
    "v_cvt_pk_bf16_f32 v179, v238, v239\n\t"
    "v_exp_f32 v240, v84\n\t"
    "v_exp_f32 v241, v85\n\t"
    "s_nop 0\n\t"
    "v_cvt_pk_bf16_f32 v180, v240, v241\n\t"
    "v_exp_f32 v242, v86\n\t"
    "v_exp_f32 v243, v87\n\t"
    "s_nop 0\n\t"
    "v_cvt_pk_bf16_f32 v181, v242, v243\n\t"
    "v_exp_f32 v232, v100\n\t"
    "v_exp_f32 v233, v101\n\t"
    "s_nop 0\n\t"
    "v_cvt_pk_bf16_f32 v182, v232, v233\n\t"
    "v_exp_f32 v234, v102\n\t"
    "v_exp_f32 v235, v103\n\t"
    "s_nop 0\n\t"
    "v_cvt_pk_bf16_f32 v183, v234, v235\n\t"
    "v_exp_f32 v236, v88\n\t"
    "v_exp_f32 v237, v89\n\t"
    "s_nop 0\n\t"
    "v_cvt_pk_bf16_f32 v184, v236, v237\n\t"
    "v_exp_f32 v238, v90\n\t"
    "v_exp_f32 v239, v91\n\t"
    "s_nop 0\n\t"
    "v_cvt_pk_bf16_f32 v185, v238, v239\n\t"
    "v_exp_f32 v240, v104\n\t"
    "v_exp_f32 v241, v105\n\t"
    "s_nop 0\n\t"
    "v_cvt_pk_bf16_f32 v186, v240, v241\n\t"
    "v_exp_f32 v242, v106\n\t"
    "v_exp_f32 v243, v107\n\t"
    "s_nop 0\n\t"
    "v_cvt_pk_bf16_f32 v187, v242, v243\n\t"
    "v_exp_f32 v232, v92\n\t"
    "v_exp_f32 v233, v93\n\t"
    "s_nop 0\n\t"
    "v_cvt_pk_bf16_f32 v188, v232, v233\n\t"
    "v_exp_f32 v234, v94\n\t"
    "v_exp_f32 v235, v95\n\t"
    "s_nop 0\n\t"
    "v_cvt_pk_bf16_f32 v189, v234, v235\n\t"
    "v_exp_f32 v236, v108\n\t"
    "v_exp_f32 v237, v109\n\t"
    "s_nop 0\n\t"
    "v_cvt_pk_bf16_f32 v190, v236, v237\n\t"
    "v_exp_f32 v238, v110\n\t"
    "v_exp_f32 v239, v111\n\t"
    "s_nop 0\n\t"
    "v_cvt_pk_bf16_f32 v191, v238, v239\n\t"
    "v_exp_f32 v240, v112\n\t"
    "v_exp_f32 v241, v113\n\t"
    "s_nop 0\n\t"
    "v_cvt_pk_bf16_f32 v192, v240, v241\n\t"
    "v_exp_f32 v242, v114\n\t"
    "v_exp_f32 v243, v115\n\t"
    "s_nop 0\n\t"
    "v_cvt_pk_bf16_f32 v193, v242, v243\n\t"
    "v_exp_f32 v232, v128\n\t"
    "v_exp_f32 v233, v129\n\t"
    "s_nop 0\n\t"
    "v_cvt_pk_bf16_f32 v194, v232, v233\n\t"
    "v_exp_f32 v234, v130\n\t"
    "v_exp_f32 v235, v131\n\t"
    "s_nop 0\n\t"
    "v_cvt_pk_bf16_f32 v195, v234, v235\n\t"
    "v_exp_f32 v236, v116\n\t"
    "v_exp_f32 v237, v117\n\t"
    "s_nop 0\n\t"
    "v_cvt_pk_bf16_f32 v196, v236, v237\n\t"
    "v_exp_f32 v238, v118\n\t"
    "v_exp_f32 v239, v119\n\t"
    "s_nop 0\n\t"
    "v_cvt_pk_bf16_f32 v197, v238, v239\n\t"
    "v_exp_f32 v240, v132\n\t"
    "v_exp_f32 v241, v133\n\t"
    "s_nop 0\n\t"
    "v_cvt_pk_bf16_f32 v198, v240, v241\n\t"
    "v_exp_f32 v242, v134\n\t"
    "v_exp_f32 v243, v135\n\t"
    "s_nop 0\n\t"
    "v_cvt_pk_bf16_f32 v199, v242, v243\n\t"
    "v_exp_f32 v232, v120\n\t"
    "v_exp_f32 v233, v121\n\t"
    "s_nop 0\n\t"
    "v_cvt_pk_bf16_f32 v200, v232, v233\n\t"
    "v_exp_f32 v234, v122\n\t"
    "v_exp_f32 v235, v123\n\t"
    "s_nop 0\n\t"
    "v_cvt_pk_bf16_f32 v201, v234, v235\n\t"
    "v_exp_f32 v236, v136\n\t"
    "v_exp_f32 v237, v137\n\t"
    "s_nop 0\n\t"
    "v_cvt_pk_bf16_f32 v202, v236, v237\n\t"
    "v_exp_f32 v238, v138\n\t"
    "v_exp_f32 v239, v139\n\t"
    "s_nop 0\n\t"
    "v_cvt_pk_bf16_f32 v203, v238, v239\n\t"
    "v_exp_f32 v240, v124\n\t"
    "v_exp_f32 v241, v125\n\t"
    "s_nop 0\n\t"
    "v_cvt_pk_bf16_f32 v204, v240, v241\n\t"
    "v_exp_f32 v242, v126\n\t"
    "v_exp_f32 v243, v127\n\t"
    "s_nop 0\n\t"
    "v_cvt_pk_bf16_f32 v205, v242, v243\n\t"
    "v_exp_f32 v232, v140\n\t"
    "v_exp_f32 v233, v141\n\t"
    "s_nop 0\n\t"
    "v_cvt_pk_bf16_f32 v206, v232, v233\n\t"
    "v_exp_f32 v234, v142\n\t"
    "v_exp_f32 v235, v143\n\t"
    "s_nop 0\n\t"
    "v_cvt_pk_bf16_f32 v207, v234, v235\n\t"
    "v_mfma_f32_16x16x32_bf16 v[228:231], a[96:99], v[176:179], v[224:227]\n\t"
    "v_mfma_f32_16x16x32_bf16 v[228:231], a[100:103], v[180:183], v[228:231]\n\t"
    "v_mfma_f32_16x16x32_bf16 v[228:231], a[104:107], v[184:187], v[228:231]\n\t"
    "v_mfma_f32_16x16x32_bf16 v[228:231], a[108:111], v[188:191], v[228:231]\n\t"
    "v_mfma_f32_16x16x32_bf16 v[228:231], a[96:99], v[192:195], v[228:231]\n\t"
    "v_mfma_f32_16x16x32_bf16 v[228:231], a[100:103], v[196:199], v[228:231]\n\t"
    "v_mfma_f32_16x16x32_bf16 v[228:231], a[104:107], v[200:203], v[228:231]\n\t"
    "v_mfma_f32_16x16x32_bf16 v[228:231], a[108:111], v[204:207], v[228:231]\n\t"
    "s_nop 15\n\t"
    "s_add_u32 s60, s60, 12\n\t"
    "s_addc_u32 s61, s61, 0\n\t"
    "s_setpc_b64 s[60:61]\n\t"
    "L_k1_end_%=:\n\t"
    "k_pga11: s_cbranch_execz k_pga_back\n\t"
    "s_nop 1\n\t"
      : [sWk] "+&s"(sWk), [sX] "+&s"(sX), [sP2] "+&s"(sP2), [vX] "+&v"(vX), "+{s[84:91]}"(tb0), "+{s[92:99]}"(tb1), "+{s83}"(tb2)
      : [srdW] "s"(srdW), [srdWS] "s"(srdWS), [srdP2] "s"(srdP2), [sPark] "s"(131328 + w * 6144), [sLds4] "s"(sm0 + w * 4096), [sLdsB] "s"(sm0), [sKV] "s"(sKV),
        [sH2] "s"(sH2), [sHa] "s"(sHa), [sHb] "s"(sHa + 4096), [sHc] "s"(sHa + 8192), [sO] "s"(sO), [sSig] "s"(sSig),
        [vW] "v"(vW), [vL16] "v"(vL16), [vRdW] "v"(vRdW), [vRdW2] "v"(vRdW2), [vH8] "v"(vH8)
      : "memory", "scc", "vcc", "s60","s61","s62","s63","s64","s65","s66","s67","s68","s69","s82",
      "v8","v9","v10","v11","v12","v13","v14","v15","v16","v17","v18","v19","v20","v21","v22","v23","v24","v25","v26","v27","v28","v29","v30","v31",
      "v32","v33","v34","v35","v36","v37","v38","v39","v40","v41","v42","v43","v44","v45","v46","v47","v48","v49","v50","v51","v52","v53","v54","v55",
      "v56","v57","v58","v59","v60","v61","v62","v63","v64","v65","v66","v67","v68","v69","v70","v71","v72","v73","v74","v75","v76","v77","v78","v79",
      "v80","v81","v82","v83","v84","v85","v86","v87","v88","v89","v90","v91","v92","v93","v94","v95","v96","v97","v98","v99","v100","v101","v102","v103",
      "v104","v105","v106","v107","v108","v109","v110","v111","v112","v113","v114","v115","v116","v117","v118","v119","v120","v121","v122","v123","v124","v125","v126","v127",
      "v128","v129","v130","v131","v132","v133","v134","v135","v136","v137","v138","v139","v140","v141","v142","v143","v144","v145","v146","v147","v148","v149","v150","v151",
      "v152","v153","v154","v155","v156","v157","v158","v159","v160","v161","v162","v163","v164","v165","v166","v167","v168","v169","v170","v171","v172","v173","v174","v175",
      "v176","v177","v178","v179","v180","v181","v182","v183","v184","v185","v186","v187","v188","v189","v190","v191","v192","v193","v194","v195","v196","v197","v198","v199",
      "v200","v201","v202","v203","v204","v205","v206","v207","v208","v209","v210","v211","v212","v213","v214","v215","v216","v217","v218","v219","v220","v221","v222","v223",
      "v224","v225","v226","v227","v228","v229","v230","v231","v232","v233","v234","v235","v236","v237","v238","v239","v240","v241","v242","v243","v244","v245","v246","v247",
      "v248","v249","v250","v251","v252","v253","v254","v255",
      "a0","a1","a2","a3","a4","a5","a6","a7","a8","a9","a10","a11","a12","a13","a14","a15","a16","a17","a18","a19","a20","a21","a22","a23",
      "a24","a25","a26","a27","a28","a29","a30","a31","a32","a33","a34","a35","a36","a37","a38","a39","a40","a41","a42","a43","a44","a45","a46","a47",
      "a48","a49","a50","a51","a52","a53","a54","a55","a56","a57","a58","a59","a60","a61","a62","a63","a64","a65","a66","a67","a68","a69","a70","a71",
      "a72","a73","a74","a75","a76","a77","a78","a79","a80","a81","a82","a83","a84","a85","a86","a87","a88","a89","a90","a91","a92","a93","a94","a95",
      "a96","a97","a98","a99","a100","a101","a102","a103","a104","a105","a106","a107","a108","a109","a110","a111","a112","a113","a114","a115","a116","a117","a118","a119",
      "a120","a121","a122","a123","a124","a125","a126","a127","a128","a129","a130","a131","a132","a133","a134","a135","a136","a137","a138","a139","a140","a141","a142","a143",
      "a144","a145","a146","a147","a148","a149","a150","a151","a152","a153","a154","a155","a156","a157","a158","a159","a160","a161","a162","a163","a164","a165","a166","a167",
      "a168","a169","a170","a171","a172","a173","a174","a175","a176","a177","a178","a179","a180","a181","a182","a183","a184","a185","a186","a187","a188","a189","a190","a191",
      "a192","a193","a194","a195","a196","a197","a198","a199","a200","a201","a202","a203","a204","a205","a206","a207","a208","a209","a210","a211","a212","a213","a214","a215",
      "a216","a217","a218","a219","a220","a221","a222","a223","a224","a225","a226","a227","a228","a229","a230","a231","a232","a233","a234","a235","a236","a237","a238","a239",
      "a240","a241","a242","a243","a244","a245","a246","a247","a248","a249","a250","a251","a252","a253","a254","a255");
  }
  {
    constexpr int SLAB2 = 2 * 8224, KGS = 2056;
    asm volatile("" : "+s"(b), "+s"(w));
    asm volatile("v_mbcnt_lo_u32_b32 %0, -1, 0\n\tv_mbcnt_hi_u32_b32 %0, -1, %0" : "=v"(lane));
    const int xcd = b & 7, idx = b >> 3;
    const int g = xcd & 1, tb = ((xcd >> 1) & 1) * 8 + (idx & 7), nb = (xcd >> 2) * 4 + (idx >> 3);
    const int kq = w >> 1, th = w & 1;
    const int rw = tb & 7, rk = nb & 1, ko = kq ^ ((nb >> 1) & 1);
    const int KD = g ? F_ : 4096; const unsigned BS = g ? H_BS : O_BS;
    const u16* W = g ? w2 : wo;
    const u32x4 srdW = {(unsigned)(size_t)W, (unsigned)((size_t)W >> 32) & 0xffffu, (unsigned)(KD * D_ * 2), 0x20000u};
    const u32x4 srdWS = {(unsigned)(size_t)ws, (unsigned)((size_t)ws >> 32) & 0xffffu, (unsigned)WS_END, 0x20000u};
    const u32x4 srdX2 = {(unsigned)(size_t)x, (unsigned)((size_t)x >> 32) & 0xffffu, (unsigned)(S_ * D_ * 2), 0x20000u};
    const u32x4 srdX = {srdX2[0], srdX2[1], g ? 0u : srdX2[2], 0x20000u};
    const u32x4 srdOut = {(unsigned)(size_t)out, (unsigned)((size_t)out >> 32) & 0xffffu, (unsigned)(S_ * D_ * 2), 0x20000u};
    const unsigned vW0 = (4 * th + (lane >> 4)) * 2048 + nb * 256 + ((lane & 15) ^ (lane >> 4)) * 16, vL16 = lane * 16;
    const unsigned abase = (unsigned)(g ? WS_H : WS_O) + (unsigned)(2 * tb + th) * BS + vL16;
    const unsigned vA0 = abase + ko * 2048, vA1 = abase + (ko ^ 1) * 2048;
    const unsigned smb = sm0 + 32256, sXl = sm0 + (3 - w) * 8192;
    const unsigned vXr = sXl + (lane & 15) * 256 + ((lane >> 4) ^ ((lane >> 2) & 3)) * 64 + (lane & 3) * 16, sXr = (tb * 128 + th * 60 + ko * 32) * 2048;
    const unsigned vRd = smb + kq * 4 * SLAB2 + (lane >> 4) * KGS + ((lane >> 2) & 3) * (256 + 16) + (lane & 3) * 64;
    const unsigned sLds = smb + kq * 4 * SLAB2 + th * 1024;
    const unsigned sAi1 = rk ? 0u - 4096u : 4096u;
    unsigned sWk = (unsigned)kq * 64 * 2048 - 128 * 2048, sA = (unsigned)kq * 8192 + rk * 4096 - (16384u - sAi1), sIt = KD / 512 - 2;
    const unsigned vXo = ((lane >> 2) & 3) * 8192 + (lane >> 4) * 64 + (lane & 3) * 16, sXo = (tb * 128 + th * 64 + ko * 32) * 2048 + nb * 256;
    const unsigned sSig = (unsigned)WS_RDY + (4 * (idx >> 2) + w) * 256 + xcd * 4, sRdy = (unsigned)WS_RDY + (2 * tb + th) * 256 + g * 128;
    const unsigned pw = (tb * 8 + nb) * 4 + w, sCnt = (unsigned)WS_CNT + pw * 128, sGg = (unsigned)WS_G + pw * 24576;
    const unsigned sRedW = smb + (kq * 4 + th) * SLAB2, sRedR = smb + ((kq ^ 1) * 4 + th) * SLAB2;
    asm volatile(
    "s_waitcnt lgkmcnt(0)\n\t"
    "s_nop 4\n\t"
    "s_sub_u32 s55, 16384, %[sAi1]\n\t"
    "s_xor_b32 s69, %[sRw], 0\n\t"
    "s_mul_i32 s84, s69, 2056\n\t"
    "s_add_u32 s84, s84, %[sLds]\n\t"
    "s_xor_b32 s69, s69, %[sRk]\n\t"
    "s_mul_i32 s92, s69, 16384\n\t"
    "s_xor_b32 s69, %[sRw], 1\n\t"
    "s_mul_i32 s85, s69, 2056\n\t"
    "s_add_u32 s85, s85, %[sLds]\n\t"
    "s_xor_b32 s69, s69, %[sRk]\n\t"
    "s_mul_i32 s93, s69, 16384\n\t"
    "s_xor_b32 s69, %[sRw], 2\n\t"
    "s_mul_i32 s86, s69, 2056\n\t"
    "s_add_u32 s86, s86, %[sLds]\n\t"
    "s_xor_b32 s69, s69, %[sRk]\n\t"
    "s_mul_i32 s94, s69, 16384\n\t"
    "s_xor_b32 s69, %[sRw], 3\n\t"
    "s_mul_i32 s87, s69, 2056\n\t"
    "s_add_u32 s87, s87, %[sLds]\n\t"
    "s_xor_b32 s69, s69, %[sRk]\n\t"
    "s_mul_i32 s95, s69, 16384\n\t"
    "s_xor_b32 s69, %[sRw], 4\n\t"
    "s_mul_i32 s88, s69, 2056\n\t"
    "s_add_u32 s88, s88, %[sLds]\n\t"
    "s_xor_b32 s69, s69, %[sRk]\n\t"
    "s_mul_i32 s96, s69, 16384\n\t"
    "s_xor_b32 s69, %[sRw], 5\n\t"
    "s_mul_i32 s89, s69, 2056\n\t"
    "s_add_u32 s89, s89, %[sLds]\n\t"
    "s_xor_b32 s69, s69, %[sRk]\n\t"
    "s_mul_i32 s97, s69, 16384\n\t"
    "s_xor_b32 s69, %[sRw], 6\n\t"
    "s_mul_i32 s90, s69, 2056\n\t"
    "s_add_u32 s90, s90, %[sLds]\n\t"
    "s_xor_b32 s69, s69, %[sRk]\n\t"
    "s_mul_i32 s98, s69, 16384\n\t"
    "s_xor_b32 s69, %[sRw], 7\n\t"
    "s_mul_i32 s91, s69, 2056\n\t"
    "s_add_u32 s91, s91, %[sLds]\n\t"
    "s_xor_b32 s69, s69, %[sRk]\n\t"
    "s_mul_i32 s99, s69, 16384\n\t"
    "v_lshrrev_b32 v30, 0, %[vL16]\n\t"
    "v_add_u32 v29, %[sRedW], v30\n\t"
    "v_bfe_u32 v24, %[vL16], 6, 2\n\t"
    "v_lshlrev_b32 v24, 4, v24\n\t"
    "v_xor_b32 v25, 16, v24\n\t"
    "v_sub_u32 v25, v25, v24\n\t"
    "v_add_u32 v25, v25, %[vRd]\n\t"
    "v_xor_b32 v26, 32, v24\n\t"
    "v_sub_u32 v26, v26, v24\n\t"
    "v_add_u32 v26, v26, %[vRd]\n\t"
    "v_xor_b32 v27, 48, v24\n\t"
    "v_sub_u32 v27, v27, v24\n\t"
    "v_add_u32 v27, v27, %[vRd]\n\t"
    "s_mov_b32 s62, 0xbfb8aa3b\n\t"
    "s_mov_b32 s63, 0xbfb8aa3b\n\t"
    "s_mov_b32 s64, 0xffff0000\n\t"
    "s_mov_b32 s60, 0\n\t"
    "s_add_u32 s66, %[sXo], 4096\n\t"
    "s_add_u32 s67, %[sXo], 32768\n\t"
    "s_add_u32 s68, %[sXo], 36864\n\t"
    "s_mov_b32 s56, 0xaaaaaaaa\n\t"
    "s_mov_b32 s58, 0xcccccccc\n\t"
    "s_mov_b32 s57, 0xaaaaaaaa\n\t"
    "s_mov_b32 s59, 0xcccccccc\n\t"
    "v_mov_b32 v23, 1\n\t"
    "v_bfe_u32 v31, %[vL16], 4, 3\n\t"
    "v_lshlrev_b32 v31, 2, v31\n\t"
    "s_waitcnt vmcnt(0)\n\t"
    "s_mov_b64 exec, 1\n\t"
    "buffer_atomic_add v23, off, %[srdWS], %[sSig]\n\t"
    "s_mov_b64 exec, -1\n\t"
    "s_barrier\n\t"
    "s_add_u32 %[sWk], %[sWk], 262144\n\t"
    "s_add_u32 m0, s84, 0\n\t"
    "s_add_u32 s69, %[sWk], s92\n\t"
    "buffer_load_dwordx4 %[vW0], %[srdW], s69 offen lds\n\t"
    "s_add_u32 m0, s85, 0\n\t"
    "s_add_u32 s69, %[sWk], s93\n\t"
    "buffer_load_dwordx4 %[vW0], %[srdW], s69 offen lds\n\t"
    "s_add_u32 m0, s86, 0\n\t"
    "s_add_u32 s69, %[sWk], s94\n\t"
    "buffer_load_dwordx4 %[vW0], %[srdW], s69 offen lds\n\t"
    "s_add_u32 m0, s87, 0\n\t"
    "s_add_u32 s69, %[sWk], s95\n\t"
    "buffer_load_dwordx4 %[vW0], %[srdW], s69 offen lds\n\t"
    "s_add_u32 m0, s88, 0\n\t"
    "s_add_u32 s69, %[sWk], s96\n\t"
    "buffer_load_dwordx4 %[vW0], %[srdW], s69 offen lds\n\t"
    "s_add_u32 m0, s89, 0\n\t"
    "s_add_u32 s69, %[sWk], s97\n\t"
    "buffer_load_dwordx4 %[vW0], %[srdW], s69 offen lds\n\t"
    "s_add_u32 m0, s90, 0\n\t"
    "s_add_u32 s69, %[sWk], s98\n\t"
    "buffer_load_dwordx4 %[vW0], %[srdW], s69 offen lds\n\t"
    "s_add_u32 m0, s91, 0\n\t"
    "s_add_u32 s69, %[sWk], s99\n\t"
    "buffer_load_dwordx4 %[vW0], %[srdW], s69 offen lds\n\t"
    "s_add_u32 %[sWk], %[sWk], 262144\n\t"
    "s_add_u32 m0, s84, 16448\n\t"
    "s_add_u32 s69, %[sWk], s92\n\t"
    "buffer_load_dwordx4 %[vW0], %[srdW], s69 offen lds\n\t"
    "s_add_u32 m0, s85, 16448\n\t"
    "s_add_u32 s69, %[sWk], s93\n\t"
    "buffer_load_dwordx4 %[vW0], %[srdW], s69 offen lds\n\t"
    "s_lshr_b32 s69, %[sXl], 13\n\t"
    "s_and_b32 s69, s69, 3\n\t"
    "s_mov_b64 exec, 0\n\t"
    "s_cmp_eq_u32 s69, 0\n\t"
    "s_cbranch_scc1 k_pgb0\n\t"
    "s_cmp_eq_u32 s69, 1\n\t"
    "s_cbranch_scc1 k_pgb1\n\t"
    "s_cmp_eq_u32 s69, 2\n\t"
    "s_cbranch_scc1 k_pgb2\n\t"
    "s_branch k_pgb3\n\t"
    "k_pgb_back:\n\t"
    "s_mov_b64 exec, -1\n\t"
    "s_add_u32 m0, s86, 16448\n\t"
    "s_add_u32 s69, %[sWk], s94\n\t"
    "buffer_load_dwordx4 %[vW0], %[srdW], s69 offen lds\n\t"
    "s_add_u32 m0, s87, 16448\n\t"
    "s_add_u32 s69, %[sWk], s95\n\t"
    "buffer_load_dwordx4 %[vW0], %[srdW], s69 offen lds\n\t"
    "buffer_load_dword v28, v31, %[srdWS], %[sRdy] offen sc1\n\t"
    "s_add_u32 m0, s88, 16448\n\t"
    "s_add_u32 s69, %[sWk], s96\n\t"
    "buffer_load_dwordx4 %[vW0], %[srdW], s69 offen lds\n\t"
    "s_add_u32 m0, s89, 16448\n\t"
    "s_add_u32 s69, %[sWk], s97\n\t"
    "buffer_load_dwordx4 %[vW0], %[srdW], s69 offen lds\n\t"
    "s_add_u32 m0, s90, 16448\n\t"
    "s_add_u32 s69, %[sWk], s98\n\t"
    "buffer_load_dwordx4 %[vW0], %[srdW], s69 offen lds\n\t"
    "s_add_u32 m0, s91, 16448\n\t"
    "s_add_u32 s69, %[sWk], s99\n\t"
    "buffer_load_dwordx4 %[vW0], %[srdW], s69 offen lds\n\t"
    "s_add_u32 %[sWk], %[sWk], 262144\n\t"
    "s_add_u32 m0, s84, 32896\n\t"
    "s_add_u32 s69, %[sWk], s92\n\t"
    "buffer_load_dwordx4 %[vW0], %[srdW], s69 offen lds\n\t"
    "s_add_u32 m0, s85, 32896\n\t"
    "s_add_u32 s69, %[sWk], s93\n\t"
    "buffer_load_dwordx4 %[vW0], %[srdW], s69 offen lds\n\t"
    "s_add_u32 m0, s86, 32896\n\t"
    "s_add_u32 s69, %[sWk], s94\n\t"
    "buffer_load_dwordx4 %[vW0], %[srdW], s69 offen lds\n\t"
    "s_waitcnt vmcnt(7)\n\t"
    "s_mov_b32 s65, 400000\n\t"
    "L_k2_spina_%=:\n\t"
    "v_cmp_gt_u32 vcc, 4, v28\n\t"
    "s_sub_u32 s65, s65, 1\n\t"
    "s_nop 2\n\t"
    "s_and_b32 s61, vcc_lo, 0xff\n\t"
    "s_cbranch_scc0 L_k2_goa_%=\n\t"
    "buffer_load_dword v28, v31, %[srdWS], %[sRdy] offen sc1\n\t"
    "s_cmp_gt_i32 s65, 0\n\t"
    "s_waitcnt vmcnt(0)\n\t"
    "s_cbranch_scc1 L_k2_spina_%=\n\t"
    "L_k2_goa_%=:\n\t"
    "s_add_u32 %[sA], %[sA], s55\n\t"
    "buffer_load_dwordx4 v[64:67], %[vA0], %[srdWS], %[sA] offen offset:0 sc1\n\t"
    "buffer_load_dwordx4 v[68:71], %[vA0], %[srdWS], %[sA] offen offset:1024 sc1\n\t"
    "buffer_load_dwordx4 v[72:75], %[vA1], %[srdWS], %[sA] offen offset:0 sc1\n\t"
    "buffer_load_dwordx4 v[76:79], %[vA1], %[srdWS], %[sA] offen offset:1024 sc1\n\t"
    "s_add_u32 %[sA], %[sA], %[sAi1]\n\t"
    "buffer_load_dwordx4 v[80:83], %[vA0], %[srdWS], %[sA] offen offset:0 sc1\n\t"
    "buffer_load_dwordx4 v[84:87], %[vA0], %[srdWS], %[sA] offen offset:1024 sc1\n\t"
    "buffer_load_dwordx4 v[88:91], %[vA1], %[srdWS], %[sA] offen offset:0 sc1\n\t"
    "buffer_load_dwordx4 v[92:95], %[vA1], %[srdWS], %[sA] offen offset:1024 sc1\n\t"
    "s_add_u32 %[sA], %[sA], s55\n\t"
    "buffer_load_dwordx4 v[96:99], %[vA0], %[srdWS], %[sA] offen offset:0 sc1\n\t"
    "buffer_load_dwordx4 v[100:103], %[vA0], %[srdWS], %[sA] offen offset:1024 sc1\n\t"
    "buffer_load_dwordx4 v[104:107], %[vA1], %[srdWS], %[sA] offen offset:0 sc1\n\t"
    "buffer_load_dwordx4 v[108:111], %[vA1], %[srdWS], %[sA] offen offset:1024 sc1\n\t"
    "s_add_u32 %[sA], %[sA], %[sAi1]\n\t"
    "buffer_load_dwordx4 v[112:115], %[vA0], %[srdWS], %[sA] offen offset:0 sc1\n\t"
    "buffer_load_dwordx4 v[116:119], %[vA0], %[srdWS], %[sA] offen offset:1024 sc1\n\t"
    "buffer_load_dwordx4 v[120:123], %[vA1], %[srdWS], %[sA] offen offset:0 sc1\n\t"
    "buffer_load_dwordx4 v[124:127], %[vA1], %[srdWS], %[sA] offen offset:1024 sc1\n\t"
    "s_add_u32 %[sA], %[sA], s55\n\t"
    "buffer_load_dwordx4 v[128:131], %[vA0], %[srdWS], %[sA] offen offset:0 sc1\n\t"
    "buffer_load_dwordx4 v[132:135], %[vA0], %[srdWS], %[sA] offen offset:1024 sc1\n\t"
    "buffer_load_dwordx4 v[136:139], %[vA1], %[srdWS], %[sA] offen offset:0 sc1\n\t"
    "s_waitcnt vmcnt(30)\n\t"
    "s_barrier\n\t"
    "ds_read_b64_tr_b16 v[32:33], %[vRd] offset:0\n\t"
    "ds_read_b64_tr_b16 v[34:35], %[vRd] offset:1024\n\t"
    "ds_read_b64_tr_b16 v[36:37], %[vRd] offset:8224\n\t"
    "ds_read_b64_tr_b16 v[38:39], %[vRd] offset:9248\n\t"
    "ds_read_b64_tr_b16 v[40:41], %[vRd] offset:8\n\t"
    "ds_read_b64_tr_b16 v[42:43], %[vRd] offset:1032\n\t"
    "ds_read_b64_tr_b16 v[44:45], %[vRd] offset:8232\n\t"
    "ds_read_b64_tr_b16 v[46:47], %[vRd] offset:9256\n\t"
    "ds_read_b64_tr_b16 v[48:49], v25 offset:0\n\t"
    "ds_read_b64_tr_b16 v[50:51], v25 offset:1024\n\t"
    "ds_read_b64_tr_b16 v[52:53], v25 offset:8224\n\t"
    "ds_read_b64_tr_b16 v[54:55], v25 offset:9248\n\t"
    "s_waitcnt vmcnt(11) lgkmcnt(8)\n\t"
    "v_mfma_f32_16x16x32_bf16 a[0:3], v[32:35], v[64:67], 0\n\t"
    "ds_read_b64_tr_b16 v[56:57], v25 offset:8\n\t"
    "v_mfma_f32_16x16x32_bf16 a[0:3], v[36:39], v[80:83], a[0:3]\n\t"
    "ds_read_b64_tr_b16 v[58:59], v25 offset:1032\n\t"
    "v_mfma_f32_16x16x32_bf16 a[4:7], v[36:39], v[84:87], 0\n\t"
    "v_mfma_f32_16x16x32_bf16 a[4:7], v[32:35], v[68:71], a[4:7]\n\t"
    "buffer_load_dwordx4 v[140:143], %[vA1], %[srdWS], %[sA] offen offset:1024 sc1\n\t"
    "v_mfma_f32_16x16x32_bf16 a[8:11], v[32:35], v[72:75], 0\n\t"
    "ds_read_b64_tr_b16 v[60:61], v25 offset:8232\n\t"
    "v_mfma_f32_16x16x32_bf16 a[8:11], v[36:39], v[88:91], a[8:11]\n\t"
    "ds_read_b64_tr_b16 v[62:63], v25 offset:9256\n\t"
    "v_mfma_f32_16x16x32_bf16 a[12:15], v[36:39], v[92:95], 0\n\t"
    "s_add_u32 m0, s87, 32896\n\t"
    "s_add_u32 s69, %[sWk], s95\n\t"
    "v_mfma_f32_16x16x32_bf16 a[12:15], v[32:35], v[76:79], a[12:15]\n\t"
    "buffer_load_dwordx4 %[vW0], %[srdW], s69 offen lds\n\t"
    "s_waitcnt lgkmcnt(8)\n\t"
    "v_mfma_f32_16x16x32_bf16 a[16:19], v[40:43], v[64:67], 0\n\t"
    "ds_read_b64_tr_b16 v[32:33], v26 offset:0\n\t"
    "v_mfma_f32_16x16x32_bf16 a[16:19], v[44:47], v[80:83], a[16:19]\n\t"
    "ds_read_b64_tr_b16 v[34:35], v26 offset:1024\n\t"
    "v_mfma_f32_16x16x32_bf16 a[20:23], v[44:47], v[84:87], 0\n\t"
    "s_add_u32 %[sA], %[sA], %[sAi1]\n\t"
    "v_mfma_f32_16x16x32_bf16 a[20:23], v[40:43], v[68:71], a[20:23]\n\t"
    "buffer_load_dwordx4 v[144:147], %[vA0], %[srdWS], %[sA] offen offset:0 sc1\n\t"
    "v_mfma_f32_16x16x32_bf16 a[24:27], v[40:43], v[72:75], 0\n\t"
    "ds_read_b64_tr_b16 v[36:37], v26 offset:8224\n\t"
    "v_mfma_f32_16x16x32_bf16 a[24:27], v[44:47], v[88:91], a[24:27]\n\t"
    "ds_read_b64_tr_b16 v[38:39], v26 offset:9248\n\t"
    "v_mfma_f32_16x16x32_bf16 a[28:31], v[44:47], v[92:95], 0\n\t"
    "s_add_u32 m0, s88, 32896\n\t"
    "s_add_u32 s69, %[sWk], s96\n\t"
    "v_mfma_f32_16x16x32_bf16 a[28:31], v[40:43], v[76:79], a[28:31]\n\t"
    "buffer_load_dwordx4 %[vW0], %[srdW], s69 offen lds\n\t"
    "s_waitcnt lgkmcnt(8)\n\t"
    "v_mfma_f32_16x16x32_bf16 a[32:35], v[48:51], v[64:67], 0\n\t"
    "ds_read_b64_tr_b16 v[40:41], v26 offset:8\n\t"
    "v_mfma_f32_16x16x32_bf16 a[32:35], v[52:55], v[80:83], a[32:35]\n\t"
    "ds_read_b64_tr_b16 v[42:43], v26 offset:1032\n\t"
    "v_mfma_f32_16x16x32_bf16 a[36:39], v[52:55], v[84:87], 0\n\t"
    "v_mfma_f32_16x16x32_bf16 a[36:39], v[48:51], v[68:71], a[36:39]\n\t"
    "buffer_load_dwordx4 v[148:151], %[vA0], %[srdWS], %[sA] offen offset:1024 sc1\n\t"
    "v_mfma_f32_16x16x32_bf16 a[40:43], v[48:51], v[72:75], 0\n\t"
    "ds_read_b64_tr_b16 v[44:45], v26 offset:8232\n\t"
    "v_mfma_f32_16x16x32_bf16 a[40:43], v[52:55], v[88:91], a[40:43]\n\t"
    "ds_read_b64_tr_b16 v[46:47], v26 offset:9256\n\t"
    "v_mfma_f32_16x16x32_bf16 a[44:47], v[52:55], v[92:95], 0\n\t"
    "s_add_u32 m0, s89, 32896\n\t"
    "s_add_u32 s69, %[sWk], s97\n\t"
    "v_mfma_f32_16x16x32_bf16 a[44:47], v[48:51], v[76:79], a[44:47]\n\t"
    "buffer_load_dwordx4 %[vW0], %[srdW], s69 offen lds\n\t"
    "s_waitcnt lgkmcnt(8)\n\t"
    "v_mfma_f32_16x16x32_bf16 a[48:51], v[56:59], v[64:67], 0\n\t"
    "ds_read_b64_tr_b16 v[48:49], v27 offset:0\n\t"
    "v_mfma_f32_16x16x32_bf16 a[48:51], v[60:63], v[80:83], a[48:51]\n\t"
    "ds_read_b64_tr_b16 v[50:51], v27 offset:1024\n\t"
    "v_mfma_f32_16x16x32_bf16 a[52:55], v[60:63], v[84:87], 0\n\t"
    "v_mfma_f32_16x16x32_bf16 a[52:55], v[56:59], v[68:71], a[52:55]\n\t"
    "buffer_load_dwordx4 v[152:155], %[vA1], %[srdWS], %[sA] offen offset:0 sc1\n\t"
    "v_mfma_f32_16x16x32_bf16 a[56:59], v[56:59], v[72:75], 0\n\t"
    "ds_read_b64_tr_b16 v[52:53], v27 offset:8224\n\t"
    "v_mfma_f32_16x16x32_bf16 a[56:59], v[60:63], v[88:91], a[56:59]\n\t"
    "ds_read_b64_tr_b16 v[54:55], v27 offset:9248\n\t"
    "v_mfma_f32_16x16x32_bf16 a[60:63], v[60:63], v[92:95], 0\n\t"
    "s_add_u32 m0, s90, 32896\n\t"
    "s_add_u32 s69, %[sWk], s98\n\t"
    "v_mfma_f32_16x16x32_bf16 a[60:63], v[56:59], v[76:79], a[60:63]\n\t"
    "buffer_load_dwordx4 %[vW0], %[srdW], s69 offen lds\n\t"
    "s_waitcnt lgkmcnt(8)\n\t"
    "v_mfma_f32_16x16x32_bf16 a[64:67], v[32:35], v[64:67], 0\n\t"
    "ds_read_b64_tr_b16 v[56:57], v27 offset:8\n\t"
    "v_mfma_f32_16x16x32_bf16 a[64:67], v[36:39], v[80:83], a[64:67]\n\t"
    "ds_read_b64_tr_b16 v[58:59], v27 offset:1032\n\t"
    "v_mfma_f32_16x16x32_bf16 a[68:71], v[36:39], v[84:87], 0\n\t"
    "v_mfma_f32_16x16x32_bf16 a[68:71], v[32:35], v[68:71], a[68:71]\n\t"
    "buffer_load_dwordx4 v[156:159], %[vA1], %[srdWS], %[sA] offen offset:1024 sc1\n\t"
    "v_mfma_f32_16x16x32_bf16 a[72:75], v[32:35], v[72:75], 0\n\t"
    "ds_read_b64_tr_b16 v[60:61], v27 offset:8232\n\t"
    "v_mfma_f32_16x16x32_bf16 a[72:75], v[36:39], v[88:91], a[72:75]\n\t"
    "ds_read_b64_tr_b16 v[62:63], v27 offset:9256\n\t"
    "v_mfma_f32_16x16x32_bf16 a[76:79], v[36:39], v[92:95], 0\n\t"
    "s_add_u32 m0, s91, 32896\n\t"
    "s_add_u32 s69, %[sWk], s99\n\t"
    "v_mfma_f32_16x16x32_bf16 a[76:79], v[32:35], v[76:79], a[76:79]\n\t"
    "buffer_load_dwordx4 %[vW0], %[srdW], s69 offen lds\n\t"
    "s_waitcnt vmcnt(32) lgkmcnt(8)\n\t"
    "s_barrier\n\t"
    "v_mfma_f32_16x16x32_bf16 a[80:83], v[40:43], v[64:67], 0\n\t"
    "ds_read_b64_tr_b16 v[32:33], %[vRd] offset:16448\n\t"
    "v_mfma_f32_16x16x32_bf16 a[80:83], v[44:47], v[80:83], a[80:83]\n\t"
    "ds_read_b64_tr_b16 v[34:35], %[vRd] offset:17472\n\t"
    "v_mfma_f32_16x16x32_bf16 a[84:87], v[44:47], v[84:87], 0\n\t"
    "s_add_u32 %[sA], %[sA], s55\n\t"
    "v_mfma_f32_16x16x32_bf16 a[84:87], v[40:43], v[68:71], a[84:87]\n\t"
    "buffer_load_dwordx4 v[160:163], %[vA0], %[srdWS], %[sA] offen offset:0 sc1\n\t"
    "v_mfma_f32_16x16x32_bf16 a[88:91], v[40:43], v[72:75], 0\n\t"
    "ds_read_b64_tr_b16 v[36:37], %[vRd] offset:24672\n\t"
    "v_mfma_f32_16x16x32_bf16 a[88:91], v[44:47], v[88:91], a[88:91]\n\t"
    "ds_read_b64_tr_b16 v[38:39], %[vRd] offset:25696\n\t"
    "v_mfma_f32_16x16x32_bf16 a[92:95], v[44:47], v[92:95], 0\n\t"
    "s_add_u32 %[sWk], %[sWk], 262144\n\t"
    "s_add_u32 m0, s84, 49344\n\t"
    "s_add_u32 s69, %[sWk], s92\n\t"
    "v_mfma_f32_16x16x32_bf16 a[92:95], v[40:43], v[76:79], a[92:95]\n\t"
    "buffer_load_dwordx4 %[vW0], %[srdW], s69 offen lds\n\t"
    "s_waitcnt lgkmcnt(8)\n\t"
    "v_mfma_f32_16x16x32_bf16 a[96:99], v[48:51], v[64:67], 0\n\t"
    "ds_read_b64_tr_b16 v[40:41], %[vRd] offset:16456\n\t"
    "v_mfma_f32_16x16x32_bf16 a[96:99], v[52:55], v[80:83], a[96:99]\n\t"
    "ds_read_b64_tr_b16 v[42:43], %[vRd] offset:17480\n\t"
    "v_mfma_f32_16x16x32_bf16 a[100:103], v[52:55], v[84:87], 0\n\t"
    "v_mfma_f32_16x16x32_bf16 a[100:103], v[48:51], v[68:71], a[100:103]\n\t"
    "buffer_load_dwordx4 v[164:167], %[vA0], %[srdWS], %[sA] offen offset:1024 sc1\n\t"
    "v_mfma_f32_16x16x32_bf16 a[104:107], v[48:51], v[72:75], 0\n\t"
    "ds_read_b64_tr_b16 v[44:45], %[vRd] offset:24680\n\t"
    "v_mfma_f32_16x16x32_bf16 a[104:107], v[52:55], v[88:91], a[104:107]\n\t"
    "ds_read_b64_tr_b16 v[46:47], %[vRd] offset:25704\n\t"
    "v_mfma_f32_16x16x32_bf16 a[108:111], v[52:55], v[92:95], 0\n\t"
    "s_add_u32 m0, s85, 49344\n\t"
    "s_add_u32 s69, %[sWk], s93\n\t"
    "v_mfma_f32_16x16x32_bf16 a[108:111], v[48:51], v[76:79], a[108:111]\n\t"
    "buffer_load_dwordx4 %[vW0], %[srdW], s69 offen lds\n\t"
    "s_waitcnt lgkmcnt(8)\n\t"
    "v_mfma_f32_16x16x32_bf16 a[112:115], v[56:59], v[64:67], 0\n\t"
    "ds_read_b64_tr_b16 v[48:49], v25 offset:16448\n\t"
    "v_mfma_f32_16x16x32_bf16 a[112:115], v[60:63], v[80:83], a[112:115]\n\t"
    "ds_read_b64_tr_b16 v[50:51], v25 offset:17472\n\t"
    "v_mfma_f32_16x16x32_bf16 a[116:119], v[60:63], v[84:87], 0\n\t"
    "v_mfma_f32_16x16x32_bf16 a[116:119], v[56:59], v[68:71], a[116:119]\n\t"
    "buffer_load_dwordx4 v[168:171], %[vA1], %[srdWS], %[sA] offen offset:0 sc1\n\t"
    "v_mfma_f32_16x16x32_bf16 a[120:123], v[56:59], v[72:75], 0\n\t"
    "ds_read_b64_tr_b16 v[52:53], v25 offset:24672\n\t"
    "v_mfma_f32_16x16x32_bf16 a[120:123], v[60:63], v[88:91], a[120:123]\n\t"
    "ds_read_b64_tr_b16 v[54:55], v25 offset:25696\n\t"
    "v_mfma_f32_16x16x32_bf16 a[124:127], v[60:63], v[92:95], 0\n\t"
    "s_add_u32 m0, s86, 49344\n\t"
    "s_add_u32 s69, %[sWk], s94\n\t"
    "v_mfma_f32_16x16x32_bf16 a[124:127], v[56:59], v[76:79], a[124:127]\n\t"
    "buffer_load_dwordx4 %[vW0], %[srdW], s69 offen lds\n\t"
    "s_waitcnt vmcnt(19) lgkmcnt(8)\n\t"
    "v_mfma_f32_16x16x32_bf16 a[0:3], v[32:35], v[96:99], a[0:3]\n\t"
    "ds_read_b64_tr_b16 v[56:57], v25 offset:16456\n\t"
    "v_mfma_f32_16x16x32_bf16 a[0:3], v[36:39], v[112:115], a[0:3]\n\t"
    "ds_read_b64_tr_b16 v[58:59], v25 offset:17480\n\t"
    "v_mfma_f32_16x16x32_bf16 a[4:7], v[36:39], v[116:119], a[4:7]\n\t"
    "v_mfma_f32_16x16x32_bf16 a[4:7], v[32:35], v[100:103], a[4:7]\n\t"
    "buffer_load_dwordx4 v[172:175], %[vA1], %[srdWS], %[sA] offen offset:1024 sc1\n\t"
    "v_mfma_f32_16x16x32_bf16 a[8:11], v[32:35], v[104:107], a[8:11]\n\t"
    "ds_read_b64_tr_b16 v[60:61], v25 offset:24680\n\t"
    "v_mfma_f32_16x16x32_bf16 a[8:11], v[36:39], v[120:123], a[8:11]\n\t"
    "ds_read_b64_tr_b16 v[62:63], v25 offset:25704\n\t"
    "v_mfma_f32_16x16x32_bf16 a[12:15], v[36:39], v[124:127], a[12:15]\n\t"
    "s_add_u32 m0, s87, 49344\n\t"
    "s_add_u32 s69, %[sWk], s95\n\t"
    "v_mfma_f32_16x16x32_bf16 a[12:15], v[32:35], v[108:111], a[12:15]\n\t"
    "buffer_load_dwordx4 %[vW0], %[srdW], s69 offen lds\n\t"
    "s_waitcnt lgkmcnt(8)\n\t"
    "v_mfma_f32_16x16x32_bf16 a[16:19], v[40:43], v[96:99], a[16:19]\n\t"
    "ds_read_b64_tr_b16 v[32:33], v26 offset:16448\n\t"
    "v_mfma_f32_16x16x32_bf16 a[16:19], v[44:47], v[112:115], a[16:19]\n\t"
    "ds_read_b64_tr_b16 v[34:35], v26 offset:17472\n\t"
    "v_mfma_f32_16x16x32_bf16 a[20:23], v[44:47], v[116:119], a[20:23]\n\t"
    "s_add_u32 %[sA], %[sA], %[sAi1]\n\t"
    "v_mfma_f32_16x16x32_bf16 a[20:23], v[40:43], v[100:103], a[20:23]\n\t"
    "buffer_load_dwordx4 v[176:179], %[vA0], %[srdWS], %[sA] offen offset:0 sc1\n\t"
    "v_mfma_f32_16x16x32_bf16 a[24:27], v[40:43], v[104:107], a[24:27]\n\t"
    "ds_read_b64_tr_b16 v[36:37], v26 offset:24672\n\t"
    "v_mfma_f32_16x16x32_bf16 a[24:27], v[44:47], v[120:123], a[24:27]\n\t"
    "ds_read_b64_tr_b16 v[38:39], v26 offset:25696\n\t"
    "k_pgb0: s_cbranch_execz k_pgb4\n\t"
    "v_mfma_f32_16x16x32_bf16 a[28:31], v[44:47], v[124:127], a[28:31]\n\t"
    "s_add_u32 m0, s88, 49344\n\t"
    "s_add_u32 s69, %[sWk], s96\n\t"
    "v_mfma_f32_16x16x32_bf16 a[28:31], v[40:43], v[108:111], a[28:31]\n\t"
    "buffer_load_dwordx4 %[vW0], %[srdW], s69 offen lds\n\t"
    "s_waitcnt lgkmcnt(8)\n\t"
    "v_mfma_f32_16x16x32_bf16 a[32:35], v[48:51], v[96:99], a[32:35]\n\t"
    "ds_read_b64_tr_b16 v[40:41], v26 offset:16456\n\t"
    "v_mfma_f32_16x16x32_bf16 a[32:35], v[52:55], v[112:115], a[32:35]\n\t"
    "ds_read_b64_tr_b16 v[42:43], v26 offset:17480\n\t"
    "v_mfma_f32_16x16x32_bf16 a[36:39], v[52:55], v[116:119], a[36:39]\n\t"
    "v_mfma_f32_16x16x32_bf16 a[36:39], v[48:51], v[100:103], a[36:39]\n\t"
    "buffer_load_dwordx4 v[180:183], %[vA0], %[srdWS], %[sA] offen offset:1024 sc1\n\t"
    "v_mfma_f32_16x16x32_bf16 a[40:43], v[48:51], v[104:107], a[40:43]\n\t"
    "ds_read_b64_tr_b16 v[44:45], v26 offset:24680\n\t"
    "v_mfma_f32_16x16x32_bf16 a[40:43], v[52:55], v[120:123], a[40:43]\n\t"
    "ds_read_b64_tr_b16 v[46:47], v26 offset:25704\n\t"
    "v_mfma_f32_16x16x32_bf16 a[44:47], v[52:55], v[124:127], a[44:47]\n\t"
    "s_add_u32 m0, s89, 49344\n\t"
    "s_add_u32 s69, %[sWk], s97\n\t"
    "v_mfma_f32_16x16x32_bf16 a[44:47], v[48:51], v[108:111], a[44:47]\n\t"
    "buffer_load_dwordx4 %[vW0], %[srdW], s69 offen lds\n\t"
    "s_waitcnt lgkmcnt(8)\n\t"
    "v_mfma_f32_16x16x32_bf16 a[48:51], v[56:59], v[96:99], a[48:51]\n\t"
    "ds_read_b64_tr_b16 v[48:49], v27 offset:16448\n\t"
    "v_mfma_f32_16x16x32_bf16 a[48:51], v[60:63], v[112:115], a[48:51]\n\t"
    "ds_read_b64_tr_b16 v[50:51], v27 offset:17472\n\t"
    "v_mfma_f32_16x16x32_bf16 a[52:55], v[60:63], v[116:119], a[52:55]\n\t"
    "v_mfma_f32_16x16x32_bf16 a[52:55], v[56:59], v[100:103], a[52:55]\n\t"
    "buffer_load_dwordx4 v[184:187], %[vA1], %[srdWS], %[sA] offen offset:0 sc1\n\t"
    "v_mfma_f32_16x16x32_bf16 a[56:59], v[56:59], v[104:107], a[56:59]\n\t"
    "ds_read_b64_tr_b16 v[52:53], v27 offset:24672\n\t"
    "v_mfma_f32_16x16x32_bf16 a[56:59], v[60:63], v[120:123], a[56:59]\n\t"
    "ds_read_b64_tr_b16 v[54:55], v27 offset:25696\n\t"
    "v_mfma_f32_16x16x32_bf16 a[60:63], v[60:63], v[124:127], a[60:63]\n\t"
    "s_add_u32 m0, s90, 49344\n\t"
    "s_add_u32 s69, %[sWk], s98\n\t"
    "v_mfma_f32_16x16x32_bf16 a[60:63], v[56:59], v[108:111], a[60:63]\n\t"
    "buffer_load_dwordx4 %[vW0], %[srdW], s69 offen lds\n\t"
    "s_waitcnt lgkmcnt(8)\n\t"
    "v_mfma_f32_16x16x32_bf16 a[64:67], v[32:35], v[96:99], a[64:67]\n\t"
    "ds_read_b64_tr_b16 v[56:57], v27 offset:16456\n\t"
    "v_mfma_f32_16x16x32_bf16 a[64:67], v[36:39], v[112:115], a[64:67]\n\t"
    "ds_read_b64_tr_b16 v[58:59], v27 offset:17480\n\t"
    "v_mfma_f32_16x16x32_bf16 a[68:71], v[36:39], v[116:119], a[68:71]\n\t"
    "v_mfma_f32_16x16x32_bf16 a[68:71], v[32:35], v[100:103], a[68:71]\n\t"
    "buffer_load_dwordx4 v[188:191], %[vA1], %[srdWS], %[sA] offen offset:1024 sc1\n\t"
    "v_mfma_f32_16x16x32_bf16 a[72:75], v[32:35], v[104:107], a[72:75]\n\t"
    "ds_read_b64_tr_b16 v[60:61], v27 offset:24680\n\t"
    "v_mfma_f32_16x16x32_bf16 a[72:75], v[36:39], v[120:123], a[72:75]\n\t"
    "ds_read_b64_tr_b16 v[62:63], v27 offset:25704\n\t"
    "v_mfma_f32_16x16x32_bf16 a[76:79], v[36:39], v[124:127], a[76:79]\n\t"
    "s_add_u32 m0, s91, 49344\n\t"
    "s_add_u32 s69, %[sWk], s99\n\t"
    "v_mfma_f32_16x16x32_bf16 a[76:79], v[32:35], v[108:111], a[76:79]\n\t"
    "buffer_load_dwordx4 %[vW0], %[srdW], s69 offen lds\n\t"
    "s_waitcnt vmcnt(16) lgkmcnt(8)\n\t"
    "s_barrier\n\t"
    "v_mfma_f32_16x16x32_bf16 a[80:83], v[40:43], v[96:99], a[80:83]\n\t"
    "ds_read_b64_tr_b16 v[32:33], %[vRd] offset:32896\n\t"
    "v_mfma_f32_16x16x32_bf16 a[80:83], v[44:47], v[112:115], a[80:83]\n\t"
    "ds_read_b64_tr_b16 v[34:35], %[vRd] offset:33920\n\t"
    "v_mfma_f32_16x16x32_bf16 a[84:87], v[44:47], v[116:119], a[84:87]\n\t"
    "s_add_u32 %[sA], %[sA], s55\n\t"
    "v_mfma_f32_16x16x32_bf16 a[84:87], v[40:43], v[100:103], a[84:87]\n\t"
    "buffer_load_dwordx4 v[64:67], %[vA0], %[srdWS], %[sA] offen offset:0 sc1\n\t"
    "v_mfma_f32_16x16x32_bf16 a[88:91], v[40:43], v[104:107], a[88:91]\n\t"
    "ds_read_b64_tr_b16 v[36:37], %[vRd] offset:41120\n\t"
    "v_mfma_f32_16x16x32_bf16 a[88:91], v[44:47], v[120:123], a[88:91]\n\t"
    "ds_read_b64_tr_b16 v[38:39], %[vRd] offset:42144\n\t"
    "v_mfma_f32_16x16x32_bf16 a[92:95], v[44:47], v[124:127], a[92:95]\n\t"
    "s_add_u32 %[sWk], %[sWk], 262144\n\t"
    "s_add_u32 m0, s84, 0\n\t"
    "s_add_u32 s69, %[sWk], s92\n\t"
    "v_mfma_f32_16x16x32_bf16 a[92:95], v[40:43], v[108:111], a[92:95]\n\t"
    "buffer_load_dwordx4 %[vW0], %[srdW], s69 offen lds\n\t"
    "s_waitcnt lgkmcnt(8)\n\t"
    "v_mfma_f32_16x16x32_bf16 a[96:99], v[48:51], v[96:99], a[96:99]\n\t"
    "ds_read_b64_tr_b16 v[40:41], %[vRd] offset:32904\n\t"
    "v_mfma_f32_16x16x32_bf16 a[96:99], v[52:55], v[112:115], a[96:99]\n\t"
    "ds_read_b64_tr_b16 v[42:43], %[vRd] offset:33928\n\t"
    "v_mfma_f32_16x16x32_bf16 a[100:103], v[52:55], v[116:119], a[100:103]\n\t"
    "v_mfma_f32_16x16x32_bf16 a[100:103], v[48:51], v[100:103], a[100:103]\n\t"
    "buffer_load_dwordx4 v[68:71], %[vA0], %[srdWS], %[sA] offen offset:1024 sc1\n\t"
    "v_mfma_f32_16x16x32_bf16 a[104:107], v[48:51], v[104:107], a[104:107]\n\t"
    "ds_read_b64_tr_b16 v[44:45], %[vRd] offset:41128\n\t"
    "v_mfma_f32_16x16x32_bf16 a[104:107], v[52:55], v[120:123], a[104:107]\n\t"
    "ds_read_b64_tr_b16 v[46:47], %[vRd] offset:42152\n\t"
    "v_mfma_f32_16x16x32_bf16 a[108:111], v[52:55], v[124:127], a[108:111]\n\t"
    "s_add_u32 m0, s85, 0\n\t"
    "s_add_u32 s69, %[sWk], s93\n\t"
    "v_mfma_f32_16x16x32_bf16 a[108:111], v[48:51], v[108:111], a[108:111]\n\t"
    "buffer_load_dwordx4 %[vW0], %[srdW], s69 offen lds\n\t"
    "s_waitcnt lgkmcnt(8)\n\t"
    "v_mfma_f32_16x16x32_bf16 a[112:115], v[56:59], v[96:99], a[112:115]\n\t"
    "ds_read_b64_tr_b16 v[48:49], v25 offset:32896\n\t"
    "v_mfma_f32_16x16x32_bf16 a[112:115], v[60:63], v[112:115], a[112:115]\n\t"
    "ds_read_b64_tr_b16 v[50:51], v25 offset:33920\n\t"
    "v_mfma_f32_16x16x32_bf16 a[116:119], v[60:63], v[116:119], a[116:119]\n\t"
    "v_mfma_f32_16x16x32_bf16 a[116:119], v[56:59], v[100:103], a[116:119]\n\t"
    "buffer_load_dwordx4 v[72:75], %[vA1], %[srdWS], %[sA] offen offset:0 sc1\n\t"
    "v_mfma_f32_16x16x32_bf16 a[120:123], v[56:59], v[104:107], a[120:123]\n\t"
    "ds_read_b64_tr_b16 v[52:53], v25 offset:41120\n\t"
    "v_mfma_f32_16x16x32_bf16 a[120:123], v[60:63], v[120:123], a[120:123]\n\t"
    "ds_read_b64_tr_b16 v[54:55], v25 offset:42144\n\t"
    "v_mfma_f32_16x16x32_bf16 a[124:127], v[60:63], v[124:127], a[124:127]\n\t"
    "s_add_u32 m0, s86, 0\n\t"
    "s_add_u32 s69, %[sWk], s94\n\t"
    "v_mfma_f32_16x16x32_bf16 a[124:127], v[56:59], v[108:111], a[124:127]\n\t"
    "buffer_load_dwordx4 %[vW0], %[srdW], s69 offen lds\n\t"
    "s_waitcnt vmcnt(23) lgkmcnt(8)\n\t"
    "v_mfma_f32_16x16x32_bf16 a[0:3], v[32:35], v[128:131], a[0:3]\n\t"
    "ds_read_b64_tr_b16 v[56:57], v25 offset:32904\n\t"
    "v_mfma_f32_16x16x32_bf16 a[0:3], v[36:39], v[144:147], a[0:3]\n\t"
    "ds_read_b64_tr_b16 v[58:59], v25 offset:33928\n\t"
    "v_mfma_f32_16x16x32_bf16 a[4:7], v[36:39], v[148:151], a[4:7]\n\t"
    "v_mfma_f32_16x16x32_bf16 a[4:7], v[32:35], v[132:135], a[4:7]\n\t"
    "buffer_load_dwordx4 v[76:79], %[vA1], %[srdWS], %[sA] offen offset:1024 sc1\n\t"
    "v_mfma_f32_16x16x32_bf16 a[8:11], v[32:35], v[136:139], a[8:11]\n\t"
    "ds_read_b64_tr_b16 v[60:61], v25 offset:41128\n\t"
    "v_mfma_f32_16x16x32_bf16 a[8:11], v[36:39], v[152:155], a[8:11]\n\t"
    "ds_read_b64_tr_b16 v[62:63], v25 offset:42152\n\t"
    "v_mfma_f32_16x16x32_bf16 a[12:15], v[36:39], v[156:159], a[12:15]\n\t"
    "s_add_u32 m0, s87, 0\n\t"
    "s_add_u32 s69, %[sWk], s95\n\t"
    "v_mfma_f32_16x16x32_bf16 a[12:15], v[32:35], v[140:143], a[12:15]\n\t"
    "buffer_load_dwordx4 %[vW0], %[srdW], s69 offen lds\n\t"
    "s_waitcnt lgkmcnt(8)\n\t"
    "v_mfma_f32_16x16x32_bf16 a[16:19], v[40:43], v[128:131], a[16:19]\n\t"
    "ds_read_b64_tr_b16 v[32:33], v26 offset:32896\n\t"
    "v_mfma_f32_16x16x32_bf16 a[16:19], v[44:47], v[144:147], a[16:19]\n\t"
    "ds_read_b64_tr_b16 v[34:35], v26 offset:33920\n\t"
    "v_mfma_f32_16x16x32_bf16 a[20:23], v[44:47], v[148:151], a[20:23]\n\t"
    "s_add_u32 %[sA], %[sA], %[sAi1]\n\t"
    "v_mfma_f32_16x16x32_bf16 a[20:23], v[40:43], v[132:135], a[20:23]\n\t"
    "buffer_load_dwordx4 v[80:83], %[vA0], %[srdWS], %[sA] offen offset:0 sc1\n\t"
    "v_mfma_f32_16x16x32_bf16 a[24:27], v[40:43], v[136:139], a[24:27]\n\t"
    "ds_read_b64_tr_b16 v[36:37], v26 offset:41120\n\t"
    "v_mfma_f32_16x16x32_bf16 a[24:27], v[44:47], v[152:155], a[24:27]\n\t"
    "ds_read_b64_tr_b16 v[38:39], v26 offset:42144\n\t"
    "v_mfma_f32_16x16x32_bf16 a[28:31], v[44:47], v[156:159], a[28:31]\n\t"
    "s_add_u32 m0, s88, 0\n\t"
    "s_add_u32 s69, %[sWk], s96\n\t"
    "v_mfma_f32_16x16x32_bf16 a[28:31], v[40:43], v[140:143], a[28:31]\n\t"
    "buffer_load_dwordx4 %[vW0], %[srdW], s69 offen lds\n\t"
    "s_waitcnt lgkmcnt(8)\n\t"
    "v_mfma_f32_16x16x32_bf16 a[32:35], v[48:51], v[128:131], a[32:35]\n\t"
    "ds_read_b64_tr_b16 v[40:41], v26 offset:32904\n\t"
    "v_mfma_f32_16x16x32_bf16 a[32:35], v[52:55], v[144:147], a[32:35]\n\t"
    "ds_read_b64_tr_b16 v[42:43], v26 offset:33928\n\t"
    "v_mfma_f32_16x16x32_bf16 a[36:39], v[52:55], v[148:151], a[36:39]\n\t"
    "v_mfma_f32_16x16x32_bf16 a[36:39], v[48:51], v[132:135], a[36:39]\n\t"
    "buffer_load_dwordx4 v[84:87], %[vA0], %[srdWS], %[sA] offen offset:1024 sc1\n\t"
    "v_mfma_f32_16x16x32_bf16 a[40:43], v[48:51], v[136:139], a[40:43]\n\t"
    "ds_read_b64_tr_b16 v[44:45], v26 offset:41128\n\t"
    "v_mfma_f32_16x16x32_bf16 a[40:43], v[52:55], v[152:155], a[40:43]\n\t"
    "ds_read_b64_tr_b16 v[46:47], v26 offset:42152\n\t"
    "v_mfma_f32_16x16x32_bf16 a[44:47], v[52:55], v[156:159], a[44:47]\n\t"
    "s_add_u32 m0, s89, 0\n\t"
    "s_add_u32 s69, %[sWk], s97\n\t"
    "v_mfma_f32_16x16x32_bf16 a[44:47], v[48:51], v[140:143], a[44:47]\n\t"
    "buffer_load_dwordx4 %[vW0], %[srdW], s69 offen lds\n\t"
    "s_waitcnt lgkmcnt(8)\n\t"
    "v_mfma_f32_16x16x32_bf16 a[48:51], v[56:59], v[128:131], a[48:51]\n\t"
    "ds_read_b64_tr_b16 v[48:49], v27 offset:32896\n\t"
    "v_mfma_f32_16x16x32_bf16 a[48:51], v[60:63], v[144:147], a[48:51]\n\t"
    "ds_read_b64_tr_b16 v[50:51], v27 offset:33920\n\t"
    "v_mfma_f32_16x16x32_bf16 a[52:55], v[60:63], v[148:151], a[52:55]\n\t"
    "v_mfma_f32_16x16x32_bf16 a[52:55], v[56:59], v[132:135], a[52:55]\n\t"
    "buffer_load_dwordx4 v[88:91], %[vA1], %[srdWS], %[sA] offen offset:0 sc1\n\t"
    "v_mfma_f32_16x16x32_bf16 a[56:59], v[56:59], v[136:139], a[56:59]\n\t"
    "ds_read_b64_tr_b16 v[52:53], v27 offset:41120\n\t"
    "v_mfma_f32_16x16x32_bf16 a[56:59], v[60:63], v[152:155], a[56:59]\n\t"
    "ds_read_b64_tr_b16 v[54:55], v27 offset:42144\n\t"
    "v_mfma_f32_16x16x32_bf16 a[60:63], v[60:63], v[156:159], a[60:63]\n\t"
    "s_add_u32 m0, s90, 0\n\t"
    "s_add_u32 s69, %[sWk], s98\n\t"
    "v_mfma_f32_16x16x32_bf16 a[60:63], v[56:59], v[140:143], a[60:63]\n\t"
    "buffer_load_dwordx4 %[vW0], %[srdW], s69 offen lds\n\t"
    "s_waitcnt lgkmcnt(8)\n\t"
    "v_mfma_f32_16x16x32_bf16 a[64:67], v[32:35], v[128:131], a[64:67]\n\t"
    "ds_read_b64_tr_b16 v[56:57], v27 offset:32904\n\t"
    "v_mfma_f32_16x16x32_bf16 a[64:67], v[36:39], v[144:147], a[64:67]\n\t"
    "ds_read_b64_tr_b16 v[58:59], v27 offset:33928\n\t"
    "v_mfma_f32_16x16x32_bf16 a[68:71], v[36:39], v[148:151], a[68:71]\n\t"
    "v_mfma_f32_16x16x32_bf16 a[68:71], v[32:35], v[132:135], a[68:71]\n\t"
    "buffer_load_dwordx4 v[92:95], %[vA1], %[srdWS], %[sA] offen offset:1024 sc1\n\t"
    "v_mfma_f32_16x16x32_bf16 a[72:75], v[32:35], v[136:139], a[72:75]\n\t"
    "ds_read_b64_tr_b16 v[60:61], v27 offset:41128\n\t"
    "v_mfma_f32_16x16x32_bf16 a[72:75], v[36:39], v[152:155], a[72:75]\n\t"
    "ds_read_b64_tr_b16 v[62:63], v27 offset:42152\n\t"
    "v_mfma_f32_16x16x32_bf16 a[76:79], v[36:39], v[156:159], a[76:79]\n\t"
    "s_add_u32 m0, s91, 0\n\t"
    "s_add_u32 s69, %[sWk], s99\n\t"
    "v_mfma_f32_16x16x32_bf16 a[76:79], v[32:35], v[140:143], a[76:79]\n\t"
    "buffer_load_dwordx4 %[vW0], %[srdW], s69 offen lds\n\t"
    "s_waitcnt vmcnt(16) lgkmcnt(8)\n\t"
    "s_barrier\n\t"
    "v_mfma_f32_16x16x32_bf16 a[80:83], v[40:43], v[128:131], a[80:83]\n\t"
    "ds_read_b64_tr_b16 v[32:33], %[vRd] offset:49344\n\t"
    "v_mfma_f32_16x16x32_bf16 a[80:83], v[44:47], v[144:147], a[80:83]\n\t"
    "ds_read_b64_tr_b16 v[34:35], %[vRd] offset:50368\n\t"
    "v_mfma_f32_16x16x32_bf16 a[84:87], v[44:47], v[148:151], a[84:87]\n\t"
    "s_add_u32 %[sA], %[sA], s55\n\t"
    "v_mfma_f32_16x16x32_bf16 a[84:87], v[40:43], v[132:135], a[84:87]\n\t"
    "buffer_load_dwordx4 v[96:99], %[vA0], %[srdWS], %[sA] offen offset:0 sc1\n\t"
    "v_mfma_f32_16x16x32_bf16 a[88:91], v[40:43], v[136:139], a[88:91]\n\t"
    "ds_read_b64_tr_b16 v[36:37], %[vRd] offset:57568\n\t"
    "v_mfma_f32_16x16x32_bf16 a[88:91], v[44:47], v[152:155], a[88:91]\n\t"
    "ds_read_b64_tr_b16 v[38:39], %[vRd] offset:58592\n\t"
    "v_mfma_f32_16x16x32_bf16 a[92:95], v[44:47], v[156:159], a[92:95]\n\t"
    "s_add_u32 %[sWk], %[sWk], 262144\n\t"
    "s_add_u32 m0, s84, 16448\n\t"
    "s_add_u32 s69, %[sWk], s92\n\t"
    "v_mfma_f32_16x16x32_bf16 a[92:95], v[40:43], v[140:143], a[92:95]\n\t"
    "buffer_load_dwordx4 %[vW0], %[srdW], s69 offen lds\n\t"
    "s_waitcnt lgkmcnt(8)\n\t"
    "v_mfma_f32_16x16x32_bf16 a[96:99], v[48:51], v[128:131], a[96:99]\n\t"
    "ds_read_b64_tr_b16 v[40:41], %[vRd] offset:49352\n\t"
    "v_mfma_f32_16x16x32_bf16 a[96:99], v[52:55], v[144:147], a[96:99]\n\t"
    "ds_read_b64_tr_b16 v[42:43], %[vRd] offset:50376\n\t"
    "v_mfma_f32_16x16x32_bf16 a[100:103], v[52:55], v[148:151], a[100:103]\n\t"
    "v_mfma_f32_16x16x32_bf16 a[100:103], v[48:51], v[132:135], a[100:103]\n\t"
    "buffer_load_dwordx4 v[100:103], %[vA0], %[srdWS], %[sA] offen offset:1024 sc1\n\t"
    "v_mfma_f32_16x16x32_bf16 a[104:107], v[48:51], v[136:139], a[104:107]\n\t"
    "ds_read_b64_tr_b16 v[44:45], %[vRd] offset:57576\n\t"
    "v_mfma_f32_16x16x32_bf16 a[104:107], v[52:55], v[152:155], a[104:107]\n\t"
    "ds_read_b64_tr_b16 v[46:47], %[vRd] offset:58600\n\t"
    "v_mfma_f32_16x16x32_bf16 a[108:111], v[52:55], v[156:159], a[108:111]\n\t"
    "s_add_u32 m0, s85, 16448\n\t"
    "s_add_u32 s69, %[sWk], s93\n\t"
    "v_mfma_f32_16x16x32_bf16 a[108:111], v[48:51], v[140:143], a[108:111]\n\t"
    "buffer_load_dwordx4 %[vW0], %[srdW], s69 offen lds\n\t"
    "s_waitcnt lgkmcnt(8)\n\t"
    "v_mfma_f32_16x16x32_bf16 a[112:115], v[56:59], v[128:131], a[112:115]\n\t"
    "ds_read_b64_tr_b16 v[48:49], v25 offset:49344\n\t"
    "v_mfma_f32_16x16x32_bf16 a[112:115], v[60:63], v[144:147], a[112:115]\n\t"
    "ds_read_b64_tr_b16 v[50:51], v25 offset:50368\n\t"
    "v_mfma_f32_16x16x32_bf16 a[116:119], v[60:63], v[148:151], a[116:119]\n\t"
    "v_mfma_f32_16x16x32_bf16 a[116:119], v[56:59], v[132:135], a[116:119]\n\t"
    "buffer_load_dwordx4 v[104:107], %[vA1], %[srdWS], %[sA] offen offset:0 sc1\n\t"
    "v_mfma_f32_16x16x32_bf16 a[120:123], v[56:59], v[136:139], a[120:123]\n\t"
    "ds_read_b64_tr_b16 v[52:53], v25 offset:57568\n\t"
    "v_mfma_f32_16x16x32_bf16 a[120:123], v[60:63], v[152:155], a[120:123]\n\t"
    "ds_read_b64_tr_b16 v[54:55], v25 offset:58592\n\t"
    "v_mfma_f32_16x16x32_bf16 a[124:127], v[60:63], v[156:159], a[124:127]\n\t"
    "s_add_u32 m0, s86, 16448\n\t"
    "s_add_u32 s69, %[sWk], s94\n\t"
    "v_mfma_f32_16x16x32_bf16 a[124:127], v[56:59], v[140:143], a[124:127]\n\t"
    "buffer_load_dwordx4 %[vW0], %[srdW], s69 offen lds\n\t"
    "s_waitcnt vmcnt(23) lgkmcnt(8)\n\t"
    "v_mfma_f32_16x16x32_bf16 a[0:3], v[32:35], v[160:163], a[0:3]\n\t"
    "ds_read_b64_tr_b16 v[56:57], v25 offset:49352\n\t"
    "v_mfma_f32_16x16x32_bf16 a[0:3], v[36:39], v[176:179], a[0:3]\n\t"
    "ds_read_b64_tr_b16 v[58:59], v25 offset:50376\n\t"
    "v_mfma_f32_16x16x32_bf16 a[4:7], v[36:39], v[180:183], a[4:7]\n\t"
    "v_mfma_f32_16x16x32_bf16 a[4:7], v[32:35], v[164:167], a[4:7]\n\t"
    "buffer_load_dwordx4 v[108:111], %[vA1], %[srdWS], %[sA] offen offset:1024 sc1\n\t"
    "v_mfma_f32_16x16x32_bf16 a[8:11], v[32:35], v[168:171], a[8:11]\n\t"
    "ds_read_b64_tr_b16 v[60:61], v25 offset:57576\n\t"
    "v_mfma_f32_16x16x32_bf16 a[8:11], v[36:39], v[184:187], a[8:11]\n\t"
    "ds_read_b64_tr_b16 v[62:63], v25 offset:58600\n\t"
    "v_mfma_f32_16x16x32_bf16 a[12:15], v[36:39], v[188:191], a[12:15]\n\t"
    "s_add_u32 m0, s87, 16448\n\t"
    "s_add_u32 s69, %[sWk], s95\n\t"
    "v_mfma_f32_16x16x32_bf16 a[12:15], v[32:35], v[172:175], a[12:15]\n\t"
    "buffer_load_dwordx4 %[vW0], %[srdW], s69 offen lds\n\t"
    "s_waitcnt lgkmcnt(8)\n\t"
    "v_mfma_f32_16x16x32_bf16 a[16:19], v[40:43], v[160:163], a[16:19]\n\t"
    "ds_read_b64_tr_b16 v[32:33], v26 offset:49344\n\t"
    "v_mfma_f32_16x16x32_bf16 a[16:19], v[44:47], v[176:179], a[16:19]\n\t"
    "ds_read_b64_tr_b16 v[34:35], v26 offset:50368\n\t"
    "v_mfma_f32_16x16x32_bf16 a[20:23], v[44:47], v[180:183], a[20:23]\n\t"
    "s_add_u32 %[sA], %[sA], %[sAi1]\n\t"
    "v_mfma_f32_16x16x32_bf16 a[20:23], v[40:43], v[164:167], a[20:23]\n\t"
    "buffer_load_dwordx4 v[112:115], %[vA0], %[srdWS], %[sA] offen offset:0 sc1\n\t"
    "v_mfma_f32_16x16x32_bf16 a[24:27], v[40:43], v[168:171], a[24:27]\n\t"
    "ds_read_b64_tr_b16 v[36:37], v26 offset:57568\n\t"
    "v_mfma_f32_16x16x32_bf16 a[24:27], v[44:47], v[184:187], a[24:27]\n\t"
    "ds_read_b64_tr_b16 v[38:39], v26 offset:58592\n\t"
    "v_mfma_f32_16x16x32_bf16 a[28:31], v[44:47], v[188:191], a[28:31]\n\t"
    "s_add_u32 m0, s88, 16448\n\t"
    "s_add_u32 s69, %[sWk], s96\n\t"
    "v_mfma_f32_16x16x32_bf16 a[28:31], v[40:43], v[172:175], a[28:31]\n\t"
    "buffer_load_dwordx4 %[vW0], %[srdW], s69 offen lds\n\t"
    "s_waitcnt lgkmcnt(8)\n\t"
    "v_mfma_f32_16x16x32_bf16 a[32:35], v[48:51], v[160:163], a[32:35]\n\t"
    "ds_read_b64_tr_b16 v[40:41], v26 offset:49352\n\t"
    "v_mfma_f32_16x16x32_bf16 a[32:35], v[52:55], v[176:179], a[32:35]\n\t"
    "ds_read_b64_tr_b16 v[42:43], v26 offset:50376\n\t"
    "v_mfma_f32_16x16x32_bf16 a[36:39], v[52:55], v[180:183], a[36:39]\n\t"
    "v_mfma_f32_16x16x32_bf16 a[36:39], v[48:51], v[164:167], a[36:39]\n\t"
    "buffer_load_dwordx4 v[116:119], %[vA0], %[srdWS], %[sA] offen offset:1024 sc1\n\t"
    "v_mfma_f32_16x16x32_bf16 a[40:43], v[48:51], v[168:171], a[40:43]\n\t"
    "ds_read_b64_tr_b16 v[44:45], v26 offset:57576\n\t"
    "v_mfma_f32_16x16x32_bf16 a[40:43], v[52:55], v[184:187], a[40:43]\n\t"
    "ds_read_b64_tr_b16 v[46:47], v26 offset:58600\n\t"
    "v_mfma_f32_16x16x32_bf16 a[44:47], v[52:55], v[188:191], a[44:47]\n\t"
    "s_add_u32 m0, s89, 16448\n\t"
    "s_add_u32 s69, %[sWk], s97\n\t"
    "v_mfma_f32_16x16x32_bf16 a[44:47], v[48:51], v[172:175], a[44:47]\n\t"
    "buffer_load_dwordx4 %[vW0], %[srdW], s69 offen lds\n\t"
    "s_waitcnt lgkmcnt(8)\n\t"
    "v_mfma_f32_16x16x32_bf16 a[48:51], v[56:59], v[160:163], a[48:51]\n\t"
    "ds_read_b64_tr_b16 v[48:49], v27 offset:49344\n\t"
    "v_mfma_f32_16x16x32_bf16 a[48:51], v[60:63], v[176:179], a[48:51]\n\t"
    "ds_read_b64_tr_b16 v[50:51], v27 offset:50368\n\t"
    "v_mfma_f32_16x16x32_bf16 a[52:55], v[60:63], v[180:183], a[52:55]\n\t"
    "v_mfma_f32_16x16x32_bf16 a[52:55], v[56:59], v[164:167], a[52:55]\n\t"
    "buffer_load_dwordx4 v[120:123], %[vA1], %[srdWS], %[sA] offen offset:0 sc1\n\t"
    "v_mfma_f32_16x16x32_bf16 a[56:59], v[56:59], v[168:171], a[56:59]\n\t"
    "ds_read_b64_tr_b16 v[52:53], v27 offset:57568\n\t"
    "v_mfma_f32_16x16x32_bf16 a[56:59], v[60:63], v[184:187], a[56:59]\n\t"
    "ds_read_b64_tr_b16 v[54:55], v27 offset:58592\n\t"
    "v_mfma_f32_16x16x32_bf16 a[60:63], v[60:63], v[188:191], a[60:63]\n\t"
    "s_add_u32 m0, s90, 16448\n\t"
    "s_add_u32 s69, %[sWk], s98\n\t"
    "v_mfma_f32_16x16x32_bf16 a[60:63], v[56:59], v[172:175], a[60:63]\n\t"
    "buffer_load_dwordx4 %[vW0], %[srdW], s69 offen lds\n\t"
    "s_waitcnt lgkmcnt(8)\n\t"
    "v_mfma_f32_16x16x32_bf16 a[64:67], v[32:35], v[160:163], a[64:67]\n\t"
    "ds_read_b64_tr_b16 v[56:57], v27 offset:49352\n\t"
    "v_mfma_f32_16x16x32_bf16 a[64:67], v[36:39], v[176:179], a[64:67]\n\t"
    "ds_read_b64_tr_b16 v[58:59], v27 offset:50376\n\t"
    "v_mfma_f32_16x16x32_bf16 a[68:71], v[36:39], v[180:183], a[68:71]\n\t"
    "v_mfma_f32_16x16x32_bf16 a[68:71], v[32:35], v[164:167], a[68:71]\n\t"
    "buffer_load_dwordx4 v[124:127], %[vA1], %[srdWS], %[sA] offen offset:1024 sc1\n\t"
    "v_mfma_f32_16x16x32_bf16 a[72:75], v[32:35], v[168:171], a[72:75]\n\t"
    "ds_read_b64_tr_b16 v[60:61], v27 offset:57576\n\t"
    "v_mfma_f32_16x16x32_bf16 a[72:75], v[36:39], v[184:187], a[72:75]\n\t"
    "ds_read_b64_tr_b16 v[62:63], v27 offset:58600\n\t"
    "v_mfma_f32_16x16x32_bf16 a[76:79], v[36:39], v[188:191], a[76:79]\n\t"
    "s_add_u32 m0, s91, 16448\n\t"
    "s_add_u32 s69, %[sWk], s99\n\t"
    "v_mfma_f32_16x16x32_bf16 a[76:79], v[32:35], v[172:175], a[76:79]\n\t"
    "buffer_load_dwordx4 %[vW0], %[srdW], s69 offen lds\n\t"
    "s_waitcnt vmcnt(16) lgkmcnt(8)\n\t"
    "s_barrier\n\t"
    "v_mfma_f32_16x16x32_bf16 a[80:83], v[40:43], v[160:163], a[80:83]\n\t"
    "ds_read_b64_tr_b16 v[32:33], %[vRd] offset:0\n\t"
    "v_mfma_f32_16x16x32_bf16 a[80:83], v[44:47], v[176:179], a[80:83]\n\t"
    "ds_read_b64_tr_b16 v[34:35], %[vRd] offset:1024\n\t"
    "v_mfma_f32_16x16x32_bf16 a[84:87], v[44:47], v[180:183], a[84:87]\n\t"
    "s_add_u32 %[sA], %[sA], s55\n\t"
    "v_mfma_f32_16x16x32_bf16 a[84:87], v[40:43], v[164:167], a[84:87]\n\t"
    "buffer_load_dwordx4 v[128:131], %[vA0], %[srdWS], %[sA] offen offset:0 sc1\n\t"
    "v_mfma_f32_16x16x32_bf16 a[88:91], v[40:43], v[168:171], a[88:91]\n\t"
    "ds_read_b64_tr_b16 v[36:37], %[vRd] offset:8224\n\t"
    "v_mfma_f32_16x16x32_bf16 a[88:91], v[44:47], v[184:187], a[88:91]\n\t"
    "ds_read_b64_tr_b16 v[38:39], %[vRd] offset:9248\n\t"
    "v_mfma_f32_16x16x32_bf16 a[92:95], v[44:47], v[188:191], a[92:95]\n\t"
    "s_add_u32 %[sWk], %[sWk], 262144\n\t"
    "s_add_u32 m0, s84, 32896\n\t"
    "s_add_u32 s69, %[sWk], s92\n\t"
    "v_mfma_f32_16x16x32_bf16 a[92:95], v[40:43], v[172:175], a[92:95]\n\t"
    "buffer_load_dwordx4 %[vW0], %[srdW], s69 offen lds\n\t"
    "s_waitcnt lgkmcnt(8)\n\t"
    "v_mfma_f32_16x16x32_bf16 a[96:99], v[48:51], v[160:163], a[96:99]\n\t"
    "ds_read_b64_tr_b16 v[40:41], %[vRd] offset:8\n\t"
    "v_mfma_f32_16x16x32_bf16 a[96:99], v[52:55], v[176:179], a[96:99]\n\t"
    "ds_read_b64_tr_b16 v[42:43], %[vRd] offset:1032\n\t"
    "v_mfma_f32_16x16x32_bf16 a[100:103], v[52:55], v[180:183], a[100:103]\n\t"
    "v_mfma_f32_16x16x32_bf16 a[100:103], v[48:51], v[164:167], a[100:103]\n\t"
    "buffer_load_dwordx4 v[132:135], %[vA0], %[srdWS], %[sA] offen offset:1024 sc1\n\t"
    "v_mfma_f32_16x16x32_bf16 a[104:107], v[48:51], v[168:171], a[104:107]\n\t"
    "ds_read_b64_tr_b16 v[44:45], %[vRd] offset:8232\n\t"
    "v_mfma_f32_16x16x32_bf16 a[104:107], v[52:55], v[184:187], a[104:107]\n\t"
    "ds_read_b64_tr_b16 v[46:47], %[vRd] offset:9256\n\t"
    "v_mfma_f32_16x16x32_bf16 a[108:111], v[52:55], v[188:191], a[108:111]\n\t"
    "s_add_u32 m0, s85, 32896\n\t"
    "s_add_u32 s69, %[sWk], s93\n\t"
    "v_mfma_f32_16x16x32_bf16 a[108:111], v[48:51], v[172:175], a[108:111]\n\t"
    "buffer_load_dwordx4 %[vW0], %[srdW], s69 offen lds\n\t"
    "s_waitcnt lgkmcnt(8)\n\t"
    "v_mfma_f32_16x16x32_bf16 a[112:115], v[56:59], v[160:163], a[112:115]\n\t"
    "ds_read_b64_tr_b16 v[48:49], v25 offset:0\n\t"
    "v_mfma_f32_16x16x32_bf16 a[112:115], v[60:63], v[176:179], a[112:115]\n\t"
    "ds_read_b64_tr_b16 v[50:51], v25 offset:1024\n\t"
    "v_mfma_f32_16x16x32_bf16 a[116:119], v[60:63], v[180:183], a[116:119]\n\t"
    "v_mfma_f32_16x16x32_bf16 a[116:119], v[56:59], v[164:167], a[116:119]\n\t"
    "buffer_load_dwordx4 v[136:139], %[vA1], %[srdWS], %[sA] offen offset:0 sc1\n\t"
    "v_mfma_f32_16x16x32_bf16 a[120:123], v[56:59], v[168:171], a[120:123]\n\t"
    "ds_read_b64_tr_b16 v[52:53], v25 offset:8224\n\t"
    "v_mfma_f32_16x16x32_bf16 a[120:123], v[60:63], v[184:187], a[120:123]\n\t"
    "ds_read_b64_tr_b16 v[54:55], v25 offset:9248\n\t"
    "v_mfma_f32_16x16x32_bf16 a[124:127], v[60:63], v[188:191], a[124:127]\n\t"
    "s_add_u32 m0, s86, 32896\n\t"
    "s_add_u32 s69, %[sWk], s94\n\t"
    "v_mfma_f32_16x16x32_bf16 a[124:127], v[56:59], v[172:175], a[124:127]\n\t"
    "buffer_load_dwordx4 %[vW0], %[srdW], s69 offen lds\n\t"
    "L_k2_loop_%=:\n\t"
    "s_waitcnt vmcnt(23) lgkmcnt(8)\n\t"
    "v_mfma_f32_16x16x32_bf16 a[0:3], v[32:35], v[64:67], a[0:3]\n\t"
    "ds_read_b64_tr_b16 v[56:57], v25 offset:8\n\t"
    "v_mfma_f32_16x16x32_bf16 a[0:3], v[36:39], v[80:83], a[0:3]\n\t"
    "ds_read_b64_tr_b16 v[58:59], v25 offset:1032\n\t"
    "v_mfma_f32_16x16x32_bf16 a[4:7], v[36:39], v[84:87], a[4:7]\n\t"
    "v_mfma_f32_16x16x32_bf16 a[4:7], v[32:35], v[68:71], a[4:7]\n\t"
    "buffer_load_dwordx4 v[140:143], %[vA1], %[srdWS], %[sA] offen offset:1024 sc1\n\t"
    "v_mfma_f32_16x16x32_bf16 a[8:11], v[32:35], v[72:75], a[8:11]\n\t"
    "ds_read_b64_tr_b16 v[60:61], v25 offset:8232\n\t"
    "v_mfma_f32_16x16x32_bf16 a[8:11], v[36:39], v[88:91], a[8:11]\n\t"
    "ds_read_b64_tr_b16 v[62:63], v25 offset:9256\n\t"
    "v_mfma_f32_16x16x32_bf16 a[12:15], v[36:39], v[92:95], a[12:15]\n\t"
    "s_add_u32 m0, s87, 32896\n\t"
    "s_add_u32 s69, %[sWk], s95\n\t"
    "v_mfma_f32_16x16x32_bf16 a[12:15], v[32:35], v[76:79], a[12:15]\n\t"
    "buffer_load_dwordx4 %[vW0], %[srdW], s69 offen lds\n\t"
    "s_waitcnt lgkmcnt(8)\n\t"
    "v_mfma_f32_16x16x32_bf16 a[16:19], v[40:43], v[64:67], a[16:19]\n\t"
    "ds_read_b64_tr_b16 v[32:33], v26 offset:0\n\t"
    "v_mfma_f32_16x16x32_bf16 a[16:19], v[44:47], v[80:83], a[16:19]\n\t"
    "ds_read_b64_tr_b16 v[34:35], v26 offset:1024\n\t"
    "v_mfma_f32_16x16x32_bf16 a[20:23], v[44:47], v[84:87], a[20:23]\n\t"
    "s_add_u32 %[sA], %[sA], %[sAi1]\n\t"
    "v_mfma_f32_16x16x32_bf16 a[20:23], v[40:43], v[68:71], a[20:23]\n\t"
    "buffer_load_dwordx4 v[144:147], %[vA0], %[srdWS], %[sA] offen offset:0 sc1\n\t"
    "v_mfma_f32_16x16x32_bf16 a[24:27], v[40:43], v[72:75], a[24:27]\n\t"
    "ds_read_b64_tr_b16 v[36:37], v26 offset:8224\n\t"
    "v_mfma_f32_16x16x32_bf16 a[24:27], v[44:47], v[88:91], a[24:27]\n\t"
    "ds_read_b64_tr_b16 v[38:39], v26 offset:9248\n\t"
    "v_mfma_f32_16x16x32_bf16 a[28:31], v[44:47], v[92:95], a[28:31]\n\t"
    "s_add_u32 m0, s88, 32896\n\t"
    "s_add_u32 s69, %[sWk], s96\n\t"
    "v_mfma_f32_16x16x32_bf16 a[28:31], v[40:43], v[76:79], a[28:31]\n\t"
    "buffer_load_dwordx4 %[vW0], %[srdW], s69 offen lds\n\t"
    "s_waitcnt lgkmcnt(8)\n\t"
    "v_mfma_f32_16x16x32_bf16 a[32:35], v[48:51], v[64:67], a[32:35]\n\t"
    "ds_read_b64_tr_b16 v[40:41], v26 offset:8\n\t"
    "v_mfma_f32_16x16x32_bf16 a[32:35], v[52:55], v[80:83], a[32:35]\n\t"
    "ds_read_b64_tr_b16 v[42:43], v26 offset:1032\n\t"
    "v_mfma_f32_16x16x32_bf16 a[36:39], v[52:55], v[84:87], a[36:39]\n\t"
    "v_mfma_f32_16x16x32_bf16 a[36:39], v[48:51], v[68:71], a[36:39]\n\t"
    "buffer_load_dwordx4 v[148:151], %[vA0], %[srdWS], %[sA] offen offset:1024 sc1\n\t"
    "v_mfma_f32_16x16x32_bf16 a[40:43], v[48:51], v[72:75], a[40:43]\n\t"
    "ds_read_b64_tr_b16 v[44:45], v26 offset:8232\n\t"
    "v_mfma_f32_16x16x32_bf16 a[40:43], v[52:55], v[88:91], a[40:43]\n\t"
    "ds_read_b64_tr_b16 v[46:47], v26 offset:9256\n\t"
    "v_mfma_f32_16x16x32_bf16 a[44:47], v[52:55], v[92:95], a[44:47]\n\t"
    "s_add_u32 m0, s89, 32896\n\t"
    "s_add_u32 s69, %[sWk], s97\n\t"
    "v_mfma_f32_16x16x32_bf16 a[44:47], v[48:51], v[76:79], a[44:47]\n\t"
    "buffer_load_dwordx4 %[vW0], %[srdW], s69 offen lds\n\t"
    "s_waitcnt lgkmcnt(8)\n\t"
    "v_mfma_f32_16x16x32_bf16 a[48:51], v[56:59], v[64:67], a[48:51]\n\t"
    "ds_read_b64_tr_b16 v[48:49], v27 offset:0\n\t"
    "v_mfma_f32_16x16x32_bf16 a[48:51], v[60:63], v[80:83], a[48:51]\n\t"
    "ds_read_b64_tr_b16 v[50:51], v27 offset:1024\n\t"
    "v_mfma_f32_16x16x32_bf16 a[52:55], v[60:63], v[84:87], a[52:55]\n\t"
    "v_mfma_f32_16x16x32_bf16 a[52:55], v[56:59], v[68:71], a[52:55]\n\t"
    "buffer_load_dwordx4 v[152:155], %[vA1], %[srdWS], %[sA] offen offset:0 sc1\n\t"
    "v_mfma_f32_16x16x32_bf16 a[56:59], v[56:59], v[72:75], a[56:59]\n\t"
    "ds_read_b64_tr_b16 v[52:53], v27 offset:8224\n\t"
    "v_mfma_f32_16x16x32_bf16 a[56:59], v[60:63], v[88:91], a[56:59]\n\t"
    "ds_read_b64_tr_b16 v[54:55], v27 offset:9248\n\t"
    "v_mfma_f32_16x16x32_bf16 a[60:63], v[60:63], v[92:95], a[60:63]\n\t"
    "s_add_u32 m0, s90, 32896\n\t"
    "s_add_u32 s69, %[sWk], s98\n\t"
    "v_mfma_f32_16x16x32_bf16 a[60:63], v[56:59], v[76:79], a[60:63]\n\t"
    "buffer_load_dwordx4 %[vW0], %[srdW], s69 offen lds\n\t"
    "s_waitcnt lgkmcnt(8)\n\t"
    "v_mfma_f32_16x16x32_bf16 a[64:67], v[32:35], v[64:67], a[64:67]\n\t"
    "ds_read_b64_tr_b16 v[56:57], v27 offset:8\n\t"
    "v_mfma_f32_16x16x32_bf16 a[64:67], v[36:39], v[80:83], a[64:67]\n\t"
    "ds_read_b64_tr_b16 v[58:59], v27 offset:1032\n\t"
    "v_mfma_f32_16x16x32_bf16 a[68:71], v[36:39], v[84:87], a[68:71]\n\t"
    "v_mfma_f32_16x16x32_bf16 a[68:71], v[32:35], v[68:71], a[68:71]\n\t"
    "buffer_load_dwordx4 v[156:159], %[vA1], %[srdWS], %[sA] offen offset:1024 sc1\n\t"
    "v_mfma_f32_16x16x32_bf16 a[72:75], v[32:35], v[72:75], a[72:75]\n\t"
    "ds_read_b64_tr_b16 v[60:61], v27 offset:8232\n\t"
    "v_mfma_f32_16x16x32_bf16 a[72:75], v[36:39], v[88:91], a[72:75]\n\t"
    "ds_read_b64_tr_b16 v[62:63], v27 offset:9256\n\t"
    "v_mfma_f32_16x16x32_bf16 a[76:79], v[36:39], v[92:95], a[76:79]\n\t"
    "s_add_u32 m0, s91, 32896\n\t"
    "s_add_u32 s69, %[sWk], s99\n\t"
    "v_mfma_f32_16x16x32_bf16 a[76:79], v[32:35], v[76:79], a[76:79]\n\t"
    "buffer_load_dwordx4 %[vW0], %[srdW], s69 offen lds\n\t"
    "s_waitcnt vmcnt(16) lgkmcnt(8)\n\t"
    "s_barrier\n\t"
    "v_mfma_f32_16x16x32_bf16 a[80:83], v[40:43], v[64:67], a[80:83]\n\t"
    "ds_read_b64_tr_b16 v[32:33], %[vRd] offset:16448\n\t"
    "v_mfma_f32_16x16x32_bf16 a[80:83], v[44:47], v[80:83], a[80:83]\n\t"
    "ds_read_b64_tr_b16 v[34:35], %[vRd] offset:17472\n\t"
    "v_mfma_f32_16x16x32_bf16 a[84:87], v[44:47], v[84:87], a[84:87]\n\t"
    "s_add_u32 %[sA], %[sA], s55\n\t"
    "v_mfma_f32_16x16x32_bf16 a[84:87], v[40:43], v[68:71], a[84:87]\n\t"
    "buffer_load_dwordx4 v[160:163], %[vA0], %[srdWS], %[sA] offen offset:0 sc1\n\t"
    "v_mfma_f32_16x16x32_bf16 a[88:91], v[40:43], v[72:75], a[88:91]\n\t"
    "ds_read_b64_tr_b16 v[36:37], %[vRd] offset:24672\n\t"
    "v_mfma_f32_16x16x32_bf16 a[88:91], v[44:47], v[88:91], a[88:91]\n\t"
    "ds_read_b64_tr_b16 v[38:39], %[vRd] offset:25696\n\t"
    "v_mfma_f32_16x16x32_bf16 a[92:95], v[44:47], v[92:95], a[92:95]\n\t"
    "s_add_u32 %[sWk], %[sWk], 262144\n\t"
    "s_add_u32 m0, s84, 49344\n\t"
    "s_add_u32 s69, %[sWk], s92\n\t"
    "v_mfma_f32_16x16x32_bf16 a[92:95], v[40:43], v[76:79], a[92:95]\n\t"
    "buffer_load_dwordx4 %[vW0], %[srdW], s69 offen lds\n\t"
    "s_waitcnt lgkmcnt(8)\n\t"
    "v_mfma_f32_16x16x32_bf16 a[96:99], v[48:51], v[64:67], a[96:99]\n\t"
    "ds_read_b64_tr_b16 v[40:41], %[vRd] offset:16456\n\t"
    "v_mfma_f32_16x16x32_bf16 a[96:99], v[52:55], v[80:83], a[96:99]\n\t"
    "ds_read_b64_tr_b16 v[42:43], %[vRd] offset:17480\n\t"
    "v_mfma_f32_16x16x32_bf16 a[100:103], v[52:55], v[84:87], a[100:103]\n\t"
    "v_mfma_f32_16x16x32_bf16 a[100:103], v[48:51], v[68:71], a[100:103]\n\t"
    "buffer_load_dwordx4 v[164:167], %[vA0], %[srdWS], %[sA] offen offset:1024 sc1\n\t"
    "v_mfma_f32_16x16x32_bf16 a[104:107], v[48:51], v[72:75], a[104:107]\n\t"
    "ds_read_b64_tr_b16 v[44:45], %[vRd] offset:24680\n\t"
    "v_mfma_f32_16x16x32_bf16 a[104:107], v[52:55], v[88:91], a[104:107]\n\t"
    "ds_read_b64_tr_b16 v[46:47], %[vRd] offset:25704\n\t"
    "v_mfma_f32_16x16x32_bf16 a[108:111], v[52:55], v[92:95], a[108:111]\n\t"
    "s_add_u32 m0, s85, 49344\n\t"
    "s_add_u32 s69, %[sWk], s93\n\t"
    "v_mfma_f32_16x16x32_bf16 a[108:111], v[48:51], v[76:79], a[108:111]\n\t"
    "buffer_load_dwordx4 %[vW0], %[srdW], s69 offen lds\n\t"
    "s_waitcnt lgkmcnt(8)\n\t"
    "v_mfma_f32_16x16x32_bf16 a[112:115], v[56:59], v[64:67], a[112:115]\n\t"
    "ds_read_b64_tr_b16 v[48:49], v25 offset:16448\n\t"
    "v_mfma_f32_16x16x32_bf16 a[112:115], v[60:63], v[80:83], a[112:115]\n\t"
    "ds_read_b64_tr_b16 v[50:51], v25 offset:17472\n\t"
    "v_mfma_f32_16x16x32_bf16 a[116:119], v[60:63], v[84:87], a[116:119]\n\t"
    "v_mfma_f32_16x16x32_bf16 a[116:119], v[56:59], v[68:71], a[116:119]\n\t"
    "buffer_load_dwordx4 v[168:171], %[vA1], %[srdWS], %[sA] offen offset:0 sc1\n\t"
    "v_mfma_f32_16x16x32_bf16 a[120:123], v[56:59], v[72:75], a[120:123]\n\t"
    "ds_read_b64_tr_b16 v[52:53], v25 offset:24672\n\t"
    "v_mfma_f32_16x16x32_bf16 a[120:123], v[60:63], v[88:91], a[120:123]\n\t"
    "ds_read_b64_tr_b16 v[54:55], v25 offset:25696\n\t"
    "v_mfma_f32_16x16x32_bf16 a[124:127], v[60:63], v[92:95], a[124:127]\n\t"
    "s_add_u32 m0, s86, 49344\n\t"
    "s_add_u32 s69, %[sWk], s94\n\t"
    "v_mfma_f32_16x16x32_bf16 a[124:127], v[56:59], v[76:79], a[124:127]\n\t"
    "buffer_load_dwordx4 %[vW0], %[srdW], s69 offen lds\n\t"
    "s_waitcnt vmcnt(23) lgkmcnt(8)\n\t"
    "v_mfma_f32_16x16x32_bf16 a[0:3], v[32:35], v[96:99], a[0:3]\n\t"
    "ds_read_b64_tr_b16 v[56:57], v25 offset:16456\n\t"
    "v_mfma_f32_16x16x32_bf16 a[0:3], v[36:39], v[112:115], a[0:3]\n\t"
    "ds_read_b64_tr_b16 v[58:59], v25 offset:17480\n\t"
    "v_mfma_f32_16x16x32_bf16 a[4:7], v[36:39], v[116:119], a[4:7]\n\t"
    "v_mfma_f32_16x16x32_bf16 a[4:7], v[32:35], v[100:103], a[4:7]\n\t"
    "buffer_load_dwordx4 v[172:175], %[vA1], %[srdWS], %[sA] offen offset:1024 sc1\n\t"
    "v_mfma_f32_16x16x32_bf16 a[8:11], v[32:35], v[104:107], a[8:11]\n\t"
    "ds_read_b64_tr_b16 v[60:61], v25 offset:24680\n\t"
    "v_mfma_f32_16x16x32_bf16 a[8:11], v[36:39], v[120:123], a[8:11]\n\t"
    "ds_read_b64_tr_b16 v[62:63], v25 offset:25704\n\t"
    "v_mfma_f32_16x16x32_bf16 a[12:15], v[36:39], v[124:127], a[12:15]\n\t"
    "s_add_u32 m0, s87, 49344\n\t"
    "s_add_u32 s69, %[sWk], s95\n\t"
    "v_mfma_f32_16x16x32_bf16 a[12:15], v[32:35], v[108:111], a[12:15]\n\t"
    "buffer_load_dwordx4 %[vW0], %[srdW], s69 offen lds\n\t"
    "s_waitcnt lgkmcnt(8)\n\t"
    "v_mfma_f32_16x16x32_bf16 a[16:19], v[40:43], v[96:99], a[16:19]\n\t"
    "ds_read_b64_tr_b16 v[32:33], v26 offset:16448\n\t"
    "v_mfma_f32_16x16x32_bf16 a[16:19], v[44:47], v[112:115], a[16:19]\n\t"
    "ds_read_b64_tr_b16 v[34:35], v26 offset:17472\n\t"
    "v_mfma_f32_16x16x32_bf16 a[20:23], v[44:47], v[116:119], a[20:23]\n\t"
    "s_add_u32 %[sA], %[sA], %[sAi1]\n\t"
    "k_pgb1: s_cbranch_execz k_pgb_back\n\t"
    "v_mfma_f32_16x16x32_bf16 a[20:23], v[40:43], v[100:103], a[20:23]\n\t"
    "buffer_load_dwordx4 v[176:179], %[vA0], %[srdWS], %[sA] offen offset:0 sc1\n\t"
    "v_mfma_f32_16x16x32_bf16 a[24:27], v[40:43], v[104:107], a[24:27]\n\t"
    "ds_read_b64_tr_b16 v[36:37], v26 offset:24672\n\t"
    "v_mfma_f32_16x16x32_bf16 a[24:27], v[44:47], v[120:123], a[24:27]\n\t"
    "ds_read_b64_tr_b16 v[38:39], v26 offset:25696\n\t"
    "v_mfma_f32_16x16x32_bf16 a[28:31], v[44:47], v[124:127], a[28:31]\n\t"
    "s_add_u32 m0, s88, 49344\n\t"
    "s_add_u32 s69, %[sWk], s96\n\t"
    "v_mfma_f32_16x16x32_bf16 a[28:31], v[40:43], v[108:111], a[28:31]\n\t"
    "buffer_load_dwordx4 %[vW0], %[srdW], s69 offen lds\n\t"
    "s_waitcnt lgkmcnt(8)\n\t"
    "v_mfma_f32_16x16x32_bf16 a[32:35], v[48:51], v[96:99], a[32:35]\n\t"
    "ds_read_b64_tr_b16 v[40:41], v26 offset:16456\n\t"
    "v_mfma_f32_16x16x32_bf16 a[32:35], v[52:55], v[112:115], a[32:35]\n\t"
    "ds_read_b64_tr_b16 v[42:43], v26 offset:17480\n\t"
    "v_mfma_f32_16x16x32_bf16 a[36:39], v[52:55], v[116:119], a[36:39]\n\t"
    "v_mfma_f32_16x16x32_bf16 a[36:39], v[48:51], v[100:103], a[36:39]\n\t"
    "buffer_load_dwordx4 v[180:183], %[vA0], %[srdWS], %[sA] offen offset:1024 sc1\n\t"
    "v_mfma_f32_16x16x32_bf16 a[40:43], v[48:51], v[104:107], a[40:43]\n\t"
    "ds_read_b64_tr_b16 v[44:45], v26 offset:24680\n\t"
    "v_mfma_f32_16x16x32_bf16 a[40:43], v[52:55], v[120:123], a[40:43]\n\t"
    "ds_read_b64_tr_b16 v[46:47], v26 offset:25704\n\t"
    "v_mfma_f32_16x16x32_bf16 a[44:47], v[52:55], v[124:127], a[44:47]\n\t"
    "s_add_u32 m0, s89, 49344\n\t"
    "s_add_u32 s69, %[sWk], s97\n\t"
    "v_mfma_f32_16x16x32_bf16 a[44:47], v[48:51], v[108:111], a[44:47]\n\t"
    "buffer_load_dwordx4 %[vW0], %[srdW], s69 offen lds\n\t"
    "s_waitcnt lgkmcnt(8)\n\t"
    "v_mfma_f32_16x16x32_bf16 a[48:51], v[56:59], v[96:99], a[48:51]\n\t"
    "ds_read_b64_tr_b16 v[48:49], v27 offset:16448\n\t"
    "v_mfma_f32_16x16x32_bf16 a[48:51], v[60:63], v[112:115], a[48:51]\n\t"
    "ds_read_b64_tr_b16 v[50:51], v27 offset:17472\n\t"
    "v_mfma_f32_16x16x32_bf16 a[52:55], v[60:63], v[116:119], a[52:55]\n\t"
    "v_mfma_f32_16x16x32_bf16 a[52:55], v[56:59], v[100:103], a[52:55]\n\t"
    "buffer_load_dwordx4 v[184:187], %[vA1], %[srdWS], %[sA] offen offset:0 sc1\n\t"
    "v_mfma_f32_16x16x32_bf16 a[56:59], v[56:59], v[104:107], a[56:59]\n\t"
    "ds_read_b64_tr_b16 v[52:53], v27 offset:24672\n\t"
    "v_mfma_f32_16x16x32_bf16 a[56:59], v[60:63], v[120:123], a[56:59]\n\t"
    "ds_read_b64_tr_b16 v[54:55], v27 offset:25696\n\t"
    "v_mfma_f32_16x16x32_bf16 a[60:63], v[60:63], v[124:127], a[60:63]\n\t"
    "s_add_u32 m0, s90, 49344\n\t"
    "s_add_u32 s69, %[sWk], s98\n\t"
    "v_mfma_f32_16x16x32_bf16 a[60:63], v[56:59], v[108:111], a[60:63]\n\t"
    "buffer_load_dwordx4 %[vW0], %[srdW], s69 offen lds\n\t"
    "s_waitcnt lgkmcnt(8)\n\t"
    "v_mfma_f32_16x16x32_bf16 a[64:67], v[32:35], v[96:99], a[64:67]\n\t"
    "ds_read_b64_tr_b16 v[56:57], v27 offset:16456\n\t"
    "v_mfma_f32_16x16x32_bf16 a[64:67], v[36:39], v[112:115], a[64:67]\n\t"
    "ds_read_b64_tr_b16 v[58:59], v27 offset:17480\n\t"
    "v_mfma_f32_16x16x32_bf16 a[68:71], v[36:39], v[116:119], a[68:71]\n\t"
    "v_mfma_f32_16x16x32_bf16 a[68:71], v[32:35], v[100:103], a[68:71]\n\t"
    "buffer_load_dwordx4 v[188:191], %[vA1], %[srdWS], %[sA] offen offset:1024 sc1\n\t"
    "v_mfma_f32_16x16x32_bf16 a[72:75], v[32:35], v[104:107], a[72:75]\n\t"
    "ds_read_b64_tr_b16 v[60:61], v27 offset:24680\n\t"
    "v_mfma_f32_16x16x32_bf16 a[72:75], v[36:39], v[120:123], a[72:75]\n\t"
    "ds_read_b64_tr_b16 v[62:63], v27 offset:25704\n\t"
    "v_mfma_f32_16x16x32_bf16 a[76:79], v[36:39], v[124:127], a[76:79]\n\t"
    "s_add_u32 m0, s91, 49344\n\t"
    "s_add_u32 s69, %[sWk], s99\n\t"
    "v_mfma_f32_16x16x32_bf16 a[76:79], v[32:35], v[108:111], a[76:79]\n\t"
    "buffer_load_dwordx4 %[vW0], %[srdW], s69 offen lds\n\t"
    "s_waitcnt vmcnt(16) lgkmcnt(8)\n\t"
    "s_barrier\n\t"
    "v_mfma_f32_16x16x32_bf16 a[80:83], v[40:43], v[96:99], a[80:83]\n\t"
    "ds_read_b64_tr_b16 v[32:33], %[vRd] offset:32896\n\t"
    "v_mfma_f32_16x16x32_bf16 a[80:83], v[44:47], v[112:115], a[80:83]\n\t"
    "ds_read_b64_tr_b16 v[34:35], %[vRd] offset:33920\n\t"
    "v_mfma_f32_16x16x32_bf16 a[84:87], v[44:47], v[116:119], a[84:87]\n\t"
    "s_add_u32 %[sA], %[sA], s55\n\t"
    "v_mfma_f32_16x16x32_bf16 a[84:87], v[40:43], v[100:103], a[84:87]\n\t"
    "buffer_load_dwordx4 v[64:67], %[vA0], %[srdWS], %[sA] offen offset:0 sc1\n\t"
    "v_mfma_f32_16x16x32_bf16 a[88:91], v[40:43], v[104:107], a[88:91]\n\t"
    "ds_read_b64_tr_b16 v[36:37], %[vRd] offset:41120\n\t"
    "v_mfma_f32_16x16x32_bf16 a[88:91], v[44:47], v[120:123], a[88:91]\n\t"
    "ds_read_b64_tr_b16 v[38:39], %[vRd] offset:42144\n\t"
    "v_mfma_f32_16x16x32_bf16 a[92:95], v[44:47], v[124:127], a[92:95]\n\t"
    "s_add_u32 %[sWk], %[sWk], 262144\n\t"
    "s_add_u32 m0, s84, 0\n\t"
    "s_add_u32 s69, %[sWk], s92\n\t"
    "v_mfma_f32_16x16x32_bf16 a[92:95], v[40:43], v[108:111], a[92:95]\n\t"
    "buffer_load_dwordx4 %[vW0], %[srdW], s69 offen lds\n\t"
    "s_waitcnt lgkmcnt(8)\n\t"
    "v_mfma_f32_16x16x32_bf16 a[96:99], v[48:51], v[96:99], a[96:99]\n\t"
    "ds_read_b64_tr_b16 v[40:41], %[vRd] offset:32904\n\t"
    "v_mfma_f32_16x16x32_bf16 a[96:99], v[52:55], v[112:115], a[96:99]\n\t"
    "ds_read_b64_tr_b16 v[42:43], %[vRd] offset:33928\n\t"
    "v_mfma_f32_16x16x32_bf16 a[100:103], v[52:55], v[116:119], a[100:103]\n\t"
    "v_mfma_f32_16x16x32_bf16 a[100:103], v[48:51], v[100:103], a[100:103]\n\t"
    "buffer_load_dwordx4 v[68:71], %[vA0], %[srdWS], %[sA] offen offset:1024 sc1\n\t"
    "v_mfma_f32_16x16x32_bf16 a[104:107], v[48:51], v[104:107], a[104:107]\n\t"
    "ds_read_b64_tr_b16 v[44:45], %[vRd] offset:41128\n\t"
    "v_mfma_f32_16x16x32_bf16 a[104:107], v[52:55], v[120:123], a[104:107]\n\t"
    "ds_read_b64_tr_b16 v[46:47], %[vRd] offset:42152\n\t"
    "v_mfma_f32_16x16x32_bf16 a[108:111], v[52:55], v[124:127], a[108:111]\n\t"
    "s_add_u32 m0, s85, 0\n\t"
    "s_add_u32 s69, %[sWk], s93\n\t"
    "v_mfma_f32_16x16x32_bf16 a[108:111], v[48:51], v[108:111], a[108:111]\n\t"
    "buffer_load_dwordx4 %[vW0], %[srdW], s69 offen lds\n\t"
    "s_waitcnt lgkmcnt(8)\n\t"
    "v_mfma_f32_16x16x32_bf16 a[112:115], v[56:59], v[96:99], a[112:115]\n\t"
    "ds_read_b64_tr_b16 v[48:49], v25 offset:32896\n\t"
    "v_mfma_f32_16x16x32_bf16 a[112:115], v[60:63], v[112:115], a[112:115]\n\t"
    "ds_read_b64_tr_b16 v[50:51], v25 offset:33920\n\t"
    "v_mfma_f32_16x16x32_bf16 a[116:119], v[60:63], v[116:119], a[116:119]\n\t"
    "v_mfma_f32_16x16x32_bf16 a[116:119], v[56:59], v[100:103], a[116:119]\n\t"
    "buffer_load_dwordx4 v[72:75], %[vA1], %[srdWS], %[sA] offen offset:0 sc1\n\t"
    "v_mfma_f32_16x16x32_bf16 a[120:123], v[56:59], v[104:107], a[120:123]\n\t"
    "ds_read_b64_tr_b16 v[52:53], v25 offset:41120\n\t"
    "v_mfma_f32_16x16x32_bf16 a[120:123], v[60:63], v[120:123], a[120:123]\n\t"
    "ds_read_b64_tr_b16 v[54:55], v25 offset:42144\n\t"
    "v_mfma_f32_16x16x32_bf16 a[124:127], v[60:63], v[124:127], a[124:127]\n\t"
    "s_add_u32 m0, s86, 0\n\t"
    "s_add_u32 s69, %[sWk], s94\n\t"
    "v_mfma_f32_16x16x32_bf16 a[124:127], v[56:59], v[108:111], a[124:127]\n\t"
    "buffer_load_dwordx4 %[vW0], %[srdW], s69 offen lds\n\t"
    "s_waitcnt vmcnt(23) lgkmcnt(8)\n\t"
    "v_mfma_f32_16x16x32_bf16 a[0:3], v[32:35], v[128:131], a[0:3]\n\t"
    "ds_read_b64_tr_b16 v[56:57], v25 offset:32904\n\t"
    "v_mfma_f32_16x16x32_bf16 a[0:3], v[36:39], v[144:147], a[0:3]\n\t"
    "ds_read_b64_tr_b16 v[58:59], v25 offset:33928\n\t"
    "v_mfma_f32_16x16x32_bf16 a[4:7], v[36:39], v[148:151], a[4:7]\n\t"
    "v_mfma_f32_16x16x32_bf16 a[4:7], v[32:35], v[132:135], a[4:7]\n\t"
    "buffer_load_dwordx4 v[76:79], %[vA1], %[srdWS], %[sA] offen offset:1024 sc1\n\t"
    "v_mfma_f32_16x16x32_bf16 a[8:11], v[32:35], v[136:139], a[8:11]\n\t"
    "ds_read_b64_tr_b16 v[60:61], v25 offset:41128\n\t"
    "v_mfma_f32_16x16x32_bf16 a[8:11], v[36:39], v[152:155], a[8:11]\n\t"
    "ds_read_b64_tr_b16 v[62:63], v25 offset:42152\n\t"
    "v_mfma_f32_16x16x32_bf16 a[12:15], v[36:39], v[156:159], a[12:15]\n\t"
    "s_add_u32 m0, s87, 0\n\t"
    "s_add_u32 s69, %[sWk], s95\n\t"
    "v_mfma_f32_16x16x32_bf16 a[12:15], v[32:35], v[140:143], a[12:15]\n\t"
    "buffer_load_dwordx4 %[vW0], %[srdW], s69 offen lds\n\t"
    "s_waitcnt lgkmcnt(8)\n\t"
    "v_mfma_f32_16x16x32_bf16 a[16:19], v[40:43], v[128:131], a[16:19]\n\t"
    "ds_read_b64_tr_b16 v[32:33], v26 offset:32896\n\t"
    "v_mfma_f32_16x16x32_bf16 a[16:19], v[44:47], v[144:147], a[16:19]\n\t"
    "ds_read_b64_tr_b16 v[34:35], v26 offset:33920\n\t"
    "v_mfma_f32_16x16x32_bf16 a[20:23], v[44:47], v[148:151], a[20:23]\n\t"
    "s_add_u32 %[sA], %[sA], %[sAi1]\n\t"
    "v_mfma_f32_16x16x32_bf16 a[20:23], v[40:43], v[132:135], a[20:23]\n\t"
    "buffer_load_dwordx4 v[80:83], %[vA0], %[srdWS], %[sA] offen offset:0 sc1\n\t"
    "v_mfma_f32_16x16x32_bf16 a[24:27], v[40:43], v[136:139], a[24:27]\n\t"
    "ds_read_b64_tr_b16 v[36:37], v26 offset:41120\n\t"
    "v_mfma_f32_16x16x32_bf16 a[24:27], v[44:47], v[152:155], a[24:27]\n\t"
    "ds_read_b64_tr_b16 v[38:39], v26 offset:42144\n\t"
    "v_mfma_f32_16x16x32_bf16 a[28:31], v[44:47], v[156:159], a[28:31]\n\t"
    "s_add_u32 m0, s88, 0\n\t"
    "s_add_u32 s69, %[sWk], s96\n\t"
    "v_mfma_f32_16x16x32_bf16 a[28:31], v[40:43], v[140:143], a[28:31]\n\t"
    "buffer_load_dwordx4 %[vW0], %[srdW], s69 offen lds\n\t"
    "s_waitcnt lgkmcnt(8)\n\t"
    "v_mfma_f32_16x16x32_bf16 a[32:35], v[48:51], v[128:131], a[32:35]\n\t"
    "ds_read_b64_tr_b16 v[40:41], v26 offset:32904\n\t"
    "v_mfma_f32_16x16x32_bf16 a[32:35], v[52:55], v[144:147], a[32:35]\n\t"
    "ds_read_b64_tr_b16 v[42:43], v26 offset:33928\n\t"
    "v_mfma_f32_16x16x32_bf16 a[36:39], v[52:55], v[148:151], a[36:39]\n\t"
    "v_mfma_f32_16x16x32_bf16 a[36:39], v[48:51], v[132:135], a[36:39]\n\t"
    "buffer_load_dwordx4 v[84:87], %[vA0], %[srdWS], %[sA] offen offset:1024 sc1\n\t"
    "v_mfma_f32_16x16x32_bf16 a[40:43], v[48:51], v[136:139], a[40:43]\n\t"
    "ds_read_b64_tr_b16 v[44:45], v26 offset:41128\n\t"
    "v_mfma_f32_16x16x32_bf16 a[40:43], v[52:55], v[152:155], a[40:43]\n\t"
    "ds_read_b64_tr_b16 v[46:47], v26 offset:42152\n\t"
    "v_mfma_f32_16x16x32_bf16 a[44:47], v[52:55], v[156:159], a[44:47]\n\t"
    "s_add_u32 m0, s89, 0\n\t"
    "s_add_u32 s69, %[sWk], s97\n\t"
    "v_mfma_f32_16x16x32_bf16 a[44:47], v[48:51], v[140:143], a[44:47]\n\t"
    "buffer_load_dwordx4 %[vW0], %[srdW], s69 offen lds\n\t"
    "s_waitcnt lgkmcnt(8)\n\t"
    "v_mfma_f32_16x16x32_bf16 a[48:51], v[56:59], v[128:131], a[48:51]\n\t"
    "ds_read_b64_tr_b16 v[48:49], v27 offset:32896\n\t"
    "v_mfma_f32_16x16x32_bf16 a[48:51], v[60:63], v[144:147], a[48:51]\n\t"
    "ds_read_b64_tr_b16 v[50:51], v27 offset:33920\n\t"
    "v_mfma_f32_16x16x32_bf16 a[52:55], v[60:63], v[148:151], a[52:55]\n\t"
    "v_mfma_f32_16x16x32_bf16 a[52:55], v[56:59], v[132:135], a[52:55]\n\t"
    "buffer_load_dwordx4 v[88:91], %[vA1], %[srdWS], %[sA] offen offset:0 sc1\n\t"
    "v_mfma_f32_16x16x32_bf16 a[56:59], v[56:59], v[136:139], a[56:59]\n\t"
    "ds_read_b64_tr_b16 v[52:53], v27 offset:41120\n\t"
    "v_mfma_f32_16x16x32_bf16 a[56:59], v[60:63], v[152:155], a[56:59]\n\t"
    "ds_read_b64_tr_b16 v[54:55], v27 offset:42144\n\t"
    "v_mfma_f32_16x16x32_bf16 a[60:63], v[60:63], v[156:159], a[60:63]\n\t"
    "s_add_u32 m0, s90, 0\n\t"
    "s_add_u32 s69, %[sWk], s98\n\t"
    "v_mfma_f32_16x16x32_bf16 a[60:63], v[56:59], v[140:143], a[60:63]\n\t"
    "buffer_load_dwordx4 %[vW0], %[srdW], s69 offen lds\n\t"
    "s_waitcnt lgkmcnt(8)\n\t"
    "v_mfma_f32_16x16x32_bf16 a[64:67], v[32:35], v[128:131], a[64:67]\n\t"
    "ds_read_b64_tr_b16 v[56:57], v27 offset:32904\n\t"
    "v_mfma_f32_16x16x32_bf16 a[64:67], v[36:39], v[144:147], a[64:67]\n\t"
    "ds_read_b64_tr_b16 v[58:59], v27 offset:33928\n\t"
    "v_mfma_f32_16x16x32_bf16 a[68:71], v[36:39], v[148:151], a[68:71]\n\t"
    "v_mfma_f32_16x16x32_bf16 a[68:71], v[32:35], v[132:135], a[68:71]\n\t"
    "buffer_load_dwordx4 v[92:95], %[vA1], %[srdWS], %[sA] offen offset:1024 sc1\n\t"
    "v_mfma_f32_16x16x32_bf16 a[72:75], v[32:35], v[136:139], a[72:75]\n\t"
    "ds_read_b64_tr_b16 v[60:61], v27 offset:41128\n\t"
    "v_mfma_f32_16x16x32_bf16 a[72:75], v[36:39], v[152:155], a[72:75]\n\t"
    "ds_read_b64_tr_b16 v[62:63], v27 offset:42152\n\t"
    "v_mfma_f32_16x16x32_bf16 a[76:79], v[36:39], v[156:159], a[76:79]\n\t"
    "s_add_u32 m0, s91, 0\n\t"
    "s_add_u32 s69, %[sWk], s99\n\t"
    "v_mfma_f32_16x16x32_bf16 a[76:79], v[32:35], v[140:143], a[76:79]\n\t"
    "buffer_load_dwordx4 %[vW0], %[srdW], s69 offen lds\n\t"
    "s_waitcnt vmcnt(16) lgkmcnt(8)\n\t"
    "s_barrier\n\t"
    "v_mfma_f32_16x16x32_bf16 a[80:83], v[40:43], v[128:131], a[80:83]\n\t"
    "ds_read_b64_tr_b16 v[32:33], %[vRd] offset:49344\n\t"
    "v_mfma_f32_16x16x32_bf16 a[80:83], v[44:47], v[144:147], a[80:83]\n\t"
    "ds_read_b64_tr_b16 v[34:35], %[vRd] offset:50368\n\t"
    "v_mfma_f32_16x16x32_bf16 a[84:87], v[44:47], v[148:151], a[84:87]\n\t"
    "s_add_u32 %[sA], %[sA], s55\n\t"
    "v_mfma_f32_16x16x32_bf16 a[84:87], v[40:43], v[132:135], a[84:87]\n\t"
    "buffer_load_dwordx4 v[96:99], %[vA0], %[srdWS], %[sA] offen offset:0 sc1\n\t"
    "v_mfma_f32_16x16x32_bf16 a[88:91], v[40:43], v[136:139], a[88:91]\n\t"
    "ds_read_b64_tr_b16 v[36:37], %[vRd] offset:57568\n\t"
    "v_mfma_f32_16x16x32_bf16 a[88:91], v[44:47], v[152:155], a[88:91]\n\t"
    "ds_read_b64_tr_b16 v[38:39], %[vRd] offset:58592\n\t"
    "v_mfma_f32_16x16x32_bf16 a[92:95], v[44:47], v[156:159], a[92:95]\n\t"
    "s_add_u32 %[sWk], %[sWk], 262144\n\t"
    "s_add_u32 m0, s84, 16448\n\t"
    "s_add_u32 s69, %[sWk], s92\n\t"
    "v_mfma_f32_16x16x32_bf16 a[92:95], v[40:43], v[140:143], a[92:95]\n\t"
    "buffer_load_dwordx4 %[vW0], %[srdW], s69 offen lds\n\t"
    "s_waitcnt lgkmcnt(8)\n\t"
    "v_mfma_f32_16x16x32_bf16 a[96:99], v[48:51], v[128:131], a[96:99]\n\t"
    "ds_read_b64_tr_b16 v[40:41], %[vRd] offset:49352\n\t"
    "v_mfma_f32_16x16x32_bf16 a[96:99], v[52:55], v[144:147], a[96:99]\n\t"
    "ds_read_b64_tr_b16 v[42:43], %[vRd] offset:50376\n\t"
    "v_mfma_f32_16x16x32_bf16 a[100:103], v[52:55], v[148:151], a[100:103]\n\t"
    "v_mfma_f32_16x16x32_bf16 a[100:103], v[48:51], v[132:135], a[100:103]\n\t"
    "buffer_load_dwordx4 v[100:103], %[vA0], %[srdWS], %[sA] offen offset:1024 sc1\n\t"
    "v_mfma_f32_16x16x32_bf16 a[104:107], v[48:51], v[136:139], a[104:107]\n\t"
    "ds_read_b64_tr_b16 v[44:45], %[vRd] offset:57576\n\t"
    "v_mfma_f32_16x16x32_bf16 a[104:107], v[52:55], v[152:155], a[104:107]\n\t"
    "ds_read_b64_tr_b16 v[46:47], %[vRd] offset:58600\n\t"
    "v_mfma_f32_16x16x32_bf16 a[108:111], v[52:55], v[156:159], a[108:111]\n\t"
    "s_add_u32 m0, s85, 16448\n\t"
    "s_add_u32 s69, %[sWk], s93\n\t"
    "v_mfma_f32_16x16x32_bf16 a[108:111], v[48:51], v[140:143], a[108:111]\n\t"
    "buffer_load_dwordx4 %[vW0], %[srdW], s69 offen lds\n\t"
    "s_waitcnt lgkmcnt(8)\n\t"
    "v_mfma_f32_16x16x32_bf16 a[112:115], v[56:59], v[128:131], a[112:115]\n\t"
    "ds_read_b64_tr_b16 v[48:49], v25 offset:49344\n\t"
    "v_mfma_f32_16x16x32_bf16 a[112:115], v[60:63], v[144:147], a[112:115]\n\t"
    "ds_read_b64_tr_b16 v[50:51], v25 offset:50368\n\t"
    "v_mfma_f32_16x16x32_bf16 a[116:119], v[60:63], v[148:151], a[116:119]\n\t"
    "v_mfma_f32_16x16x32_bf16 a[116:119], v[56:59], v[132:135], a[116:119]\n\t"
    "buffer_load_dwordx4 v[104:107], %[vA1], %[srdWS], %[sA] offen offset:0 sc1\n\t"
    "v_mfma_f32_16x16x32_bf16 a[120:123], v[56:59], v[136:139], a[120:123]\n\t"
    "ds_read_b64_tr_b16 v[52:53], v25 offset:57568\n\t"
    "v_mfma_f32_16x16x32_bf16 a[120:123], v[60:63], v[152:155], a[120:123]\n\t"
    "ds_read_b64_tr_b16 v[54:55], v25 offset:58592\n\t"
    "v_mfma_f32_16x16x32_bf16 a[124:127], v[60:63], v[156:159], a[124:127]\n\t"
    "s_add_u32 m0, s86, 16448\n\t"
    "s_add_u32 s69, %[sWk], s94\n\t"
    "v_mfma_f32_16x16x32_bf16 a[124:127], v[56:59], v[140:143], a[124:127]\n\t"
    "buffer_load_dwordx4 %[vW0], %[srdW], s69 offen lds\n\t"
    "s_waitcnt vmcnt(23) lgkmcnt(8)\n\t"
    "v_mfma_f32_16x16x32_bf16 a[0:3], v[32:35], v[160:163], a[0:3]\n\t"
    "ds_read_b64_tr_b16 v[56:57], v25 offset:49352\n\t"
    "v_mfma_f32_16x16x32_bf16 a[0:3], v[36:39], v[176:179], a[0:3]\n\t"
    "ds_read_b64_tr_b16 v[58:59], v25 offset:50376\n\t"
    "v_mfma_f32_16x16x32_bf16 a[4:7], v[36:39], v[180:183], a[4:7]\n\t"
    "v_mfma_f32_16x16x32_bf16 a[4:7], v[32:35], v[164:167], a[4:7]\n\t"
    "buffer_load_dwordx4 v[108:111], %[vA1], %[srdWS], %[sA] offen offset:1024 sc1\n\t"
    "v_mfma_f32_16x16x32_bf16 a[8:11], v[32:35], v[168:171], a[8:11]\n\t"
    "ds_read_b64_tr_b16 v[60:61], v25 offset:57576\n\t"
    "v_mfma_f32_16x16x32_bf16 a[8:11], v[36:39], v[184:187], a[8:11]\n\t"
    "ds_read_b64_tr_b16 v[62:63], v25 offset:58600\n\t"
    "v_mfma_f32_16x16x32_bf16 a[12:15], v[36:39], v[188:191], a[12:15]\n\t"
    "s_add_u32 m0, s87, 16448\n\t"
    "s_add_u32 s69, %[sWk], s95\n\t"
    "v_mfma_f32_16x16x32_bf16 a[12:15], v[32:35], v[172:175], a[12:15]\n\t"
    "buffer_load_dwordx4 %[vW0], %[srdW], s69 offen lds\n\t"
    "s_waitcnt lgkmcnt(8)\n\t"
    "v_mfma_f32_16x16x32_bf16 a[16:19], v[40:43], v[160:163], a[16:19]\n\t"
    "ds_read_b64_tr_b16 v[32:33], v26 offset:49344\n\t"
    "v_mfma_f32_16x16x32_bf16 a[16:19], v[44:47], v[176:179], a[16:19]\n\t"
    "ds_read_b64_tr_b16 v[34:35], v26 offset:50368\n\t"
    "v_mfma_f32_16x16x32_bf16 a[20:23], v[44:47], v[180:183], a[20:23]\n\t"
    "s_add_u32 %[sA], %[sA], %[sAi1]\n\t"
    "v_mfma_f32_16x16x32_bf16 a[20:23], v[40:43], v[164:167], a[20:23]\n\t"
    "buffer_load_dwordx4 v[112:115], %[vA0], %[srdWS], %[sA] offen offset:0 sc1\n\t"
    "v_mfma_f32_16x16x32_bf16 a[24:27], v[40:43], v[168:171], a[24:27]\n\t"
    "ds_read_b64_tr_b16 v[36:37], v26 offset:57568\n\t"
    "v_mfma_f32_16x16x32_bf16 a[24:27], v[44:47], v[184:187], a[24:27]\n\t"
    "ds_read_b64_tr_b16 v[38:39], v26 offset:58592\n\t"
    "v_mfma_f32_16x16x32_bf16 a[28:31], v[44:47], v[188:191], a[28:31]\n\t"
    "s_add_u32 m0, s88, 16448\n\t"
    "s_add_u32 s69, %[sWk], s96\n\t"
    "v_mfma_f32_16x16x32_bf16 a[28:31], v[40:43], v[172:175], a[28:31]\n\t"
    "buffer_load_dwordx4 %[vW0], %[srdW], s69 offen lds\n\t"
    "s_waitcnt lgkmcnt(8)\n\t"
    "v_mfma_f32_16x16x32_bf16 a[32:35], v[48:51], v[160:163], a[32:35]\n\t"
    "ds_read_b64_tr_b16 v[40:41], v26 offset:49352\n\t"
    "v_mfma_f32_16x16x32_bf16 a[32:35], v[52:55], v[176:179], a[32:35]\n\t"
    "ds_read_b64_tr_b16 v[42:43], v26 offset:50376\n\t"
    "v_mfma_f32_16x16x32_bf16 a[36:39], v[52:55], v[180:183], a[36:39]\n\t"
    "v_mfma_f32_16x16x32_bf16 a[36:39], v[48:51], v[164:167], a[36:39]\n\t"
    "buffer_load_dwordx4 v[116:119], %[vA0], %[srdWS], %[sA] offen offset:1024 sc1\n\t"
    "v_mfma_f32_16x16x32_bf16 a[40:43], v[48:51], v[168:171], a[40:43]\n\t"
    "ds_read_b64_tr_b16 v[44:45], v26 offset:57576\n\t"
    "v_mfma_f32_16x16x32_bf16 a[40:43], v[52:55], v[184:187], a[40:43]\n\t"
    "ds_read_b64_tr_b16 v[46:47], v26 offset:58600\n\t"
    "v_mfma_f32_16x16x32_bf16 a[44:47], v[52:55], v[188:191], a[44:47]\n\t"
    "s_add_u32 m0, s89, 16448\n\t"
    "s_add_u32 s69, %[sWk], s97\n\t"
    "v_mfma_f32_16x16x32_bf16 a[44:47], v[48:51], v[172:175], a[44:47]\n\t"
    "buffer_load_dwordx4 %[vW0], %[srdW], s69 offen lds\n\t"
    "s_waitcnt lgkmcnt(8)\n\t"
    "v_mfma_f32_16x16x32_bf16 a[48:51], v[56:59], v[160:163], a[48:51]\n\t"
    "ds_read_b64_tr_b16 v[48:49], v27 offset:49344\n\t"
    "v_mfma_f32_16x16x32_bf16 a[48:51], v[60:63], v[176:179], a[48:51]\n\t"
    "ds_read_b64_tr_b16 v[50:51], v27 offset:50368\n\t"
    "v_mfma_f32_16x16x32_bf16 a[52:55], v[60:63], v[180:183], a[52:55]\n\t"
    "v_mfma_f32_16x16x32_bf16 a[52:55], v[56:59], v[164:167], a[52:55]\n\t"
    "buffer_load_dwordx4 v[120:123], %[vA1], %[srdWS], %[sA] offen offset:0 sc1\n\t"
    "v_mfma_f32_16x16x32_bf16 a[56:59], v[56:59], v[168:171], a[56:59]\n\t"
    "ds_read_b64_tr_b16 v[52:53], v27 offset:57568\n\t"
    "v_mfma_f32_16x16x32_bf16 a[56:59], v[60:63], v[184:187], a[56:59]\n\t"
    "ds_read_b64_tr_b16 v[54:55], v27 offset:58592\n\t"
    "v_mfma_f32_16x16x32_bf16 a[60:63], v[60:63], v[188:191], a[60:63]\n\t"
    "s_add_u32 m0, s90, 16448\n\t"
    "s_add_u32 s69, %[sWk], s98\n\t"
    "v_mfma_f32_16x16x32_bf16 a[60:63], v[56:59], v[172:175], a[60:63]\n\t"
    "buffer_load_dwordx4 %[vW0], %[srdW], s69 offen lds\n\t"
    "s_waitcnt lgkmcnt(8)\n\t"
    "v_mfma_f32_16x16x32_bf16 a[64:67], v[32:35], v[160:163], a[64:67]\n\t"
    "ds_read_b64_tr_b16 v[56:57], v27 offset:49352\n\t"
    "v_mfma_f32_16x16x32_bf16 a[64:67], v[36:39], v[176:179], a[64:67]\n\t"
    "ds_read_b64_tr_b16 v[58:59], v27 offset:50376\n\t"
    "v_mfma_f32_16x16x32_bf16 a[68:71], v[36:39], v[180:183], a[68:71]\n\t"
    "v_mfma_f32_16x16x32_bf16 a[68:71], v[32:35], v[164:167], a[68:71]\n\t"
    "buffer_load_dwordx4 v[124:127], %[vA1], %[srdWS], %[sA] offen offset:1024 sc1\n\t"
    "v_mfma_f32_16x16x32_bf16 a[72:75], v[32:35], v[168:171], a[72:75]\n\t"
    "ds_read_b64_tr_b16 v[60:61], v27 offset:57576\n\t"
    "v_mfma_f32_16x16x32_bf16 a[72:75], v[36:39], v[184:187], a[72:75]\n\t"
    "ds_read_b64_tr_b16 v[62:63], v27 offset:58600\n\t"
    "v_mfma_f32_16x16x32_bf16 a[76:79], v[36:39], v[188:191], a[76:79]\n\t"
    "s_add_u32 m0, s91, 16448\n\t"
    "s_add_u32 s69, %[sWk], s99\n\t"
    "v_mfma_f32_16x16x32_bf16 a[76:79], v[32:35], v[172:175], a[76:79]\n\t"
    "buffer_load_dwordx4 %[vW0], %[srdW], s69 offen lds\n\t"
    "s_waitcnt vmcnt(16) lgkmcnt(8)\n\t"
    "s_barrier\n\t"
    "v_mfma_f32_16x16x32_bf16 a[80:83], v[40:43], v[160:163], a[80:83]\n\t"
    "ds_read_b64_tr_b16 v[32:33], %[vRd] offset:0\n\t"
    "v_mfma_f32_16x16x32_bf16 a[80:83], v[44:47], v[176:179], a[80:83]\n\t"
    "ds_read_b64_tr_b16 v[34:35], %[vRd] offset:1024\n\t"
    "v_mfma_f32_16x16x32_bf16 a[84:87], v[44:47], v[180:183], a[84:87]\n\t"
    "s_add_u32 %[sA], %[sA], s55\n\t"
    "v_mfma_f32_16x16x32_bf16 a[84:87], v[40:43], v[164:167], a[84:87]\n\t"
    "buffer_load_dwordx4 v[128:131], %[vA0], %[srdWS], %[sA] offen offset:0 sc1\n\t"
    "v_mfma_f32_16x16x32_bf16 a[88:91], v[40:43], v[168:171], a[88:91]\n\t"
    "ds_read_b64_tr_b16 v[36:37], %[vRd] offset:8224\n\t"
    "v_mfma_f32_16x16x32_bf16 a[88:91], v[44:47], v[184:187], a[88:91]\n\t"
    "ds_read_b64_tr_b16 v[38:39], %[vRd] offset:9248\n\t"
    "v_mfma_f32_16x16x32_bf16 a[92:95], v[44:47], v[188:191], a[92:95]\n\t"
    "s_add_u32 %[sWk], %[sWk], 262144\n\t"
    "s_add_u32 m0, s84, 32896\n\t"
    "s_add_u32 s69, %[sWk], s92\n\t"
    "v_mfma_f32_16x16x32_bf16 a[92:95], v[40:43], v[172:175], a[92:95]\n\t"
    "buffer_load_dwordx4 %[vW0], %[srdW], s69 offen lds\n\t"
    "s_waitcnt lgkmcnt(8)\n\t"
    "v_mfma_f32_16x16x32_bf16 a[96:99], v[48:51], v[160:163], a[96:99]\n\t"
    "ds_read_b64_tr_b16 v[40:41], %[vRd] offset:8\n\t"
    "v_mfma_f32_16x16x32_bf16 a[96:99], v[52:55], v[176:179], a[96:99]\n\t"
    "ds_read_b64_tr_b16 v[42:43], %[vRd] offset:1032\n\t"
    "v_mfma_f32_16x16x32_bf16 a[100:103], v[52:55], v[180:183], a[100:103]\n\t"
    "v_mfma_f32_16x16x32_bf16 a[100:103], v[48:51], v[164:167], a[100:103]\n\t"
    "buffer_load_dwordx4 v[132:135], %[vA0], %[srdWS], %[sA] offen offset:1024 sc1\n\t"
    "v_mfma_f32_16x16x32_bf16 a[104:107], v[48:51], v[168:171], a[104:107]\n\t"
    "ds_read_b64_tr_b16 v[44:45], %[vRd] offset:8232\n\t"
    "v_mfma_f32_16x16x32_bf16 a[104:107], v[52:55], v[184:187], a[104:107]\n\t"
    "ds_read_b64_tr_b16 v[46:47], %[vRd] offset:9256\n\t"
    "v_mfma_f32_16x16x32_bf16 a[108:111], v[52:55], v[188:191], a[108:111]\n\t"
    "s_add_u32 m0, s85, 32896\n\t"
    "s_add_u32 s69, %[sWk], s93\n\t"
    "v_mfma_f32_16x16x32_bf16 a[108:111], v[48:51], v[172:175], a[108:111]\n\t"
    "buffer_load_dwordx4 %[vW0], %[srdW], s69 offen lds\n\t"
    "s_waitcnt lgkmcnt(8)\n\t"
    "v_mfma_f32_16x16x32_bf16 a[112:115], v[56:59], v[160:163], a[112:115]\n\t"
    "ds_read_b64_tr_b16 v[48:49], v25 offset:0\n\t"
    "v_mfma_f32_16x16x32_bf16 a[112:115], v[60:63], v[176:179], a[112:115]\n\t"
    "ds_read_b64_tr_b16 v[50:51], v25 offset:1024\n\t"
    "v_mfma_f32_16x16x32_bf16 a[116:119], v[60:63], v[180:183], a[116:119]\n\t"
    "v_mfma_f32_16x16x32_bf16 a[116:119], v[56:59], v[164:167], a[116:119]\n\t"
    "buffer_load_dwordx4 v[136:139], %[vA1], %[srdWS], %[sA] offen offset:0 sc1\n\t"
    "v_mfma_f32_16x16x32_bf16 a[120:123], v[56:59], v[168:171], a[120:123]\n\t"
    "ds_read_b64_tr_b16 v[52:53], v25 offset:8224\n\t"
    "v_mfma_f32_16x16x32_bf16 a[120:123], v[60:63], v[184:187], a[120:123]\n\t"
    "ds_read_b64_tr_b16 v[54:55], v25 offset:9248\n\t"
    "v_mfma_f32_16x16x32_bf16 a[124:127], v[60:63], v[188:191], a[124:127]\n\t"
    "s_add_u32 m0, s86, 32896\n\t"
    "s_add_u32 s69, %[sWk], s94\n\t"
    "v_mfma_f32_16x16x32_bf16 a[124:127], v[56:59], v[172:175], a[124:127]\n\t"
    "buffer_load_dwordx4 %[vW0], %[srdW], s69 offen lds\n\t"
    "s_sub_u32 %[sIt], %[sIt], 1\n\t"
    "s_cmp_lg_u32 %[sIt], 0\n\t"
    "s_cbranch_scc1 L_k2_loop_%=\n\t"
    "s_waitcnt vmcnt(23) lgkmcnt(8)\n\t"
    "v_mfma_f32_16x16x32_bf16 a[0:3], v[32:35], v[64:67], a[0:3]\n\t"
    "ds_read_b64_tr_b16 v[56:57], v25 offset:8\n\t"
    "v_mfma_f32_16x16x32_bf16 a[0:3], v[36:39], v[80:83], a[0:3]\n\t"
    "ds_read_b64_tr_b16 v[58:59], v25 offset:1032\n\t"
    "v_mfma_f32_16x16x32_bf16 a[4:7], v[36:39], v[84:87], a[4:7]\n\t"
    "v_mfma_f32_16x16x32_bf16 a[4:7], v[32:35], v[68:71], a[4:7]\n\t"
    "buffer_load_dwordx4 v[140:143], %[vA1], %[srdWS], %[sA] offen offset:1024 sc1\n\t"
    "v_mfma_f32_16x16x32_bf16 a[8:11], v[32:35], v[72:75], a[8:11]\n\t"
    "ds_read_b64_tr_b16 v[60:61], v25 offset:8232\n\t"
    "v_mfma_f32_16x16x32_bf16 a[8:11], v[36:39], v[88:91], a[8:11]\n\t"
    "ds_read_b64_tr_b16 v[62:63], v25 offset:9256\n\t"
    "v_mfma_f32_16x16x32_bf16 a[12:15], v[36:39], v[92:95], a[12:15]\n\t"
    "s_add_u32 m0, s87, 32896\n\t"
    "s_add_u32 s69, %[sWk], s95\n\t"
    "v_mfma_f32_16x16x32_bf16 a[12:15], v[32:35], v[76:79], a[12:15]\n\t"
    "buffer_load_dwordx4 %[vW0], %[srdW], s69 offen lds\n\t"
    "s_waitcnt lgkmcnt(8)\n\t"
    "v_mfma_f32_16x16x32_bf16 a[16:19], v[40:43], v[64:67], a[16:19]\n\t"
    "ds_read_b64_tr_b16 v[32:33], v26 offset:0\n\t"
    "v_mfma_f32_16x16x32_bf16 a[16:19], v[44:47], v[80:83], a[16:19]\n\t"
    "ds_read_b64_tr_b16 v[34:35], v26 offset:1024\n\t"
    "v_mfma_f32_16x16x32_bf16 a[20:23], v[44:47], v[84:87], a[20:23]\n\t"
    "s_add_u32 %[sA], %[sA], %[sAi1]\n\t"
    "v_mfma_f32_16x16x32_bf16 a[20:23], v[40:43], v[68:71], a[20:23]\n\t"
    "buffer_load_dwordx4 v[144:147], %[vA0], %[srdWS], %[sA] offen offset:0 sc1\n\t"
    "v_mfma_f32_16x16x32_bf16 a[24:27], v[40:43], v[72:75], a[24:27]\n\t"
    "ds_read_b64_tr_b16 v[36:37], v26 offset:8224\n\t"
    "v_mfma_f32_16x16x32_bf16 a[24:27], v[44:47], v[88:91], a[24:27]\n\t"
    "ds_read_b64_tr_b16 v[38:39], v26 offset:9248\n\t"
    "v_mfma_f32_16x16x32_bf16 a[28:31], v[44:47], v[92:95], a[28:31]\n\t"
    "s_add_u32 m0, s88, 32896\n\t"
    "s_add_u32 s69, %[sWk], s96\n\t"
    "v_mfma_f32_16x16x32_bf16 a[28:31], v[40:43], v[76:79], a[28:31]\n\t"
    "buffer_load_dwordx4 %[vW0], %[srdW], s69 offen lds\n\t"
    "s_waitcnt lgkmcnt(8)\n\t"
    "v_mfma_f32_16x16x32_bf16 a[32:35], v[48:51], v[64:67], a[32:35]\n\t"
    "ds_read_b64_tr_b16 v[40:41], v26 offset:8\n\t"
    "v_mfma_f32_16x16x32_bf16 a[32:35], v[52:55], v[80:83], a[32:35]\n\t"
    "ds_read_b64_tr_b16 v[42:43], v26 offset:1032\n\t"
    "v_mfma_f32_16x16x32_bf16 a[36:39], v[52:55], v[84:87], a[36:39]\n\t"
    "v_mfma_f32_16x16x32_bf16 a[36:39], v[48:51], v[68:71], a[36:39]\n\t"
    "buffer_load_dwordx4 v[148:151], %[vA0], %[srdWS], %[sA] offen offset:1024 sc1\n\t"
    "v_mfma_f32_16x16x32_bf16 a[40:43], v[48:51], v[72:75], a[40:43]\n\t"
    "ds_read_b64_tr_b16 v[44:45], v26 offset:8232\n\t"
    "v_mfma_f32_16x16x32_bf16 a[40:43], v[52:55], v[88:91], a[40:43]\n\t"
    "ds_read_b64_tr_b16 v[46:47], v26 offset:9256\n\t"
    "v_mfma_f32_16x16x32_bf16 a[44:47], v[52:55], v[92:95], a[44:47]\n\t"
    "s_add_u32 m0, s89, 32896\n\t"
    "s_add_u32 s69, %[sWk], s97\n\t"
    "v_mfma_f32_16x16x32_bf16 a[44:47], v[48:51], v[76:79], a[44:47]\n\t"
    "buffer_load_dwordx4 %[vW0], %[srdW], s69 offen lds\n\t"
    "s_waitcnt lgkmcnt(8)\n\t"
    "v_mfma_f32_16x16x32_bf16 a[48:51], v[56:59], v[64:67], a[48:51]\n\t"
    "ds_read_b64_tr_b16 v[48:49], v27 offset:0\n\t"
    "v_mfma_f32_16x16x32_bf16 a[48:51], v[60:63], v[80:83], a[48:51]\n\t"
    "ds_read_b64_tr_b16 v[50:51], v27 offset:1024\n\t"
    "v_mfma_f32_16x16x32_bf16 a[52:55], v[60:63], v[84:87], a[52:55]\n\t"
    "v_mfma_f32_16x16x32_bf16 a[52:55], v[56:59], v[68:71], a[52:55]\n\t"
    "buffer_load_dwordx4 v[152:155], %[vA1], %[srdWS], %[sA] offen offset:0 sc1\n\t"
    "v_mfma_f32_16x16x32_bf16 a[56:59], v[56:59], v[72:75], a[56:59]\n\t"
    "ds_read_b64_tr_b16 v[52:53], v27 offset:8224\n\t"
    "v_mfma_f32_16x16x32_bf16 a[56:59], v[60:63], v[88:91], a[56:59]\n\t"
    "ds_read_b64_tr_b16 v[54:55], v27 offset:9248\n\t"
    "v_mfma_f32_16x16x32_bf16 a[60:63], v[60:63], v[92:95], a[60:63]\n\t"
    "s_add_u32 m0, s90, 32896\n\t"
    "s_add_u32 s69, %[sWk], s98\n\t"
    "v_mfma_f32_16x16x32_bf16 a[60:63], v[56:59], v[76:79], a[60:63]\n\t"
    "buffer_load_dwordx4 %[vW0], %[srdW], s69 offen lds\n\t"
    "s_waitcnt lgkmcnt(8)\n\t"
    "v_mfma_f32_16x16x32_bf16 a[64:67], v[32:35], v[64:67], a[64:67]\n\t"
    "ds_read_b64_tr_b16 v[56:57], v27 offset:8\n\t"
    "v_mfma_f32_16x16x32_bf16 a[64:67], v[36:39], v[80:83], a[64:67]\n\t"
    "ds_read_b64_tr_b16 v[58:59], v27 offset:1032\n\t"
    "v_mfma_f32_16x16x32_bf16 a[68:71], v[36:39], v[84:87], a[68:71]\n\t"
    "v_mfma_f32_16x16x32_bf16 a[68:71], v[32:35], v[68:71], a[68:71]\n\t"
    "buffer_load_dwordx4 v[156:159], %[vA1], %[srdWS], %[sA] offen offset:1024 sc1\n\t"
    "v_mfma_f32_16x16x32_bf16 a[72:75], v[32:35], v[72:75], a[72:75]\n\t"
    "ds_read_b64_tr_b16 v[60:61], v27 offset:8232\n\t"
    "v_mfma_f32_16x16x32_bf16 a[72:75], v[36:39], v[88:91], a[72:75]\n\t"
    "ds_read_b64_tr_b16 v[62:63], v27 offset:9256\n\t"
    "v_mfma_f32_16x16x32_bf16 a[76:79], v[36:39], v[92:95], a[76:79]\n\t"
    "s_add_u32 m0, s91, 32896\n\t"
    "s_add_u32 s69, %[sWk], s99\n\t"
    "v_mfma_f32_16x16x32_bf16 a[76:79], v[32:35], v[76:79], a[76:79]\n\t"
    "buffer_load_dwordx4 %[vW0], %[srdW], s69 offen lds\n\t"
    "s_waitcnt vmcnt(16) lgkmcnt(8)\n\t"
    "s_barrier\n\t"
    "v_mfma_f32_16x16x32_bf16 a[80:83], v[40:43], v[64:67], a[80:83]\n\t"
    "ds_read_b64_tr_b16 v[32:33], %[vRd] offset:16448\n\t"
    "v_mfma_f32_16x16x32_bf16 a[80:83], v[44:47], v[80:83], a[80:83]\n\t"
    "ds_read_b64_tr_b16 v[34:35], %[vRd] offset:17472\n\t"
    "v_mfma_f32_16x16x32_bf16 a[84:87], v[44:47], v[84:87], a[84:87]\n\t"
    "s_add_u32 %[sA], %[sA], s55\n\t"
    "v_mfma_f32_16x16x32_bf16 a[84:87], v[40:43], v[68:71], a[84:87]\n\t"
    "buffer_load_dwordx4 v[160:163], %[vA0], %[srdWS], %[sA] offen offset:0 sc1\n\t"
    "v_mfma_f32_16x16x32_bf16 a[88:91], v[40:43], v[72:75], a[88:91]\n\t"
    "ds_read_b64_tr_b16 v[36:37], %[vRd] offset:24672\n\t"
    "v_mfma_f32_16x16x32_bf16 a[88:91], v[44:47], v[88:91], a[88:91]\n\t"
    "ds_read_b64_tr_b16 v[38:39], %[vRd] offset:25696\n\t"
    "v_mfma_f32_16x16x32_bf16 a[92:95], v[44:47], v[92:95], a[92:95]\n\t"
    "s_add_u32 %[sWk], %[sWk], 262144\n\t"
    "s_add_u32 m0, s84, 49344\n\t"
    "s_add_u32 s69, %[sWk], s92\n\t"
    "v_mfma_f32_16x16x32_bf16 a[92:95], v[40:43], v[76:79], a[92:95]\n\t"
    "buffer_load_dwordx4 %[vW0], %[srdW], s69 offen lds\n\t"
    "s_waitcnt lgkmcnt(8)\n\t"
    "v_mfma_f32_16x16x32_bf16 a[96:99], v[48:51], v[64:67], a[96:99]\n\t"
    "ds_read_b64_tr_b16 v[40:41], %[vRd] offset:16456\n\t"
    "v_mfma_f32_16x16x32_bf16 a[96:99], v[52:55], v[80:83], a[96:99]\n\t"
    "ds_read_b64_tr_b16 v[42:43], %[vRd] offset:17480\n\t"
    "v_mfma_f32_16x16x32_bf16 a[100:103], v[52:55], v[84:87], a[100:103]\n\t"
    "v_mfma_f32_16x16x32_bf16 a[100:103], v[48:51], v[68:71], a[100:103]\n\t"
    "buffer_load_dwordx4 v[164:167], %[vA0], %[srdWS], %[sA] offen offset:1024 sc1\n\t"
    "v_mfma_f32_16x16x32_bf16 a[104:107], v[48:51], v[72:75], a[104:107]\n\t"
    "ds_read_b64_tr_b16 v[44:45], %[vRd] offset:24680\n\t"
    "v_mfma_f32_16x16x32_bf16 a[104:107], v[52:55], v[88:91], a[104:107]\n\t"
    "ds_read_b64_tr_b16 v[46:47], %[vRd] offset:25704\n\t"
    "v_mfma_f32_16x16x32_bf16 a[108:111], v[52:55], v[92:95], a[108:111]\n\t"
    "s_add_u32 m0, s85, 49344\n\t"
    "s_add_u32 s69, %[sWk], s93\n\t"
    "v_mfma_f32_16x16x32_bf16 a[108:111], v[48:51], v[76:79], a[108:111]\n\t"
    "buffer_load_dwordx4 %[vW0], %[srdW], s69 offen lds\n\t"
    "s_waitcnt lgkmcnt(8)\n\t"
    "v_mfma_f32_16x16x32_bf16 a[112:115], v[56:59], v[64:67], a[112:115]\n\t"
    "ds_read_b64_tr_b16 v[48:49], v25 offset:16448\n\t"
    "v_mfma_f32_16x16x32_bf16 a[112:115], v[60:63], v[80:83], a[112:115]\n\t"
    "ds_read_b64_tr_b16 v[50:51], v25 offset:17472\n\t"
    "v_mfma_f32_16x16x32_bf16 a[116:119], v[60:63], v[84:87], a[116:119]\n\t"
    "v_mfma_f32_16x16x32_bf16 a[116:119], v[56:59], v[68:71], a[116:119]\n\t"
    "buffer_load_dwordx4 v[168:171], %[vA1], %[srdWS], %[sA] offen offset:0 sc1\n\t"
    "v_mfma_f32_16x16x32_bf16 a[120:123], v[56:59], v[72:75], a[120:123]\n\t"
    "ds_read_b64_tr_b16 v[52:53], v25 offset:24672\n\t"
    "v_mfma_f32_16x16x32_bf16 a[120:123], v[60:63], v[88:91], a[120:123]\n\t"
    "ds_read_b64_tr_b16 v[54:55], v25 offset:25696\n\t"
    "v_mfma_f32_16x16x32_bf16 a[124:127], v[60:63], v[92:95], a[124:127]\n\t"
    "s_add_u32 m0, s86, 49344\n\t"
    "s_add_u32 s69, %[sWk], s94\n\t"
    "v_mfma_f32_16x16x32_bf16 a[124:127], v[56:59], v[76:79], a[124:127]\n\t"
    "buffer_load_dwordx4 %[vW0], %[srdW], s69 offen lds\n\t"
    "s_waitcnt vmcnt(23) lgkmcnt(8)\n\t"
    "v_mfma_f32_16x16x32_bf16 a[0:3], v[32:35], v[96:99], a[0:3]\n\t"
    "ds_read_b64_tr_b16 v[56:57], v25 offset:16456\n\t"
    "v_mfma_f32_16x16x32_bf16 a[0:3], v[36:39], v[112:115], a[0:3]\n\t"
    "ds_read_b64_tr_b16 v[58:59], v25 offset:17480\n\t"
    "v_mfma_f32_16x16x32_bf16 a[4:7], v[36:39], v[116:119], a[4:7]\n\t"
    "v_mfma_f32_16x16x32_bf16 a[4:7], v[32:35], v[100:103], a[4:7]\n\t"
    "buffer_load_dwordx4 v[172:175], %[vA1], %[srdWS], %[sA] offen offset:1024 sc1\n\t"
    "v_mfma_f32_16x16x32_bf16 a[8:11], v[32:35], v[104:107], a[8:11]\n\t"
    "ds_read_b64_tr_b16 v[60:61], v25 offset:24680\n\t"
    "v_mfma_f32_16x16x32_bf16 a[8:11], v[36:39], v[120:123], a[8:11]\n\t"
    "ds_read_b64_tr_b16 v[62:63], v25 offset:25704\n\t"
    "v_mfma_f32_16x16x32_bf16 a[12:15], v[36:39], v[124:127], a[12:15]\n\t"
    "s_add_u32 m0, s87, 49344\n\t"
    "s_add_u32 s69, %[sWk], s95\n\t"
    "k_pgb2: s_cbranch_execz k_pgb_back\n\t"
    "v_mfma_f32_16x16x32_bf16 a[12:15], v[32:35], v[108:111], a[12:15]\n\t"
    "buffer_load_dwordx4 %[vW0], %[srdW], s69 offen lds\n\t"
    "s_waitcnt lgkmcnt(8)\n\t"
    "v_mfma_f32_16x16x32_bf16 a[16:19], v[40:43], v[96:99], a[16:19]\n\t"
    "ds_read_b64_tr_b16 v[32:33], v26 offset:16448\n\t"
    "v_mfma_f32_16x16x32_bf16 a[16:19], v[44:47], v[112:115], a[16:19]\n\t"
    "ds_read_b64_tr_b16 v[34:35], v26 offset:17472\n\t"
    "v_mfma_f32_16x16x32_bf16 a[20:23], v[44:47], v[116:119], a[20:23]\n\t"
    "s_add_u32 %[sA], %[sA], %[sAi1]\n\t"
    "v_mfma_f32_16x16x32_bf16 a[20:23], v[40:43], v[100:103], a[20:23]\n\t"
    "buffer_load_dwordx4 v[176:179], %[vA0], %[srdWS], %[sA] offen offset:0 sc1\n\t"
    "v_mfma_f32_16x16x32_bf16 a[24:27], v[40:43], v[104:107], a[24:27]\n\t"
    "ds_read_b64_tr_b16 v[36:37], v26 offset:24672\n\t"
    "v_mfma_f32_16x16x32_bf16 a[24:27], v[44:47], v[120:123], a[24:27]\n\t"
    "ds_read_b64_tr_b16 v[38:39], v26 offset:25696\n\t"
    "v_mfma_f32_16x16x32_bf16 a[28:31], v[44:47], v[124:127], a[28:31]\n\t"
    "s_add_u32 m0, s88, 49344\n\t"
    "s_add_u32 s69, %[sWk], s96\n\t"
    "v_mfma_f32_16x16x32_bf16 a[28:31], v[40:43], v[108:111], a[28:31]\n\t"
    "buffer_load_dwordx4 %[vW0], %[srdW], s69 offen lds\n\t"
    "s_waitcnt lgkmcnt(8)\n\t"
    "v_mfma_f32_16x16x32_bf16 a[32:35], v[48:51], v[96:99], a[32:35]\n\t"
    "ds_read_b64_tr_b16 v[40:41], v26 offset:16456\n\t"
    "v_mfma_f32_16x16x32_bf16 a[32:35], v[52:55], v[112:115], a[32:35]\n\t"
    "ds_read_b64_tr_b16 v[42:43], v26 offset:17480\n\t"
    "v_mfma_f32_16x16x32_bf16 a[36:39], v[52:55], v[116:119], a[36:39]\n\t"
    "v_mfma_f32_16x16x32_bf16 a[36:39], v[48:51], v[100:103], a[36:39]\n\t"
    "buffer_load_dwordx4 v[180:183], %[vA0], %[srdWS], %[sA] offen offset:1024 sc1\n\t"
    "v_mfma_f32_16x16x32_bf16 a[40:43], v[48:51], v[104:107], a[40:43]\n\t"
    "ds_read_b64_tr_b16 v[44:45], v26 offset:24680\n\t"
    "v_mfma_f32_16x16x32_bf16 a[40:43], v[52:55], v[120:123], a[40:43]\n\t"
    "ds_read_b64_tr_b16 v[46:47], v26 offset:25704\n\t"
    "v_mfma_f32_16x16x32_bf16 a[44:47], v[52:55], v[124:127], a[44:47]\n\t"
    "s_add_u32 m0, s89, 49344\n\t"
    "s_add_u32 s69, %[sWk], s97\n\t"
    "v_mfma_f32_16x16x32_bf16 a[44:47], v[48:51], v[108:111], a[44:47]\n\t"
    "buffer_load_dwordx4 %[vW0], %[srdW], s69 offen lds\n\t"
    "s_waitcnt lgkmcnt(8)\n\t"
    "v_mfma_f32_16x16x32_bf16 a[48:51], v[56:59], v[96:99], a[48:51]\n\t"
    "ds_read_b64_tr_b16 v[48:49], v27 offset:16448\n\t"
    "v_mfma_f32_16x16x32_bf16 a[48:51], v[60:63], v[112:115], a[48:51]\n\t"
    "ds_read_b64_tr_b16 v[50:51], v27 offset:17472\n\t"
    "v_mfma_f32_16x16x32_bf16 a[52:55], v[60:63], v[116:119], a[52:55]\n\t"
    "v_mfma_f32_16x16x32_bf16 a[52:55], v[56:59], v[100:103], a[52:55]\n\t"
    "buffer_load_dwordx4 v[184:187], %[vA1], %[srdWS], %[sA] offen offset:0 sc1\n\t"
    "v_mfma_f32_16x16x32_bf16 a[56:59], v[56:59], v[104:107], a[56:59]\n\t"
    "ds_read_b64_tr_b16 v[52:53], v27 offset:24672\n\t"
    "v_mfma_f32_16x16x32_bf16 a[56:59], v[60:63], v[120:123], a[56:59]\n\t"
    "ds_read_b64_tr_b16 v[54:55], v27 offset:25696\n\t"
    "v_mfma_f32_16x16x32_bf16 a[60:63], v[60:63], v[124:127], a[60:63]\n\t"
    "s_add_u32 m0, s90, 49344\n\t"
    "s_add_u32 s69, %[sWk], s98\n\t"
    "v_mfma_f32_16x16x32_bf16 a[60:63], v[56:59], v[108:111], a[60:63]\n\t"
    "buffer_load_dwordx4 %[vW0], %[srdW], s69 offen lds\n\t"
    "s_waitcnt lgkmcnt(8)\n\t"
    "v_mfma_f32_16x16x32_bf16 a[64:67], v[32:35], v[96:99], a[64:67]\n\t"
    "ds_read_b64_tr_b16 v[56:57], v27 offset:16456\n\t"
    "v_mfma_f32_16x16x32_bf16 a[64:67], v[36:39], v[112:115], a[64:67]\n\t"
    "ds_read_b64_tr_b16 v[58:59], v27 offset:17480\n\t"
    "v_mfma_f32_16x16x32_bf16 a[68:71], v[36:39], v[116:119], a[68:71]\n\t"
    "v_mfma_f32_16x16x32_bf16 a[68:71], v[32:35], v[100:103], a[68:71]\n\t"
    "buffer_load_dwordx4 v[188:191], %[vA1], %[srdWS], %[sA] offen offset:1024 sc1\n\t"
    "v_mfma_f32_16x16x32_bf16 a[72:75], v[32:35], v[104:107], a[72:75]\n\t"
    "ds_read_b64_tr_b16 v[60:61], v27 offset:24680\n\t"
    "v_mfma_f32_16x16x32_bf16 a[72:75], v[36:39], v[120:123], a[72:75]\n\t"
    "ds_read_b64_tr_b16 v[62:63], v27 offset:25704\n\t"
    "v_mfma_f32_16x16x32_bf16 a[76:79], v[36:39], v[124:127], a[76:79]\n\t"
    "s_add_u32 m0, s91, 49344\n\t"
    "s_add_u32 s69, %[sWk], s99\n\t"
    "v_mfma_f32_16x16x32_bf16 a[76:79], v[32:35], v[108:111], a[76:79]\n\t"
    "buffer_load_dwordx4 %[vW0], %[srdW], s69 offen lds\n\t"
    "s_waitcnt vmcnt(16) lgkmcnt(8)\n\t"
    "s_barrier\n\t"
    "v_mfma_f32_16x16x32_bf16 a[80:83], v[40:43], v[96:99], a[80:83]\n\t"
    "ds_read_b64_tr_b16 v[32:33], %[vRd] offset:32896\n\t"
    "s_add_u32 m0, %[sXl], 0\n\t"
    "s_add_u32 s69, %[sXr], 0\n\t"
    "v_mfma_f32_16x16x32_bf16 a[80:83], v[44:47], v[112:115], a[80:83]\n\t"
    "ds_read_b64_tr_b16 v[34:35], %[vRd] offset:33920\n\t"
    "buffer_load_dwordx4 %[vW0], %[srdX], s69 offen lds\n\t"
    "v_mfma_f32_16x16x32_bf16 a[84:87], v[44:47], v[116:119], a[84:87]\n\t"
    "s_add_u32 m0, %[sXl], 1024\n\t"
    "s_add_u32 s69, %[sXr], 8192\n\t"
    "v_mfma_f32_16x16x32_bf16 a[84:87], v[40:43], v[100:103], a[84:87]\n\t"
    "v_xor_b32 v31, 64, %[vW0]\n\t"
    "v_mfma_f32_16x16x32_bf16 a[88:91], v[40:43], v[104:107], a[88:91]\n\t"
    "ds_read_b64_tr_b16 v[36:37], %[vRd] offset:41120\n\t"
    "v_mfma_f32_16x16x32_bf16 a[88:91], v[44:47], v[120:123], a[88:91]\n\t"
    "ds_read_b64_tr_b16 v[38:39], %[vRd] offset:42144\n\t"
    "buffer_load_dwordx4 v31, %[srdX], s69 offen lds\n\t"
    "v_mfma_f32_16x16x32_bf16 a[92:95], v[44:47], v[124:127], a[92:95]\n\t"
    "s_add_u32 m0, %[sXl], 2048\n\t"
    "s_add_u32 s69, %[sXr], 16384\n\t"
    "v_mfma_f32_16x16x32_bf16 a[92:95], v[40:43], v[108:111], a[92:95]\n\t"
    "v_xor_b32 v31, 128, %[vW0]\n\t"
    "s_waitcnt lgkmcnt(8)\n\t"
    "v_mfma_f32_16x16x32_bf16 a[96:99], v[48:51], v[96:99], a[96:99]\n\t"
    "ds_read_b64_tr_b16 v[40:41], %[vRd] offset:32904\n\t"
    "v_mfma_f32_16x16x32_bf16 a[96:99], v[52:55], v[112:115], a[96:99]\n\t"
    "ds_read_b64_tr_b16 v[42:43], %[vRd] offset:33928\n\t"
    "buffer_load_dwordx4 v31, %[srdX], s69 offen lds\n\t"
    "v_mfma_f32_16x16x32_bf16 a[100:103], v[52:55], v[116:119], a[100:103]\n\t"
    "s_add_u32 m0, %[sXl], 3072\n\t"
    "s_add_u32 s69, %[sXr], 24576\n\t"
    "v_mfma_f32_16x16x32_bf16 a[100:103], v[48:51], v[100:103], a[100:103]\n\t"
    "v_xor_b32 v31, 192, %[vW0]\n\t"
    "v_mfma_f32_16x16x32_bf16 a[104:107], v[48:51], v[104:107], a[104:107]\n\t"
    "ds_read_b64_tr_b16 v[44:45], %[vRd] offset:41128\n\t"
    "v_mfma_f32_16x16x32_bf16 a[104:107], v[52:55], v[120:123], a[104:107]\n\t"
    "ds_read_b64_tr_b16 v[46:47], %[vRd] offset:42152\n\t"
    "buffer_load_dwordx4 v31, %[srdX], s69 offen lds\n\t"
    "v_mfma_f32_16x16x32_bf16 a[108:111], v[52:55], v[124:127], a[108:111]\n\t"
    "s_add_u32 m0, %[sXl], 4096\n\t"
    "s_add_u32 s69, %[sXr], 32768\n\t"
    "v_mfma_f32_16x16x32_bf16 a[108:111], v[48:51], v[108:111], a[108:111]\n\t"
    "s_waitcnt lgkmcnt(8)\n\t"
    "v_mfma_f32_16x16x32_bf16 a[112:115], v[56:59], v[96:99], a[112:115]\n\t"
    "ds_read_b64_tr_b16 v[48:49], v25 offset:32896\n\t"
    "v_mfma_f32_16x16x32_bf16 a[112:115], v[60:63], v[112:115], a[112:115]\n\t"
    "ds_read_b64_tr_b16 v[50:51], v25 offset:33920\n\t"
    "buffer_load_dwordx4 %[vW0], %[srdX], s69 offen lds\n\t"
    "v_mfma_f32_16x16x32_bf16 a[116:119], v[60:63], v[116:119], a[116:119]\n\t"
    "s_add_u32 m0, %[sXl], 5120\n\t"
    "s_add_u32 s69, %[sXr], 40960\n\t"
    "v_mfma_f32_16x16x32_bf16 a[116:119], v[56:59], v[100:103], a[116:119]\n\t"
    "v_xor_b32 v31, 64, %[vW0]\n\t"
    "v_mfma_f32_16x16x32_bf16 a[120:123], v[56:59], v[104:107], a[120:123]\n\t"
    "ds_read_b64_tr_b16 v[52:53], v25 offset:41120\n\t"
    "v_mfma_f32_16x16x32_bf16 a[120:123], v[60:63], v[120:123], a[120:123]\n\t"
    "ds_read_b64_tr_b16 v[54:55], v25 offset:42144\n\t"
    "buffer_load_dwordx4 v31, %[srdX], s69 offen lds\n\t"
    "v_mfma_f32_16x16x32_bf16 a[124:127], v[60:63], v[124:127], a[124:127]\n\t"
    "s_add_u32 m0, %[sXl], 6144\n\t"
    "s_add_u32 s69, %[sXr], 49152\n\t"
    "v_mfma_f32_16x16x32_bf16 a[124:127], v[56:59], v[108:111], a[124:127]\n\t"
    "v_xor_b32 v31, 128, %[vW0]\n\t"
    "s_waitcnt vmcnt(23) lgkmcnt(8)\n\t"
    "v_mfma_f32_16x16x32_bf16 a[0:3], v[32:35], v[128:131], a[0:3]\n\t"
    "ds_read_b64_tr_b16 v[56:57], v25 offset:32904\n\t"
    "v_mfma_f32_16x16x32_bf16 a[0:3], v[36:39], v[144:147], a[0:3]\n\t"
    "ds_read_b64_tr_b16 v[58:59], v25 offset:33928\n\t"
    "buffer_load_dwordx4 v31, %[srdX], s69 offen lds\n\t"
    "v_mfma_f32_16x16x32_bf16 a[4:7], v[36:39], v[148:151], a[4:7]\n\t"
    "s_add_u32 m0, %[sXl], 7168\n\t"
    "s_add_u32 s69, %[sXr], 57344\n\t"
    "v_mfma_f32_16x16x32_bf16 a[4:7], v[32:35], v[132:135], a[4:7]\n\t"
    "v_xor_b32 v31, 192, %[vW0]\n\t"
    "v_mfma_f32_16x16x32_bf16 a[8:11], v[32:35], v[136:139], a[8:11]\n\t"
    "ds_read_b64_tr_b16 v[60:61], v25 offset:41128\n\t"
    "v_mfma_f32_16x16x32_bf16 a[8:11], v[36:39], v[152:155], a[8:11]\n\t"
    "ds_read_b64_tr_b16 v[62:63], v25 offset:42152\n\t"
    "buffer_load_dwordx4 v31, %[srdX], s69 offen lds\n\t"
    "v_mfma_f32_16x16x32_bf16 a[12:15], v[36:39], v[156:159], a[12:15]\n\t"
    "v_mfma_f32_16x16x32_bf16 a[12:15], v[32:35], v[140:143], a[12:15]\n\t"
    "s_waitcnt lgkmcnt(8)\n\t"
    "v_mfma_f32_16x16x32_bf16 a[16:19], v[40:43], v[128:131], a[16:19]\n\t"
    "ds_read_b64_tr_b16 v[32:33], v26 offset:32896\n\t"
    "v_mfma_f32_16x16x32_bf16 a[16:19], v[44:47], v[144:147], a[16:19]\n\t"
    "ds_read_b64_tr_b16 v[34:35], v26 offset:33920\n\t"
    "v_mfma_f32_16x16x32_bf16 a[20:23], v[44:47], v[148:151], a[20:23]\n\t"
    "v_mfma_f32_16x16x32_bf16 a[20:23], v[40:43], v[132:135], a[20:23]\n\t"
    "v_mfma_f32_16x16x32_bf16 a[24:27], v[40:43], v[136:139], a[24:27]\n\t"
    "ds_read_b64_tr_b16 v[36:37], v26 offset:41120\n\t"
    "v_mfma_f32_16x16x32_bf16 a[24:27], v[44:47], v[152:155], a[24:27]\n\t"
    "ds_read_b64_tr_b16 v[38:39], v26 offset:42144\n\t"
    "v_mfma_f32_16x16x32_bf16 a[28:31], v[44:47], v[156:159], a[28:31]\n\t"
    "v_mfma_f32_16x16x32_bf16 a[28:31], v[40:43], v[140:143], a[28:31]\n\t"
    "s_waitcnt lgkmcnt(8)\n\t"
    "v_mfma_f32_16x16x32_bf16 a[32:35], v[48:51], v[128:131], a[32:35]\n\t"
    "ds_read_b64_tr_b16 v[40:41], v26 offset:32904\n\t"
    "v_mfma_f32_16x16x32_bf16 a[32:35], v[52:55], v[144:147], a[32:35]\n\t"
    "ds_read_b64_tr_b16 v[42:43], v26 offset:33928\n\t"
    "v_mfma_f32_16x16x32_bf16 a[36:39], v[52:55], v[148:151], a[36:39]\n\t"
    "v_mfma_f32_16x16x32_bf16 a[36:39], v[48:51], v[132:135], a[36:39]\n\t"
    "v_mfma_f32_16x16x32_bf16 a[40:43], v[48:51], v[136:139], a[40:43]\n\t"
    "ds_read_b64_tr_b16 v[44:45], v26 offset:41128\n\t"
    "v_mfma_f32_16x16x32_bf16 a[40:43], v[52:55], v[152:155], a[40:43]\n\t"
    "ds_read_b64_tr_b16 v[46:47], v26 offset:42152\n\t"
    "v_mfma_f32_16x16x32_bf16 a[44:47], v[52:55], v[156:159], a[44:47]\n\t"
    "v_mfma_f32_16x16x32_bf16 a[44:47], v[48:51], v[140:143], a[44:47]\n\t"
    "s_waitcnt lgkmcnt(8)\n\t"
    "v_mfma_f32_16x16x32_bf16 a[48:51], v[56:59], v[128:131], a[48:51]\n\t"
    "ds_read_b64_tr_b16 v[48:49], v27 offset:32896\n\t"
    "v_mfma_f32_16x16x32_bf16 a[48:51], v[60:63], v[144:147], a[48:51]\n\t"
    "ds_read_b64_tr_b16 v[50:51], v27 offset:33920\n\t"
    "v_mfma_f32_16x16x32_bf16 a[52:55], v[60:63], v[148:151], a[52:55]\n\t"
    "v_mfma_f32_16x16x32_bf16 a[52:55], v[56:59], v[132:135], a[52:55]\n\t"
    "v_mfma_f32_16x16x32_bf16 a[56:59], v[56:59], v[136:139], a[56:59]\n\t"
    "ds_read_b64_tr_b16 v[52:53], v27 offset:41120\n\t"
    "v_mfma_f32_16x16x32_bf16 a[56:59], v[60:63], v[152:155], a[56:59]\n\t"
    "ds_read_b64_tr_b16 v[54:55], v27 offset:42144\n\t"
    "v_mfma_f32_16x16x32_bf16 a[60:63], v[60:63], v[156:159], a[60:63]\n\t"
    "v_mfma_f32_16x16x32_bf16 a[60:63], v[56:59], v[140:143], a[60:63]\n\t"
    "s_waitcnt lgkmcnt(8)\n\t"
    "v_mfma_f32_16x16x32_bf16 a[64:67], v[32:35], v[128:131], a[64:67]\n\t"
    "ds_read_b64_tr_b16 v[56:57], v27 offset:32904\n\t"
    "v_mfma_f32_16x16x32_bf16 a[64:67], v[36:39], v[144:147], a[64:67]\n\t"
    "ds_read_b64_tr_b16 v[58:59], v27 offset:33928\n\t"
    "v_mfma_f32_16x16x32_bf16 a[68:71], v[36:39], v[148:151], a[68:71]\n\t"
    "v_mfma_f32_16x16x32_bf16 a[68:71], v[32:35], v[132:135], a[68:71]\n\t"
    "v_mfma_f32_16x16x32_bf16 a[72:75], v[32:35], v[136:139], a[72:75]\n\t"
    "ds_read_b64_tr_b16 v[60:61], v27 offset:41128\n\t"
    "v_mfma_f32_16x16x32_bf16 a[72:75], v[36:39], v[152:155], a[72:75]\n\t"
    "ds_read_b64_tr_b16 v[62:63], v27 offset:42152\n\t"
    "v_mfma_f32_16x16x32_bf16 a[76:79], v[36:39], v[156:159], a[76:79]\n\t"
    "v_mfma_f32_16x16x32_bf16 a[76:79], v[32:35], v[140:143], a[76:79]\n\t"
    "s_waitcnt vmcnt(8) lgkmcnt(8)\n\t"
    "s_barrier\n\t"
    "v_mfma_f32_16x16x32_bf16 a[80:83], v[40:43], v[128:131], a[80:83]\n\t"
    "ds_read_b64_tr_b16 v[32:33], %[vRd] offset:49344\n\t"
    "v_mfma_f32_16x16x32_bf16 a[80:83], v[44:47], v[144:147], a[80:83]\n\t"
    "ds_read_b64_tr_b16 v[34:35], %[vRd] offset:50368\n\t"
    "v_mfma_f32_16x16x32_bf16 a[84:87], v[44:47], v[148:151], a[84:87]\n\t"
    "v_mfma_f32_16x16x32_bf16 a[84:87], v[40:43], v[132:135], a[84:87]\n\t"
    "v_mfma_f32_16x16x32_bf16 a[88:91], v[40:43], v[136:139], a[88:91]\n\t"
    "ds_read_b64_tr_b16 v[36:37], %[vRd] offset:57568\n\t"
    "v_mfma_f32_16x16x32_bf16 a[88:91], v[44:47], v[152:155], a[88:91]\n\t"
    "ds_read_b64_tr_b16 v[38:39], %[vRd] offset:58592\n\t"
    "v_mfma_f32_16x16x32_bf16 a[92:95], v[44:47], v[156:159], a[92:95]\n\t"
    "v_mfma_f32_16x16x32_bf16 a[92:95], v[40:43], v[140:143], a[92:95]\n\t"
    "s_waitcnt vmcnt(0) lgkmcnt(8)\n\t"
    "v_mfma_f32_16x16x32_bf16 a[96:99], v[48:51], v[128:131], a[96:99]\n\t"
    "ds_read_b64_tr_b16 v[40:41], %[vRd] offset:49352\n\t"
    "ds_read_b128 v[192:195], %[vXr] offset:0\n\t"
    "v_mfma_f32_16x16x32_bf16 a[96:99], v[52:55], v[144:147], a[96:99]\n\t"
    "ds_read_b64_tr_b16 v[42:43], %[vRd] offset:50376\n\t"
    "ds_read_b128 v[208:211], %[vXr] offset:4096\n\t"
    "v_mfma_f32_16x16x32_bf16 a[100:103], v[52:55], v[148:151], a[100:103]\n\t"
    "v_xor_b32 v31, 16, %[vXr]\n\t"
    "ds_read_b128 v[196:199], v31 offset:0\n\t"
    "v_mfma_f32_16x16x32_bf16 a[100:103], v[48:51], v[132:135], a[100:103]\n\t"
    "ds_read_b128 v[212:215], v31 offset:4096\n\t"
    "v_xor_b32 v31, 32, %[vXr]\n\t"
    "v_mfma_f32_16x16x32_bf16 a[104:107], v[48:51], v[136:139], a[104:107]\n\t"
    "ds_read_b64_tr_b16 v[44:45], %[vRd] offset:57576\n\t"
    "ds_read_b128 v[200:203], v31 offset:0\n\t"
    "v_mfma_f32_16x16x32_bf16 a[104:107], v[52:55], v[152:155], a[104:107]\n\t"
    "ds_read_b64_tr_b16 v[46:47], %[vRd] offset:58600\n\t"
    "ds_read_b128 v[216:219], v31 offset:4096\n\t"
    "v_mfma_f32_16x16x32_bf16 a[108:111], v[52:55], v[156:159], a[108:111]\n\t"
    "v_xor_b32 v31, 48, %[vXr]\n\t"
    "ds_read_b128 v[204:207], v31 offset:0\n\t"
    "v_mfma_f32_16x16x32_bf16 a[108:111], v[48:51], v[140:143], a[108:111]\n\t"
    "ds_read_b128 v[220:223], v31 offset:4096\n\t"
    "s_waitcnt lgkmcnt(15)\n\t"
    "v_mfma_f32_16x16x32_bf16 a[112:115], v[56:59], v[128:131], a[112:115]\n\t"
    "ds_read_b64_tr_b16 v[48:49], v25 offset:49344\n\t"
    "v_mfma_f32_16x16x32_bf16 a[112:115], v[60:63], v[144:147], a[112:115]\n\t"
    "ds_read_b64_tr_b16 v[50:51], v25 offset:50368\n\t"
    "v_mfma_f32_16x16x32_bf16 a[116:119], v[60:63], v[148:151], a[116:119]\n\t"
    "v_mfma_f32_16x16x32_bf16 a[116:119], v[56:59], v[132:135], a[116:119]\n\t"
    "v_mfma_f32_16x16x32_bf16 a[120:123], v[56:59], v[136:139], a[120:123]\n\t"
    "ds_read_b64_tr_b16 v[52:53], v25 offset:57568\n\t"
    "v_mfma_f32_16x16x32_bf16 a[120:123], v[60:63], v[152:155], a[120:123]\n\t"
    "ds_read_b64_tr_b16 v[54:55], v25 offset:58592\n\t"
    "v_mfma_f32_16x16x32_bf16 a[124:127], v[60:63], v[156:159], a[124:127]\n\t"
    "v_mfma_f32_16x16x32_bf16 a[124:127], v[56:59], v[140:143], a[124:127]\n\t"
    "s_waitcnt vmcnt(9) lgkmcnt(4)\n\t"
    "v_mfma_f32_16x16x32_bf16 a[0:3], v[32:35], v[160:163], a[0:3]\n\t"
    "ds_read_b64_tr_b16 v[56:57], v25 offset:49352\n\t"
    "buffer_load_dword v28, off, %[srdWS], %[sCnt] sc1\n\t"
    "v_mfma_f32_16x16x32_bf16 a[0:3], v[36:39], v[176:179], a[0:3]\n\t"
    "ds_read_b64_tr_b16 v[58:59], v25 offset:50376\n\t"
    "v_lshlrev_b32 v64, 16, v192\n\t"
    "v_mfma_f32_16x16x32_bf16 a[4:7], v[36:39], v[180:183], a[4:7]\n\t"
    "v_and_b32 v65, s64, v192\n\t"
    "v_lshlrev_b32 v66, 16, v193\n\t"
    "v_mfma_f32_16x16x32_bf16 a[4:7], v[32:35], v[164:167], a[4:7]\n\t"
    "v_and_b32 v67, s64, v193\n\t"
    "v_lshlrev_b32 v68, 16, v194\n\t"
    "v_mfma_f32_16x16x32_bf16 a[8:11], v[32:35], v[168:171], a[8:11]\n\t"
    "ds_read_b64_tr_b16 v[60:61], v25 offset:57576\n\t"
    "v_and_b32 v69, s64, v194\n\t"
    "v_mfma_f32_16x16x32_bf16 a[8:11], v[36:39], v[184:187], a[8:11]\n\t"
    "ds_read_b64_tr_b16 v[62:63], v25 offset:58600\n\t"
    "v_lshlrev_b32 v70, 16, v195\n\t"
    "v_mfma_f32_16x16x32_bf16 a[12:15], v[36:39], v[188:191], a[12:15]\n\t"
    "v_and_b32 v71, s64, v195\n\t"
    "v_lshlrev_b32 v72, 16, v196\n\t"
    "v_mfma_f32_16x16x32_bf16 a[12:15], v[32:35], v[172:175], a[12:15]\n\t"
    "v_and_b32 v73, s64, v196\n\t"
    "s_waitcnt lgkmcnt(11)\n\t"
    "v_mfma_f32_16x16x32_bf16 a[16:19], v[40:43], v[160:163], a[16:19]\n\t"
    "ds_read_b64_tr_b16 v[32:33], v26 offset:49344\n\t"
    "v_lshlrev_b32 v74, 16, v197\n\t"
    "v_mfma_f32_16x16x32_bf16 a[16:19], v[44:47], v[176:179], a[16:19]\n\t"
    "ds_read_b64_tr_b16 v[34:35], v26 offset:50368\n\t"
    "v_and_b32 v75, s64, v197\n\t"
    "v_mfma_f32_16x16x32_bf16 a[20:23], v[44:47], v[180:183], a[20:23]\n\t"
    "v_lshlrev_b32 v76, 16, v198\n\t"
    "v_and_b32 v77, s64, v198\n\t"
    "v_mfma_f32_16x16x32_bf16 a[20:23], v[40:43], v[164:167], a[20:23]\n\t"
    "ds_write_b128 v29, a[8:11] offset:0\n\t"
    "v_mfma_f32_16x16x32_bf16 a[24:27], v[40:43], v[168:171], a[24:27]\n\t"
    "ds_read_b64_tr_b16 v[36:37], v26 offset:57568\n\t"
    "v_lshlrev_b32 v78, 16, v199\n\t"
    "v_mfma_f32_16x16x32_bf16 a[24:27], v[44:47], v[184:187], a[24:27]\n\t"
    "ds_read_b64_tr_b16 v[38:39], v26 offset:58592\n\t"
    "v_and_b32 v79, s64, v199\n\t"
    "v_mfma_f32_16x16x32_bf16 a[28:31], v[44:47], v[188:191], a[28:31]\n\t"
    "ds_write_b128 v29, a[12:15] offset:8192\n\t"
    "v_mfma_f32_16x16x32_bf16 a[28:31], v[40:43], v[172:175], a[28:31]\n\t"
    "v_lshlrev_b32 v80, 16, v200\n\t"
    "s_waitcnt lgkmcnt(10)\n\t"
    "v_mfma_f32_16x16x32_bf16 a[32:35], v[48:51], v[160:163], a[32:35]\n\t"
    "ds_read_b64_tr_b16 v[40:41], v26 offset:49352\n\t"
    "v_and_b32 v81, s64, v200\n\t"
    "v_mfma_f32_16x16x32_bf16 a[32:35], v[52:55], v[176:179], a[32:35]\n\t"
    "ds_read_b64_tr_b16 v[42:43], v26 offset:50376\n\t"
    "v_lshlrev_b32 v82, 16, v201\n\t"
    "v_mfma_f32_16x16x32_bf16 a[36:39], v[52:55], v[180:183], a[36:39]\n\t"
    "v_and_b32 v83, s64, v201\n\t"
    "v_lshlrev_b32 v84, 16, v202\n\t"
    "v_mfma_f32_16x16x32_bf16 a[36:39], v[48:51], v[164:167], a[36:39]\n\t"
    "ds_write_b128 v29, a[24:27] offset:1024\n\t"
    "v_mfma_f32_16x16x32_bf16 a[40:43], v[48:51], v[168:171], a[40:43]\n\t"
    "ds_read_b64_tr_b16 v[44:45], v26 offset:57576\n\t"
    "v_and_b32 v85, s64, v202\n\t"
    "v_mfma_f32_16x16x32_bf16 a[40:43], v[52:55], v[184:187], a[40:43]\n\t"
    "ds_read_b64_tr_b16 v[46:47], v26 offset:58600\n\t"
    "v_lshlrev_b32 v86, 16, v203\n\t"
    "v_mfma_f32_16x16x32_bf16 a[44:47], v[52:55], v[188:191], a[44:47]\n\t"
    "ds_write_b128 v29, a[28:31] offset:9216\n\t"
    "v_mfma_f32_16x16x32_bf16 a[44:47], v[48:51], v[172:175], a[44:47]\n\t"
    "v_and_b32 v87, s64, v203\n\t"
    "s_waitcnt lgkmcnt(12)\n\t"
    "v_mfma_f32_16x16x32_bf16 a[48:51], v[56:59], v[160:163], a[48:51]\n\t"
    "ds_read_b64_tr_b16 v[48:49], v27 offset:49344\n\t"
    "v_lshlrev_b32 v88, 16, v204\n\t"
    "v_mfma_f32_16x16x32_bf16 a[48:51], v[60:63], v[176:179], a[48:51]\n\t"
    "ds_read_b64_tr_b16 v[50:51], v27 offset:50368\n\t"
    "v_and_b32 v89, s64, v204\n\t"
    "v_mfma_f32_16x16x32_bf16 a[52:55], v[60:63], v[180:183], a[52:55]\n\t"
    "v_lshlrev_b32 v90, 16, v205\n\t"
    "v_and_b32 v91, s64, v205\n\t"
    "v_mfma_f32_16x16x32_bf16 a[52:55], v[56:59], v[164:167], a[52:55]\n\t"
    "ds_write_b128 v29, a[40:43] offset:2048\n\t"
    "v_mfma_f32_16x16x32_bf16 a[56:59], v[56:59], v[168:171], a[56:59]\n\t"
    "ds_read_b64_tr_b16 v[52:53], v27 offset:57568\n\t"
    "v_lshlrev_b32 v92, 16, v206\n\t"
    "v_mfma_f32_16x16x32_bf16 a[56:59], v[60:63], v[184:187], a[56:59]\n\t"
    "ds_read_b64_tr_b16 v[54:55], v27 offset:58592\n\t"
    "v_and_b32 v93, s64, v206\n\t"
    "v_mfma_f32_16x16x32_bf16 a[60:63], v[60:63], v[188:191], a[60:63]\n\t"
    "ds_write_b128 v29, a[44:47] offset:10240\n\t"
    "v_mfma_f32_16x16x32_bf16 a[60:63], v[56:59], v[172:175], a[60:63]\n\t"
    "v_lshlrev_b32 v94, 16, v207\n\t"
    "s_waitcnt lgkmcnt(13)\n\t"
    "s_cmp_lg_u32 %[sG], 0\n\t"
    "s_cbranch_scc1 L_k2_ea_%=\n\t"
    "s_waitcnt vmcnt(0)\n\t"
    "v_readfirstlane_b32 s60, v28\n\t"
    "s_nop 3\n\t"
    "s_cmp_eq_u32 s60, 0\n\t"
    "s_cbranch_scc1 L_k2_na_%=\n\t"
    "s_add_u32 s61, %[sGg], 4096\n\t"
    "buffer_load_dwordx4 v[224:227], %[vL16], %[srdWS], %[sGg] offen offset:0 sc1\n\t"
    "buffer_load_dwordx4 v[228:231], %[vL16], %[srdWS], %[sGg] offen offset:1024 sc1\n\t"
    "buffer_load_dwordx4 v[232:235], %[vL16], %[srdWS], %[sGg] offen offset:2048 sc1\n\t"
    "buffer_load_dwordx4 v[236:239], %[vL16], %[srdWS], %[sGg] offen offset:3072 sc1\n\t"
    "buffer_load_dwordx4 v[240:243], %[vL16], %[srdWS], s61 offen offset:0 sc1\n\t"
    "buffer_load_dwordx4 v[244:247], %[vL16], %[srdWS], s61 offen offset:1024 sc1\n\t"
    "buffer_load_dwordx4 v[248:251], %[vL16], %[srdWS], s61 offen offset:2048 sc1\n\t"
    "buffer_load_dwordx4 v[252:255], %[vL16], %[srdWS], s61 offen offset:3072 sc1\n\t"
    "s_branch L_k2_ea_%=\n\t"
    "L_k2_na_%=:\n\t"
    "buffer_load_dword v28, off, %[srdWS], %[sCnt] sc1\n\t"
    "L_k2_ea_%=:\n\t"
    "v_mfma_f32_16x16x32_bf16 a[64:67], v[32:35], v[160:163], a[64:67]\n\t"
    "ds_read_b64_tr_b16 v[56:57], v27 offset:49352\n\t"
    "v_and_b32 v95, s64, v207\n\t"
    "v_mfma_f32_16x16x32_bf16 a[64:67], v[36:39], v[176:179], a[64:67]\n\t"
    "ds_read_b64_tr_b16 v[58:59], v27 offset:50376\n\t"
    "v_lshlrev_b32 v96, 16, v208\n\t"
    "v_mfma_f32_16x16x32_bf16 a[68:71], v[36:39], v[180:183], a[68:71]\n\t"
    "v_and_b32 v97, s64, v208\n\t"
    "v_lshlrev_b32 v98, 16, v209\n\t"
    "v_mfma_f32_16x16x32_bf16 a[68:71], v[32:35], v[164:167], a[68:71]\n\t"
    "ds_write_b128 v29, a[56:59] offset:3072\n\t"
    "v_mfma_f32_16x16x32_bf16 a[72:75], v[32:35], v[168:171], a[72:75]\n\t"
    "ds_read_b64_tr_b16 v[60:61], v27 offset:57576\n\t"
    "v_and_b32 v99, s64, v209\n\t"
    "v_mfma_f32_16x16x32_bf16 a[72:75], v[36:39], v[184:187], a[72:75]\n\t"
    "ds_read_b64_tr_b16 v[62:63], v27 offset:58600\n\t"
    "v_lshlrev_b32 v100, 16, v210\n\t"
    "v_mfma_f32_16x16x32_bf16 a[76:79], v[36:39], v[188:191], a[76:79]\n\t"
    "ds_write_b128 v29, a[60:63] offset:11264\n\t"
    "v_mfma_f32_16x16x32_bf16 a[76:79], v[32:35], v[172:175], a[76:79]\n\t"
    "v_and_b32 v101, s64, v210\n\t"
    "s_waitcnt lgkmcnt(13)\n\t"
    "v_mfma_f32_16x16x32_bf16 a[80:83], v[40:43], v[160:163], a[80:83]\n\t"
    "v_lshlrev_b32 v102, 16, v211\n\t"
    "v_and_b32 v103, s64, v211\n\t"
    "v_mfma_f32_16x16x32_bf16 a[80:83], v[44:47], v[176:179], a[80:83]\n\t"
    "v_lshlrev_b32 v104, 16, v212\n\t"
    "v_and_b32 v105, s64, v212\n\t"
    "v_mfma_f32_16x16x32_bf16 a[84:87], v[44:47], v[180:183], a[84:87]\n\t"
    "v_lshlrev_b32 v106, 16, v213\n\t"
    "v_and_b32 v107, s64, v213\n\t"
    "v_mfma_f32_16x16x32_bf16 a[84:87], v[40:43], v[164:167], a[84:87]\n\t"
    "ds_write_b128 v29, a[72:75] offset:4096\n\t"
    "v_mfma_f32_16x16x32_bf16 a[88:91], v[40:43], v[168:171], a[88:91]\n\t"
    "ds_write_b128 v29, a[76:79] offset:12288\n\t"
    "v_mfma_f32_16x16x32_bf16 a[88:91], v[44:47], v[184:187], a[88:91]\n\t"
    "v_lshlrev_b32 v108, 16, v214\n\t"
    "v_and_b32 v109, s64, v214\n\t"
    "v_mfma_f32_16x16x32_bf16 a[92:95], v[44:47], v[188:191], a[92:95]\n\t"
    "v_lshlrev_b32 v110, 16, v215\n\t"
    "v_and_b32 v111, s64, v215\n\t"
    "v_mfma_f32_16x16x32_bf16 a[92:95], v[40:43], v[172:175], a[92:95]\n\t"
    "v_lshlrev_b32 v112, 16, v216\n\t"
    "s_waitcnt lgkmcnt(9)\n\t"
    "v_mfma_f32_16x16x32_bf16 a[96:99], v[48:51], v[160:163], a[96:99]\n\t"
    "v_and_b32 v113, s64, v216\n\t"
    "v_lshlrev_b32 v114, 16, v217\n\t"
    "v_mfma_f32_16x16x32_bf16 a[96:99], v[52:55], v[176:179], a[96:99]\n\t"
    "v_and_b32 v115, s64, v217\n\t"
    "v_lshlrev_b32 v116, 16, v218\n\t"
    "v_mfma_f32_16x16x32_bf16 a[100:103], v[52:55], v[180:183], a[100:103]\n\t"
    "v_and_b32 v117, s64, v218\n\t"
    "v_lshlrev_b32 v118, 16, v219\n\t"
    "v_mfma_f32_16x16x32_bf16 a[100:103], v[48:51], v[164:167], a[100:103]\n\t"
    "ds_write_b128 v29, a[88:91] offset:5120\n\t"
    "v_mfma_f32_16x16x32_bf16 a[104:107], v[48:51], v[168:171], a[104:107]\n\t"
    "ds_write_b128 v29, a[92:95] offset:13312\n\t"
    "v_mfma_f32_16x16x32_bf16 a[104:107], v[52:55], v[184:187], a[104:107]\n\t"
    "v_and_b32 v119, s64, v219\n\t"
    "v_lshlrev_b32 v120, 16, v220\n\t"
    "v_mfma_f32_16x16x32_bf16 a[108:111], v[52:55], v[188:191], a[108:111]\n\t"
    "v_and_b32 v121, s64, v220\n\t"
    "v_lshlrev_b32 v122, 16, v221\n\t"
    "v_mfma_f32_16x16x32_bf16 a[108:111], v[48:51], v[172:175], a[108:111]\n\t"
    "v_and_b32 v123, s64, v221\n\t"
    "s_waitcnt lgkmcnt(5)\n\t"
    "v_mfma_f32_16x16x32_bf16 a[112:115], v[56:59], v[160:163], a[112:115]\n\t"
    "v_lshlrev_b32 v124, 16, v222\n\t"
    "v_and_b32 v125, s64, v222\n\t"
    "v_mfma_f32_16x16x32_bf16 a[112:115], v[60:63], v[176:179], a[112:115]\n\t"
    "v_lshlrev_b32 v126, 16, v223\n\t"
    "v_and_b32 v127, s64, v223\n\t"
    "v_mfma_f32_16x16x32_bf16 a[116:119], v[60:63], v[180:183], a[116:119]\n\t"
    "v_accvgpr_read_b32 v128, a0\n\t"
    "v_accvgpr_read_b32 v129, a1\n\t"
    "v_mfma_f32_16x16x32_bf16 a[116:119], v[56:59], v[164:167], a[116:119]\n\t"
    "ds_write_b128 v29, a[104:107] offset:6144\n\t"
    "v_mfma_f32_16x16x32_bf16 a[120:123], v[56:59], v[168:171], a[120:123]\n\t"
    "ds_write_b128 v29, a[108:111] offset:14336\n\t"
    "v_mfma_f32_16x16x32_bf16 a[120:123], v[60:63], v[184:187], a[120:123]\n\t"
    "v_accvgpr_read_b32 v130, a2\n\t"
    "v_accvgpr_read_b32 v131, a3\n\t"
    "v_mfma_f32_16x16x32_bf16 a[124:127], v[60:63], v[188:191], a[124:127]\n\t"
    "v_accvgpr_read_b32 v144, a4\n\t"
    "v_accvgpr_read_b32 v145, a5\n\t"
    "v_mfma_f32_16x16x32_bf16 a[124:127], v[56:59], v[172:175], a[124:127]\n\t"
    "v_accvgpr_read_b32 v146, a6\n\t"
    "v_accvgpr_read_b32 v147, a7\n\t"
    "ds_write_b128 v29, a[120:123] offset:7168\n\t"
    "v_accvgpr_read_b32 v132, a16\n\t"
    "s_nop 3\n\t"
    "ds_write_b128 v29, a[124:127] offset:15360\n\t"
    "v_accvgpr_read_b32 v133, a17\n\t"
    "v_accvgpr_read_b32 v134, a18\n\t"
    "v_accvgpr_read_b32 v135, a19\n\t"
    "v_accvgpr_read_b32 v148, a20\n\t"
    "v_accvgpr_read_b32 v149, a21\n\t"
    "v_accvgpr_read_b32 v150, a22\n\t"
    "v_accvgpr_read_b32 v151, a23\n\t"
    "v_accvgpr_read_b32 v136, a32\n\t"
    "v_accvgpr_read_b32 v137, a33\n\t"
    "v_accvgpr_read_b32 v138, a34\n\t"
    "v_accvgpr_read_b32 v139, a35\n\t"
    "v_accvgpr_read_b32 v152, a36\n\t"
    "v_accvgpr_read_b32 v153, a37\n\t"
    "v_accvgpr_read_b32 v154, a38\n\t"
    "v_accvgpr_read_b32 v155, a39\n\t"
    "v_accvgpr_read_b32 v140, a48\n\t"
    "v_accvgpr_read_b32 v141, a49\n\t"
    "v_accvgpr_read_b32 v142, a50\n\t"
    "v_accvgpr_read_b32 v143, a51\n\t"
    "v_accvgpr_read_b32 v156, a52\n\t"
    "v_accvgpr_read_b32 v157, a53\n\t"
    "v_accvgpr_read_b32 v158, a54\n\t"
    "v_accvgpr_read_b32 v159, a55\n\t"
    "v_accvgpr_read_b32 v192, a64\n\t"
    "v_accvgpr_read_b32 v193, a65\n\t"
    "v_accvgpr_read_b32 v194, a66\n\t"
    "v_accvgpr_read_b32 v195, a67\n\t"
    "v_accvgpr_read_b32 v208, a68\n\t"
    "v_accvgpr_read_b32 v209, a69\n\t"
    "v_accvgpr_read_b32 v210, a70\n\t"
    "v_accvgpr_read_b32 v211, a71\n\t"
    "v_accvgpr_read_b32 v196, a80\n\t"
    "v_accvgpr_read_b32 v197, a81\n\t"
    "v_accvgpr_read_b32 v198, a82\n\t"
    "v_accvgpr_read_b32 v199, a83\n\t"
    "v_accvgpr_read_b32 v212, a84\n\t"
    "v_accvgpr_read_b32 v213, a85\n\t"
    "v_accvgpr_read_b32 v214, a86\n\t"
    "v_accvgpr_read_b32 v215, a87\n\t"
    "v_accvgpr_read_b32 v200, a96\n\t"
    "v_accvgpr_read_b32 v201, a97\n\t"
    "v_accvgpr_read_b32 v202, a98\n\t"
    "v_accvgpr_read_b32 v203, a99\n\t"
    "v_accvgpr_read_b32 v216, a100\n\t"
    "v_accvgpr_read_b32 v217, a101\n\t"
    "v_accvgpr_read_b32 v218, a102\n\t"
    "v_accvgpr_read_b32 v219, a103\n\t"
    "v_accvgpr_read_b32 v204, a112\n\t"
    "v_accvgpr_read_b32 v205, a113\n\t"
    "v_accvgpr_read_b32 v206, a114\n\t"
    "v_accvgpr_read_b32 v207, a115\n\t"
    "v_accvgpr_read_b32 v220, a116\n\t"
    "v_accvgpr_read_b32 v221, a117\n\t"
    "v_accvgpr_read_b32 v222, a118\n\t"
    "v_accvgpr_read_b32 v223, a119\n\t"
    "v_add_u32 v29, %[sRedR], v30\n\t"
    "s_cmp_lg_u32 s60, 0\n\t"
    "s_cbranch_scc1 L_k2_eb_%=\n\t"
    "s_cmp_lg_u32 %[sG], 0\n\t"
    "s_cbranch_scc1 L_k2_eb_%=\n\t"
    "s_waitcnt vmcnt(0)\n\t"
    "v_readfirstlane_b32 s60, v28\n\t"
    "s_nop 3\n\t"
    "s_cmp_eq_u32 s60, 0\n\t"
    "s_cbranch_scc1 L_k2_nb_%=\n\t"
    "s_add_u32 s61, %[sGg], 4096\n\t"
    "buffer_load_dwordx4 v[224:227], %[vL16], %[srdWS], %[sGg] offen offset:0 sc1\n\t"
    "buffer_load_dwordx4 v[228:231], %[vL16], %[srdWS], %[sGg] offen offset:1024 sc1\n\t"
    "buffer_load_dwordx4 v[232:235], %[vL16], %[srdWS], %[sGg] offen offset:2048 sc1\n\t"
    "buffer_load_dwordx4 v[236:239], %[vL16], %[srdWS], %[sGg] offen offset:3072 sc1\n\t"
    "buffer_load_dwordx4 v[240:243], %[vL16], %[srdWS], s61 offen offset:0 sc1\n\t"
    "buffer_load_dwordx4 v[244:247], %[vL16], %[srdWS], s61 offen offset:1024 sc1\n\t"
    "buffer_load_dwordx4 v[248:251], %[vL16], %[srdWS], s61 offen offset:2048 sc1\n\t"
    "buffer_load_dwordx4 v[252:255], %[vL16], %[srdWS], s61 offen offset:3072 sc1\n\t"
    "s_branch L_k2_eb_%=\n\t"
    "L_k2_nb_%=:\n\t"
    "buffer_load_dword v28, off, %[srdWS], %[sCnt] sc1\n\t"
    "L_k2_eb_%=:\n\t"
    "s_waitcnt lgkmcnt(0)\n\t"
    "s_barrier\n\t"
    "ds_read_b128 v[32:35], v29 offset:0\n\t"
    "ds_read_b128 v[36:39], v29 offset:1024\n\t"
    "ds_read_b128 v[40:43], v29 offset:2048\n\t"
    "ds_read_b128 v[44:47], v29 offset:3072\n\t"
    "ds_read_b128 v[48:51], v29 offset:4096\n\t"
    "ds_read_b128 v[52:55], v29 offset:5120\n\t"
    "ds_read_b128 v[56:59], v29 offset:6144\n\t"
    "ds_read_b128 v[60:63], v29 offset:7168\n\t"
    "ds_read_b128 v[160:163], v29 offset:8192\n\t"
    "ds_read_b128 v[164:167], v29 offset:9216\n\t"
    "ds_read_b128 v[168:171], v29 offset:10240\n\t"
    "ds_read_b128 v[172:175], v29 offset:11264\n\t"
    "ds_read_b128 v[176:179], v29 offset:12288\n\t"
    "ds_read_b128 v[180:183], v29 offset:13312\n\t"
    "ds_read_b128 v[184:187], v29 offset:14336\n\t"
    "ds_read_b128 v[188:191], v29 offset:15360\n\t"
    "s_waitcnt lgkmcnt(12)\n\t"
    "v_pk_add_f32 v[128:129], v[128:129], v[32:33]\n\t"
    "v_pk_add_f32 v[130:131], v[130:131], v[34:35]\n\t"
    "v_pk_add_f32 v[132:133], v[132:133], v[36:37]\n\t"
    "v_pk_add_f32 v[134:135], v[134:135], v[38:39]\n\t"
    "v_pk_add_f32 v[136:137], v[136:137], v[40:41]\n\t"
    "v_pk_add_f32 v[138:139], v[138:139], v[42:43]\n\t"
    "v_pk_add_f32 v[140:141], v[140:141], v[44:45]\n\t"
    "v_pk_add_f32 v[142:143], v[142:143], v[46:47]\n\t"
    "s_waitcnt lgkmcnt(8)\n\t"
    "v_pk_add_f32 v[192:193], v[192:193], v[48:49]\n\t"
    "v_pk_add_f32 v[194:195], v[194:195], v[50:51]\n\t"
    "v_pk_add_f32 v[196:197], v[196:197], v[52:53]\n\t"
    "v_pk_add_f32 v[198:199], v[198:199], v[54:55]\n\t"
    "v_pk_add_f32 v[200:201], v[200:201], v[56:57]\n\t"
    "v_pk_add_f32 v[202:203], v[202:203], v[58:59]\n\t"
    "v_pk_add_f32 v[204:205], v[204:205], v[60:61]\n\t"
    "v_pk_add_f32 v[206:207], v[206:207], v[62:63]\n\t"
    "s_waitcnt lgkmcnt(4)\n\t"
    "v_pk_add_f32 v[144:145], v[144:145], v[160:161]\n\t"
    "v_pk_add_f32 v[146:147], v[146:147], v[162:163]\n\t"
    "v_pk_add_f32 v[148:149], v[148:149], v[164:165]\n\t"
    "k_pgb3: s_cbranch_execz k_pgb_back\n\t"
    "v_pk_add_f32 v[150:151], v[150:151], v[166:167]\n\t"
    "v_pk_add_f32 v[152:153], v[152:153], v[168:169]\n\t"
    "v_pk_add_f32 v[154:155], v[154:155], v[170:171]\n\t"
    "v_pk_add_f32 v[156:157], v[156:157], v[172:173]\n\t"
    "v_pk_add_f32 v[158:159], v[158:159], v[174:175]\n\t"
    "s_waitcnt lgkmcnt(0)\n\t"
    "v_pk_add_f32 v[208:209], v[208:209], v[176:177]\n\t"
    "v_pk_add_f32 v[210:211], v[210:211], v[178:179]\n\t"
    "v_pk_add_f32 v[212:213], v[212:213], v[180:181]\n\t"
    "v_pk_add_f32 v[214:215], v[214:215], v[182:183]\n\t"
    "v_pk_add_f32 v[216:217], v[216:217], v[184:185]\n\t"
    "v_pk_add_f32 v[218:219], v[218:219], v[186:187]\n\t"
    "v_pk_add_f32 v[220:221], v[220:221], v[188:189]\n\t"
    "v_pk_add_f32 v[222:223], v[222:223], v[190:191]\n\t"
    "s_cmp_lg_u32 s60, 0\n\t"
    "s_cbranch_scc1 L_k2_comb_%=\n\t"
    "s_cmp_lg_u32 %[sG], 0\n\t"
    "s_cbranch_scc1 L_k2_g_%=\n\t"
    "s_mov_b32 s65, 400\n\t"
    "L_k2_poll_%=:\n\t"
    "s_waitcnt vmcnt(0)\n\t"
    "v_readfirstlane_b32 s60, v28\n\t"
    "s_sub_u32 s65, s65, 1\n\t"
    "s_nop 2\n\t"
    "s_cmp_lg_u32 s60, 0\n\t"
    "s_cbranch_scc1 L_k2_pl_%=\n\t"
    "buffer_load_dword v28, off, %[srdWS], %[sCnt] sc1\n\t"
    "s_cmp_gt_i32 s65, 0\n\t"
    "s_cbranch_scc1 L_k2_poll_%=\n\t"
    "s_add_u32 s61, %[sGg], 8192\n\t"
    "buffer_store_dwordx4 v[128:131], %[vL16], %[srdWS], s61 offen offset:0 sc1\n\t"
    "buffer_store_dwordx4 v[132:135], %[vL16], %[srdWS], s61 offen offset:1024 sc1\n\t"
    "buffer_store_dwordx4 v[136:139], %[vL16], %[srdWS], s61 offen offset:2048 sc1\n\t"
    "buffer_store_dwordx4 v[140:143], %[vL16], %[srdWS], s61 offen offset:3072 sc1\n\t"
    "s_add_u32 s61, %[sGg], 12288\n\t"
    "buffer_store_dwordx4 v[192:195], %[vL16], %[srdWS], s61 offen offset:0 sc1\n\t"
    "buffer_store_dwordx4 v[196:199], %[vL16], %[srdWS], s61 offen offset:1024 sc1\n\t"
    "buffer_store_dwordx4 v[200:203], %[vL16], %[srdWS], s61 offen offset:2048 sc1\n\t"
    "buffer_store_dwordx4 v[204:207], %[vL16], %[srdWS], s61 offen offset:3072 sc1\n\t"
    "s_add_u32 s61, %[sGg], 16384\n\t"
    "buffer_store_dwordx4 v[144:147], %[vL16], %[srdWS], s61 offen offset:0 sc1\n\t"
    "buffer_store_dwordx4 v[148:151], %[vL16], %[srdWS], s61 offen offset:1024 sc1\n\t"
    "buffer_store_dwordx4 v[152:155], %[vL16], %[srdWS], s61 offen offset:2048 sc1\n\t"
    "buffer_store_dwordx4 v[156:159], %[vL16], %[srdWS], s61 offen offset:3072 sc1\n\t"
    "s_add_u32 s61, %[sGg], 20480\n\t"
    "buffer_store_dwordx4 v[208:211], %[vL16], %[srdWS], s61 offen offset:0 sc1\n\t"
    "buffer_store_dwordx4 v[212:215], %[vL16], %[srdWS], s61 offen offset:1024 sc1\n\t"
    "buffer_store_dwordx4 v[216:219], %[vL16], %[srdWS], s61 offen offset:2048 sc1\n\t"
    "buffer_store_dwordx4 v[220:223], %[vL16], %[srdWS], s61 offen offset:3072 sc1\n\t"
    "s_waitcnt vmcnt(0)\n\t"
    "v_mov_b32 v28, 1\n\t"
    "s_mov_b64 exec, 1\n\t"
    "buffer_atomic_add v28, off, %[srdWS], %[sCnt] sc0\n\t"
    "s_waitcnt vmcnt(0)\n\t"
    "v_readfirstlane_b32 s60, v28\n\t"
    "s_mov_b64 exec, -1\n\t"
    "s_nop 3\n\t"
    "s_cmp_eq_u32 s60, 0\n\t"
    "s_cbranch_scc1 L_k2_end_%=\n\t"
    "L_k2_pl_%=:\n\t"
    "s_add_u32 s61, %[sGg], 4096\n\t"
    "buffer_load_dwordx4 v[224:227], %[vL16], %[srdWS], %[sGg] offen offset:0 sc1\n\t"
    "buffer_load_dwordx4 v[228:231], %[vL16], %[srdWS], %[sGg] offen offset:1024 sc1\n\t"
    "buffer_load_dwordx4 v[232:235], %[vL16], %[srdWS], %[sGg] offen offset:2048 sc1\n\t"
    "buffer_load_dwordx4 v[236:239], %[vL16], %[srdWS], %[sGg] offen offset:3072 sc1\n\t"
    "buffer_load_dwordx4 v[240:243], %[vL16], %[srdWS], s61 offen offset:0 sc1\n\t"
    "buffer_load_dwordx4 v[244:247], %[vL16], %[srdWS], s61 offen offset:1024 sc1\n\t"
    "buffer_load_dwordx4 v[248:251], %[vL16], %[srdWS], s61 offen offset:2048 sc1\n\t"
    "buffer_load_dwordx4 v[252:255], %[vL16], %[srdWS], s61 offen offset:3072 sc1\n\t"
    "L_k2_comb_%=:\n\t"
    "s_waitcnt vmcnt(0)\n\t"
    "v_lshlrev_b32 v32, 16, v224\n\t"
    "v_and_b32 v33, s64, v224\n\t"
    "v_lshlrev_b32 v34, 16, v225\n\t"
    "v_and_b32 v35, s64, v225\n\t"
    "v_lshlrev_b32 v36, 16, v226\n\t"
    "v_and_b32 v37, s64, v226\n\t"
    "v_lshlrev_b32 v38, 16, v227\n\t"
    "v_and_b32 v39, s64, v227\n\t"
    "v_pk_fma_f32 v[32:33], v[32:33], v[128:129], v[64:65]\n\t"
    "v_pk_fma_f32 v[34:35], v[34:35], v[130:131], v[66:67]\n\t"
    "v_pk_fma_f32 v[36:37], v[36:37], v[132:133], v[68:69]\n\t"
    "v_pk_fma_f32 v[38:39], v[38:39], v[134:135], v[70:71]\n\t"
    "v_cvt_pk_bf16_f32 v224, v32, v33\n\t"
    "v_cvt_pk_bf16_f32 v225, v34, v35\n\t"
    "v_cvt_pk_bf16_f32 v226, v36, v37\n\t"
    "v_cvt_pk_bf16_f32 v227, v38, v39\n\t"
    "v_lshlrev_b32 v40, 16, v228\n\t"
    "v_and_b32 v41, s64, v228\n\t"
    "v_lshlrev_b32 v42, 16, v229\n\t"
    "v_and_b32 v43, s64, v229\n\t"
    "v_lshlrev_b32 v44, 16, v230\n\t"
    "v_and_b32 v45, s64, v230\n\t"
    "v_lshlrev_b32 v46, 16, v231\n\t"
    "v_and_b32 v47, s64, v231\n\t"
    "v_pk_fma_f32 v[40:41], v[40:41], v[136:137], v[72:73]\n\t"
    "v_pk_fma_f32 v[42:43], v[42:43], v[138:139], v[74:75]\n\t"
    "v_pk_fma_f32 v[44:45], v[44:45], v[140:141], v[76:77]\n\t"
    "v_pk_fma_f32 v[46:47], v[46:47], v[142:143], v[78:79]\n\t"
    "v_cvt_pk_bf16_f32 v228, v40, v41\n\t"
    "v_cvt_pk_bf16_f32 v229, v42, v43\n\t"
    "v_cvt_pk_bf16_f32 v230, v44, v45\n\t"
    "v_cvt_pk_bf16_f32 v231, v46, v47\n\t"
    "v_lshlrev_b32 v48, 16, v232\n\t"
    "v_and_b32 v49, s64, v232\n\t"
    "v_lshlrev_b32 v50, 16, v233\n\t"
    "v_and_b32 v51, s64, v233\n\t"
    "v_lshlrev_b32 v52, 16, v234\n\t"
    "v_and_b32 v53, s64, v234\n\t"
    "v_lshlrev_b32 v54, 16, v235\n\t"
    "v_and_b32 v55, s64, v235\n\t"
    "v_pk_fma_f32 v[48:49], v[48:49], v[192:193], v[80:81]\n\t"
    "v_pk_fma_f32 v[50:51], v[50:51], v[194:195], v[82:83]\n\t"
    "v_pk_fma_f32 v[52:53], v[52:53], v[196:197], v[84:85]\n\t"
    "v_pk_fma_f32 v[54:55], v[54:55], v[198:199], v[86:87]\n\t"
    "v_cvt_pk_bf16_f32 v232, v48, v49\n\t"
    "v_cvt_pk_bf16_f32 v233, v50, v51\n\t"
    "v_cvt_pk_bf16_f32 v234, v52, v53\n\t"
    "v_cvt_pk_bf16_f32 v235, v54, v55\n\t"
    "v_lshlrev_b32 v56, 16, v236\n\t"
    "v_and_b32 v57, s64, v236\n\t"
    "v_lshlrev_b32 v58, 16, v237\n\t"
    "v_and_b32 v59, s64, v237\n\t"
    "v_lshlrev_b32 v60, 16, v238\n\t"
    "v_and_b32 v61, s64, v238\n\t"
    "v_lshlrev_b32 v62, 16, v239\n\t"
    "v_and_b32 v63, s64, v239\n\t"
    "v_pk_fma_f32 v[56:57], v[56:57], v[200:201], v[88:89]\n\t"
    "v_pk_fma_f32 v[58:59], v[58:59], v[202:203], v[90:91]\n\t"
    "v_pk_fma_f32 v[60:61], v[60:61], v[204:205], v[92:93]\n\t"
    "v_pk_fma_f32 v[62:63], v[62:63], v[206:207], v[94:95]\n\t"
    "v_cvt_pk_bf16_f32 v236, v56, v57\n\t"
    "v_cvt_pk_bf16_f32 v237, v58, v59\n\t"
    "v_cvt_pk_bf16_f32 v238, v60, v61\n\t"
    "v_cvt_pk_bf16_f32 v239, v62, v63\n\t"
    "s_mov_b64 vcc, s[56:57]\n\t"
    "v_cndmask_b32_dpp v68, v224, v228, vcc quad_perm:[1,0,3,2] row_mask:0xf bank_mask:0xf\n\t"
    "v_cndmask_b32_dpp v69, v225, v229, vcc quad_perm:[1,0,3,2] row_mask:0xf bank_mask:0xf\n\t"
    "v_cndmask_b32_dpp v70, v226, v230, vcc quad_perm:[1,0,3,2] row_mask:0xf bank_mask:0xf\n\t"
    "v_cndmask_b32_dpp v71, v227, v231, vcc quad_perm:[1,0,3,2] row_mask:0xf bank_mask:0xf\n\t"
    "v_cndmask_b32_dpp v76, v232, v236, vcc quad_perm:[1,0,3,2] row_mask:0xf bank_mask:0xf\n\t"
    "v_cndmask_b32_dpp v77, v233, v237, vcc quad_perm:[1,0,3,2] row_mask:0xf bank_mask:0xf\n\t"
    "v_cndmask_b32_dpp v78, v234, v238, vcc quad_perm:[1,0,3,2] row_mask:0xf bank_mask:0xf\n\t"
    "v_cndmask_b32_dpp v79, v235, v239, vcc quad_perm:[1,0,3,2] row_mask:0xf bank_mask:0xf\n\t"
    "s_not_b64 vcc, s[56:57]\n\t"
    "v_cndmask_b32_dpp v64, v228, v224, vcc quad_perm:[1,0,3,2] row_mask:0xf bank_mask:0xf\n\t"
    "v_cndmask_b32_dpp v65, v229, v225, vcc quad_perm:[1,0,3,2] row_mask:0xf bank_mask:0xf\n\t"
    "v_cndmask_b32_dpp v66, v230, v226, vcc quad_perm:[1,0,3,2] row_mask:0xf bank_mask:0xf\n\t"
    "v_cndmask_b32_dpp v67, v231, v227, vcc quad_perm:[1,0,3,2] row_mask:0xf bank_mask:0xf\n\t"
    "v_cndmask_b32_dpp v72, v236, v232, vcc quad_perm:[1,0,3,2] row_mask:0xf bank_mask:0xf\n\t"
    "v_cndmask_b32_dpp v73, v237, v233, vcc quad_perm:[1,0,3,2] row_mask:0xf bank_mask:0xf\n\t"
    "v_cndmask_b32_dpp v74, v238, v234, vcc quad_perm:[1,0,3,2] row_mask:0xf bank_mask:0xf\n\t"
    "v_cndmask_b32_dpp v75, v239, v235, vcc quad_perm:[1,0,3,2] row_mask:0xf bank_mask:0xf\n\t"
    "s_mov_b64 vcc, s[58:59]\n\t"
    "v_cndmask_b32_dpp v232, v64, v72, vcc quad_perm:[2,3,0,1] row_mask:0xf bank_mask:0xf\n\t"
    "v_cndmask_b32_dpp v233, v65, v73, vcc quad_perm:[2,3,0,1] row_mask:0xf bank_mask:0xf\n\t"
    "v_cndmask_b32_dpp v234, v66, v74, vcc quad_perm:[2,3,0,1] row_mask:0xf bank_mask:0xf\n\t"
    "v_cndmask_b32_dpp v235, v67, v75, vcc quad_perm:[2,3,0,1] row_mask:0xf bank_mask:0xf\n\t"
    "v_cndmask_b32_dpp v236, v68, v76, vcc quad_perm:[2,3,0,1] row_mask:0xf bank_mask:0xf\n\t"
    "v_cndmask_b32_dpp v237, v69, v77, vcc quad_perm:[2,3,0,1] row_mask:0xf bank_mask:0xf\n\t"
    "v_cndmask_b32_dpp v238, v70, v78, vcc quad_perm:[2,3,0,1] row_mask:0xf bank_mask:0xf\n\t"
    "v_cndmask_b32_dpp v239, v71, v79, vcc quad_perm:[2,3,0,1] row_mask:0xf bank_mask:0xf\n\t"
    "s_not_b64 vcc, s[58:59]\n\t"
    "v_cndmask_b32_dpp v224, v72, v64, vcc quad_perm:[2,3,0,1] row_mask:0xf bank_mask:0xf\n\t"
    "v_cndmask_b32_dpp v225, v73, v65, vcc quad_perm:[2,3,0,1] row_mask:0xf bank_mask:0xf\n\t"
    "v_cndmask_b32_dpp v226, v74, v66, vcc quad_perm:[2,3,0,1] row_mask:0xf bank_mask:0xf\n\t"
    "v_cndmask_b32_dpp v227, v75, v67, vcc quad_perm:[2,3,0,1] row_mask:0xf bank_mask:0xf\n\t"
    "v_cndmask_b32_dpp v228, v76, v68, vcc quad_perm:[2,3,0,1] row_mask:0xf bank_mask:0xf\n\t"
    "v_cndmask_b32_dpp v229, v77, v69, vcc quad_perm:[2,3,0,1] row_mask:0xf bank_mask:0xf\n\t"
    "v_cndmask_b32_dpp v230, v78, v70, vcc quad_perm:[2,3,0,1] row_mask:0xf bank_mask:0xf\n\t"
    "v_cndmask_b32_dpp v231, v79, v71, vcc quad_perm:[2,3,0,1] row_mask:0xf bank_mask:0xf\n\t"
    "buffer_store_dwordx4 v[224:227], %[vXo], %[srdOut], %[sXo] offen offset:0 sc1\n\t"
    "buffer_store_dwordx4 v[228:231], %[vXo], %[srdOut], %[sXo] offen offset:2048 sc1\n\t"
    "buffer_store_dwordx4 v[232:235], %[vXo], %[srdOut], s66 offen offset:0 sc1\n\t"
    "buffer_store_dwordx4 v[236:239], %[vXo], %[srdOut], s66 offen offset:2048 sc1\n\t"
    "v_lshlrev_b32 v160, 16, v240\n\t"
    "v_and_b32 v161, s64, v240\n\t"
    "v_lshlrev_b32 v162, 16, v241\n\t"
    "v_and_b32 v163, s64, v241\n\t"
    "v_lshlrev_b32 v164, 16, v242\n\t"
    "v_and_b32 v165, s64, v242\n\t"
    "v_lshlrev_b32 v166, 16, v243\n\t"
    "v_and_b32 v167, s64, v243\n\t"
    "v_pk_fma_f32 v[160:161], v[160:161], v[144:145], v[96:97]\n\t"
    "v_pk_fma_f32 v[162:163], v[162:163], v[146:147], v[98:99]\n\t"
    "v_pk_fma_f32 v[164:165], v[164:165], v[148:149], v[100:101]\n\t"
    "v_pk_fma_f32 v[166:167], v[166:167], v[150:151], v[102:103]\n\t"
    "v_cvt_pk_bf16_f32 v240, v160, v161\n\t"
    "v_cvt_pk_bf16_f32 v241, v162, v163\n\t"
    "v_cvt_pk_bf16_f32 v242, v164, v165\n\t"
    "v_cvt_pk_bf16_f32 v243, v166, v167\n\t"
    "v_lshlrev_b32 v168, 16, v244\n\t"
    "v_and_b32 v169, s64, v244\n\t"
    "v_lshlrev_b32 v170, 16, v245\n\t"
    "v_and_b32 v171, s64, v245\n\t"
    "v_lshlrev_b32 v172, 16, v246\n\t"
    "v_and_b32 v173, s64, v246\n\t"
    "v_lshlrev_b32 v174, 16, v247\n\t"
    "v_and_b32 v175, s64, v247\n\t"
    "v_pk_fma_f32 v[168:169], v[168:169], v[152:153], v[104:105]\n\t"
    "v_pk_fma_f32 v[170:171], v[170:171], v[154:155], v[106:107]\n\t"
    "v_pk_fma_f32 v[172:173], v[172:173], v[156:157], v[108:109]\n\t"
    "v_pk_fma_f32 v[174:175], v[174:175], v[158:159], v[110:111]\n\t"
    "v_cvt_pk_bf16_f32 v244, v168, v169\n\t"
    "v_cvt_pk_bf16_f32 v245, v170, v171\n\t"
    "v_cvt_pk_bf16_f32 v246, v172, v173\n\t"
    "v_cvt_pk_bf16_f32 v247, v174, v175\n\t"
    "v_lshlrev_b32 v176, 16, v248\n\t"
    "v_and_b32 v177, s64, v248\n\t"
    "v_lshlrev_b32 v178, 16, v249\n\t"
    "v_and_b32 v179, s64, v249\n\t"
    "v_lshlrev_b32 v180, 16, v250\n\t"
    "v_and_b32 v181, s64, v250\n\t"
    "v_lshlrev_b32 v182, 16, v251\n\t"
    "v_and_b32 v183, s64, v251\n\t"
    "v_pk_fma_f32 v[176:177], v[176:177], v[208:209], v[112:113]\n\t"
    "v_pk_fma_f32 v[178:179], v[178:179], v[210:211], v[114:115]\n\t"
    "v_pk_fma_f32 v[180:181], v[180:181], v[212:213], v[116:117]\n\t"
    "v_pk_fma_f32 v[182:183], v[182:183], v[214:215], v[118:119]\n\t"
    "v_cvt_pk_bf16_f32 v248, v176, v177\n\t"
    "v_cvt_pk_bf16_f32 v249, v178, v179\n\t"
    "v_cvt_pk_bf16_f32 v250, v180, v181\n\t"
    "v_cvt_pk_bf16_f32 v251, v182, v183\n\t"
    "v_lshlrev_b32 v184, 16, v252\n\t"
    "v_and_b32 v185, s64, v252\n\t"
    "v_lshlrev_b32 v186, 16, v253\n\t"
    "v_and_b32 v187, s64, v253\n\t"
    "v_lshlrev_b32 v188, 16, v254\n\t"
    "v_and_b32 v189, s64, v254\n\t"
    "v_lshlrev_b32 v190, 16, v255\n\t"
    "v_and_b32 v191, s64, v255\n\t"
    "v_pk_fma_f32 v[184:185], v[184:185], v[216:217], v[120:121]\n\t"
    "v_pk_fma_f32 v[186:187], v[186:187], v[218:219], v[122:123]\n\t"
    "v_pk_fma_f32 v[188:189], v[188:189], v[220:221], v[124:125]\n\t"
    "v_pk_fma_f32 v[190:191], v[190:191], v[222:223], v[126:127]\n\t"
    "v_cvt_pk_bf16_f32 v252, v184, v185\n\t"
    "v_cvt_pk_bf16_f32 v253, v186, v187\n\t"
    "v_cvt_pk_bf16_f32 v254, v188, v189\n\t"
    "v_cvt_pk_bf16_f32 v255, v190, v191\n\t"
    "s_mov_b64 vcc, s[56:57]\n\t"
    "v_cndmask_b32_dpp v100, v240, v244, vcc quad_perm:[1,0,3,2] row_mask:0xf bank_mask:0xf\n\t"
    "v_cndmask_b32_dpp v101, v241, v245, vcc quad_perm:[1,0,3,2] row_mask:0xf bank_mask:0xf\n\t"
    "v_cndmask_b32_dpp v102, v242, v246, vcc quad_perm:[1,0,3,2] row_mask:0xf bank_mask:0xf\n\t"
    "v_cndmask_b32_dpp v103, v243, v247, vcc quad_perm:[1,0,3,2] row_mask:0xf bank_mask:0xf\n\t"
    "v_cndmask_b32_dpp v108, v248, v252, vcc quad_perm:[1,0,3,2] row_mask:0xf bank_mask:0xf\n\t"
    "v_cndmask_b32_dpp v109, v249, v253, vcc quad_perm:[1,0,3,2] row_mask:0xf bank_mask:0xf\n\t"
    "v_cndmask_b32_dpp v110, v250, v254, vcc quad_perm:[1,0,3,2] row_mask:0xf bank_mask:0xf\n\t"
    "v_cndmask_b32_dpp v111, v251, v255, vcc quad_perm:[1,0,3,2] row_mask:0xf bank_mask:0xf\n\t"
    "s_not_b64 vcc, s[56:57]\n\t"
    "v_cndmask_b32_dpp v96, v244, v240, vcc quad_perm:[1,0,3,2] row_mask:0xf bank_mask:0xf\n\t"
    "v_cndmask_b32_dpp v97, v245, v241, vcc quad_perm:[1,0,3,2] row_mask:0xf bank_mask:0xf\n\t"
    "v_cndmask_b32_dpp v98, v246, v242, vcc quad_perm:[1,0,3,2] row_mask:0xf bank_mask:0xf\n\t"
    "v_cndmask_b32_dpp v99, v247, v243, vcc quad_perm:[1,0,3,2] row_mask:0xf bank_mask:0xf\n\t"
    "v_cndmask_b32_dpp v104, v252, v248, vcc quad_perm:[1,0,3,2] row_mask:0xf bank_mask:0xf\n\t"
    "v_cndmask_b32_dpp v105, v253, v249, vcc quad_perm:[1,0,3,2] row_mask:0xf bank_mask:0xf\n\t"
    "v_cndmask_b32_dpp v106, v254, v250, vcc quad_perm:[1,0,3,2] row_mask:0xf bank_mask:0xf\n\t"
    "v_cndmask_b32_dpp v107, v255, v251, vcc quad_perm:[1,0,3,2] row_mask:0xf bank_mask:0xf\n\t"
    "s_mov_b64 vcc, s[58:59]\n\t"
    "v_cndmask_b32_dpp v248, v96, v104, vcc quad_perm:[2,3,0,1] row_mask:0xf bank_mask:0xf\n\t"
    "v_cndmask_b32_dpp v249, v97, v105, vcc quad_perm:[2,3,0,1] row_mask:0xf bank_mask:0xf\n\t"
    "v_cndmask_b32_dpp v250, v98, v106, vcc quad_perm:[2,3,0,1] row_mask:0xf bank_mask:0xf\n\t"
    "v_cndmask_b32_dpp v251, v99, v107, vcc quad_perm:[2,3,0,1] row_mask:0xf bank_mask:0xf\n\t"
    "v_cndmask_b32_dpp v252, v100, v108, vcc quad_perm:[2,3,0,1] row_mask:0xf bank_mask:0xf\n\t"
    "v_cndmask_b32_dpp v253, v101, v109, vcc quad_perm:[2,3,0,1] row_mask:0xf bank_mask:0xf\n\t"
    "v_cndmask_b32_dpp v254, v102, v110, vcc quad_perm:[2,3,0,1] row_mask:0xf bank_mask:0xf\n\t"
    "v_cndmask_b32_dpp v255, v103, v111, vcc quad_perm:[2,3,0,1] row_mask:0xf bank_mask:0xf\n\t"
    "s_not_b64 vcc, s[58:59]\n\t"
    "v_cndmask_b32_dpp v240, v104, v96, vcc quad_perm:[2,3,0,1] row_mask:0xf bank_mask:0xf\n\t"
    "v_cndmask_b32_dpp v241, v105, v97, vcc quad_perm:[2,3,0,1] row_mask:0xf bank_mask:0xf\n\t"
    "v_cndmask_b32_dpp v242, v106, v98, vcc quad_perm:[2,3,0,1] row_mask:0xf bank_mask:0xf\n\t"
    "v_cndmask_b32_dpp v243, v107, v99, vcc quad_perm:[2,3,0,1] row_mask:0xf bank_mask:0xf\n\t"
    "v_cndmask_b32_dpp v244, v108, v100, vcc quad_perm:[2,3,0,1] row_mask:0xf bank_mask:0xf\n\t"
    "v_cndmask_b32_dpp v245, v109, v101, vcc quad_perm:[2,3,0,1] row_mask:0xf bank_mask:0xf\n\t"
    "v_cndmask_b32_dpp v246, v110, v102, vcc quad_perm:[2,3,0,1] row_mask:0xf bank_mask:0xf\n\t"
    "v_cndmask_b32_dpp v247, v111, v103, vcc quad_perm:[2,3,0,1] row_mask:0xf bank_mask:0xf\n\t"
    "buffer_store_dwordx4 v[240:243], %[vXo], %[srdOut], s67 offen offset:0 sc1\n\t"
    "buffer_store_dwordx4 v[244:247], %[vXo], %[srdOut], s67 offen offset:2048 sc1\n\t"
    "buffer_store_dwordx4 v[248:251], %[vXo], %[srdOut], s68 offen offset:0 sc1\n\t"
    "buffer_store_dwordx4 v[252:255], %[vXo], %[srdOut], s68 offen offset:2048 sc1\n\t"
    "s_branch L_k2_end_%=\n\t"
    "L_k2_g_%=:\n\t"
    "s_add_u32 s61, %[sGg], 4096\n\t"
    "v_pk_mul_f32 v[32:33], v[128:129], s[62:63]\n\t"
    "v_pk_mul_f32 v[34:35], v[130:131], s[62:63]\n\t"
    "v_pk_mul_f32 v[36:37], v[132:133], s[62:63]\n\t"
    "v_pk_mul_f32 v[38:39], v[134:135], s[62:63]\n\t"
    "v_pk_mul_f32 v[40:41], v[136:137], s[62:63]\n\t"
    "v_pk_mul_f32 v[42:43], v[138:139], s[62:63]\n\t"
    "v_pk_mul_f32 v[44:45], v[140:141], s[62:63]\n\t"
    "v_pk_mul_f32 v[46:47], v[142:143], s[62:63]\n\t"
    "v_exp_f32 v32, v32\n\t"
    "v_exp_f32 v33, v33\n\t"
    "v_exp_f32 v34, v34\n\t"
    "v_exp_f32 v35, v35\n\t"
    "v_exp_f32 v36, v36\n\t"
    "v_exp_f32 v37, v37\n\t"
    "v_exp_f32 v38, v38\n\t"
    "v_exp_f32 v39, v39\n\t"
    "v_exp_f32 v40, v40\n\t"
    "v_exp_f32 v41, v41\n\t"
    "v_exp_f32 v42, v42\n\t"
    "v_exp_f32 v43, v43\n\t"
    "v_exp_f32 v44, v44\n\t"
    "v_exp_f32 v45, v45\n\t"
    "v_exp_f32 v46, v46\n\t"
    "v_exp_f32 v47, v47\n\t"
    "v_pk_add_f32 v[32:33], v[32:33], 1.0 op_sel_hi:[1,0]\n\t"
    "v_pk_add_f32 v[34:35], v[34:35], 1.0 op_sel_hi:[1,0]\n\t"
    "v_pk_add_f32 v[36:37], v[36:37], 1.0 op_sel_hi:[1,0]\n\t"
    "v_pk_add_f32 v[38:39], v[38:39], 1.0 op_sel_hi:[1,0]\n\t"
    "v_pk_add_f32 v[40:41], v[40:41], 1.0 op_sel_hi:[1,0]\n\t"
    "v_pk_add_f32 v[42:43], v[42:43], 1.0 op_sel_hi:[1,0]\n\t"
    "v_pk_add_f32 v[44:45], v[44:45], 1.0 op_sel_hi:[1,0]\n\t"
    "v_pk_add_f32 v[46:47], v[46:47], 1.0 op_sel_hi:[1,0]\n\t"
    "v_rcp_f32 v32, v32\n\t"
    "v_rcp_f32 v33, v33\n\t"
    "v_rcp_f32 v34, v34\n\t"
    "v_rcp_f32 v35, v35\n\t"
    "v_rcp_f32 v36, v36\n\t"
    "v_rcp_f32 v37, v37\n\t"
    "v_rcp_f32 v38, v38\n\t"
    "v_rcp_f32 v39, v39\n\t"
    "v_rcp_f32 v40, v40\n\t"
    "v_rcp_f32 v41, v41\n\t"
    "v_rcp_f32 v42, v42\n\t"
    "v_rcp_f32 v43, v43\n\t"
    "v_rcp_f32 v44, v44\n\t"
    "v_rcp_f32 v45, v45\n\t"
    "v_rcp_f32 v46, v46\n\t"
    "v_rcp_f32 v47, v47\n\t"
    "v_pk_mul_f32 v[32:33], v[32:33], v[128:129]\n\t"
    "v_pk_mul_f32 v[34:35], v[34:35], v[130:131]\n\t"
    "v_pk_mul_f32 v[36:37], v[36:37], v[132:133]\n\t"
    "v_pk_mul_f32 v[38:39], v[38:39], v[134:135]\n\t"
    "v_pk_mul_f32 v[40:41], v[40:41], v[136:137]\n\t"
    "v_pk_mul_f32 v[42:43], v[42:43], v[138:139]\n\t"
    "v_pk_mul_f32 v[44:45], v[44:45], v[140:141]\n\t"
    "v_pk_mul_f32 v[46:47], v[46:47], v[142:143]\n\t"
    "v_cvt_pk_bf16_f32 v224, v32, v33\n\t"
    "v_cvt_pk_bf16_f32 v225, v34, v35\n\t"
    "v_cvt_pk_bf16_f32 v226, v36, v37\n\t"
    "v_cvt_pk_bf16_f32 v227, v38, v39\n\t"
    "v_cvt_pk_bf16_f32 v228, v40, v41\n\t"
    "v_cvt_pk_bf16_f32 v229, v42, v43\n\t"
    "v_cvt_pk_bf16_f32 v230, v44, v45\n\t"
    "v_cvt_pk_bf16_f32 v231, v46, v47\n\t"
    "buffer_store_dwordx4 v[224:227], %[vL16], %[srdWS], %[sGg] offen offset:0 sc1\n\t"
    "buffer_store_dwordx4 v[228:231], %[vL16], %[srdWS], %[sGg] offen offset:1024 sc1\n\t"
    "v_pk_mul_f32 v[48:49], v[192:193], s[62:63]\n\t"
    "v_pk_mul_f32 v[50:51], v[194:195], s[62:63]\n\t"
    "v_pk_mul_f32 v[52:53], v[196:197], s[62:63]\n\t"
    "v_pk_mul_f32 v[54:55], v[198:199], s[62:63]\n\t"
    "v_pk_mul_f32 v[56:57], v[200:201], s[62:63]\n\t"
    "v_pk_mul_f32 v[58:59], v[202:203], s[62:63]\n\t"
    "v_pk_mul_f32 v[60:61], v[204:205], s[62:63]\n\t"
    "v_pk_mul_f32 v[62:63], v[206:207], s[62:63]\n\t"
    "v_exp_f32 v48, v48\n\t"
    "v_exp_f32 v49, v49\n\t"
    "v_exp_f32 v50, v50\n\t"
    "v_exp_f32 v51, v51\n\t"
    "v_exp_f32 v52, v52\n\t"
    "v_exp_f32 v53, v53\n\t"
    "v_exp_f32 v54, v54\n\t"
    "v_exp_f32 v55, v55\n\t"
    "v_exp_f32 v56, v56\n\t"
    "v_exp_f32 v57, v57\n\t"
    "v_exp_f32 v58, v58\n\t"
    "v_exp_f32 v59, v59\n\t"
    "v_exp_f32 v60, v60\n\t"
    "v_exp_f32 v61, v61\n\t"
    "v_exp_f32 v62, v62\n\t"
    "v_exp_f32 v63, v63\n\t"
    "v_pk_add_f32 v[48:49], v[48:49], 1.0 op_sel_hi:[1,0]\n\t"
    "v_pk_add_f32 v[50:51], v[50:51], 1.0 op_sel_hi:[1,0]\n\t"
    "v_pk_add_f32 v[52:53], v[52:53], 1.0 op_sel_hi:[1,0]\n\t"
    "v_pk_add_f32 v[54:55], v[54:55], 1.0 op_sel_hi:[1,0]\n\t"
    "v_pk_add_f32 v[56:57], v[56:57], 1.0 op_sel_hi:[1,0]\n\t"
    "v_pk_add_f32 v[58:59], v[58:59], 1.0 op_sel_hi:[1,0]\n\t"
    "v_pk_add_f32 v[60:61], v[60:61], 1.0 op_sel_hi:[1,0]\n\t"
    "v_pk_add_f32 v[62:63], v[62:63], 1.0 op_sel_hi:[1,0]\n\t"
    "v_rcp_f32 v48, v48\n\t"
    "v_rcp_f32 v49, v49\n\t"
    "v_rcp_f32 v50, v50\n\t"
    "v_rcp_f32 v51, v51\n\t"
    "v_rcp_f32 v52, v52\n\t"
    "v_rcp_f32 v53, v53\n\t"
    "v_rcp_f32 v54, v54\n\t"
    "v_rcp_f32 v55, v55\n\t"
    "v_rcp_f32 v56, v56\n\t"
    "v_rcp_f32 v57, v57\n\t"
    "v_rcp_f32 v58, v58\n\t"
    "v_rcp_f32 v59, v59\n\t"
    "v_rcp_f32 v60, v60\n\t"
    "v_rcp_f32 v61, v61\n\t"
    "v_rcp_f32 v62, v62\n\t"
    "v_rcp_f32 v63, v63\n\t"
    "v_pk_mul_f32 v[48:49], v[48:49], v[192:193]\n\t"
    "v_pk_mul_f32 v[50:51], v[50:51], v[194:195]\n\t"
    "v_pk_mul_f32 v[52:53], v[52:53], v[196:197]\n\t"
    "v_pk_mul_f32 v[54:55], v[54:55], v[198:199]\n\t"
    "v_pk_mul_f32 v[56:57], v[56:57], v[200:201]\n\t"
    "v_pk_mul_f32 v[58:59], v[58:59], v[202:203]\n\t"
    "v_pk_mul_f32 v[60:61], v[60:61], v[204:205]\n\t"
    "v_pk_mul_f32 v[62:63], v[62:63], v[206:207]\n\t"
    "v_cvt_pk_bf16_f32 v232, v48, v49\n\t"
    "v_cvt_pk_bf16_f32 v233, v50, v51\n\t"
    "v_cvt_pk_bf16_f32 v234, v52, v53\n\t"
    "v_cvt_pk_bf16_f32 v235, v54, v55\n\t"
    "v_cvt_pk_bf16_f32 v236, v56, v57\n\t"
    "v_cvt_pk_bf16_f32 v237, v58, v59\n\t"
    "v_cvt_pk_bf16_f32 v238, v60, v61\n\t"
    "v_cvt_pk_bf16_f32 v239, v62, v63\n\t"
    "buffer_store_dwordx4 v[232:235], %[vL16], %[srdWS], %[sGg] offen offset:2048 sc1\n\t"
    "buffer_store_dwordx4 v[236:239], %[vL16], %[srdWS], %[sGg] offen offset:3072 sc1\n\t"
    "v_pk_mul_f32 v[160:161], v[144:145], s[62:63]\n\t"
    "v_pk_mul_f32 v[162:163], v[146:147], s[62:63]\n\t"
    "v_pk_mul_f32 v[164:165], v[148:149], s[62:63]\n\t"
    "v_pk_mul_f32 v[166:167], v[150:151], s[62:63]\n\t"
    "v_pk_mul_f32 v[168:169], v[152:153], s[62:63]\n\t"
    "v_pk_mul_f32 v[170:171], v[154:155], s[62:63]\n\t"
    "v_pk_mul_f32 v[172:173], v[156:157], s[62:63]\n\t"
    "v_pk_mul_f32 v[174:175], v[158:159], s[62:63]\n\t"
    "v_exp_f32 v160, v160\n\t"
    "v_exp_f32 v161, v161\n\t"
    "v_exp_f32 v162, v162\n\t"
    "v_exp_f32 v163, v163\n\t"
    "v_exp_f32 v164, v164\n\t"
    "v_exp_f32 v165, v165\n\t"
    "v_exp_f32 v166, v166\n\t"
    "v_exp_f32 v167, v167\n\t"
    "v_exp_f32 v168, v168\n\t"
    "v_exp_f32 v169, v169\n\t"
    "v_exp_f32 v170, v170\n\t"
    "v_exp_f32 v171, v171\n\t"
    "v_exp_f32 v172, v172\n\t"
    "v_exp_f32 v173, v173\n\t"
    "v_exp_f32 v174, v174\n\t"
    "v_exp_f32 v175, v175\n\t"
    "v_pk_add_f32 v[160:161], v[160:161], 1.0 op_sel_hi:[1,0]\n\t"
    "v_pk_add_f32 v[162:163], v[162:163], 1.0 op_sel_hi:[1,0]\n\t"
    "v_pk_add_f32 v[164:165], v[164:165], 1.0 op_sel_hi:[1,0]\n\t"
    "v_pk_add_f32 v[166:167], v[166:167], 1.0 op_sel_hi:[1,0]\n\t"
    "v_pk_add_f32 v[168:169], v[168:169], 1.0 op_sel_hi:[1,0]\n\t"
    "v_pk_add_f32 v[170:171], v[170:171], 1.0 op_sel_hi:[1,0]\n\t"
    "v_pk_add_f32 v[172:173], v[172:173], 1.0 op_sel_hi:[1,0]\n\t"
    "v_pk_add_f32 v[174:175], v[174:175], 1.0 op_sel_hi:[1,0]\n\t"
    "v_rcp_f32 v160, v160\n\t"
    "v_rcp_f32 v161, v161\n\t"
    "v_rcp_f32 v162, v162\n\t"
    "v_rcp_f32 v163, v163\n\t"
    "v_rcp_f32 v164, v164\n\t"
    "v_rcp_f32 v165, v165\n\t"
    "v_rcp_f32 v166, v166\n\t"
    "v_rcp_f32 v167, v167\n\t"
    "v_rcp_f32 v168, v168\n\t"
    "v_rcp_f32 v169, v169\n\t"
    "v_rcp_f32 v170, v170\n\t"
    "v_rcp_f32 v171, v171\n\t"
    "v_rcp_f32 v172, v172\n\t"
    "v_rcp_f32 v173, v173\n\t"
    "v_rcp_f32 v174, v174\n\t"
    "v_rcp_f32 v175, v175\n\t"
    "v_pk_mul_f32 v[160:161], v[160:161], v[144:145]\n\t"
    "v_pk_mul_f32 v[162:163], v[162:163], v[146:147]\n\t"
    "v_pk_mul_f32 v[164:165], v[164:165], v[148:149]\n\t"
    "v_pk_mul_f32 v[166:167], v[166:167], v[150:151]\n\t"
    "v_pk_mul_f32 v[168:169], v[168:169], v[152:153]\n\t"
    "v_pk_mul_f32 v[170:171], v[170:171], v[154:155]\n\t"
    "v_pk_mul_f32 v[172:173], v[172:173], v[156:157]\n\t"
    "v_pk_mul_f32 v[174:175], v[174:175], v[158:159]\n\t"
    "v_cvt_pk_bf16_f32 v240, v160, v161\n\t"
    "v_cvt_pk_bf16_f32 v241, v162, v163\n\t"
    "v_cvt_pk_bf16_f32 v242, v164, v165\n\t"
    "v_cvt_pk_bf16_f32 v243, v166, v167\n\t"
    "v_cvt_pk_bf16_f32 v244, v168, v169\n\t"
    "v_cvt_pk_bf16_f32 v245, v170, v171\n\t"
    "v_cvt_pk_bf16_f32 v246, v172, v173\n\t"
    "v_cvt_pk_bf16_f32 v247, v174, v175\n\t"
    "buffer_store_dwordx4 v[240:243], %[vL16], %[srdWS], s61 offen offset:0 sc1\n\t"
    "buffer_store_dwordx4 v[244:247], %[vL16], %[srdWS], s61 offen offset:1024 sc1\n\t"
    "v_pk_mul_f32 v[176:177], v[208:209], s[62:63]\n\t"
    "v_pk_mul_f32 v[178:179], v[210:211], s[62:63]\n\t"
    "v_pk_mul_f32 v[180:181], v[212:213], s[62:63]\n\t"
    "v_pk_mul_f32 v[182:183], v[214:215], s[62:63]\n\t"
    "v_pk_mul_f32 v[184:185], v[216:217], s[62:63]\n\t"
    "v_pk_mul_f32 v[186:187], v[218:219], s[62:63]\n\t"
    "v_pk_mul_f32 v[188:189], v[220:221], s[62:63]\n\t"
    "v_pk_mul_f32 v[190:191], v[222:223], s[62:63]\n\t"
    "v_exp_f32 v176, v176\n\t"
    "v_exp_f32 v177, v177\n\t"
    "v_exp_f32 v178, v178\n\t"
    "v_exp_f32 v179, v179\n\t"
    "v_exp_f32 v180, v180\n\t"
    "v_exp_f32 v181, v181\n\t"
    "v_exp_f32 v182, v182\n\t"
    "v_exp_f32 v183, v183\n\t"
    "v_exp_f32 v184, v184\n\t"
    "v_exp_f32 v185, v185\n\t"
    "v_exp_f32 v186, v186\n\t"
    "v_exp_f32 v187, v187\n\t"
    "v_exp_f32 v188, v188\n\t"
    "v_exp_f32 v189, v189\n\t"
    "v_exp_f32 v190, v190\n\t"
    "v_exp_f32 v191, v191\n\t"
    "v_pk_add_f32 v[176:177], v[176:177], 1.0 op_sel_hi:[1,0]\n\t"
    "v_pk_add_f32 v[178:179], v[178:179], 1.0 op_sel_hi:[1,0]\n\t"
    "v_pk_add_f32 v[180:181], v[180:181], 1.0 op_sel_hi:[1,0]\n\t"
    "v_pk_add_f32 v[182:183], v[182:183], 1.0 op_sel_hi:[1,0]\n\t"
    "v_pk_add_f32 v[184:185], v[184:185], 1.0 op_sel_hi:[1,0]\n\t"
    "v_pk_add_f32 v[186:187], v[186:187], 1.0 op_sel_hi:[1,0]\n\t"
    "v_pk_add_f32 v[188:189], v[188:189], 1.0 op_sel_hi:[1,0]\n\t"
    "v_pk_add_f32 v[190:191], v[190:191], 1.0 op_sel_hi:[1,0]\n\t"
    "v_rcp_f32 v176, v176\n\t"
    "v_rcp_f32 v177, v177\n\t"
    "v_rcp_f32 v178, v178\n\t"
    "v_rcp_f32 v179, v179\n\t"
    "v_rcp_f32 v180, v180\n\t"
    "v_rcp_f32 v181, v181\n\t"
    "v_rcp_f32 v182, v182\n\t"
    "v_rcp_f32 v183, v183\n\t"
    "v_rcp_f32 v184, v184\n\t"
    "v_rcp_f32 v185, v185\n\t"
    "v_rcp_f32 v186, v186\n\t"
    "v_rcp_f32 v187, v187\n\t"
    "v_rcp_f32 v188, v188\n\t"
    "v_rcp_f32 v189, v189\n\t"
    "v_rcp_f32 v190, v190\n\t"
    "v_rcp_f32 v191, v191\n\t"
    "v_pk_mul_f32 v[176:177], v[176:177], v[208:209]\n\t"
    "v_pk_mul_f32 v[178:179], v[178:179], v[210:211]\n\t"
    "v_pk_mul_f32 v[180:181], v[180:181], v[212:213]\n\t"
    "v_pk_mul_f32 v[182:183], v[182:183], v[214:215]\n\t"
    "v_pk_mul_f32 v[184:185], v[184:185], v[216:217]\n\t"
    "v_pk_mul_f32 v[186:187], v[186:187], v[218:219]\n\t"
    "v_pk_mul_f32 v[188:189], v[188:189], v[220:221]\n\t"
    "v_pk_mul_f32 v[190:191], v[190:191], v[222:223]\n\t"
    "v_cvt_pk_bf16_f32 v248, v176, v177\n\t"
    "v_cvt_pk_bf16_f32 v249, v178, v179\n\t"
    "v_cvt_pk_bf16_f32 v250, v180, v181\n\t"
    "v_cvt_pk_bf16_f32 v251, v182, v183\n\t"
    "v_cvt_pk_bf16_f32 v252, v184, v185\n\t"
    "v_cvt_pk_bf16_f32 v253, v186, v187\n\t"
    "v_cvt_pk_bf16_f32 v254, v188, v189\n\t"
    "v_cvt_pk_bf16_f32 v255, v190, v191\n\t"
    "buffer_store_dwordx4 v[248:251], %[vL16], %[srdWS], s61 offen offset:2048 sc1\n\t"
    "buffer_store_dwordx4 v[252:255], %[vL16], %[srdWS], s61 offen offset:3072 sc1\n\t"
    "s_waitcnt vmcnt(0)\n\t"
    "v_mov_b32 v28, 1\n\t"
    "s_mov_b64 exec, 1\n\t"
    "buffer_atomic_add v28, off, %[srdWS], %[sCnt] sc0\n\t"
    "s_waitcnt vmcnt(0)\n\t"
    "v_readfirstlane_b32 s60, v28\n\t"
    "s_mov_b64 exec, -1\n\t"
    "s_nop 3\n\t"
    "s_cmp_eq_u32 s60, 0\n\t"
    "s_cbranch_scc1 L_k2_end_%=\n\t"
    "buffer_load_dwordx4 v[192:195], %[vXo], %[srdX2], %[sXo] offen offset:0\n\t"
    "buffer_load_dwordx4 v[196:199], %[vXo], %[srdX2], %[sXo] offen offset:2048\n\t"
    "buffer_load_dwordx4 v[200:203], %[vXo], %[srdX2], s66 offen offset:0\n\t"
    "buffer_load_dwordx4 v[204:207], %[vXo], %[srdX2], s66 offen offset:2048\n\t"
    "buffer_load_dwordx4 v[208:211], %[vXo], %[srdX2], s67 offen offset:0\n\t"
    "buffer_load_dwordx4 v[212:215], %[vXo], %[srdX2], s67 offen offset:2048\n\t"
    "buffer_load_dwordx4 v[216:219], %[vXo], %[srdX2], s68 offen offset:0\n\t"
    "buffer_load_dwordx4 v[220:223], %[vXo], %[srdX2], s68 offen offset:2048\n\t"
    "s_add_u32 s61, %[sGg], 8192\n\t"
    "buffer_load_dwordx4 v[32:35], %[vL16], %[srdWS], s61 offen offset:0 sc1\n\t"
    "buffer_load_dwordx4 v[36:39], %[vL16], %[srdWS], s61 offen offset:1024 sc1\n\t"
    "buffer_load_dwordx4 v[40:43], %[vL16], %[srdWS], s61 offen offset:2048 sc1\n\t"
    "buffer_load_dwordx4 v[44:47], %[vL16], %[srdWS], s61 offen offset:3072 sc1\n\t"
    "s_add_u32 s61, %[sGg], 12288\n\t"
    "buffer_load_dwordx4 v[48:51], %[vL16], %[srdWS], s61 offen offset:0 sc1\n\t"
    "buffer_load_dwordx4 v[52:55], %[vL16], %[srdWS], s61 offen offset:1024 sc1\n\t"
    "buffer_load_dwordx4 v[56:59], %[vL16], %[srdWS], s61 offen offset:2048 sc1\n\t"
    "buffer_load_dwordx4 v[60:63], %[vL16], %[srdWS], s61 offen offset:3072 sc1\n\t"
    "s_add_u32 s61, %[sGg], 16384\n\t"
    "buffer_load_dwordx4 v[160:163], %[vL16], %[srdWS], s61 offen offset:0 sc1\n\t"
    "buffer_load_dwordx4 v[164:167], %[vL16], %[srdWS], s61 offen offset:1024 sc1\n\t"
    "buffer_load_dwordx4 v[168:171], %[vL16], %[srdWS], s61 offen offset:2048 sc1\n\t"
    "buffer_load_dwordx4 v[172:175], %[vL16], %[srdWS], s61 offen offset:3072 sc1\n\t"
    "s_add_u32 s61, %[sGg], 20480\n\t"
    "buffer_load_dwordx4 v[176:179], %[vL16], %[srdWS], s61 offen offset:0 sc1\n\t"
    "buffer_load_dwordx4 v[180:183], %[vL16], %[srdWS], s61 offen offset:1024 sc1\n\t"
    "buffer_load_dwordx4 v[184:187], %[vL16], %[srdWS], s61 offen offset:2048 sc1\n\t"
    "buffer_load_dwordx4 v[188:191], %[vL16], %[srdWS], s61 offen offset:3072 sc1\n\t"
    "s_waitcnt vmcnt(0)\n\t"
    "s_mov_b64 vcc, s[56:57]\n\t"
    "v_cndmask_b32_dpp v68, v192, v196, vcc quad_perm:[1,0,3,2] row_mask:0xf bank_mask:0xf\n\t"
    "v_cndmask_b32_dpp v69, v193, v197, vcc quad_perm:[1,0,3,2] row_mask:0xf bank_mask:0xf\n\t"
    "v_cndmask_b32_dpp v70, v194, v198, vcc quad_perm:[1,0,3,2] row_mask:0xf bank_mask:0xf\n\t"
    "v_cndmask_b32_dpp v71, v195, v199, vcc quad_perm:[1,0,3,2] row_mask:0xf bank_mask:0xf\n\t"
    "v_cndmask_b32_dpp v76, v200, v204, vcc quad_perm:[1,0,3,2] row_mask:0xf bank_mask:0xf\n\t"
    "v_cndmask_b32_dpp v77, v201, v205, vcc quad_perm:[1,0,3,2] row_mask:0xf bank_mask:0xf\n\t"
    "v_cndmask_b32_dpp v78, v202, v206, vcc quad_perm:[1,0,3,2] row_mask:0xf bank_mask:0xf\n\t"
    "v_cndmask_b32_dpp v79, v203, v207, vcc quad_perm:[1,0,3,2] row_mask:0xf bank_mask:0xf\n\t"
    "s_not_b64 vcc, s[56:57]\n\t"
    "v_cndmask_b32_dpp v64, v196, v192, vcc quad_perm:[1,0,3,2] row_mask:0xf bank_mask:0xf\n\t"
    "v_cndmask_b32_dpp v65, v197, v193, vcc quad_perm:[1,0,3,2] row_mask:0xf bank_mask:0xf\n\t"
    "v_cndmask_b32_dpp v66, v198, v194, vcc quad_perm:[1,0,3,2] row_mask:0xf bank_mask:0xf\n\t"
    "v_cndmask_b32_dpp v67, v199, v195, vcc quad_perm:[1,0,3,2] row_mask:0xf bank_mask:0xf\n\t"
    "v_cndmask_b32_dpp v72, v204, v200, vcc quad_perm:[1,0,3,2] row_mask:0xf bank_mask:0xf\n\t"
    "v_cndmask_b32_dpp v73, v205, v201, vcc quad_perm:[1,0,3,2] row_mask:0xf bank_mask:0xf\n\t"
    "v_cndmask_b32_dpp v74, v206, v202, vcc quad_perm:[1,0,3,2] row_mask:0xf bank_mask:0xf\n\t"
    "v_cndmask_b32_dpp v75, v207, v203, vcc quad_perm:[1,0,3,2] row_mask:0xf bank_mask:0xf\n\t"
    "s_mov_b64 vcc, s[58:59]\n\t"
    "v_cndmask_b32_dpp v200, v64, v72, vcc quad_perm:[2,3,0,1] row_mask:0xf bank_mask:0xf\n\t"
    "v_cndmask_b32_dpp v201, v65, v73, vcc quad_perm:[2,3,0,1] row_mask:0xf bank_mask:0xf\n\t"
    "v_cndmask_b32_dpp v202, v66, v74, vcc quad_perm:[2,3,0,1] row_mask:0xf bank_mask:0xf\n\t"
    "v_cndmask_b32_dpp v203, v67, v75, vcc quad_perm:[2,3,0,1] row_mask:0xf bank_mask:0xf\n\t"
    "v_cndmask_b32_dpp v204, v68, v76, vcc quad_perm:[2,3,0,1] row_mask:0xf bank_mask:0xf\n\t"
    "v_cndmask_b32_dpp v205, v69, v77, vcc quad_perm:[2,3,0,1] row_mask:0xf bank_mask:0xf\n\t"
    "v_cndmask_b32_dpp v206, v70, v78, vcc quad_perm:[2,3,0,1] row_mask:0xf bank_mask:0xf\n\t"
    "v_cndmask_b32_dpp v207, v71, v79, vcc quad_perm:[2,3,0,1] row_mask:0xf bank_mask:0xf\n\t"
    "s_not_b64 vcc, s[58:59]\n\t"
    "v_cndmask_b32_dpp v192, v72, v64, vcc quad_perm:[2,3,0,1] row_mask:0xf bank_mask:0xf\n\t"
    "v_cndmask_b32_dpp v193, v73, v65, vcc quad_perm:[2,3,0,1] row_mask:0xf bank_mask:0xf\n\t"
    "v_cndmask_b32_dpp v194, v74, v66, vcc quad_perm:[2,3,0,1] row_mask:0xf bank_mask:0xf\n\t"
    "v_cndmask_b32_dpp v195, v75, v67, vcc quad_perm:[2,3,0,1] row_mask:0xf bank_mask:0xf\n\t"
    "v_cndmask_b32_dpp v196, v76, v68, vcc quad_perm:[2,3,0,1] row_mask:0xf bank_mask:0xf\n\t"
    "v_cndmask_b32_dpp v197, v77, v69, vcc quad_perm:[2,3,0,1] row_mask:0xf bank_mask:0xf\n\t"
    "v_cndmask_b32_dpp v198, v78, v70, vcc quad_perm:[2,3,0,1] row_mask:0xf bank_mask:0xf\n\t"
    "v_cndmask_b32_dpp v199, v79, v71, vcc quad_perm:[2,3,0,1] row_mask:0xf bank_mask:0xf\n\t"
    "v_lshlrev_b32 v64, 16, v192\n\t"
    "v_and_b32 v65, s64, v192\n\t"
    "v_lshlrev_b32 v66, 16, v193\n\t"
    "v_and_b32 v67, s64, v193\n\t"
    "v_lshlrev_b32 v68, 16, v194\n\t"
    "v_and_b32 v69, s64, v194\n\t"
    "v_lshlrev_b32 v70, 16, v195\n\t"
    "v_and_b32 v71, s64, v195\n\t"
    "v_lshlrev_b32 v72, 16, v196\n\t"
    "v_and_b32 v73, s64, v196\n\t"
    "v_lshlrev_b32 v74, 16, v197\n\t"
    "v_and_b32 v75, s64, v197\n\t"
    "v_lshlrev_b32 v76, 16, v198\n\t"
    "v_and_b32 v77, s64, v198\n\t"
    "v_lshlrev_b32 v78, 16, v199\n\t"
    "v_and_b32 v79, s64, v199\n\t"
    "v_lshlrev_b32 v80, 16, v200\n\t"
    "v_and_b32 v81, s64, v200\n\t"
    "v_lshlrev_b32 v82, 16, v201\n\t"
    "v_and_b32 v83, s64, v201\n\t"
    "v_lshlrev_b32 v84, 16, v202\n\t"
    "v_and_b32 v85, s64, v202\n\t"
    "v_lshlrev_b32 v86, 16, v203\n\t"
    "v_and_b32 v87, s64, v203\n\t"
    "v_lshlrev_b32 v88, 16, v204\n\t"
    "v_and_b32 v89, s64, v204\n\t"
    "v_lshlrev_b32 v90, 16, v205\n\t"
    "k_pgb4: s_cbranch_execz k_pgb_back\n\t"
    "v_and_b32 v91, s64, v205\n\t"
    "v_lshlrev_b32 v92, 16, v206\n\t"
    "v_and_b32 v93, s64, v206\n\t"
    "v_lshlrev_b32 v94, 16, v207\n\t"
    "v_and_b32 v95, s64, v207\n\t"
    "s_mov_b64 vcc, s[56:57]\n\t"
    "v_cndmask_b32_dpp v100, v208, v212, vcc quad_perm:[1,0,3,2] row_mask:0xf bank_mask:0xf\n\t"
    "v_cndmask_b32_dpp v101, v209, v213, vcc quad_perm:[1,0,3,2] row_mask:0xf bank_mask:0xf\n\t"
    "v_cndmask_b32_dpp v102, v210, v214, vcc quad_perm:[1,0,3,2] row_mask:0xf bank_mask:0xf\n\t"
    "v_cndmask_b32_dpp v103, v211, v215, vcc quad_perm:[1,0,3,2] row_mask:0xf bank_mask:0xf\n\t"
    "v_cndmask_b32_dpp v108, v216, v220, vcc quad_perm:[1,0,3,2] row_mask:0xf bank_mask:0xf\n\t"
    "v_cndmask_b32_dpp v109, v217, v221, vcc quad_perm:[1,0,3,2] row_mask:0xf bank_mask:0xf\n\t"
    "v_cndmask_b32_dpp v110, v218, v222, vcc quad_perm:[1,0,3,2] row_mask:0xf bank_mask:0xf\n\t"
    "v_cndmask_b32_dpp v111, v219, v223, vcc quad_perm:[1,0,3,2] row_mask:0xf bank_mask:0xf\n\t"
    "s_not_b64 vcc, s[56:57]\n\t"
    "v_cndmask_b32_dpp v96, v212, v208, vcc quad_perm:[1,0,3,2] row_mask:0xf bank_mask:0xf\n\t"
    "v_cndmask_b32_dpp v97, v213, v209, vcc quad_perm:[1,0,3,2] row_mask:0xf bank_mask:0xf\n\t"
    "v_cndmask_b32_dpp v98, v214, v210, vcc quad_perm:[1,0,3,2] row_mask:0xf bank_mask:0xf\n\t"
    "v_cndmask_b32_dpp v99, v215, v211, vcc quad_perm:[1,0,3,2] row_mask:0xf bank_mask:0xf\n\t"
    "v_cndmask_b32_dpp v104, v220, v216, vcc quad_perm:[1,0,3,2] row_mask:0xf bank_mask:0xf\n\t"
    "v_cndmask_b32_dpp v105, v221, v217, vcc quad_perm:[1,0,3,2] row_mask:0xf bank_mask:0xf\n\t"
    "v_cndmask_b32_dpp v106, v222, v218, vcc quad_perm:[1,0,3,2] row_mask:0xf bank_mask:0xf\n\t"
    "v_cndmask_b32_dpp v107, v223, v219, vcc quad_perm:[1,0,3,2] row_mask:0xf bank_mask:0xf\n\t"
    "s_mov_b64 vcc, s[58:59]\n\t"
    "v_cndmask_b32_dpp v216, v96, v104, vcc quad_perm:[2,3,0,1] row_mask:0xf bank_mask:0xf\n\t"
    "v_cndmask_b32_dpp v217, v97, v105, vcc quad_perm:[2,3,0,1] row_mask:0xf bank_mask:0xf\n\t"
    "v_cndmask_b32_dpp v218, v98, v106, vcc quad_perm:[2,3,0,1] row_mask:0xf bank_mask:0xf\n\t"
    "v_cndmask_b32_dpp v219, v99, v107, vcc quad_perm:[2,3,0,1] row_mask:0xf bank_mask:0xf\n\t"
    "v_cndmask_b32_dpp v220, v100, v108, vcc quad_perm:[2,3,0,1] row_mask:0xf bank_mask:0xf\n\t"
    "v_cndmask_b32_dpp v221, v101, v109, vcc quad_perm:[2,3,0,1] row_mask:0xf bank_mask:0xf\n\t"
    "v_cndmask_b32_dpp v222, v102, v110, vcc quad_perm:[2,3,0,1] row_mask:0xf bank_mask:0xf\n\t"
    "v_cndmask_b32_dpp v223, v103, v111, vcc quad_perm:[2,3,0,1] row_mask:0xf bank_mask:0xf\n\t"
    "s_not_b64 vcc, s[58:59]\n\t"
    "v_cndmask_b32_dpp v208, v104, v96, vcc quad_perm:[2,3,0,1] row_mask:0xf bank_mask:0xf\n\t"
    "v_cndmask_b32_dpp v209, v105, v97, vcc quad_perm:[2,3,0,1] row_mask:0xf bank_mask:0xf\n\t"
    "v_cndmask_b32_dpp v210, v106, v98, vcc quad_perm:[2,3,0,1] row_mask:0xf bank_mask:0xf\n\t"
    "v_cndmask_b32_dpp v211, v107, v99, vcc quad_perm:[2,3,0,1] row_mask:0xf bank_mask:0xf\n\t"
    "v_cndmask_b32_dpp v212, v108, v100, vcc quad_perm:[2,3,0,1] row_mask:0xf bank_mask:0xf\n\t"
    "v_cndmask_b32_dpp v213, v109, v101, vcc quad_perm:[2,3,0,1] row_mask:0xf bank_mask:0xf\n\t"
    "v_cndmask_b32_dpp v214, v110, v102, vcc quad_perm:[2,3,0,1] row_mask:0xf bank_mask:0xf\n\t"
    "v_cndmask_b32_dpp v215, v111, v103, vcc quad_perm:[2,3,0,1] row_mask:0xf bank_mask:0xf\n\t"
    "v_lshlrev_b32 v96, 16, v208\n\t"
    "v_and_b32 v97, s64, v208\n\t"
    "v_lshlrev_b32 v98, 16, v209\n\t"
    "v_and_b32 v99, s64, v209\n\t"
    "v_lshlrev_b32 v100, 16, v210\n\t"
    "v_and_b32 v101, s64, v210\n\t"
    "v_lshlrev_b32 v102, 16, v211\n\t"
    "v_and_b32 v103, s64, v211\n\t"
    "v_lshlrev_b32 v104, 16, v212\n\t"
    "v_and_b32 v105, s64, v212\n\t"
    "v_lshlrev_b32 v106, 16, v213\n\t"
    "v_and_b32 v107, s64, v213\n\t"
    "v_lshlrev_b32 v108, 16, v214\n\t"
    "v_and_b32 v109, s64, v214\n\t"
    "v_lshlrev_b32 v110, 16, v215\n\t"
    "v_and_b32 v111, s64, v215\n\t"
    "v_lshlrev_b32 v112, 16, v216\n\t"
    "v_and_b32 v113, s64, v216\n\t"
    "v_lshlrev_b32 v114, 16, v217\n\t"
    "v_and_b32 v115, s64, v217\n\t"
    "v_lshlrev_b32 v116, 16, v218\n\t"
    "v_and_b32 v117, s64, v218\n\t"
    "v_lshlrev_b32 v118, 16, v219\n\t"
    "v_and_b32 v119, s64, v219\n\t"
    "v_lshlrev_b32 v120, 16, v220\n\t"
    "v_and_b32 v121, s64, v220\n\t"
    "v_lshlrev_b32 v122, 16, v221\n\t"
    "v_and_b32 v123, s64, v221\n\t"
    "v_lshlrev_b32 v124, 16, v222\n\t"
    "v_and_b32 v125, s64, v222\n\t"
    "v_lshlrev_b32 v126, 16, v223\n\t"
    "v_and_b32 v127, s64, v223\n\t"
    "s_waitcnt vmcnt(0)\n\t"
    "v_lshlrev_b32 v128, 16, v224\n\t"
    "v_and_b32 v129, s64, v224\n\t"
    "v_lshlrev_b32 v130, 16, v225\n\t"
    "v_and_b32 v131, s64, v225\n\t"
    "v_lshlrev_b32 v132, 16, v226\n\t"
    "v_and_b32 v133, s64, v226\n\t"
    "v_lshlrev_b32 v134, 16, v227\n\t"
    "v_and_b32 v135, s64, v227\n\t"
    "v_pk_fma_f32 v[128:129], v[128:129], v[32:33], v[64:65]\n\t"
    "v_pk_fma_f32 v[130:131], v[130:131], v[34:35], v[66:67]\n\t"
    "v_pk_fma_f32 v[132:133], v[132:133], v[36:37], v[68:69]\n\t"
    "v_pk_fma_f32 v[134:135], v[134:135], v[38:39], v[70:71]\n\t"
    "v_cvt_pk_bf16_f32 v224, v128, v129\n\t"
    "v_cvt_pk_bf16_f32 v225, v130, v131\n\t"
    "v_cvt_pk_bf16_f32 v226, v132, v133\n\t"
    "v_cvt_pk_bf16_f32 v227, v134, v135\n\t"
    "v_lshlrev_b32 v136, 16, v228\n\t"
    "v_and_b32 v137, s64, v228\n\t"
    "v_lshlrev_b32 v138, 16, v229\n\t"
    "v_and_b32 v139, s64, v229\n\t"
    "v_lshlrev_b32 v140, 16, v230\n\t"
    "v_and_b32 v141, s64, v230\n\t"
    "v_lshlrev_b32 v142, 16, v231\n\t"
    "v_and_b32 v143, s64, v231\n\t"
    "v_pk_fma_f32 v[136:137], v[136:137], v[40:41], v[72:73]\n\t"
    "v_pk_fma_f32 v[138:139], v[138:139], v[42:43], v[74:75]\n\t"
    "v_pk_fma_f32 v[140:141], v[140:141], v[44:45], v[76:77]\n\t"
    "v_pk_fma_f32 v[142:143], v[142:143], v[46:47], v[78:79]\n\t"
    "v_cvt_pk_bf16_f32 v228, v136, v137\n\t"
    "v_cvt_pk_bf16_f32 v229, v138, v139\n\t"
    "v_cvt_pk_bf16_f32 v230, v140, v141\n\t"
    "v_cvt_pk_bf16_f32 v231, v142, v143\n\t"
    "v_lshlrev_b32 v192, 16, v232\n\t"
    "v_and_b32 v193, s64, v232\n\t"
    "v_lshlrev_b32 v194, 16, v233\n\t"
    "v_and_b32 v195, s64, v233\n\t"
    "v_lshlrev_b32 v196, 16, v234\n\t"
    "v_and_b32 v197, s64, v234\n\t"
    "v_lshlrev_b32 v198, 16, v235\n\t"
    "v_and_b32 v199, s64, v235\n\t"
    "v_pk_fma_f32 v[192:193], v[192:193], v[48:49], v[80:81]\n\t"
    "v_pk_fma_f32 v[194:195], v[194:195], v[50:51], v[82:83]\n\t"
    "v_pk_fma_f32 v[196:197], v[196:197], v[52:53], v[84:85]\n\t"
    "v_pk_fma_f32 v[198:199], v[198:199], v[54:55], v[86:87]\n\t"
    "v_cvt_pk_bf16_f32 v232, v192, v193\n\t"
    "v_cvt_pk_bf16_f32 v233, v194, v195\n\t"
    "v_cvt_pk_bf16_f32 v234, v196, v197\n\t"
    "v_cvt_pk_bf16_f32 v235, v198, v199\n\t"
    "v_lshlrev_b32 v200, 16, v236\n\t"
    "v_and_b32 v201, s64, v236\n\t"
    "v_lshlrev_b32 v202, 16, v237\n\t"
    "v_and_b32 v203, s64, v237\n\t"
    "v_lshlrev_b32 v204, 16, v238\n\t"
    "v_and_b32 v205, s64, v238\n\t"
    "v_lshlrev_b32 v206, 16, v239\n\t"
    "v_and_b32 v207, s64, v239\n\t"
    "v_pk_fma_f32 v[200:201], v[200:201], v[56:57], v[88:89]\n\t"
    "v_pk_fma_f32 v[202:203], v[202:203], v[58:59], v[90:91]\n\t"
    "v_pk_fma_f32 v[204:205], v[204:205], v[60:61], v[92:93]\n\t"
    "v_pk_fma_f32 v[206:207], v[206:207], v[62:63], v[94:95]\n\t"
    "v_cvt_pk_bf16_f32 v236, v200, v201\n\t"
    "v_cvt_pk_bf16_f32 v237, v202, v203\n\t"
    "v_cvt_pk_bf16_f32 v238, v204, v205\n\t"
    "v_cvt_pk_bf16_f32 v239, v206, v207\n\t"
    "s_mov_b64 vcc, s[56:57]\n\t"
    "v_cndmask_b32_dpp v68, v224, v228, vcc quad_perm:[1,0,3,2] row_mask:0xf bank_mask:0xf\n\t"
    "v_cndmask_b32_dpp v69, v225, v229, vcc quad_perm:[1,0,3,2] row_mask:0xf bank_mask:0xf\n\t"
    "v_cndmask_b32_dpp v70, v226, v230, vcc quad_perm:[1,0,3,2] row_mask:0xf bank_mask:0xf\n\t"
    "v_cndmask_b32_dpp v71, v227, v231, vcc quad_perm:[1,0,3,2] row_mask:0xf bank_mask:0xf\n\t"
    "v_cndmask_b32_dpp v76, v232, v236, vcc quad_perm:[1,0,3,2] row_mask:0xf bank_mask:0xf\n\t"
    "v_cndmask_b32_dpp v77, v233, v237, vcc quad_perm:[1,0,3,2] row_mask:0xf bank_mask:0xf\n\t"
    "v_cndmask_b32_dpp v78, v234, v238, vcc quad_perm:[1,0,3,2] row_mask:0xf bank_mask:0xf\n\t"
    "v_cndmask_b32_dpp v79, v235, v239, vcc quad_perm:[1,0,3,2] row_mask:0xf bank_mask:0xf\n\t"
    "s_not_b64 vcc, s[56:57]\n\t"
    "v_cndmask_b32_dpp v64, v228, v224, vcc quad_perm:[1,0,3,2] row_mask:0xf bank_mask:0xf\n\t"
    "v_cndmask_b32_dpp v65, v229, v225, vcc quad_perm:[1,0,3,2] row_mask:0xf bank_mask:0xf\n\t"
    "v_cndmask_b32_dpp v66, v230, v226, vcc quad_perm:[1,0,3,2] row_mask:0xf bank_mask:0xf\n\t"
    "v_cndmask_b32_dpp v67, v231, v227, vcc quad_perm:[1,0,3,2] row_mask:0xf bank_mask:0xf\n\t"
    "v_cndmask_b32_dpp v72, v236, v232, vcc quad_perm:[1,0,3,2] row_mask:0xf bank_mask:0xf\n\t"
    "v_cndmask_b32_dpp v73, v237, v233, vcc quad_perm:[1,0,3,2] row_mask:0xf bank_mask:0xf\n\t"
    "v_cndmask_b32_dpp v74, v238, v234, vcc quad_perm:[1,0,3,2] row_mask:0xf bank_mask:0xf\n\t"
    "v_cndmask_b32_dpp v75, v239, v235, vcc quad_perm:[1,0,3,2] row_mask:0xf bank_mask:0xf\n\t"
    "s_mov_b64 vcc, s[58:59]\n\t"
    "v_cndmask_b32_dpp v232, v64, v72, vcc quad_perm:[2,3,0,1] row_mask:0xf bank_mask:0xf\n\t"
    "v_cndmask_b32_dpp v233, v65, v73, vcc quad_perm:[2,3,0,1] row_mask:0xf bank_mask:0xf\n\t"
    "v_cndmask_b32_dpp v234, v66, v74, vcc quad_perm:[2,3,0,1] row_mask:0xf bank_mask:0xf\n\t"
    "v_cndmask_b32_dpp v235, v67, v75, vcc quad_perm:[2,3,0,1] row_mask:0xf bank_mask:0xf\n\t"
    "v_cndmask_b32_dpp v236, v68, v76, vcc quad_perm:[2,3,0,1] row_mask:0xf bank_mask:0xf\n\t"
    "v_cndmask_b32_dpp v237, v69, v77, vcc quad_perm:[2,3,0,1] row_mask:0xf bank_mask:0xf\n\t"
    "v_cndmask_b32_dpp v238, v70, v78, vcc quad_perm:[2,3,0,1] row_mask:0xf bank_mask:0xf\n\t"
    "v_cndmask_b32_dpp v239, v71, v79, vcc quad_perm:[2,3,0,1] row_mask:0xf bank_mask:0xf\n\t"
    "s_not_b64 vcc, s[58:59]\n\t"
    "v_cndmask_b32_dpp v224, v72, v64, vcc quad_perm:[2,3,0,1] row_mask:0xf bank_mask:0xf\n\t"
    "v_cndmask_b32_dpp v225, v73, v65, vcc quad_perm:[2,3,0,1] row_mask:0xf bank_mask:0xf\n\t"
    "v_cndmask_b32_dpp v226, v74, v66, vcc quad_perm:[2,3,0,1] row_mask:0xf bank_mask:0xf\n\t"
    "v_cndmask_b32_dpp v227, v75, v67, vcc quad_perm:[2,3,0,1] row_mask:0xf bank_mask:0xf\n\t"
    "v_cndmask_b32_dpp v228, v76, v68, vcc quad_perm:[2,3,0,1] row_mask:0xf bank_mask:0xf\n\t"
    "v_cndmask_b32_dpp v229, v77, v69, vcc quad_perm:[2,3,0,1] row_mask:0xf bank_mask:0xf\n\t"
    "v_cndmask_b32_dpp v230, v78, v70, vcc quad_perm:[2,3,0,1] row_mask:0xf bank_mask:0xf\n\t"
    "v_cndmask_b32_dpp v231, v79, v71, vcc quad_perm:[2,3,0,1] row_mask:0xf bank_mask:0xf\n\t"
    "buffer_store_dwordx4 v[224:227], %[vXo], %[srdOut], %[sXo] offen offset:0 sc1\n\t"
    "buffer_store_dwordx4 v[228:231], %[vXo], %[srdOut], %[sXo] offen offset:2048 sc1\n\t"
    "buffer_store_dwordx4 v[232:235], %[vXo], %[srdOut], s66 offen offset:0 sc1\n\t"
    "buffer_store_dwordx4 v[236:239], %[vXo], %[srdOut], s66 offen offset:2048 sc1\n\t"
    "v_lshlrev_b32 v144, 16, v240\n\t"
    "v_and_b32 v145, s64, v240\n\t"
    "v_lshlrev_b32 v146, 16, v241\n\t"
    "v_and_b32 v147, s64, v241\n\t"
    "v_lshlrev_b32 v148, 16, v242\n\t"
    "v_and_b32 v149, s64, v242\n\t"
    "v_lshlrev_b32 v150, 16, v243\n\t"
    "v_and_b32 v151, s64, v243\n\t"
    "v_pk_fma_f32 v[144:145], v[144:145], v[160:161], v[96:97]\n\t"
    "v_pk_fma_f32 v[146:147], v[146:147], v[162:163], v[98:99]\n\t"
    "v_pk_fma_f32 v[148:149], v[148:149], v[164:165], v[100:101]\n\t"
    "v_pk_fma_f32 v[150:151], v[150:151], v[166:167], v[102:103]\n\t"
    "v_cvt_pk_bf16_f32 v240, v144, v145\n\t"
    "v_cvt_pk_bf16_f32 v241, v146, v147\n\t"
    "v_cvt_pk_bf16_f32 v242, v148, v149\n\t"
    "v_cvt_pk_bf16_f32 v243, v150, v151\n\t"
    "v_lshlrev_b32 v152, 16, v244\n\t"
    "v_and_b32 v153, s64, v244\n\t"
    "v_lshlrev_b32 v154, 16, v245\n\t"
    "v_and_b32 v155, s64, v245\n\t"
    "v_lshlrev_b32 v156, 16, v246\n\t"
    "v_and_b32 v157, s64, v246\n\t"
    "v_lshlrev_b32 v158, 16, v247\n\t"
    "v_and_b32 v159, s64, v247\n\t"
    "v_pk_fma_f32 v[152:153], v[152:153], v[168:169], v[104:105]\n\t"
    "v_pk_fma_f32 v[154:155], v[154:155], v[170:171], v[106:107]\n\t"
    "v_pk_fma_f32 v[156:157], v[156:157], v[172:173], v[108:109]\n\t"
    "v_pk_fma_f32 v[158:159], v[158:159], v[174:175], v[110:111]\n\t"
    "v_cvt_pk_bf16_f32 v244, v152, v153\n\t"
    "v_cvt_pk_bf16_f32 v245, v154, v155\n\t"
    "v_cvt_pk_bf16_f32 v246, v156, v157\n\t"
    "v_cvt_pk_bf16_f32 v247, v158, v159\n\t"
    "v_lshlrev_b32 v208, 16, v248\n\t"
    "v_and_b32 v209, s64, v248\n\t"
    "v_lshlrev_b32 v210, 16, v249\n\t"
    "v_and_b32 v211, s64, v249\n\t"
    "v_lshlrev_b32 v212, 16, v250\n\t"
    "v_and_b32 v213, s64, v250\n\t"
    "v_lshlrev_b32 v214, 16, v251\n\t"
    "v_and_b32 v215, s64, v251\n\t"
    "v_pk_fma_f32 v[208:209], v[208:209], v[176:177], v[112:113]\n\t"
    "v_pk_fma_f32 v[210:211], v[210:211], v[178:179], v[114:115]\n\t"
    "v_pk_fma_f32 v[212:213], v[212:213], v[180:181], v[116:117]\n\t"
    "v_pk_fma_f32 v[214:215], v[214:215], v[182:183], v[118:119]\n\t"
    "v_cvt_pk_bf16_f32 v248, v208, v209\n\t"
    "v_cvt_pk_bf16_f32 v249, v210, v211\n\t"
    "v_cvt_pk_bf16_f32 v250, v212, v213\n\t"
    "v_cvt_pk_bf16_f32 v251, v214, v215\n\t"
    "v_lshlrev_b32 v216, 16, v252\n\t"
    "v_and_b32 v217, s64, v252\n\t"
    "v_lshlrev_b32 v218, 16, v253\n\t"
    "v_and_b32 v219, s64, v253\n\t"
    "v_lshlrev_b32 v220, 16, v254\n\t"
    "v_and_b32 v221, s64, v254\n\t"
    "v_lshlrev_b32 v222, 16, v255\n\t"
    "v_and_b32 v223, s64, v255\n\t"
    "v_pk_fma_f32 v[216:217], v[216:217], v[184:185], v[120:121]\n\t"
    "v_pk_fma_f32 v[218:219], v[218:219], v[186:187], v[122:123]\n\t"
    "v_pk_fma_f32 v[220:221], v[220:221], v[188:189], v[124:125]\n\t"
    "v_pk_fma_f32 v[222:223], v[222:223], v[190:191], v[126:127]\n\t"
    "v_cvt_pk_bf16_f32 v252, v216, v217\n\t"
    "v_cvt_pk_bf16_f32 v253, v218, v219\n\t"
    "v_cvt_pk_bf16_f32 v254, v220, v221\n\t"
    "v_cvt_pk_bf16_f32 v255, v222, v223\n\t"
    "s_mov_b64 vcc, s[56:57]\n\t"
    "v_cndmask_b32_dpp v100, v240, v244, vcc quad_perm:[1,0,3,2] row_mask:0xf bank_mask:0xf\n\t"
    "v_cndmask_b32_dpp v101, v241, v245, vcc quad_perm:[1,0,3,2] row_mask:0xf bank_mask:0xf\n\t"
    "v_cndmask_b32_dpp v102, v242, v246, vcc quad_perm:[1,0,3,2] row_mask:0xf bank_mask:0xf\n\t"
    "v_cndmask_b32_dpp v103, v243, v247, vcc quad_perm:[1,0,3,2] row_mask:0xf bank_mask:0xf\n\t"
    "v_cndmask_b32_dpp v108, v248, v252, vcc quad_perm:[1,0,3,2] row_mask:0xf bank_mask:0xf\n\t"
    "v_cndmask_b32_dpp v109, v249, v253, vcc quad_perm:[1,0,3,2] row_mask:0xf bank_mask:0xf\n\t"
    "v_cndmask_b32_dpp v110, v250, v254, vcc quad_perm:[1,0,3,2] row_mask:0xf bank_mask:0xf\n\t"
    "v_cndmask_b32_dpp v111, v251, v255, vcc quad_perm:[1,0,3,2] row_mask:0xf bank_mask:0xf\n\t"
    "s_not_b64 vcc, s[56:57]\n\t"
    "v_cndmask_b32_dpp v96, v244, v240, vcc quad_perm:[1,0,3,2] row_mask:0xf bank_mask:0xf\n\t"
    "v_cndmask_b32_dpp v97, v245, v241, vcc quad_perm:[1,0,3,2] row_mask:0xf bank_mask:0xf\n\t"
    "v_cndmask_b32_dpp v98, v246, v242, vcc quad_perm:[1,0,3,2] row_mask:0xf bank_mask:0xf\n\t"
    "v_cndmask_b32_dpp v99, v247, v243, vcc quad_perm:[1,0,3,2] row_mask:0xf bank_mask:0xf\n\t"
    "v_cndmask_b32_dpp v104, v252, v248, vcc quad_perm:[1,0,3,2] row_mask:0xf bank_mask:0xf\n\t"
    "v_cndmask_b32_dpp v105, v253, v249, vcc quad_perm:[1,0,3,2] row_mask:0xf bank_mask:0xf\n\t"
    "v_cndmask_b32_dpp v106, v254, v250, vcc quad_perm:[1,0,3,2] row_mask:0xf bank_mask:0xf\n\t"
    "v_cndmask_b32_dpp v107, v255, v251, vcc quad_perm:[1,0,3,2] row_mask:0xf bank_mask:0xf\n\t"
    "s_mov_b64 vcc, s[58:59]\n\t"
    "v_cndmask_b32_dpp v248, v96, v104, vcc quad_perm:[2,3,0,1] row_mask:0xf bank_mask:0xf\n\t"
    "v_cndmask_b32_dpp v249, v97, v105, vcc quad_perm:[2,3,0,1] row_mask:0xf bank_mask:0xf\n\t"
    "v_cndmask_b32_dpp v250, v98, v106, vcc quad_perm:[2,3,0,1] row_mask:0xf bank_mask:0xf\n\t"
    "v_cndmask_b32_dpp v251, v99, v107, vcc quad_perm:[2,3,0,1] row_mask:0xf bank_mask:0xf\n\t"
    "v_cndmask_b32_dpp v252, v100, v108, vcc quad_perm:[2,3,0,1] row_mask:0xf bank_mask:0xf\n\t"
    "v_cndmask_b32_dpp v253, v101, v109, vcc quad_perm:[2,3,0,1] row_mask:0xf bank_mask:0xf\n\t"
    "v_cndmask_b32_dpp v254, v102, v110, vcc quad_perm:[2,3,0,1] row_mask:0xf bank_mask:0xf\n\t"
    "v_cndmask_b32_dpp v255, v103, v111, vcc quad_perm:[2,3,0,1] row_mask:0xf bank_mask:0xf\n\t"
    "s_not_b64 vcc, s[58:59]\n\t"
    "v_cndmask_b32_dpp v240, v104, v96, vcc quad_perm:[2,3,0,1] row_mask:0xf bank_mask:0xf\n\t"
    "v_cndmask_b32_dpp v241, v105, v97, vcc quad_perm:[2,3,0,1] row_mask:0xf bank_mask:0xf\n\t"
    "v_cndmask_b32_dpp v242, v106, v98, vcc quad_perm:[2,3,0,1] row_mask:0xf bank_mask:0xf\n\t"
    "v_cndmask_b32_dpp v243, v107, v99, vcc quad_perm:[2,3,0,1] row_mask:0xf bank_mask:0xf\n\t"
    "v_cndmask_b32_dpp v244, v108, v100, vcc quad_perm:[2,3,0,1] row_mask:0xf bank_mask:0xf\n\t"
    "v_cndmask_b32_dpp v245, v109, v101, vcc quad_perm:[2,3,0,1] row_mask:0xf bank_mask:0xf\n\t"
    "v_cndmask_b32_dpp v246, v110, v102, vcc quad_perm:[2,3,0,1] row_mask:0xf bank_mask:0xf\n\t"
    "v_cndmask_b32_dpp v247, v111, v103, vcc quad_perm:[2,3,0,1] row_mask:0xf bank_mask:0xf\n\t"
    "buffer_store_dwordx4 v[240:243], %[vXo], %[srdOut], s67 offen offset:0 sc1\n\t"
    "buffer_store_dwordx4 v[244:247], %[vXo], %[srdOut], s67 offen offset:2048 sc1\n\t"
    "buffer_store_dwordx4 v[248:251], %[vXo], %[srdOut], s68 offen offset:0 sc1\n\t"
    "buffer_store_dwordx4 v[252:255], %[vXo], %[srdOut], s68 offen offset:2048 sc1\n\t"
    "L_k2_end_%=:\n\t"
    "k_code_end:\n\t"
      : [sWk] "+&s"(sWk), [sA] "+&s"(sA), [sIt] "+&s"(sIt)
      : [srdW] "s"(srdW), [srdWS] "s"(srdWS), [srdX] "s"(srdX), [srdX2] "s"(srdX2), [srdOut] "s"(srdOut),
        [vW0] "v"(vW0), [vA0] "v"(vA0), [vA1] "v"(vA1), [vRd] "v"(vRd), [vL16] "v"(vL16), [vXo] "v"(vXo), [vXr] "v"(vXr),
        [sXl] "s"(sXl), [sXr] "s"(sXr), [sLds] "s"(sLds), [sG] "s"(g), [sCnt] "s"(sCnt), [sGg] "s"(sGg), [sXo] "s"(sXo), [sRedW] "s"(sRedW), [sRedR] "s"(sRedR),
        [sRw] "s"(rw), [sRk] "s"(4 * rk), [sAi1] "s"(sAi1), [sSig] "s"(sSig), [sRdy] "s"(sRdy)
      : "memory", "scc", "vcc",
      "s52","s53","s54","s55","s56","s57","s58","s59","s60","s61","s62","s63","s64","s65","s66","s67","s68","s69","s84","s85","s86","s87","s88","s89",
      "s90","s91","s92","s93","s94","s95","s96","s97","s98","s99",
      "v23","v24","v25","v26","v27","v28","v29","v30","v31","v32","v33","v34","v35","v36","v37","v38","v39","v40","v41","v42","v43","v44","v45","v46",
      "v47","v48","v49","v50","v51","v52","v53","v54","v55","v56","v57","v58","v59","v60","v61","v62","v63","v64","v65","v66","v67","v68","v69","v70",
      "v71","v72","v73","v74","v75","v76","v77","v78","v79","v80","v81","v82","v83","v84","v85","v86","v87","v88","v89","v90","v91","v92","v93","v94",
      "v95","v96","v97","v98","v99","v100","v101","v102","v103","v104","v105","v106","v107","v108","v109","v110","v111","v112","v113","v114","v115","v116","v117","v118",
      "v119","v120","v121","v122","v123","v124","v125","v126","v127","v128","v129","v130","v131","v132","v133","v134","v135","v136","v137","v138","v139","v140","v141","v142",
      "v143","v144","v145","v146","v147","v148","v149","v150","v151","v152","v153","v154","v155","v156","v157","v158","v159","v160","v161","v162","v163","v164","v165","v166",
      "v167","v168","v169","v170","v171","v172","v173","v174","v175","v176","v177","v178","v179","v180","v181","v182","v183","v184","v185","v186","v187","v188","v189","v190",
      "v191","v192","v193","v194","v195","v196","v197","v198","v199","v200","v201","v202","v203","v204","v205","v206","v207","v208","v209","v210","v211","v212","v213","v214",
      "v215","v216","v217","v218","v219","v220","v221","v222","v223","v224","v225","v226","v227","v228","v229","v230","v231","v232","v233","v234","v235","v236","v237","v238",
      "v239","v240","v241","v242","v243","v244","v245","v246","v247","v248","v249","v250","v251","v252","v253","v254","v255",
      "a0","a1","a2","a3","a4","a5","a6","a7","a8","a9","a10","a11","a12","a13","a14","a15","a16","a17","a18","a19","a20","a21","a22","a23",
      "a24","a25","a26","a27","a28","a29","a30","a31","a32","a33","a34","a35","a36","a37","a38","a39","a40","a41","a42","a43","a44","a45","a46","a47",
      "a48","a49","a50","a51","a52","a53","a54","a55","a56","a57","a58","a59","a60","a61","a62","a63","a64","a65","a66","a67","a68","a69","a70","a71",
      "a72","a73","a74","a75","a76","a77","a78","a79","a80","a81","a82","a83","a84","a85","a86","a87","a88","a89","a90","a91","a92","a93","a94","a95",
      "a96","a97","a98","a99","a100","a101","a102","a103","a104","a105","a106","a107","a108","a109","a110","a111","a112","a113","a114","a115","a116","a117","a118","a119",
      "a120","a121","a122","a123","a124","a125","a126","a127");
  }
}

extern "C" void kernel_launch(void* const* d_in, const int* in_sizes, int n_in, void* d_out, int out_size, void* d_ws, size_t ws_size, hipStream_t stream) {
  const u16* x = (const u16*)d_in[0]; const u16* kc = (const u16*)d_in[1]; const u16* vc = (const u16*)d_in[2];
  const u16* wq = (const u16*)d_in[3]; const u16* wo = (const u16*)d_in[4]; const u16* w1 = (const u16*)d_in[6]; const u16* w2 = (const u16*)d_in[7];
  char* ws = (char*)d_ws;
  prep_kernel<<<448, 256, 0, stream>>>(x, kc, vc, ws);
  main_kernel<<<256, 256, 0, stream>>>(x, wq, w1, wo, w2, ws, (u16*)d_out);
}
```
